```python
import torch

def _softmax(x, axis=-1):
    return torch.softmax(x, dim=axis)
def _gelu(x):
    return torch.nn.functional.gelu(x, approximate="tanh")
def _silu(x):
    return torch.nn.functional.silu(x)
def _sigmoid(x):
    return torch.sigmoid(x)
def _layernorm(x, axis=-1, eps=1e-5):
    m = x.mean(dim=axis, keepdim=True)
    v = x.var(dim=axis, keepdim=True, unbiased=False)
    return (x - m) / torch.sqrt(v + eps)
def _scatter_add(tgt, idx, src, axis=0):
    return tgt.clone().index_add_(axis, idx, src)
def _segment_sum(src, seg, n):
    out = torch.zeros((n,) + src.shape[1:], dtype=src.dtype)
    return out.index_add_(0, seg, src)
def _pad(x, pw):
    flat = tuple(v for pair in reversed(pw) for v in pair)
    return torch.nn.functional.pad(x, flat)

kernel_name = 'gated_mix__compressed_kv_cla__mixer_s_mlp__prompt'

def setup_inputs():

    _w = (lambda a: (lambda t: t.to(torch.bfloat16) if t.is_floating_point() else t)(torch.as_tensor(a)))
    def _A_setup():
        B, H, Sq, Sk, D, M = 1, 16, 2048, 2048, 256, 448
        Dc = 4 * D
        return {
            "x": _w((lambda *s: torch.randn(*s, dtype=torch.bfloat16) * 0.05)(B, Sq, M)),
            "c_kv": _w((lambda *s: torch.randn(*s, dtype=torch.bfloat16) * 1.6)(B, Sk, Dc)),
            "w_q": _w((lambda *s: torch.randn(*s, dtype=torch.bfloat16) * 6.4)(M, H, D) * (M ** -0.5)),
            "w_uk": _w((lambda *s: torch.randn(*s, dtype=torch.bfloat16) * 6.4)(Dc, H, D) * (Dc ** -0.5)),
            "w_uv": _w((lambda *s: torch.randn(*s, dtype=torch.bfloat16) * 1.6)(Dc, H, D) * (Dc ** -0.5)),
            "w_o": _w((lambda *s: torch.randn(*s, dtype=torch.bfloat16) * 1.6)(H, D, M) * ((H * D) ** -0.5)),
        }
    _A_in = _A_setup()

    def _B_setup():
        B, S, D, F = 1, 2048, 448, 1792
        return {
            "x":  _w((lambda *s: torch.randn(*s, dtype=torch.bfloat16) * 0.05)(B, S, D)),
            "w1": _w((lambda *s: torch.randn(*s, dtype=torch.bfloat16) * 3.2)(D, D) * (D ** -0.5)),
            "w2": _w((lambda *s: torch.randn(*s, dtype=torch.bfloat16) * 3.2)(D, D) * (D ** -0.5)),
            "w3": _w((lambda *s: torch.randn(*s, dtype=torch.bfloat16) * 3.2)(D, F) * (D ** -0.5)),
            "w4": _w((lambda *s: torch.randn(*s, dtype=torch.bfloat16) * 1.6)(F, D) * (F ** -0.5)),
        }
    _B_in = _B_setup()

    return {
        "A_x": _A_in["x"],
        "A_c_kv": _A_in["c_kv"],
        "A_w_q": _A_in["w_q"],
        "A_w_uk": _A_in["w_uk"],
        "A_w_uv": _A_in["w_uv"],
        "A_w_o": _A_in["w_o"],
        "B_x": _B_in["x"],
        "B_w1": _B_in["w1"],
        "B_w2": _B_in["w2"],
        "B_w3": _B_in["w3"],
        "B_w4": _B_in["w4"],
        "gate_w": _w((lambda *s: torch.randn(*s, dtype=torch.bfloat16) * 0.05)(448) * 16.0),
    }

def reference(A_x, A_c_kv, A_w_q, A_w_uk, A_w_uv, A_w_o, B_x, B_w1, B_w2, B_w3, B_w4, gate_w):
    def _A_ref(x, c_kv, w_q, w_uk, w_uv, w_o):
        B, Sq, M = x.shape[0], x.shape[1], x.shape[2]
        Sk = c_kv.shape[1]
        H, D = w_q.shape[1], w_q.shape[2]
        q = torch.einsum("bqm,mhd->bhqd", x, w_q)
        k = torch.einsum("bkc,chd->bhkd", c_kv, w_uk)
        v = torch.einsum("bkc,chd->bhkd", c_kv, w_uv)
        scores = torch.einsum("bhqd,bhkd->bhqk", q, k) * (D ** -0.5)
        off = Sk - Sq
        qi = torch.arange(Sq)[:, None]
        kj = torch.arange(Sk)[None, :]
        cmask = kj <= (qi + off)
        scores = torch.where(cmask[None, None], scores, -1e9)
        p = _softmax(scores, axis=-1)
        ctx = torch.einsum("bhqk,bhkd->bhqd", p, v)
        return torch.einsum("bhqd,hdm->bqm", ctx, w_o)

    def _B_ref(x, w1, w2, w3, w4):
        B, S, D = x.shape
        h = _gelu(_layernorm(x, axis=-1) @ w1)
        c = D // 4
        g0 = _pad(h[:, :, 0*c:1*c], ((0, 0), (1, 0), (0, 0)))[:, :S, :]
        g1 = _pad(h[:, :, 1*c:2*c], ((0, 0), (0, 1), (0, 0)))[:, 1:, :]
        g2 = _pad(h[:, :, 2*c:3*c], ((0, 0), (2, 0), (0, 0)))[:, :S, :]
        g3 = _pad(h[:, :, 3*c:   ], ((0, 0), (0, 2), (0, 0)))[:, 2:, :]
        hs = torch.concatenate([g0, g1, g2, g3], axis=-1) @ w2
        x1 = x + hs
        h2 = _gelu(_layernorm(x1, axis=-1) @ w3) @ w4
        return x1 + h2

    x = A_x
    g = _sigmoid((x * gate_w).sum(-1, keepdims=True))
    a = _A_ref(x, A_c_kv, A_w_q, A_w_uk, A_w_uv, A_w_o)
    b = _B_ref(x, B_w1, B_w2, B_w3, B_w4)
    return g * a + (1 - g) * b
```

```cpp
#include <hip/hip_runtime.h>
#include <hip/hip_bf16.h>
typedef unsigned u32;
typedef unsigned long long u64;
typedef __attribute__((ext_vector_type(4))) unsigned u32x4;
typedef __attribute__((ext_vector_type(2))) unsigned u32x2;
typedef __attribute__((ext_vector_type(4))) float f32x4;
typedef __attribute__((ext_vector_type(2))) float f32x2;
typedef __attribute__((ext_vector_type(8))) __bf16 bf16x8;
typedef __attribute__((ext_vector_type(2))) __bf16 bf16x2;
#define DI __device__ __forceinline__
#define MB (1u << 20)
#define OFF_Q (32 * MB)
#define OFF_CTX (48 * MB)
#define OFF_XN (72 * MB)
#define OFF_X1 (74 * MB)
#define OFF_AUX (80 * MB)
#define OFF_WF (96 * MB)
#define OFF_W3 (104 * MB)
#define OFF_H (78 * MB)
#define OFF_W2 (108 * MB)
#define OFF_FL (OFF_AUX + 8192)
#define OFF_P (128 * MB)
#define CSTR 188416

DI f32x4 mma(u32x4 a, u32x4 b, f32x4 c) {
  return __builtin_amdgcn_mfma_f32_16x16x32_bf16(__builtin_bit_cast(bf16x8, a), __builtin_bit_cast(bf16x8, b), c, 0, 0, 0);
}
DI u32 pk2(float a, float b) { f32x2 v = {a, b}; return __builtin_bit_cast(u32, __builtin_convertvector(v, bf16x2)); }
DI float blo(u32 v) { return __builtin_bit_cast(float, v << 16); }
DI float bhi(u32 v) { return __builtin_bit_cast(float, v & 0xffff0000u); }
DI float gelu(float x) {
  float t = x * __builtin_fmaf(x * x, -0.10294324f, -2.3022082f);
  return x * __builtin_amdgcn_rcpf(1.0f + __builtin_amdgcn_exp2f(t));
}
template <class T> DI u32x4 trf(const T* w, int f) {
  u32 sel = (f & 1) ? 0x07060302u : 0x05040100u;
  u32x4 r;
#pragma unroll
  for (int d = 0; d < 4; d++) r[d] = __builtin_amdgcn_perm(w[2 * d + 1][f >> 1], w[2 * d][f >> 1], sel);
  return r;
}
typedef __amdgpu_buffer_rsrc_t rsrc;
DI rsrc mkr(const char* p, u32 n) { return __builtin_amdgcn_make_buffer_rsrc((void*)p, 0, n, 0x20000); }
DI u32x4 bld(rsrc r, u32 v, u32 s) { return __builtin_amdgcn_raw_buffer_load_b128(r, v, s, 0); }
DI float dot2(u32 a, u32 b, float c) { return __builtin_amdgcn_fdot2_f32_bf16(__builtin_bit_cast(bf16x2, a), __builtin_bit_cast(bf16x2, b), c, false); }
DI float xsum(float v) {
  u32 a = __builtin_bit_cast(u32, v);
  auto p = __builtin_amdgcn_permlane32_swap(a, a, false, false);
  a = __builtin_bit_cast(u32, __builtin_bit_cast(float, (u32)p[0]) + __builtin_bit_cast(float, (u32)p[1]));
  auto q = __builtin_amdgcn_permlane16_swap(a, a, false, false);
  return __builtin_bit_cast(float, (u32)q[0]) + __builtin_bit_cast(float, (u32)q[1]);
}

template <class F, class F2> DI void b1(F mid, F2 mid2, char* sm, int tid, int wg, const char* __restrict__ x, const char* __restrict__ w1, const char* __restrict__ gw, char* __restrict__ ws) {
  const int l = tid & 63, w = __builtin_amdgcn_readfirstlane(tid >> 6), i = l & 15, g = l >> 4;
  const int cg = wg & 3, blk = (wg >> 2) * 4 + w;
  const int row = blk * 16 + i + ((cg == 0) ? -1 : (cg == 1) ? 1 : (cg == 2) ? -2 : 2);
  const bool ok = (u32)row < 2048u;
  u32x4 xa[14], wb[4][8];
  const u32x4 gv = *(const u32x4*)(gw + (tid < 56 ? tid : 0) * 16);
#pragma unroll
  for (int s = 0; s < 14; s++) xa[s] = *(const u32x4*)(x + (ok ? row : 0) * 896 + s * 64 + g * 16);
  const char* wp = w1 + (32 * w + 8 * g) * 896 + (112 * cg + 8 * (i < 14 ? i : 13)) * 2;
#pragma unroll
  for (int t = 0; t < 4; t++)
    if (t < 3 || w < 2) {
#pragma unroll
      for (int e = 0; e < 8; e++) wb[t][e] = *(const u32x4*)(wp + (128 * t + e) * 896);
    }
  __builtin_amdgcn_sched_barrier(0);
  if (tid < 56) *(u32x4*)(sm + 114688 + tid * 16) = gv;
  const u32x4 ones = {0x3f803f80u, 0x3f803f80u, 0x3f803f80u, 0x3f803f80u};
  f32x4 t1 = {}, t2 = {};
#pragma unroll
  for (int s = 0; s < 14; s++)
#pragma unroll
    for (int e = 0; e < 4; e++) { t1[e] = dot2(xa[s][e], ones[0], t1[e]); t2[e] = dot2(xa[s][e], xa[s][e], t2[e]); }
  const float mu1 = xsum((t1[0] + t1[1]) + (t1[2] + t1[3])) * (1.0f / 448);
  const float rs1 = __builtin_amdgcn_rsqf(__builtin_fmaxf(xsum((t2[0] + t2[1]) + (t2[2] + t2[3])) * (1.0f / 448) - mu1 * mu1, 0.0f) + 1e-5f);
  f32x4 acc[8] = {}, cs[8] = {}, ga = {};
#pragma unroll
  for (int t = 0; t < 4; t++)
    if (t < 3 || w < 2) {
#pragma unroll
      for (int f = 0; f < 8; f++) {
        const u32x4 wf = trf(wb[t], f);
        *(u32x4*)(sm + ((4 * t + w) * 8 + f) * 1024 + l * 16) = wf;
        cs[f] = mma(wf, ones, cs[f]);
      }
    }
  if (i == 0) {
#pragma unroll
    for (int f = 0; f < 8; f++) *(f32x4*)(sm + 115712 + ((w * 8 + f) * 4 + g) * 16) = cs[f];
  }
  asm volatile("s_waitcnt lgkmcnt(0)\n s_barrier" ::: "memory");
  mid();
  u32x4 wf[2][9];
#pragma unroll
  for (int s = -1; s < 14; s++) {
#pragma unroll
    for (int f = 0; f < 9; f++) {
      if (s < 13) wf[(s + 1) & 1][f] = *(const u32x4*)(f < 8 ? sm + ((s + 1) * 8 + f) * 1024 + l * 16 : sm + 114688 + (s + 1) * 64 + g * 16);
      if (s >= 0) { if (f < 8) acc[f] = mma(wf[s & 1][f], xa[s], acc[f]); else ga = mma(wf[s & 1][8], xa[s], ga); }
      __builtin_amdgcn_sched_barrier(0);
    }
  }
#pragma unroll
  for (int f = 0; f < 8; f++) {
    const f32x4* p = (const f32x4*)(sm + 115712 + (f * 4 + g) * 16);
    cs[f] = (p[0] + p[32]) + (p[64] + p[96]);
  }
  mid2();
  if (cg < 2 && ok && g == 0) *(float*)(ws + OFF_AUX + row * 4) = __builtin_amdgcn_rcpf(1.0f + __builtin_amdgcn_exp2f(-1.44269504f * ga[0]));
  if (tid < 4) *(u32x4*)(ws + OFF_FL + (wg + 128 * tid) * 128) = u32x4{0, 0, 0, 0};
#pragma unroll
  for (int r = 0; r < 4; r++) {
    u32x4 o;
#pragma unroll
    for (int e = 0; e < 4; e++)
      o[e] = ok ? pk2(gelu(rs1 * (acc[2 * e][r] - mu1 * cs[2 * e][r])), gelu(rs1 * (acc[2 * e + 1][r] - mu1 * cs[2 * e + 1][r]))) : 0u;
    const int c0 = 112 * cg + 32 * g + 8 * r;
    if (4 * g + r < 14) *(u32x4*)(ws + OFF_H + ((blk * 14 + (c0 >> 5)) * 64 + ((c0 >> 3) & 3) * 16 + i) * 16) = o;
  }
}

DI void b2(char* sm, int tid, int blk0, const char* __restrict__ x, char* __restrict__ ws) {
  const int l = tid & 63, w = __builtin_amdgcn_readfirstlane(tid >> 6), i = l & 15, g = l >> 4;
  float* red = (float*)(sm + 114688);
  constexpr int D = 6;
  u32x4 wb[D][7];
  f32x4 acc[4][7] = {};
  float og[4];
  const u32 l16 = l * 16;
  const rsrc rw = mkr(ws + OFF_W2, 401408);
#define FR(f) ((f) < 6 ? 6 * w + (f) : 24 + w)
  {
#pragma unroll
    for (int s = 0; s < 14; s++)
      asm volatile("s_mov_b32 m0, %0\n s_nop 0\n global_load_lds_dwordx4 %1, %2" ::"s"((u32)(size_t)sm + (w * 14 + s) * 1024), "v"(l16),
                   "s"(ws + OFF_H + ((blk0 + w) * 14 + s) * 1024) : "memory");
    auto ldw = [&](int s) {
#pragma unroll
      for (int f = 0; f < 7; f++) wb[s % D][f] = bld(rw, l16, (s * 28 + FR(f)) * 1024);
    };
#pragma unroll
    for (int s = 0; s < D - 1; s++) ldw(s);
    __builtin_amdgcn_sched_barrier(0);
    asm volatile("s_waitcnt vmcnt(%0)\n s_barrier" ::"n"(7 * (D - 1)) : "memory");
    const rsrc rx = mkr(x + blk0 * 14336, 57344);
    u32x4 xf[16], id[2];
#pragma unroll
    for (int p = 0; p < 2; p++)
#pragma unroll
      for (int d = 0; d < 4; d++) id[p][d] = ((i >> 1) + 8 * p == 4 * g + d) ? ((i & 1) ? 0x3f800000u : 0x3f80u) : 0u;
    u32x4 a[2][4];
#pragma unroll
    for (int q = 0; q < 4; q++) a[0][q] = *(const u32x4*)(sm + q * 14336 + l16);
#pragma unroll
    for (int s = 0; s < 14; s++) {
      if (s == 12) {
#pragma unroll
        for (int q = 0; q < 4; q++) og[q] = *(const float*)(ws + OFF_AUX + (blk0 + q) * 64 + (u32)(i * 4));
      }
      if (s < 13) {
#pragma unroll
        for (int q = 0; q < 4; q++) a[(s + 1) & 1][q] = *(const u32x4*)(sm + (q * 14 + s + 1) * 1024 + l16);
      }
      __builtin_amdgcn_sched_barrier(0);
#pragma unroll
      for (int f = 0; f < 7; f++) {
        if (s + D - 1 < 14) wb[(s + D - 1) % D][f] = bld(rw, l16, ((s + D - 1) * 28 + FR(f)) * 1024);
        else if ((s + D - 15) * 7 + f < 16) {
          const int k = (s + D - 15) * 7 + f;
          xf[k] = bld(rx, i * 896 + g * 16, (k & 3) * 14336 + (k < 12 ? 3 * w + (k >> 2) : 12 + (w >> 1)) * 64);
        }
#pragma unroll
        for (int q = 0; q < 4; q++) acc[q][f] = mma(wb[s % D][f], a[s & 1][q], acc[q][f]);
        __builtin_amdgcn_sched_barrier(0);
      }
    }
#pragma unroll
    for (int f = 0; f < 7; f++)
#pragma unroll
      for (int q = 0; q < 4; q++) acc[q][f] = mma(f < 6 ? id[f & 1] : (w & 1) ? id[1] : id[0], xf[(f >> 1) * 4 + q], acc[q][f]);
  }
#pragma unroll
  for (int q = 0; q < 4; q++) { og[q] = 1.0f - og[q]; asm volatile("" : "+v"(og[q])); }
#pragma unroll
  for (int q = 0; q < 4; q++) {
    f32x4 t = acc[q][0];
#pragma unroll
    for (int f = 1; f < 7; f++) t += acc[q][f];
    float mw = xsum((t[0] + t[1]) + (t[2] + t[3])) * (1.0f / 112);
    asm volatile("" : "+v"(mw));
#pragma unroll
    for (int f = 0; f < 7; f++)
      asm volatile("global_store_dwordx4 %0, %1, %2 offset:%3 sc1\n s_nop 1" ::"v"(l16), "v"(acc[q][f] * og[q]),
                   "s"(ws + OFF_X1 + ((blk0 + q) * 28 + FR(f < 6 ? 3 : 6)) * 1024), "n"(f < 6 ? (f - 3) * 1024 : 0) : "memory");
    t = f32x4{0, 0, 0, 0};
#pragma unroll
    for (int f = 0; f < 7; f++) { const f32x4 d = acc[q][f] - mw; t = __builtin_elementwise_fma(d, d, t); }
    const float s2 = xsum((t[0] + t[1]) + (t[2] + t[3]));
    if (g == 0) { red[(q * 16 + i) * 8 + w] = mw; red[(q * 16 + i) * 8 + 4 + w] = s2; }
  }
  asm volatile("s_waitcnt lgkmcnt(0)\n s_barrier" ::: "memory");
#pragma unroll
  for (int q = 0; q < 4; q++) {
    const f32x4 m4 = *(f32x4*)(red + (q * 16 + i) * 8), v4 = *(f32x4*)(red + (q * 16 + i) * 8 + 4);
    const float mu = (m4[0] + m4[1] + m4[2] + m4[3]) * 0.25f;
    const f32x4 dm = m4 - mu;
    const float var = (v4[0] + v4[1] + v4[2] + v4[3]) * (1.0f / 448) + (dm[0] * dm[0] + dm[1] * dm[1] + dm[2] * dm[2] + dm[3] * dm[3]) * 0.25f;
    const float rs = __builtin_amdgcn_rsqf(var + 1e-5f) * 0.46866869f;
    float nm = -mu * rs;
    asm volatile("" : "+v"(nm));
    u32x2 o[7];
#pragma unroll
    for (int f = 0; f < 7; f++) { const f32x4 v = acc[q][f] * rs + nm; o[f] = u32x2{pk2(v[0], v[1]), pk2(v[2], v[3])}; }
    char* p = ws + OFF_XN + (blk0 + q) * 14336;
#pragma unroll
    for (int u = 0; u < 3; u++)
      asm volatile("global_store_dwordx4 %0, %1, %2 offset:%3 sc1\n s_nop 1" ::"v"(l16), "v"(u32x4{o[2 * u][0], o[2 * u][1], o[2 * u + 1][0], o[2 * u + 1][1]}),
                   "s"(p + 3072 * w), "n"(u * 1024) : "memory");
    asm volatile("global_store_dwordx2 %0, %1, %2 sc1" ::"v"(l16), "v"(o[6]), "s"(p + (12 + (w >> 1)) * 1024 + (w & 1) * 8) : "memory");
  }
  asm volatile("s_waitcnt vmcnt(0) lgkmcnt(0)\n s_barrier" ::: "memory");
  if (tid == 0) __hip_atomic_store((u32*)(ws + OFF_FL + (128 + (blk0 >> 2)) * 128), 1u, __ATOMIC_RELAXED, __HIP_MEMORY_SCOPE_AGENT);
}

template <int ST> DI void relay(int u0, int wvi, u32 smo, const char* wo, const char* w2, const char* w3, const char* w4, char* ws) {
  const char* p[2];
  u32 rl[2];
#pragma unroll
  for (int k = 0; k < 2; k++) {
    int u = u0 + 512 * k; u = u < 1778 ? u : u0;
    const int v = u - 1386, s = u / 7;
    if (ST) p[k] = ws + (u < 1288 ? OFF_WF + u * 4096 : u < 1386 ? OFF_W2 + (u - 1288) * 4096 : OFF_W3 + v * 4096);
    else p[k] = u < 1386 ? (u < 896 ? wo : u < 1288 ? w4 - 128 * (32 * 896) : w2 - 184 * (32 * 896)) + s * (32 * 896) + (u - 7 * s) * 128 : w3 + (v % 14) * (32 * 3584) + (v / 14) * 128;
    rl[k] = u < 1288 ? 896 : u < 1386 ? 897 : 3584;
  }
  if (ST) asm volatile(
"v_mbcnt_lo_u32_b32 v43, -1, 0\n"
"v_mbcnt_hi_u32_b32 v43, -1, v43\n"
"v_lshl_add_u32 v43, %[wv], 6, v43\n"
"v_and_b32 v40, 63, v43\n"
"s_lshl_b32 s68, %[wv], 13\n"
"s_add_u32 s69, s68, %[sm]\n"
"s_mov_b32 m0, s69\n"
"s_and_b32 s71, %[ld0], -2\n"
"s_cmp_eq_u32 %[ld0], s71\n"
"s_cselect_b32 s68, 9, 10\n"
"s_cselect_b32 s71, 11, 9\n"
"s_lshl_b32 s71, 1, s71\n"
"v_lshrrev_b32 v41, 4, v40\n"
"v_lshlrev_b32 v41, s68, v41\n"
"v_bfe_u32 v42, v40, 2, 2\n"
"v_lshl_add_u32 v41, v42, 7, v41\n"
"v_and_b32 v42, 3, v40\n"
"v_lshl_add_u32 v41, v42, 3, v41\n"
"v_add_u32 v41, s69, v41\n"
"v_add_u32 v42, s71, v41\n"
"ds_read_b64_tr_b16 v[44:45], v41 offset:0\n"
"ds_read_b64_tr_b16 v[46:47], v42 offset:0\n"
"ds_read_b64_tr_b16 v[48:49], v41 offset:32\n"
"ds_read_b64_tr_b16 v[50:51], v42 offset:32\n"
"ds_read_b64_tr_b16 v[52:53], v41 offset:64\n"
"ds_read_b64_tr_b16 v[54:55], v42 offset:64\n"
"ds_read_b64_tr_b16 v[56:57], v41 offset:96\n"
"ds_read_b64_tr_b16 v[58:59], v42 offset:96\n"
"s_and_b32 s71, %[ld1], -2\n"
"s_cmp_eq_u32 %[ld1], s71\n"
"s_cselect_b32 s68, 9, 10\n"
"s_cselect_b32 s71, 11, 9\n"
"s_lshl_b32 s71, 1, s71\n"
"v_lshrrev_b32 v41, 4, v40\n"
"v_lshlrev_b32 v41, s68, v41\n"
"v_bfe_u32 v42, v40, 2, 2\n"
"v_lshl_add_u32 v41, v42, 7, v41\n"
"v_and_b32 v42, 3, v40\n"
"v_lshl_add_u32 v41, v42, 3, v41\n"
"v_add_u32 v41, s69, v41\n"
"v_add_u32 v42, s71, v41\n"
"ds_read_b64_tr_b16 v[60:61], v41 offset:4096\n"
"ds_read_b64_tr_b16 v[62:63], v42 offset:4096\n"
"ds_read_b64_tr_b16 v[64:65], v41 offset:4128\n"
"ds_read_b64_tr_b16 v[66:67], v42 offset:4128\n"
"ds_read_b64_tr_b16 v[68:69], v41 offset:4160\n"
"ds_read_b64_tr_b16 v[70:71], v42 offset:4160\n"
"ds_read_b64_tr_b16 v[72:73], v41 offset:4192\n"
"ds_read_b64_tr_b16 v[74:75], v42 offset:4192\n"
"v_lshlrev_b32 v40, 4, v40\n"
"s_waitcnt lgkmcnt(0)\n"
"global_store_dwordx4 v40, v[44:47], %[p0] offset:0\n"
"global_store_dwordx4 v40, v[48:51], %[p0] offset:1024\n"
"global_store_dwordx4 v40, v[52:55], %[p0] offset:2048\n"
"global_store_dwordx4 v40, v[56:59], %[p0] offset:3072\n"
"global_store_dwordx4 v40, v[60:63], %[p1] offset:0\n"
"global_store_dwordx4 v40, v[64:67], %[p1] offset:1024\n"
"global_store_dwordx4 v40, v[68:71], %[p1] offset:2048\n"
"global_store_dwordx4 v40, v[72:75], %[p1] offset:3072\n"
"s_nop 1\n"
      ::[p0] "s"(p[0]), [p1] "s"(p[1]), [ld0] "s"(rl[0]), [ld1] "s"(rl[1]), [sm] "s"(smo + 131072), [wv] "s"(wvi) : "v40","v41","v42","v43","v44","v45","v46","v47","v48","v49","v50","v51","v52","v53","v54","v55","v56","v57","v58","v59","v60","v61","v62","v63","v64","v65","v66","v67","v68","v69","v70","v71","v72","v73","v74","v75","s68","s69","s70","s71","memory","vcc","scc");
  else asm volatile(
"v_mbcnt_lo_u32_b32 v43, -1, 0\n"
"v_mbcnt_hi_u32_b32 v43, -1, v43\n"
"v_lshl_add_u32 v43, %[wv], 6, v43\n"
"v_and_b32 v40, 63, v43\n"
"s_lshl_b32 s68, %[wv], 13\n"
"s_add_u32 s69, s68, %[sm]\n"
"s_mov_b32 m0, s69\n"
"s_and_b32 s71, %[ld0], -2\n"
"v_lshrrev_b32 v41, 3, v40\n"
"v_mul_u32_u24 v41, s71, v41\n"
"v_and_b32 v42, 7, v40\n"
"v_lshl_add_u32 v41, v42, 4, v41\n"
"s_lshl_b32 s68, s71, 3\n"
"global_load_lds_dwordx4 v41, %[p0]\n"
"s_add_u32 m0, m0, 1024\n"
"v_add_u32 v41, s68, v41\n"
"global_load_lds_dwordx4 v41, %[p0]\n"
"s_add_u32 m0, m0, 1024\n"
"v_add_u32 v41, s68, v41\n"
"global_load_lds_dwordx4 v41, %[p0]\n"
"s_add_u32 m0, m0, 1024\n"
"v_add_u32 v41, s68, v41\n"
"global_load_lds_dwordx4 v41, %[p0]\n"
"s_add_u32 m0, m0, 1024\n"
"v_add_u32 v41, s68, v41\n"
"s_and_b32 s71, %[ld1], -2\n"
"v_lshrrev_b32 v41, 3, v40\n"
"v_mul_u32_u24 v41, s71, v41\n"
"v_and_b32 v42, 7, v40\n"
"v_lshl_add_u32 v41, v42, 4, v41\n"
"s_lshl_b32 s68, s71, 3\n"
"global_load_lds_dwordx4 v41, %[p1]\n"
"s_add_u32 m0, m0, 1024\n"
"v_add_u32 v41, s68, v41\n"
"global_load_lds_dwordx4 v41, %[p1]\n"
"s_add_u32 m0, m0, 1024\n"
"v_add_u32 v41, s68, v41\n"
"global_load_lds_dwordx4 v41, %[p1]\n"
"s_add_u32 m0, m0, 1024\n"
"v_add_u32 v41, s68, v41\n"
"global_load_lds_dwordx4 v41, %[p1]\n"
"s_add_u32 m0, m0, 1024\n"
"v_add_u32 v41, s68, v41\n"
      ::[p0] "s"(p[0]), [p1] "s"(p[1]), [ld0] "s"(rl[0]), [ld1] "s"(rl[1]), [sm] "s"(smo + 131072), [wv] "s"(wvi) : "v40","v41","v42","v43","v44","v45","v46","v47","v48","v49","v50","v51","v52","v53","v54","v55","v56","v57","v58","v59","v60","v61","v62","v63","v64","v65","v66","v67","v68","v69","v70","v71","v72","v73","v74","v75","s68","s69","s70","s71","memory","vcc","scc");
}

__global__ __launch_bounds__(256, 1) void k1(const char* __restrict__ x, const char* __restrict__ c, const char* __restrict__ wq,
                                             const char* __restrict__ wk, const char* __restrict__ wv, const char* __restrict__ w1,
                                             const char* __restrict__ w2, const char* __restrict__ gw, const char* __restrict__ wo,
                                             const char* __restrict__ w3, const char* __restrict__ w4, char* __restrict__ ws) {
  __shared__ __attribute__((aligned(1024))) char sm[163840];
  const int xc = blockIdx.x & 7, id = blockIdx.x >> 3, wvi = __builtin_amdgcn_readfirstlane(threadIdx.x >> 6);
  const int h = 2 * xc + (id & 1), isv = (id >> 1) & 1, kb = id >> 2;
  const u32 smo = (u32)(size_t)sm, wn = 1024 * 8192 - h * 512;
  const char* cp = c + kb * (256 * 2048);
  char* op = ws + h * (2 * MB) + kb * (4 * 65536);
  if (!isv) {
    const int kw = (xc * 16 + kb * 2 + (id & 1)) * 4 + wvi;
    const char* wm = wq + (kw < 448 ? kw : 0) * 8192;
    const char *wp = wk + h * 512, *ncp = x + kb * (256 * 896), *nwp = wq + h * 512;
    char* nop = ws + OFF_Q + h * MB + kb * (4 * 32768);
    asm volatile(
"v_mbcnt_lo_u32_b32 v1, -1, 0\n"
"v_mbcnt_hi_u32_b32 v1, -1, v1\n"
"v_lshl_add_u32 v1, %[wv], 6, v1\n"
"s_mov_b64 s[36:37], %[c]\n"
"s_mov_b32 s39, 0x20000\n"
"s_mov_b64 s[40:41], %[w]\n"
"s_mov_b32 s43, 0x20000\n"
"s_mov_b64 s[44:45], %[o]\n"
"s_mov_b32 s47, 0x20000\n"
"s_mov_b64 s[48:49], %[nc]\n"
"s_mov_b32 s51, 0x20000\n"
"s_mov_b64 s[52:53], %[nw]\n"
"s_mov_b32 s55, 0x20000\n"
"s_mov_b64 s[56:57], %[no]\n"
"s_mov_b32 s59, 0x20000\n"
"s_mov_b32 s38, 524288\n"
"s_mov_b32 s42, %[wn]\n"
"s_mov_b32 s46, 262144\n"
"s_mov_b32 s50, 229376\n"
"s_sub_u32 s54, %[wn], 4718592\n"
"s_mov_b32 s58, 131072\n"
"s_mov_b32 s62, 16\n"
"s_mov_b32 s63, 16\n"
"s_mov_b32 s74, 0x3db8aa3b\n"
"s_mov_b32 s75, 0x3db8aa3b\n"
"s_mov_b32 s60, 0\n"
"s_mov_b32 s61, 0\n"
"v_lshrrev_b32 v41, 6, v1\n"
"v_and_b32 v40, 63, v1\n"
"s_nop 0\n"
"v_readfirstlane_b32 s76, v41\n"
"s_lshl_b32 s68, s76, 13\n"
"s_add_u32 s64, s68, %[sm]\n"
"s_add_u32 s65, s64, 32768\n"
"s_add_u32 s66, s64, 65536\n"
"s_add_u32 s67, s64, 98304\n"
"v_lshrrev_b32 v41, 4, v40\n"
"v_add_u32 v41, 0, v41\n"
"v_xor_b32 v41, v41, v40\n"
"v_and_b32 v41, 7, v41\n"
"v_lshlrev_b32 v41, 4, v41\n"
"s_lshl_b32 s68, s76, 6\n"
"s_add_u32 s68, s68, 0\n"
"v_lshrrev_b32 v42, 3, v40\n"
"v_add_u32 v42, s68, v42\n"
"v_lshl_add_u32 v12, v42, 11, v41\n"
"v_mul_u32_u24 v42, 896, v42\n"
"v_add_u32 v28, v41, v42\n"
"s_lshl_b32 s68, s76, 4\n"
"s_add_u32 s68, s68, 0\n"
"v_lshrrev_b32 v42, 5, v40\n"
"v_add_u32 v42, s68, v42\n"
"v_and_b32 v41, 3, v42\n"
"v_or_b32 v41, 0, v41\n"
"v_lshlrev_b32 v41, 1, v41\n"
"v_xor_b32 v41, v41, v40\n"
"v_and_b32 v41, 31, v41\n"
"v_lshlrev_b32 v41, 4, v41\n"
"v_lshl_add_u32 v20, v42, 13, v41\n"
"v_lshrrev_b32 v41, 4, v40\n"
"v_add_u32 v41, 4, v41\n"
"v_xor_b32 v41, v41, v40\n"
"v_and_b32 v41, 7, v41\n"
"v_lshlrev_b32 v41, 4, v41\n"
"s_lshl_b32 s68, s76, 6\n"
"s_add_u32 s68, s68, 8\n"
"v_lshrrev_b32 v42, 3, v40\n"
"v_add_u32 v42, s68, v42\n"
"v_lshl_add_u32 v13, v42, 11, v41\n"
"v_mul_u32_u24 v42, 896, v42\n"
"v_add_u32 v29, v41, v42\n"
"s_lshl_b32 s68, s76, 4\n"
"s_add_u32 s68, s68, 2\n"
"v_lshrrev_b32 v42, 5, v40\n"
"v_add_u32 v42, s68, v42\n"
"v_and_b32 v41, 3, v42\n"
"v_or_b32 v41, 0, v41\n"
"v_lshlrev_b32 v41, 1, v41\n"
"v_xor_b32 v41, v41, v40\n"
"v_and_b32 v41, 31, v41\n"
"v_lshlrev_b32 v41, 4, v41\n"
"v_lshl_add_u32 v21, v42, 13, v41\n"
"v_add_u32 v14, 32768, v12\n"
"v_add_u32 v22, 32768, v20\n"
"v_add_u32 v15, 32768, v13\n"
"v_add_u32 v23, 32768, v21\n"
"v_add_u32 v16, 32768, v14\n"
"v_xor_b32 v41, 128, v22\n"
"v_add_u32 v24, 32768, v41\n"
"v_add_u32 v17, 32768, v15\n"
"v_xor_b32 v41, 128, v23\n"
"v_add_u32 v25, 32768, v41\n"
"v_add_u32 v18, 32768, v16\n"
"v_add_u32 v26, 32768, v24\n"
"v_add_u32 v19, 32768, v17\n"
"v_add_u32 v27, 32768, v25\n"
"s_mov_b32 m0, s64\n"
"v_add_u32 v30, 14336, v28\n"
"v_add_u32 v31, 14336, v29\n"
"v_add_u32 v32, 14336, v30\n"
"buffer_load_dwordx4 v12, s[36:39], s60 offen lds\n"
"s_add_u32 m0, m0, 1024\n"
"v_add_u32 v33, 14336, v31\n"
"v_add_u32 v34, 14336, v32\n"
"v_add_u32 v35, 14336, v33\n"
"buffer_load_dwordx4 v13, s[36:39], s60 offen lds\n"
"s_add_u32 m0, m0, 1024\n"
"v_lshlrev_b32 v76, 7, v40\n"
"s_mov_b64 s[80:81], %[wm]\n"
"s_mov_b32 s82, 8192\n"
"buffer_load_dwordx4 v14, s[36:39], s60 offen lds\n"
"s_add_u32 m0, m0, 1024\n"
"s_mov_b32 s83, 0x20000\n"
"v_subrev_u32 v76, 81920, v76\n"
"s_lshr_b32 s68, s76, 1\n"
"buffer_load_dwordx4 v15, s[36:39], s60 offen lds\n"
"s_add_u32 m0, m0, 1024\n"
"s_lshl_b32 s68, s68, 14\n"
"s_add_u32 s68, s68, %[sm]\n"
"v_and_b32 v41, 15, v40\n"
"buffer_load_dwordx4 v16, s[36:39], s60 offen lds\n"
"s_add_u32 m0, m0, 1024\n"
"v_lshrrev_b32 v42, 4, v40\n"
"v_lshrrev_b32 v43, 1, v41\n"
"v_add_u32 v44, 0, v42\n"
"buffer_load_dwordx4 v17, s[36:39], s60 offen lds\n"
"s_add_u32 m0, m0, 1024\n"
"v_xor_b32 v44, v44, v43\n"
"v_lshlrev_b32 v44, 4, v44\n"
"v_lshl_add_u32 v44, v41, 7, v44\n"
"buffer_load_dwordx4 v18, s[36:39], s60 offen lds\n"
"s_add_u32 m0, m0, 1024\n"
"v_add_u32 v2, s68, v44\n"
"v_add_u32 v44, 4, v42\n"
"v_xor_b32 v44, v44, v43\n"
"buffer_load_dwordx4 v19, s[36:39], s60 offen lds\n"
"s_mov_b32 m0, s66\n"
"v_lshlrev_b32 v44, 4, v44\n"
"v_lshl_add_u32 v44, v41, 7, v44\n"
"v_add_u32 v3, s68, v44\n"
"buffer_load_dwordx4 v20, s[40:43], s61 offen lds\n"
"s_add_u32 m0, m0, 1024\n"
"s_and_b32 s68, s76, 1\n"
"s_lshl_b32 s68, s68, 8\n"
"s_add_u32 s68, s68, %[sm]\n"
"buffer_load_dwordx4 v21, s[40:43], s61 offen lds\n"
"s_add_u32 m0, m0, 1024\n"
"s_add_u32 s68, s68, 65536\n"
"v_lshrrev_b32 v41, 2, v41\n"
"v_lshl_add_u32 v43, v42, 3, v41\n"
"buffer_load_dwordx4 v22, s[40:43], s61 offen lds\n"
"s_add_u32 m0, m0, 1024\n"
"v_lshlrev_b32 v43, 9, v43\n"
"v_and_b32 v44, 3, v40\n"
"v_lshl_add_u32 v43, v44, 3, v43\n"
"buffer_load_dwordx4 v23, s[40:43], s61 offen lds\n"
"s_add_u32 m0, m0, 1024\n"
"v_add_u32 v43, s68, v43\n"
"v_and_b32 v42, 1, v42\n"
"v_lshl_add_u32 v41, v42, 2, v41\n"
"buffer_load_dwordx4 v24, s[40:43], s61 offen lds\n"
"s_add_u32 m0, m0, 1024\n"
"v_xor_b32 v44, 0, v41\n"
"v_lshl_add_u32 v4, v44, 5, v43\n"
"v_xor_b32 v44, 1, v41\n"
"buffer_load_dwordx4 v25, s[40:43], s61 offen lds\n"
"s_add_u32 m0, m0, 1024\n"
"v_lshl_add_u32 v5, v44, 5, v43\n"
"v_xor_b32 v44, 2, v41\n"
"v_lshl_add_u32 v6, v44, 5, v43\n"
"buffer_load_dwordx4 v26, s[40:43], s61 offen lds\n"
"s_add_u32 m0, m0, 1024\n"
"v_xor_b32 v44, 3, v41\n"
"v_lshl_add_u32 v7, v44, 5, v43\n"
"v_xor_b32 v44, 4, v41\n"
"buffer_load_dwordx4 v27, s[40:43], s61 offen lds\n"
"s_add_u32 s60, s60, 128\n"
"s_add_u32 s61, s61, 524288\n"
"s_mov_b32 m0, s65\n"
"v_lshl_add_u32 v8, v44, 5, v43\n"
"v_xor_b32 v44, 5, v41\n"
"v_lshl_add_u32 v9, v44, 5, v43\n"
"buffer_load_dwordx4 v12, s[36:39], s60 offen lds\n"
"s_add_u32 m0, m0, 1024\n"
"v_xor_b32 v44, 6, v41\n"
"v_lshl_add_u32 v10, v44, 5, v43\n"
"v_xor_b32 v44, 7, v41\n"
"buffer_load_dwordx4 v13, s[36:39], s60 offen lds\n"
"s_add_u32 m0, m0, 1024\n"
"v_lshl_add_u32 v11, v44, 5, v43\n"
"v_lshlrev_b32 v36, 4, v40\n"
"buffer_load_dwordx4 v14, s[36:39], s60 offen lds\n"
"s_add_u32 m0, m0, 1024\n"
"s_nop 0\n"
"buffer_load_dwordx4 v15, s[36:39], s60 offen lds\n"
"s_add_u32 m0, m0, 1024\n"
"s_nop 0\n"
"buffer_load_dwordx4 v16, s[36:39], s60 offen lds\n"
"s_add_u32 m0, m0, 1024\n"
"s_nop 0\n"
"buffer_load_dwordx4 v17, s[36:39], s60 offen lds\n"
"s_add_u32 m0, m0, 1024\n"
"s_nop 0\n"
"buffer_load_dwordx4 v18, s[36:39], s60 offen lds\n"
"s_add_u32 m0, m0, 1024\n"
"s_nop 0\n"
"buffer_load_dwordx4 v19, s[36:39], s60 offen lds\n"
"s_mov_b32 m0, s67\n"
"s_nop 0\n"
"buffer_load_dwordx4 v20, s[40:43], s61 offen lds\n"
"s_add_u32 m0, m0, 1024\n"
"s_nop 0\n"
"buffer_load_dwordx4 v21, s[40:43], s61 offen lds\n"
"s_add_u32 m0, m0, 1024\n"
"s_nop 0\n"
"buffer_load_dwordx4 v22, s[40:43], s61 offen lds\n"
"s_add_u32 m0, m0, 1024\n"
"s_nop 0\n"
"buffer_load_dwordx4 v23, s[40:43], s61 offen lds\n"
"s_add_u32 m0, m0, 1024\n"
"s_nop 0\n"
"buffer_load_dwordx4 v24, s[40:43], s61 offen lds\n"
"s_add_u32 m0, m0, 1024\n"
"s_nop 0\n"
"buffer_load_dwordx4 v25, s[40:43], s61 offen lds\n"
"s_add_u32 m0, m0, 1024\n"
"s_nop 0\n"
"buffer_load_dwordx4 v26, s[40:43], s61 offen lds\n"
"s_add_u32 m0, m0, 1024\n"
"s_nop 0\n"
"buffer_load_dwordx4 v27, s[40:43], s61 offen lds\n"
"s_add_u32 s60, s60, 128\n"
"s_add_u32 s61, s61, 524288\n"
"s_waitcnt vmcnt(16)\n"
"s_barrier\n"
"ds_read_b64_tr_b16 v[160:161], v4 offset:0\n"
"ds_read_b64_tr_b16 v[162:163], v4 offset:2048\n"
"ds_read_b64_tr_b16 v[164:165], v5 offset:0\n"
"ds_read_b64_tr_b16 v[166:167], v5 offset:2048\n"
"ds_read_b128 v[128:131], v2 offset:0\n"
"ds_read_b128 v[132:135], v2 offset:2048\n"
"ds_read_b128 v[136:139], v2 offset:4096\n"
"ds_read_b128 v[140:143], v2 offset:6144\n"
"ds_read_b128 v[144:147], v2 offset:8192\n"
"ds_read_b128 v[148:151], v2 offset:10240\n"
"ds_read_b128 v[152:155], v2 offset:12288\n"
"ds_read_b128 v[156:159], v2 offset:14336\n"
"ds_read_b64_tr_b16 v[168:169], v6 offset:0\n"
"ds_read_b64_tr_b16 v[170:171], v6 offset:2048\n"
"ds_read_b64_tr_b16 v[172:173], v7 offset:0\n"
"ds_read_b64_tr_b16 v[174:175], v7 offset:2048\n"
"ds_read_b64_tr_b16 v[176:177], v8 offset:0\n"
"ds_read_b64_tr_b16 v[178:179], v8 offset:2048\n"
"ds_read_b64_tr_b16 v[180:181], v9 offset:0\n"
"ds_read_b64_tr_b16 v[182:183], v9 offset:2048\n"
"ds_read_b64_tr_b16 v[184:185], v10 offset:0\n"
"ds_read_b64_tr_b16 v[186:187], v10 offset:2048\n"
"ds_read_b64_tr_b16 v[188:189], v11 offset:0\n"
"ds_read_b64_tr_b16 v[190:191], v11 offset:2048\n"
"s_waitcnt lgkmcnt(0)\n"
"L_peel%=:\n"
"v_mfma_f32_16x16x32_bf16 a[0:3], v[160:163], v[128:131], 0\n"
"ds_read_b64_tr_b16 v[224:225], v4 offset:16384\n"
"v_mfma_f32_16x16x32_bf16 a[4:7], v[164:167], v[128:131], 0\n"
"ds_read_b64_tr_b16 v[226:227], v4 offset:18432\n"
"v_mfma_f32_16x16x32_bf16 a[32:35], v[160:163], v[132:135], 0\n"
"ds_read_b64_tr_b16 v[228:229], v5 offset:16384\n"
"v_mfma_f32_16x16x32_bf16 a[36:39], v[164:167], v[132:135], 0\n"
"ds_read_b64_tr_b16 v[230:231], v5 offset:18432\n"
"v_mfma_f32_16x16x32_bf16 a[64:67], v[160:163], v[136:139], 0\n"
"ds_read_b128 v[192:195], v3 offset:0\n"
"v_mfma_f32_16x16x32_bf16 a[68:71], v[164:167], v[136:139], 0\n"
"ds_read_b128 v[196:199], v3 offset:2048\n"
"v_mfma_f32_16x16x32_bf16 a[96:99], v[160:163], v[140:143], 0\n"
"ds_read_b128 v[200:203], v3 offset:4096\n"
"v_mfma_f32_16x16x32_bf16 a[100:103], v[164:167], v[140:143], 0\n"
"ds_read_b128 v[204:207], v3 offset:6144\n"
"v_mfma_f32_16x16x32_bf16 a[128:131], v[160:163], v[144:147], 0\n"
"ds_read_b128 v[208:211], v3 offset:8192\n"
"v_mfma_f32_16x16x32_bf16 a[132:135], v[164:167], v[144:147], 0\n"
"ds_read_b128 v[212:215], v3 offset:10240\n"
"v_mfma_f32_16x16x32_bf16 a[160:163], v[160:163], v[148:151], 0\n"
"ds_read_b128 v[216:219], v3 offset:12288\n"
"v_mfma_f32_16x16x32_bf16 a[164:167], v[164:167], v[148:151], 0\n"
"ds_read_b128 v[220:223], v3 offset:14336\n"
"v_mfma_f32_16x16x32_bf16 a[192:195], v[160:163], v[152:155], 0\n"
"ds_read_b64_tr_b16 v[232:233], v6 offset:16384\n"
"v_mfma_f32_16x16x32_bf16 a[196:199], v[164:167], v[152:155], 0\n"
"ds_read_b64_tr_b16 v[234:235], v6 offset:18432\n"
"v_mfma_f32_16x16x32_bf16 a[224:227], v[160:163], v[156:159], 0\n"
"ds_read_b64_tr_b16 v[236:237], v7 offset:16384\n"
"v_mfma_f32_16x16x32_bf16 a[228:231], v[164:167], v[156:159], 0\n"
"ds_read_b64_tr_b16 v[238:239], v7 offset:18432\n"
"v_mfma_f32_16x16x32_bf16 a[8:11], v[168:171], v[128:131], 0\n"
"ds_read_b64_tr_b16 v[240:241], v8 offset:16384\n"
"v_mfma_f32_16x16x32_bf16 a[12:15], v[172:175], v[128:131], 0\n"
"ds_read_b64_tr_b16 v[242:243], v8 offset:18432\n"
"v_mfma_f32_16x16x32_bf16 a[40:43], v[168:171], v[132:135], 0\n"
"ds_read_b64_tr_b16 v[244:245], v9 offset:16384\n"
"v_mfma_f32_16x16x32_bf16 a[44:47], v[172:175], v[132:135], 0\n"
"ds_read_b64_tr_b16 v[246:247], v9 offset:18432\n"
"v_mfma_f32_16x16x32_bf16 a[72:75], v[168:171], v[136:139], 0\n"
"ds_read_b64_tr_b16 v[248:249], v10 offset:16384\n"
"v_mfma_f32_16x16x32_bf16 a[76:79], v[172:175], v[136:139], 0\n"
"ds_read_b64_tr_b16 v[250:251], v10 offset:18432\n"
"v_mfma_f32_16x16x32_bf16 a[104:107], v[168:171], v[140:143], 0\n"
"ds_read_b64_tr_b16 v[252:253], v11 offset:16384\n"
"v_mfma_f32_16x16x32_bf16 a[108:111], v[172:175], v[140:143], 0\n"
"ds_read_b64_tr_b16 v[254:255], v11 offset:18432\n"
"v_mfma_f32_16x16x32_bf16 a[136:139], v[168:171], v[144:147], 0\n"
"v_mfma_f32_16x16x32_bf16 a[140:143], v[172:175], v[144:147], 0\n"
"v_mfma_f32_16x16x32_bf16 a[168:171], v[168:171], v[148:151], 0\n"
"v_mfma_f32_16x16x32_bf16 a[172:175], v[172:175], v[148:151], 0\n"
"s_waitcnt lgkmcnt(0)\n"
"s_barrier\n"
"v_mfma_f32_16x16x32_bf16 a[200:203], v[168:171], v[152:155], 0\n"
"s_mov_b32 m0, s64\n"
"v_mfma_f32_16x16x32_bf16 a[204:207], v[172:175], v[152:155], 0\n"
"buffer_load_dwordx4 v12, s[36:39], s60 offen lds\n"
"v_mfma_f32_16x16x32_bf16 a[232:235], v[168:171], v[156:159], 0\n"
"v_mfma_f32_16x16x32_bf16 a[236:239], v[172:175], v[156:159], 0\n"
"v_mfma_f32_16x16x32_bf16 a[16:19], v[176:179], v[128:131], 0\n"
"v_mfma_f32_16x16x32_bf16 a[20:23], v[180:183], v[128:131], 0\n"
"v_mfma_f32_16x16x32_bf16 a[48:51], v[176:179], v[132:135], 0\n"
"s_add_u32 m0, m0, 1024\n"
"v_mfma_f32_16x16x32_bf16 a[52:55], v[180:183], v[132:135], 0\n"
"buffer_load_dwordx4 v13, s[36:39], s60 offen lds\n"
"v_mfma_f32_16x16x32_bf16 a[80:83], v[176:179], v[136:139], 0\n"
"v_mfma_f32_16x16x32_bf16 a[84:87], v[180:183], v[136:139], 0\n"
"v_mfma_f32_16x16x32_bf16 a[112:115], v[176:179], v[140:143], 0\n"
"v_mfma_f32_16x16x32_bf16 a[116:119], v[180:183], v[140:143], 0\n"
"v_mfma_f32_16x16x32_bf16 a[144:147], v[176:179], v[144:147], 0\n"
"s_add_u32 m0, m0, 1024\n"
"v_mfma_f32_16x16x32_bf16 a[148:151], v[180:183], v[144:147], 0\n"
"buffer_load_dwordx4 v14, s[36:39], s60 offen lds\n"
"v_mfma_f32_16x16x32_bf16 a[176:179], v[176:179], v[148:151], 0\n"
"v_mfma_f32_16x16x32_bf16 a[180:183], v[180:183], v[148:151], 0\n"
"v_mfma_f32_16x16x32_bf16 a[208:211], v[176:179], v[152:155], 0\n"
"v_mfma_f32_16x16x32_bf16 a[212:215], v[180:183], v[152:155], 0\n"
"v_mfma_f32_16x16x32_bf16 a[240:243], v[176:179], v[156:159], 0\n"
"s_add_u32 m0, m0, 1024\n"
"v_mfma_f32_16x16x32_bf16 a[244:247], v[180:183], v[156:159], 0\n"
"buffer_load_dwordx4 v15, s[36:39], s60 offen lds\n"
"v_mfma_f32_16x16x32_bf16 a[24:27], v[184:187], v[128:131], 0\n"
"v_mfma_f32_16x16x32_bf16 a[28:31], v[188:191], v[128:131], 0\n"
"v_mfma_f32_16x16x32_bf16 a[56:59], v[184:187], v[132:135], 0\n"
"v_mfma_f32_16x16x32_bf16 a[60:63], v[188:191], v[132:135], 0\n"
"v_mfma_f32_16x16x32_bf16 a[88:91], v[184:187], v[136:139], 0\n"
"s_add_u32 m0, m0, 1024\n"
"v_mfma_f32_16x16x32_bf16 a[92:95], v[188:191], v[136:139], 0\n"
"buffer_load_dwordx4 v16, s[36:39], s60 offen lds\n"
"v_mfma_f32_16x16x32_bf16 a[120:123], v[184:187], v[140:143], 0\n"
"v_mfma_f32_16x16x32_bf16 a[124:127], v[188:191], v[140:143], 0\n"
"v_mfma_f32_16x16x32_bf16 a[152:155], v[184:187], v[144:147], 0\n"
"v_mfma_f32_16x16x32_bf16 a[156:159], v[188:191], v[144:147], 0\n"
"v_mfma_f32_16x16x32_bf16 a[184:187], v[184:187], v[148:151], 0\n"
"s_add_u32 m0, m0, 1024\n"
"v_mfma_f32_16x16x32_bf16 a[188:191], v[188:191], v[148:151], 0\n"
"buffer_load_dwordx4 v17, s[36:39], s60 offen lds\n"
"v_mfma_f32_16x16x32_bf16 a[216:219], v[184:187], v[152:155], 0\n"
"v_mfma_f32_16x16x32_bf16 a[220:223], v[188:191], v[152:155], 0\n"
"v_mfma_f32_16x16x32_bf16 a[248:251], v[184:187], v[156:159], 0\n"
"v_mfma_f32_16x16x32_bf16 a[252:255], v[188:191], v[156:159], 0\n"
"v_mfma_f32_16x16x32_bf16 a[0:3], v[224:227], v[192:195], a[0:3]\n"
"s_add_u32 m0, m0, 1024\n"
"v_mfma_f32_16x16x32_bf16 a[4:7], v[228:231], v[192:195], a[4:7]\n"
"buffer_load_dwordx4 v18, s[36:39], s60 offen lds\n"
"v_mfma_f32_16x16x32_bf16 a[32:35], v[224:227], v[196:199], a[32:35]\n"
"v_mfma_f32_16x16x32_bf16 a[36:39], v[228:231], v[196:199], a[36:39]\n"
"v_mfma_f32_16x16x32_bf16 a[64:67], v[224:227], v[200:203], a[64:67]\n"
"v_mfma_f32_16x16x32_bf16 a[68:71], v[228:231], v[200:203], a[68:71]\n"
"v_mfma_f32_16x16x32_bf16 a[96:99], v[224:227], v[204:207], a[96:99]\n"
"s_add_u32 m0, m0, 1024\n"
"v_mfma_f32_16x16x32_bf16 a[100:103], v[228:231], v[204:207], a[100:103]\n"
"buffer_load_dwordx4 v19, s[36:39], s60 offen lds\n"
"v_mfma_f32_16x16x32_bf16 a[128:131], v[224:227], v[208:211], a[128:131]\n"
"v_mfma_f32_16x16x32_bf16 a[132:135], v[228:231], v[208:211], a[132:135]\n"
"v_mfma_f32_16x16x32_bf16 a[160:163], v[224:227], v[212:215], a[160:163]\n"
"v_mfma_f32_16x16x32_bf16 a[164:167], v[228:231], v[212:215], a[164:167]\n"
"v_mfma_f32_16x16x32_bf16 a[192:195], v[224:227], v[216:219], a[192:195]\n"
"s_mov_b32 m0, s66\n"
"v_mfma_f32_16x16x32_bf16 a[196:199], v[228:231], v[216:219], a[196:199]\n"
"buffer_load_dwordx4 v20, s[40:43], s61 offen lds\n"
"v_mfma_f32_16x16x32_bf16 a[224:227], v[224:227], v[220:223], a[224:227]\n"
"v_mfma_f32_16x16x32_bf16 a[228:231], v[228:231], v[220:223], a[228:231]\n"
"v_mfma_f32_16x16x32_bf16 a[8:11], v[232:235], v[192:195], a[8:11]\n"
"v_mfma_f32_16x16x32_bf16 a[12:15], v[236:239], v[192:195], a[12:15]\n"
"v_mfma_f32_16x16x32_bf16 a[40:43], v[232:235], v[196:199], a[40:43]\n"
"s_add_u32 m0, m0, 1024\n"
"v_mfma_f32_16x16x32_bf16 a[44:47], v[236:239], v[196:199], a[44:47]\n"
"buffer_load_dwordx4 v21, s[40:43], s61 offen lds\n"
"v_mfma_f32_16x16x32_bf16 a[72:75], v[232:235], v[200:203], a[72:75]\n"
"v_mfma_f32_16x16x32_bf16 a[76:79], v[236:239], v[200:203], a[76:79]\n"
"v_mfma_f32_16x16x32_bf16 a[104:107], v[232:235], v[204:207], a[104:107]\n"
"v_mfma_f32_16x16x32_bf16 a[108:111], v[236:239], v[204:207], a[108:111]\n"
"v_mfma_f32_16x16x32_bf16 a[136:139], v[232:235], v[208:211], a[136:139]\n"
"s_add_u32 m0, m0, 1024\n"
"v_mfma_f32_16x16x32_bf16 a[140:143], v[236:239], v[208:211], a[140:143]\n"
"buffer_load_dwordx4 v22, s[40:43], s61 offen lds\n"
"v_mfma_f32_16x16x32_bf16 a[168:171], v[232:235], v[212:215], a[168:171]\n"
"v_mfma_f32_16x16x32_bf16 a[172:175], v[236:239], v[212:215], a[172:175]\n"
"v_mfma_f32_16x16x32_bf16 a[200:203], v[232:235], v[216:219], a[200:203]\n"
"v_mfma_f32_16x16x32_bf16 a[204:207], v[236:239], v[216:219], a[204:207]\n"
"v_mfma_f32_16x16x32_bf16 a[232:235], v[232:235], v[220:223], a[232:235]\n"
"s_add_u32 m0, m0, 1024\n"
"v_mfma_f32_16x16x32_bf16 a[236:239], v[236:239], v[220:223], a[236:239]\n"
"buffer_load_dwordx4 v23, s[40:43], s61 offen lds\n"
"v_mfma_f32_16x16x32_bf16 a[16:19], v[240:243], v[192:195], a[16:19]\n"
"s_waitcnt vmcnt(12)\n"
"s_barrier\n"
"v_mfma_f32_16x16x32_bf16 a[20:23], v[244:247], v[192:195], a[20:23]\n"
"ds_read_b64_tr_b16 v[160:161], v4 offset:32768\n"
"v_mfma_f32_16x16x32_bf16 a[48:51], v[240:243], v[196:199], a[48:51]\n"
"buffer_load_dword v37, v76, s[80:83], 0 offen\n"
"ds_read_b64_tr_b16 v[162:163], v4 offset:34816\n"
"v_mfma_f32_16x16x32_bf16 a[52:55], v[244:247], v[196:199], a[52:55]\n"
"v_add_u32 v76, 8192, v76\n"
"ds_read_b64_tr_b16 v[164:165], v5 offset:32768\n"
"v_mfma_f32_16x16x32_bf16 a[80:83], v[240:243], v[200:203], a[80:83]\n"
"s_add_u32 m0, m0, 1024\n"
"ds_read_b64_tr_b16 v[166:167], v5 offset:34816\n"
"v_mfma_f32_16x16x32_bf16 a[84:87], v[244:247], v[200:203], a[84:87]\n"
"buffer_load_dwordx4 v24, s[40:43], s61 offen lds\n"
"ds_read_b128 v[128:131], v2 offset:32768\n"
"v_mfma_f32_16x16x32_bf16 a[112:115], v[240:243], v[204:207], a[112:115]\n"
"ds_read_b128 v[132:135], v2 offset:34816\n"
"v_mfma_f32_16x16x32_bf16 a[116:119], v[244:247], v[204:207], a[116:119]\n"
"ds_read_b128 v[136:139], v2 offset:36864\n"
"v_mfma_f32_16x16x32_bf16 a[144:147], v[240:243], v[208:211], a[144:147]\n"
"ds_read_b128 v[140:143], v2 offset:38912\n"
"v_mfma_f32_16x16x32_bf16 a[148:151], v[244:247], v[208:211], a[148:151]\n"
"ds_read_b128 v[144:147], v2 offset:40960\n"
"v_mfma_f32_16x16x32_bf16 a[176:179], v[240:243], v[212:215], a[176:179]\n"
"s_add_u32 m0, m0, 1024\n"
"ds_read_b128 v[148:151], v2 offset:43008\n"
"v_mfma_f32_16x16x32_bf16 a[180:183], v[244:247], v[212:215], a[180:183]\n"
"buffer_load_dwordx4 v25, s[40:43], s61 offen lds\n"
"ds_read_b128 v[152:155], v2 offset:45056\n"
"v_mfma_f32_16x16x32_bf16 a[208:211], v[240:243], v[216:219], a[208:211]\n"
"ds_read_b128 v[156:159], v2 offset:47104\n"
"v_mfma_f32_16x16x32_bf16 a[212:215], v[244:247], v[216:219], a[212:215]\n"
"ds_read_b64_tr_b16 v[168:169], v6 offset:32768\n"
"v_mfma_f32_16x16x32_bf16 a[240:243], v[240:243], v[220:223], a[240:243]\n"
"ds_read_b64_tr_b16 v[170:171], v6 offset:34816\n"
"v_mfma_f32_16x16x32_bf16 a[244:247], v[244:247], v[220:223], a[244:247]\n"
"ds_read_b64_tr_b16 v[172:173], v7 offset:32768\n"
"v_mfma_f32_16x16x32_bf16 a[24:27], v[248:251], v[192:195], a[24:27]\n"
"s_add_u32 m0, m0, 1024\n"
"ds_read_b64_tr_b16 v[174:175], v7 offset:34816\n"
"v_mfma_f32_16x16x32_bf16 a[28:31], v[252:255], v[192:195], a[28:31]\n"
"buffer_load_dwordx4 v26, s[40:43], s61 offen lds\n"
"ds_read_b64_tr_b16 v[176:177], v8 offset:32768\n"
"v_mfma_f32_16x16x32_bf16 a[56:59], v[248:251], v[196:199], a[56:59]\n"
"ds_read_b64_tr_b16 v[178:179], v8 offset:34816\n"
"v_mfma_f32_16x16x32_bf16 a[60:63], v[252:255], v[196:199], a[60:63]\n"
"ds_read_b64_tr_b16 v[180:181], v9 offset:32768\n"
"v_mfma_f32_16x16x32_bf16 a[88:91], v[248:251], v[200:203], a[88:91]\n"
"ds_read_b64_tr_b16 v[182:183], v9 offset:34816\n"
"v_mfma_f32_16x16x32_bf16 a[92:95], v[252:255], v[200:203], a[92:95]\n"
"ds_read_b64_tr_b16 v[184:185], v10 offset:32768\n"
"v_mfma_f32_16x16x32_bf16 a[120:123], v[248:251], v[204:207], a[120:123]\n"
"s_add_u32 m0, m0, 1024\n"
"ds_read_b64_tr_b16 v[186:187], v10 offset:34816\n"
"v_mfma_f32_16x16x32_bf16 a[124:127], v[252:255], v[204:207], a[124:127]\n"
"buffer_load_dwordx4 v27, s[40:43], s61 offen lds\n"
"s_add_u32 s60, s60, 128\n"
"s_add_u32 s61, s61, 524288\n"
"ds_read_b64_tr_b16 v[188:189], v11 offset:32768\n"
"v_mfma_f32_16x16x32_bf16 a[152:155], v[248:251], v[208:211], a[152:155]\n"
"ds_read_b64_tr_b16 v[190:191], v11 offset:34816\n"
"v_mfma_f32_16x16x32_bf16 a[156:159], v[252:255], v[208:211], a[156:159]\n"
"v_mfma_f32_16x16x32_bf16 a[184:187], v[248:251], v[212:215], a[184:187]\n"
"v_mfma_f32_16x16x32_bf16 a[188:191], v[252:255], v[212:215], a[188:191]\n"
"v_mfma_f32_16x16x32_bf16 a[216:219], v[248:251], v[216:219], a[216:219]\n"
"v_mfma_f32_16x16x32_bf16 a[220:223], v[252:255], v[216:219], a[220:223]\n"
"v_mfma_f32_16x16x32_bf16 a[248:251], v[248:251], v[220:223], a[248:251]\n"
"s_sub_u32 s62, s62, 1\n"
"s_cmp_eq_u32 s62, 0\n"
"v_mfma_f32_16x16x32_bf16 a[252:255], v[252:255], v[220:223], a[252:255]\n"
"s_waitcnt lgkmcnt(0)\n"
"s_branch L_mid%=\n"
"L_top%=:\n"
"s_cmp_lg_u32 s62, 2\n"
"s_cbranch_scc1 L_ns0%=\n"
"s_mov_b64 s[36:37], s[48:49]\n"
"s_mov_b64 s[38:39], s[50:51]\n"
"s_mov_b64 s[40:41], s[52:53]\n"
"s_mov_b64 s[42:43], s[54:55]\n"
"s_mov_b32 s50, 0\n"
"s_mov_b32 s54, 0\n"
"s_mov_b32 s60, 0\n"
"s_mov_b32 s61, 0\n"
"v_mov_b32 v12, v28\n"
"v_mov_b32 v13, v29\n"
"v_mov_b32 v14, v30\n"
"v_mov_b32 v15, v31\n"
"v_mov_b32 v16, v32\n"
"v_mov_b32 v17, v33\n"
"v_mov_b32 v18, v34\n"
"v_mov_b32 v19, v35\n"
"L_ns0%=:\n"
"v_mfma_f32_16x16x32_bf16 a[0:3], v[160:163], v[128:131], a[0:3]\n"
"ds_read_b64_tr_b16 v[224:225], v4 offset:16384\n"
"v_mfma_f32_16x16x32_bf16 a[4:7], v[164:167], v[128:131], a[4:7]\n"
"ds_read_b64_tr_b16 v[226:227], v4 offset:18432\n"
"v_mfma_f32_16x16x32_bf16 a[32:35], v[160:163], v[132:135], a[32:35]\n"
"ds_read_b64_tr_b16 v[228:229], v5 offset:16384\n"
"v_mfma_f32_16x16x32_bf16 a[36:39], v[164:167], v[132:135], a[36:39]\n"
"ds_read_b64_tr_b16 v[230:231], v5 offset:18432\n"
"v_mfma_f32_16x16x32_bf16 a[64:67], v[160:163], v[136:139], a[64:67]\n"
"ds_read_b128 v[192:195], v3 offset:0\n"
"v_mfma_f32_16x16x32_bf16 a[68:71], v[164:167], v[136:139], a[68:71]\n"
"ds_read_b128 v[196:199], v3 offset:2048\n"
"v_mfma_f32_16x16x32_bf16 a[96:99], v[160:163], v[140:143], a[96:99]\n"
"ds_read_b128 v[200:203], v3 offset:4096\n"
"v_mfma_f32_16x16x32_bf16 a[100:103], v[164:167], v[140:143], a[100:103]\n"
"ds_read_b128 v[204:207], v3 offset:6144\n"
"v_mfma_f32_16x16x32_bf16 a[128:131], v[160:163], v[144:147], a[128:131]\n"
"ds_read_b128 v[208:211], v3 offset:8192\n"
"v_mfma_f32_16x16x32_bf16 a[132:135], v[164:167], v[144:147], a[132:135]\n"
"ds_read_b128 v[212:215], v3 offset:10240\n"
"v_mfma_f32_16x16x32_bf16 a[160:163], v[160:163], v[148:151], a[160:163]\n"
"ds_read_b128 v[216:219], v3 offset:12288\n"
"v_mfma_f32_16x16x32_bf16 a[164:167], v[164:167], v[148:151], a[164:167]\n"
"ds_read_b128 v[220:223], v3 offset:14336\n"
"v_mfma_f32_16x16x32_bf16 a[192:195], v[160:163], v[152:155], a[192:195]\n"
"ds_read_b64_tr_b16 v[232:233], v6 offset:16384\n"
"v_mfma_f32_16x16x32_bf16 a[196:199], v[164:167], v[152:155], a[196:199]\n"
"ds_read_b64_tr_b16 v[234:235], v6 offset:18432\n"
"v_mfma_f32_16x16x32_bf16 a[224:227], v[160:163], v[156:159], a[224:227]\n"
"ds_read_b64_tr_b16 v[236:237], v7 offset:16384\n"
"v_mfma_f32_16x16x32_bf16 a[228:231], v[164:167], v[156:159], a[228:231]\n"
"ds_read_b64_tr_b16 v[238:239], v7 offset:18432\n"
"v_mfma_f32_16x16x32_bf16 a[8:11], v[168:171], v[128:131], a[8:11]\n"
"ds_read_b64_tr_b16 v[240:241], v8 offset:16384\n"
"v_mfma_f32_16x16x32_bf16 a[12:15], v[172:175], v[128:131], a[12:15]\n"
"ds_read_b64_tr_b16 v[242:243], v8 offset:18432\n"
"v_mfma_f32_16x16x32_bf16 a[40:43], v[168:171], v[132:135], a[40:43]\n"
"ds_read_b64_tr_b16 v[244:245], v9 offset:16384\n"
"v_mfma_f32_16x16x32_bf16 a[44:47], v[172:175], v[132:135], a[44:47]\n"
"ds_read_b64_tr_b16 v[246:247], v9 offset:18432\n"
"v_mfma_f32_16x16x32_bf16 a[72:75], v[168:171], v[136:139], a[72:75]\n"
"ds_read_b64_tr_b16 v[248:249], v10 offset:16384\n"
"v_mfma_f32_16x16x32_bf16 a[76:79], v[172:175], v[136:139], a[76:79]\n"
"ds_read_b64_tr_b16 v[250:251], v10 offset:18432\n"
"v_mfma_f32_16x16x32_bf16 a[104:107], v[168:171], v[140:143], a[104:107]\n"
"ds_read_b64_tr_b16 v[252:253], v11 offset:16384\n"
"v_mfma_f32_16x16x32_bf16 a[108:111], v[172:175], v[140:143], a[108:111]\n"
"ds_read_b64_tr_b16 v[254:255], v11 offset:18432\n"
"v_mfma_f32_16x16x32_bf16 a[136:139], v[168:171], v[144:147], a[136:139]\n"
"v_mfma_f32_16x16x32_bf16 a[140:143], v[172:175], v[144:147], a[140:143]\n"
"v_mfma_f32_16x16x32_bf16 a[168:171], v[168:171], v[148:151], a[168:171]\n"
"v_mfma_f32_16x16x32_bf16 a[172:175], v[172:175], v[148:151], a[172:175]\n"
"s_waitcnt lgkmcnt(0)\n"
"s_barrier\n"
"v_mfma_f32_16x16x32_bf16 a[200:203], v[168:171], v[152:155], a[200:203]\n"
"s_mov_b32 m0, s64\n"
"v_mfma_f32_16x16x32_bf16 a[204:207], v[172:175], v[152:155], a[204:207]\n"
"buffer_load_dwordx4 v12, s[36:39], s60 offen lds\n"
"v_mfma_f32_16x16x32_bf16 a[232:235], v[168:171], v[156:159], a[232:235]\n"
"v_mfma_f32_16x16x32_bf16 a[236:239], v[172:175], v[156:159], a[236:239]\n"
"v_mfma_f32_16x16x32_bf16 a[16:19], v[176:179], v[128:131], a[16:19]\n"
"v_mfma_f32_16x16x32_bf16 a[20:23], v[180:183], v[128:131], a[20:23]\n"
"v_mfma_f32_16x16x32_bf16 a[48:51], v[176:179], v[132:135], a[48:51]\n"
"s_add_u32 m0, m0, 1024\n"
"v_mfma_f32_16x16x32_bf16 a[52:55], v[180:183], v[132:135], a[52:55]\n"
"buffer_load_dwordx4 v13, s[36:39], s60 offen lds\n"
"v_mfma_f32_16x16x32_bf16 a[80:83], v[176:179], v[136:139], a[80:83]\n"
"v_mfma_f32_16x16x32_bf16 a[84:87], v[180:183], v[136:139], a[84:87]\n"
"v_mfma_f32_16x16x32_bf16 a[112:115], v[176:179], v[140:143], a[112:115]\n"
"v_mfma_f32_16x16x32_bf16 a[116:119], v[180:183], v[140:143], a[116:119]\n"
"v_mfma_f32_16x16x32_bf16 a[144:147], v[176:179], v[144:147], a[144:147]\n"
"s_add_u32 m0, m0, 1024\n"
"v_mfma_f32_16x16x32_bf16 a[148:151], v[180:183], v[144:147], a[148:151]\n"
"buffer_load_dwordx4 v14, s[36:39], s60 offen lds\n"
"v_mfma_f32_16x16x32_bf16 a[176:179], v[176:179], v[148:151], a[176:179]\n"
"v_mfma_f32_16x16x32_bf16 a[180:183], v[180:183], v[148:151], a[180:183]\n"
"v_mfma_f32_16x16x32_bf16 a[208:211], v[176:179], v[152:155], a[208:211]\n"
"v_mfma_f32_16x16x32_bf16 a[212:215], v[180:183], v[152:155], a[212:215]\n"
"v_mfma_f32_16x16x32_bf16 a[240:243], v[176:179], v[156:159], a[240:243]\n"
"s_add_u32 m0, m0, 1024\n"
"v_mfma_f32_16x16x32_bf16 a[244:247], v[180:183], v[156:159], a[244:247]\n"
"buffer_load_dwordx4 v15, s[36:39], s60 offen lds\n"
"v_mfma_f32_16x16x32_bf16 a[24:27], v[184:187], v[128:131], a[24:27]\n"
"v_mfma_f32_16x16x32_bf16 a[28:31], v[188:191], v[128:131], a[28:31]\n"
"v_mfma_f32_16x16x32_bf16 a[56:59], v[184:187], v[132:135], a[56:59]\n"
"v_mfma_f32_16x16x32_bf16 a[60:63], v[188:191], v[132:135], a[60:63]\n"
"v_mfma_f32_16x16x32_bf16 a[88:91], v[184:187], v[136:139], a[88:91]\n"
"s_add_u32 m0, m0, 1024\n"
"v_mfma_f32_16x16x32_bf16 a[92:95], v[188:191], v[136:139], a[92:95]\n"
"buffer_load_dwordx4 v16, s[36:39], s60 offen lds\n"
"v_mfma_f32_16x16x32_bf16 a[120:123], v[184:187], v[140:143], a[120:123]\n"
"v_mfma_f32_16x16x32_bf16 a[124:127], v[188:191], v[140:143], a[124:127]\n"
"v_mfma_f32_16x16x32_bf16 a[152:155], v[184:187], v[144:147], a[152:155]\n"
"v_mfma_f32_16x16x32_bf16 a[156:159], v[188:191], v[144:147], a[156:159]\n"
"v_mfma_f32_16x16x32_bf16 a[184:187], v[184:187], v[148:151], a[184:187]\n"
"s_add_u32 m0, m0, 1024\n"
"v_mfma_f32_16x16x32_bf16 a[188:191], v[188:191], v[148:151], a[188:191]\n"
"buffer_load_dwordx4 v17, s[36:39], s60 offen lds\n"
"v_mfma_f32_16x16x32_bf16 a[216:219], v[184:187], v[152:155], a[216:219]\n"
"v_mfma_f32_16x16x32_bf16 a[220:223], v[188:191], v[152:155], a[220:223]\n"
"v_mfma_f32_16x16x32_bf16 a[248:251], v[184:187], v[156:159], a[248:251]\n"
"v_mfma_f32_16x16x32_bf16 a[252:255], v[188:191], v[156:159], a[252:255]\n"
"v_mfma_f32_16x16x32_bf16 a[0:3], v[224:227], v[192:195], a[0:3]\n"
"s_add_u32 m0, m0, 1024\n"
"v_mfma_f32_16x16x32_bf16 a[4:7], v[228:231], v[192:195], a[4:7]\n"
"buffer_load_dwordx4 v18, s[36:39], s60 offen lds\n"
"v_mfma_f32_16x16x32_bf16 a[32:35], v[224:227], v[196:199], a[32:35]\n"
"v_mfma_f32_16x16x32_bf16 a[36:39], v[228:231], v[196:199], a[36:39]\n"
"v_mfma_f32_16x16x32_bf16 a[64:67], v[224:227], v[200:203], a[64:67]\n"
"v_mfma_f32_16x16x32_bf16 a[68:71], v[228:231], v[200:203], a[68:71]\n"
"v_mfma_f32_16x16x32_bf16 a[96:99], v[224:227], v[204:207], a[96:99]\n"
"s_add_u32 m0, m0, 1024\n"
"v_mfma_f32_16x16x32_bf16 a[100:103], v[228:231], v[204:207], a[100:103]\n"
"buffer_load_dwordx4 v19, s[36:39], s60 offen lds\n"
"v_mfma_f32_16x16x32_bf16 a[128:131], v[224:227], v[208:211], a[128:131]\n"
"v_mfma_f32_16x16x32_bf16 a[132:135], v[228:231], v[208:211], a[132:135]\n"
"v_mfma_f32_16x16x32_bf16 a[160:163], v[224:227], v[212:215], a[160:163]\n"
"v_mfma_f32_16x16x32_bf16 a[164:167], v[228:231], v[212:215], a[164:167]\n"
"v_mfma_f32_16x16x32_bf16 a[192:195], v[224:227], v[216:219], a[192:195]\n"
"s_mov_b32 m0, s66\n"
"v_mfma_f32_16x16x32_bf16 a[196:199], v[228:231], v[216:219], a[196:199]\n"
"buffer_load_dwordx4 v20, s[40:43], s61 offen lds\n"
"v_mfma_f32_16x16x32_bf16 a[224:227], v[224:227], v[220:223], a[224:227]\n"
"v_mfma_f32_16x16x32_bf16 a[228:231], v[228:231], v[220:223], a[228:231]\n"
"v_mfma_f32_16x16x32_bf16 a[8:11], v[232:235], v[192:195], a[8:11]\n"
"v_mfma_f32_16x16x32_bf16 a[12:15], v[236:239], v[192:195], a[12:15]\n"
"v_mfma_f32_16x16x32_bf16 a[40:43], v[232:235], v[196:199], a[40:43]\n"
"s_add_u32 m0, m0, 1024\n"
"v_mfma_f32_16x16x32_bf16 a[44:47], v[236:239], v[196:199], a[44:47]\n"
"buffer_load_dwordx4 v21, s[40:43], s61 offen lds\n"
"v_mfma_f32_16x16x32_bf16 a[72:75], v[232:235], v[200:203], a[72:75]\n"
"v_mfma_f32_16x16x32_bf16 a[76:79], v[236:239], v[200:203], a[76:79]\n"
"v_mfma_f32_16x16x32_bf16 a[104:107], v[232:235], v[204:207], a[104:107]\n"
"v_mfma_f32_16x16x32_bf16 a[108:111], v[236:239], v[204:207], a[108:111]\n"
"v_mfma_f32_16x16x32_bf16 a[136:139], v[232:235], v[208:211], a[136:139]\n"
"s_add_u32 m0, m0, 1024\n"
"v_mfma_f32_16x16x32_bf16 a[140:143], v[236:239], v[208:211], a[140:143]\n"
"buffer_load_dwordx4 v22, s[40:43], s61 offen lds\n"
"v_mfma_f32_16x16x32_bf16 a[168:171], v[232:235], v[212:215], a[168:171]\n"
"v_mfma_f32_16x16x32_bf16 a[172:175], v[236:239], v[212:215], a[172:175]\n"
"v_mfma_f32_16x16x32_bf16 a[200:203], v[232:235], v[216:219], a[200:203]\n"
"v_mfma_f32_16x16x32_bf16 a[204:207], v[236:239], v[216:219], a[204:207]\n"
"v_mfma_f32_16x16x32_bf16 a[232:235], v[232:235], v[220:223], a[232:235]\n"
"s_add_u32 m0, m0, 1024\n"
"v_mfma_f32_16x16x32_bf16 a[236:239], v[236:239], v[220:223], a[236:239]\n"
"buffer_load_dwordx4 v23, s[40:43], s61 offen lds\n"
"v_mfma_f32_16x16x32_bf16 a[16:19], v[240:243], v[192:195], a[16:19]\n"
"s_waitcnt vmcnt(12)\n"
"s_barrier\n"
"v_mfma_f32_16x16x32_bf16 a[20:23], v[244:247], v[192:195], a[20:23]\n"
"ds_read_b64_tr_b16 v[160:161], v4 offset:32768\n"
"v_mfma_f32_16x16x32_bf16 a[48:51], v[240:243], v[196:199], a[48:51]\n"
"buffer_load_dword v37, v76, s[80:83], 0 offen\n"
"ds_read_b64_tr_b16 v[162:163], v4 offset:34816\n"
"v_mfma_f32_16x16x32_bf16 a[52:55], v[244:247], v[196:199], a[52:55]\n"
"v_add_u32 v76, 8192, v76\n"
"ds_read_b64_tr_b16 v[164:165], v5 offset:32768\n"
"v_mfma_f32_16x16x32_bf16 a[80:83], v[240:243], v[200:203], a[80:83]\n"
"s_add_u32 m0, m0, 1024\n"
"ds_read_b64_tr_b16 v[166:167], v5 offset:34816\n"
"v_mfma_f32_16x16x32_bf16 a[84:87], v[244:247], v[200:203], a[84:87]\n"
"buffer_load_dwordx4 v24, s[40:43], s61 offen lds\n"
"ds_read_b128 v[128:131], v2 offset:32768\n"
"v_mfma_f32_16x16x32_bf16 a[112:115], v[240:243], v[204:207], a[112:115]\n"
"ds_read_b128 v[132:135], v2 offset:34816\n"
"v_mfma_f32_16x16x32_bf16 a[116:119], v[244:247], v[204:207], a[116:119]\n"
"ds_read_b128 v[136:139], v2 offset:36864\n"
"v_mfma_f32_16x16x32_bf16 a[144:147], v[240:243], v[208:211], a[144:147]\n"
"ds_read_b128 v[140:143], v2 offset:38912\n"
"v_mfma_f32_16x16x32_bf16 a[148:151], v[244:247], v[208:211], a[148:151]\n"
"ds_read_b128 v[144:147], v2 offset:40960\n"
"v_mfma_f32_16x16x32_bf16 a[176:179], v[240:243], v[212:215], a[176:179]\n"
"s_add_u32 m0, m0, 1024\n"
"ds_read_b128 v[148:151], v2 offset:43008\n"
"v_mfma_f32_16x16x32_bf16 a[180:183], v[244:247], v[212:215], a[180:183]\n"
"buffer_load_dwordx4 v25, s[40:43], s61 offen lds\n"
"ds_read_b128 v[152:155], v2 offset:45056\n"
"v_mfma_f32_16x16x32_bf16 a[208:211], v[240:243], v[216:219], a[208:211]\n"
"ds_read_b128 v[156:159], v2 offset:47104\n"
"v_mfma_f32_16x16x32_bf16 a[212:215], v[244:247], v[216:219], a[212:215]\n"
"ds_read_b64_tr_b16 v[168:169], v6 offset:32768\n"
"v_mfma_f32_16x16x32_bf16 a[240:243], v[240:243], v[220:223], a[240:243]\n"
"ds_read_b64_tr_b16 v[170:171], v6 offset:34816\n"
"v_mfma_f32_16x16x32_bf16 a[244:247], v[244:247], v[220:223], a[244:247]\n"
"ds_read_b64_tr_b16 v[172:173], v7 offset:32768\n"
"v_mfma_f32_16x16x32_bf16 a[24:27], v[248:251], v[192:195], a[24:27]\n"
"s_add_u32 m0, m0, 1024\n"
"ds_read_b64_tr_b16 v[174:175], v7 offset:34816\n"
"v_mfma_f32_16x16x32_bf16 a[28:31], v[252:255], v[192:195], a[28:31]\n"
"buffer_load_dwordx4 v26, s[40:43], s61 offen lds\n"
"ds_read_b64_tr_b16 v[176:177], v8 offset:32768\n"
"v_mfma_f32_16x16x32_bf16 a[56:59], v[248:251], v[196:199], a[56:59]\n"
"ds_read_b64_tr_b16 v[178:179], v8 offset:34816\n"
"v_mfma_f32_16x16x32_bf16 a[60:63], v[252:255], v[196:199], a[60:63]\n"
"ds_read_b64_tr_b16 v[180:181], v9 offset:32768\n"
"v_mfma_f32_16x16x32_bf16 a[88:91], v[248:251], v[200:203], a[88:91]\n"
"ds_read_b64_tr_b16 v[182:183], v9 offset:34816\n"
"v_mfma_f32_16x16x32_bf16 a[92:95], v[252:255], v[200:203], a[92:95]\n"
"ds_read_b64_tr_b16 v[184:185], v10 offset:32768\n"
"v_mfma_f32_16x16x32_bf16 a[120:123], v[248:251], v[204:207], a[120:123]\n"
"s_add_u32 m0, m0, 1024\n"
"ds_read_b64_tr_b16 v[186:187], v10 offset:34816\n"
"v_mfma_f32_16x16x32_bf16 a[124:127], v[252:255], v[204:207], a[124:127]\n"
"buffer_load_dwordx4 v27, s[40:43], s61 offen lds\n"
"s_add_u32 s60, s60, 128\n"
"s_add_u32 s61, s61, 524288\n"
"ds_read_b64_tr_b16 v[188:189], v11 offset:32768\n"
"v_mfma_f32_16x16x32_bf16 a[152:155], v[248:251], v[208:211], a[152:155]\n"
"ds_read_b64_tr_b16 v[190:191], v11 offset:34816\n"
"v_mfma_f32_16x16x32_bf16 a[156:159], v[252:255], v[208:211], a[156:159]\n"
"v_mfma_f32_16x16x32_bf16 a[184:187], v[248:251], v[212:215], a[184:187]\n"
"v_mfma_f32_16x16x32_bf16 a[188:191], v[252:255], v[212:215], a[188:191]\n"
"v_mfma_f32_16x16x32_bf16 a[216:219], v[248:251], v[216:219], a[216:219]\n"
"v_mfma_f32_16x16x32_bf16 a[220:223], v[252:255], v[216:219], a[220:223]\n"
"v_mfma_f32_16x16x32_bf16 a[248:251], v[248:251], v[220:223], a[248:251]\n"
"s_sub_u32 s62, s62, 1\n"
"s_cmp_eq_u32 s62, 0\n"
"v_mfma_f32_16x16x32_bf16 a[252:255], v[252:255], v[220:223], a[252:255]\n"
"s_waitcnt lgkmcnt(0)\n"
"s_cbranch_scc1 L_epi%=\n"
"L_mid%=:\n"
"s_cmp_lg_u32 s62, 2\n"
"s_cbranch_scc1 L_ns1%=\n"
"s_mov_b64 s[36:37], s[48:49]\n"
"s_mov_b64 s[38:39], s[50:51]\n"
"s_mov_b64 s[40:41], s[52:53]\n"
"s_mov_b64 s[42:43], s[54:55]\n"
"s_mov_b32 s50, 0\n"
"s_mov_b32 s54, 0\n"
"s_mov_b32 s60, 0\n"
"s_mov_b32 s61, 0\n"
"v_mov_b32 v12, v28\n"
"v_mov_b32 v13, v29\n"
"v_mov_b32 v14, v30\n"
"v_mov_b32 v15, v31\n"
"v_mov_b32 v16, v32\n"
"v_mov_b32 v17, v33\n"
"v_mov_b32 v18, v34\n"
"v_mov_b32 v19, v35\n"
"L_ns1%=:\n"
"v_mfma_f32_16x16x32_bf16 a[0:3], v[160:163], v[128:131], a[0:3]\n"
"ds_read_b64_tr_b16 v[224:225], v4 offset:49152\n"
"v_mfma_f32_16x16x32_bf16 a[4:7], v[164:167], v[128:131], a[4:7]\n"
"ds_read_b64_tr_b16 v[226:227], v4 offset:51200\n"
"v_mfma_f32_16x16x32_bf16 a[32:35], v[160:163], v[132:135], a[32:35]\n"
"ds_read_b64_tr_b16 v[228:229], v5 offset:49152\n"
"v_mfma_f32_16x16x32_bf16 a[36:39], v[164:167], v[132:135], a[36:39]\n"
"ds_read_b64_tr_b16 v[230:231], v5 offset:51200\n"
"v_mfma_f32_16x16x32_bf16 a[64:67], v[160:163], v[136:139], a[64:67]\n"
"ds_read_b128 v[192:195], v3 offset:32768\n"
"v_mfma_f32_16x16x32_bf16 a[68:71], v[164:167], v[136:139], a[68:71]\n"
"ds_read_b128 v[196:199], v3 offset:34816\n"
"v_mfma_f32_16x16x32_bf16 a[96:99], v[160:163], v[140:143], a[96:99]\n"
"ds_read_b128 v[200:203], v3 offset:36864\n"
"v_mfma_f32_16x16x32_bf16 a[100:103], v[164:167], v[140:143], a[100:103]\n"
"ds_read_b128 v[204:207], v3 offset:38912\n"
"v_mfma_f32_16x16x32_bf16 a[128:131], v[160:163], v[144:147], a[128:131]\n"
"ds_read_b128 v[208:211], v3 offset:40960\n"
"v_mfma_f32_16x16x32_bf16 a[132:135], v[164:167], v[144:147], a[132:135]\n"
"ds_read_b128 v[212:215], v3 offset:43008\n"
"v_mfma_f32_16x16x32_bf16 a[160:163], v[160:163], v[148:151], a[160:163]\n"
"ds_read_b128 v[216:219], v3 offset:45056\n"
"v_mfma_f32_16x16x32_bf16 a[164:167], v[164:167], v[148:151], a[164:167]\n"
"ds_read_b128 v[220:223], v3 offset:47104\n"
"v_mfma_f32_16x16x32_bf16 a[192:195], v[160:163], v[152:155], a[192:195]\n"
"ds_read_b64_tr_b16 v[232:233], v6 offset:49152\n"
"v_mfma_f32_16x16x32_bf16 a[196:199], v[164:167], v[152:155], a[196:199]\n"
"ds_read_b64_tr_b16 v[234:235], v6 offset:51200\n"
"v_mfma_f32_16x16x32_bf16 a[224:227], v[160:163], v[156:159], a[224:227]\n"
"ds_read_b64_tr_b16 v[236:237], v7 offset:49152\n"
"v_mfma_f32_16x16x32_bf16 a[228:231], v[164:167], v[156:159], a[228:231]\n"
"ds_read_b64_tr_b16 v[238:239], v7 offset:51200\n"
"v_mfma_f32_16x16x32_bf16 a[8:11], v[168:171], v[128:131], a[8:11]\n"
"ds_read_b64_tr_b16 v[240:241], v8 offset:49152\n"
"v_mfma_f32_16x16x32_bf16 a[12:15], v[172:175], v[128:131], a[12:15]\n"
"ds_read_b64_tr_b16 v[242:243], v8 offset:51200\n"
"v_mfma_f32_16x16x32_bf16 a[40:43], v[168:171], v[132:135], a[40:43]\n"
"ds_read_b64_tr_b16 v[244:245], v9 offset:49152\n"
"v_mfma_f32_16x16x32_bf16 a[44:47], v[172:175], v[132:135], a[44:47]\n"
"ds_read_b64_tr_b16 v[246:247], v9 offset:51200\n"
"v_mfma_f32_16x16x32_bf16 a[72:75], v[168:171], v[136:139], a[72:75]\n"
"ds_read_b64_tr_b16 v[248:249], v10 offset:49152\n"
"v_mfma_f32_16x16x32_bf16 a[76:79], v[172:175], v[136:139], a[76:79]\n"
"ds_read_b64_tr_b16 v[250:251], v10 offset:51200\n"
"v_mfma_f32_16x16x32_bf16 a[104:107], v[168:171], v[140:143], a[104:107]\n"
"ds_read_b64_tr_b16 v[252:253], v11 offset:49152\n"
"v_mfma_f32_16x16x32_bf16 a[108:111], v[172:175], v[140:143], a[108:111]\n"
"ds_read_b64_tr_b16 v[254:255], v11 offset:51200\n"
"v_mfma_f32_16x16x32_bf16 a[136:139], v[168:171], v[144:147], a[136:139]\n"
"v_mfma_f32_16x16x32_bf16 a[140:143], v[172:175], v[144:147], a[140:143]\n"
"v_mfma_f32_16x16x32_bf16 a[168:171], v[168:171], v[148:151], a[168:171]\n"
"v_mfma_f32_16x16x32_bf16 a[172:175], v[172:175], v[148:151], a[172:175]\n"
"s_waitcnt lgkmcnt(0)\n"
"s_barrier\n"
"v_mfma_f32_16x16x32_bf16 a[200:203], v[168:171], v[152:155], a[200:203]\n"
"s_mov_b32 m0, s65\n"
"v_mfma_f32_16x16x32_bf16 a[204:207], v[172:175], v[152:155], a[204:207]\n"
"buffer_load_dwordx4 v12, s[36:39], s60 offen lds\n"
"v_mfma_f32_16x16x32_bf16 a[232:235], v[168:171], v[156:159], a[232:235]\n"
"v_mfma_f32_16x16x32_bf16 a[236:239], v[172:175], v[156:159], a[236:239]\n"
"v_mfma_f32_16x16x32_bf16 a[16:19], v[176:179], v[128:131], a[16:19]\n"
"v_mfma_f32_16x16x32_bf16 a[20:23], v[180:183], v[128:131], a[20:23]\n"
"v_mfma_f32_16x16x32_bf16 a[48:51], v[176:179], v[132:135], a[48:51]\n"
"s_add_u32 m0, m0, 1024\n"
"v_mfma_f32_16x16x32_bf16 a[52:55], v[180:183], v[132:135], a[52:55]\n"
"buffer_load_dwordx4 v13, s[36:39], s60 offen lds\n"
"v_mfma_f32_16x16x32_bf16 a[80:83], v[176:179], v[136:139], a[80:83]\n"
"v_mfma_f32_16x16x32_bf16 a[84:87], v[180:183], v[136:139], a[84:87]\n"
"v_mfma_f32_16x16x32_bf16 a[112:115], v[176:179], v[140:143], a[112:115]\n"
"v_mfma_f32_16x16x32_bf16 a[116:119], v[180:183], v[140:143], a[116:119]\n"
"v_mfma_f32_16x16x32_bf16 a[144:147], v[176:179], v[144:147], a[144:147]\n"
"s_add_u32 m0, m0, 1024\n"
"v_mfma_f32_16x16x32_bf16 a[148:151], v[180:183], v[144:147], a[148:151]\n"
"buffer_load_dwordx4 v14, s[36:39], s60 offen lds\n"
"v_mfma_f32_16x16x32_bf16 a[176:179], v[176:179], v[148:151], a[176:179]\n"
"v_mfma_f32_16x16x32_bf16 a[180:183], v[180:183], v[148:151], a[180:183]\n"
"v_mfma_f32_16x16x32_bf16 a[208:211], v[176:179], v[152:155], a[208:211]\n"
"v_mfma_f32_16x16x32_bf16 a[212:215], v[180:183], v[152:155], a[212:215]\n"
"v_mfma_f32_16x16x32_bf16 a[240:243], v[176:179], v[156:159], a[240:243]\n"
"s_add_u32 m0, m0, 1024\n"
"v_mfma_f32_16x16x32_bf16 a[244:247], v[180:183], v[156:159], a[244:247]\n"
"buffer_load_dwordx4 v15, s[36:39], s60 offen lds\n"
"v_mfma_f32_16x16x32_bf16 a[24:27], v[184:187], v[128:131], a[24:27]\n"
"v_mfma_f32_16x16x32_bf16 a[28:31], v[188:191], v[128:131], a[28:31]\n"
"v_mfma_f32_16x16x32_bf16 a[56:59], v[184:187], v[132:135], a[56:59]\n"
"v_mfma_f32_16x16x32_bf16 a[60:63], v[188:191], v[132:135], a[60:63]\n"
"v_mfma_f32_16x16x32_bf16 a[88:91], v[184:187], v[136:139], a[88:91]\n"
"s_add_u32 m0, m0, 1024\n"
"v_mfma_f32_16x16x32_bf16 a[92:95], v[188:191], v[136:139], a[92:95]\n"
"buffer_load_dwordx4 v16, s[36:39], s60 offen lds\n"
"v_mfma_f32_16x16x32_bf16 a[120:123], v[184:187], v[140:143], a[120:123]\n"
"v_mfma_f32_16x16x32_bf16 a[124:127], v[188:191], v[140:143], a[124:127]\n"
"v_mfma_f32_16x16x32_bf16 a[152:155], v[184:187], v[144:147], a[152:155]\n"
"v_mfma_f32_16x16x32_bf16 a[156:159], v[188:191], v[144:147], a[156:159]\n"
"v_mfma_f32_16x16x32_bf16 a[184:187], v[184:187], v[148:151], a[184:187]\n"
"s_add_u32 m0, m0, 1024\n"
"v_mfma_f32_16x16x32_bf16 a[188:191], v[188:191], v[148:151], a[188:191]\n"
"buffer_load_dwordx4 v17, s[36:39], s60 offen lds\n"
"v_mfma_f32_16x16x32_bf16 a[216:219], v[184:187], v[152:155], a[216:219]\n"
"v_mfma_f32_16x16x32_bf16 a[220:223], v[188:191], v[152:155], a[220:223]\n"
"v_mfma_f32_16x16x32_bf16 a[248:251], v[184:187], v[156:159], a[248:251]\n"
"v_mfma_f32_16x16x32_bf16 a[252:255], v[188:191], v[156:159], a[252:255]\n"
"v_mfma_f32_16x16x32_bf16 a[0:3], v[224:227], v[192:195], a[0:3]\n"
"s_add_u32 m0, m0, 1024\n"
"v_mfma_f32_16x16x32_bf16 a[4:7], v[228:231], v[192:195], a[4:7]\n"
"buffer_load_dwordx4 v18, s[36:39], s60 offen lds\n"
"v_mfma_f32_16x16x32_bf16 a[32:35], v[224:227], v[196:199], a[32:35]\n"
"v_mfma_f32_16x16x32_bf16 a[36:39], v[228:231], v[196:199], a[36:39]\n"
"v_mfma_f32_16x16x32_bf16 a[64:67], v[224:227], v[200:203], a[64:67]\n"
"v_mfma_f32_16x16x32_bf16 a[68:71], v[228:231], v[200:203], a[68:71]\n"
"v_mfma_f32_16x16x32_bf16 a[96:99], v[224:227], v[204:207], a[96:99]\n"
"s_add_u32 m0, m0, 1024\n"
"v_mfma_f32_16x16x32_bf16 a[100:103], v[228:231], v[204:207], a[100:103]\n"
"buffer_load_dwordx4 v19, s[36:39], s60 offen lds\n"
"v_mfma_f32_16x16x32_bf16 a[128:131], v[224:227], v[208:211], a[128:131]\n"
"v_mfma_f32_16x16x32_bf16 a[132:135], v[228:231], v[208:211], a[132:135]\n"
"v_mfma_f32_16x16x32_bf16 a[160:163], v[224:227], v[212:215], a[160:163]\n"
"v_mfma_f32_16x16x32_bf16 a[164:167], v[228:231], v[212:215], a[164:167]\n"
"v_mfma_f32_16x16x32_bf16 a[192:195], v[224:227], v[216:219], a[192:195]\n"
"s_mov_b32 m0, s67\n"
"v_mfma_f32_16x16x32_bf16 a[196:199], v[228:231], v[216:219], a[196:199]\n"
"buffer_load_dwordx4 v20, s[40:43], s61 offen lds\n"
"v_mfma_f32_16x16x32_bf16 a[224:227], v[224:227], v[220:223], a[224:227]\n"
"v_mfma_f32_16x16x32_bf16 a[228:231], v[228:231], v[220:223], a[228:231]\n"
"v_mfma_f32_16x16x32_bf16 a[8:11], v[232:235], v[192:195], a[8:11]\n"
"v_mfma_f32_16x16x32_bf16 a[12:15], v[236:239], v[192:195], a[12:15]\n"
"v_mfma_f32_16x16x32_bf16 a[40:43], v[232:235], v[196:199], a[40:43]\n"
"s_add_u32 m0, m0, 1024\n"
"v_mfma_f32_16x16x32_bf16 a[44:47], v[236:239], v[196:199], a[44:47]\n"
"buffer_load_dwordx4 v21, s[40:43], s61 offen lds\n"
"v_mfma_f32_16x16x32_bf16 a[72:75], v[232:235], v[200:203], a[72:75]\n"
"v_mfma_f32_16x16x32_bf16 a[76:79], v[236:239], v[200:203], a[76:79]\n"
"v_mfma_f32_16x16x32_bf16 a[104:107], v[232:235], v[204:207], a[104:107]\n"
"v_mfma_f32_16x16x32_bf16 a[108:111], v[236:239], v[204:207], a[108:111]\n"
"v_mfma_f32_16x16x32_bf16 a[136:139], v[232:235], v[208:211], a[136:139]\n"
"s_add_u32 m0, m0, 1024\n"
"v_mfma_f32_16x16x32_bf16 a[140:143], v[236:239], v[208:211], a[140:143]\n"
"buffer_load_dwordx4 v22, s[40:43], s61 offen lds\n"
"v_mfma_f32_16x16x32_bf16 a[168:171], v[232:235], v[212:215], a[168:171]\n"
"v_mfma_f32_16x16x32_bf16 a[172:175], v[236:239], v[212:215], a[172:175]\n"
"v_mfma_f32_16x16x32_bf16 a[200:203], v[232:235], v[216:219], a[200:203]\n"
"v_mfma_f32_16x16x32_bf16 a[204:207], v[236:239], v[216:219], a[204:207]\n"
"v_mfma_f32_16x16x32_bf16 a[232:235], v[232:235], v[220:223], a[232:235]\n"
"s_add_u32 m0, m0, 1024\n"
"v_mfma_f32_16x16x32_bf16 a[236:239], v[236:239], v[220:223], a[236:239]\n"
"buffer_load_dwordx4 v23, s[40:43], s61 offen lds\n"
"v_mfma_f32_16x16x32_bf16 a[16:19], v[240:243], v[192:195], a[16:19]\n"
"s_waitcnt vmcnt(12)\n"
"s_barrier\n"
"v_mfma_f32_16x16x32_bf16 a[20:23], v[244:247], v[192:195], a[20:23]\n"
"ds_read_b64_tr_b16 v[160:161], v4 offset:0\n"
"v_mfma_f32_16x16x32_bf16 a[48:51], v[240:243], v[196:199], a[48:51]\n"
"buffer_load_dword v37, v76, s[80:83], 0 offen\n"
"ds_read_b64_tr_b16 v[162:163], v4 offset:2048\n"
"v_mfma_f32_16x16x32_bf16 a[52:55], v[244:247], v[196:199], a[52:55]\n"
"v_add_u32 v76, 8192, v76\n"
"ds_read_b64_tr_b16 v[164:165], v5 offset:0\n"
"v_mfma_f32_16x16x32_bf16 a[80:83], v[240:243], v[200:203], a[80:83]\n"
"s_add_u32 m0, m0, 1024\n"
"ds_read_b64_tr_b16 v[166:167], v5 offset:2048\n"
"v_mfma_f32_16x16x32_bf16 a[84:87], v[244:247], v[200:203], a[84:87]\n"
"buffer_load_dwordx4 v24, s[40:43], s61 offen lds\n"
"ds_read_b128 v[128:131], v2 offset:0\n"
"v_mfma_f32_16x16x32_bf16 a[112:115], v[240:243], v[204:207], a[112:115]\n"
"ds_read_b128 v[132:135], v2 offset:2048\n"
"v_mfma_f32_16x16x32_bf16 a[116:119], v[244:247], v[204:207], a[116:119]\n"
"ds_read_b128 v[136:139], v2 offset:4096\n"
"v_mfma_f32_16x16x32_bf16 a[144:147], v[240:243], v[208:211], a[144:147]\n"
"ds_read_b128 v[140:143], v2 offset:6144\n"
"v_mfma_f32_16x16x32_bf16 a[148:151], v[244:247], v[208:211], a[148:151]\n"
"ds_read_b128 v[144:147], v2 offset:8192\n"
"v_mfma_f32_16x16x32_bf16 a[176:179], v[240:243], v[212:215], a[176:179]\n"
"s_add_u32 m0, m0, 1024\n"
"ds_read_b128 v[148:151], v2 offset:10240\n"
"v_mfma_f32_16x16x32_bf16 a[180:183], v[244:247], v[212:215], a[180:183]\n"
"buffer_load_dwordx4 v25, s[40:43], s61 offen lds\n"
"ds_read_b128 v[152:155], v2 offset:12288\n"
"v_mfma_f32_16x16x32_bf16 a[208:211], v[240:243], v[216:219], a[208:211]\n"
"ds_read_b128 v[156:159], v2 offset:14336\n"
"v_mfma_f32_16x16x32_bf16 a[212:215], v[244:247], v[216:219], a[212:215]\n"
"ds_read_b64_tr_b16 v[168:169], v6 offset:0\n"
"v_mfma_f32_16x16x32_bf16 a[240:243], v[240:243], v[220:223], a[240:243]\n"
"ds_read_b64_tr_b16 v[170:171], v6 offset:2048\n"
"v_mfma_f32_16x16x32_bf16 a[244:247], v[244:247], v[220:223], a[244:247]\n"
"ds_read_b64_tr_b16 v[172:173], v7 offset:0\n"
"v_mfma_f32_16x16x32_bf16 a[24:27], v[248:251], v[192:195], a[24:27]\n"
"s_add_u32 m0, m0, 1024\n"
"ds_read_b64_tr_b16 v[174:175], v7 offset:2048\n"
"v_mfma_f32_16x16x32_bf16 a[28:31], v[252:255], v[192:195], a[28:31]\n"
"buffer_load_dwordx4 v26, s[40:43], s61 offen lds\n"
"ds_read_b64_tr_b16 v[176:177], v8 offset:0\n"
"v_mfma_f32_16x16x32_bf16 a[56:59], v[248:251], v[196:199], a[56:59]\n"
"ds_read_b64_tr_b16 v[178:179], v8 offset:2048\n"
"v_mfma_f32_16x16x32_bf16 a[60:63], v[252:255], v[196:199], a[60:63]\n"
"ds_read_b64_tr_b16 v[180:181], v9 offset:0\n"
"v_mfma_f32_16x16x32_bf16 a[88:91], v[248:251], v[200:203], a[88:91]\n"
"ds_read_b64_tr_b16 v[182:183], v9 offset:2048\n"
"v_mfma_f32_16x16x32_bf16 a[92:95], v[252:255], v[200:203], a[92:95]\n"
"ds_read_b64_tr_b16 v[184:185], v10 offset:0\n"
"v_mfma_f32_16x16x32_bf16 a[120:123], v[248:251], v[204:207], a[120:123]\n"
"s_add_u32 m0, m0, 1024\n"
"ds_read_b64_tr_b16 v[186:187], v10 offset:2048\n"
"v_mfma_f32_16x16x32_bf16 a[124:127], v[252:255], v[204:207], a[124:127]\n"
"buffer_load_dwordx4 v27, s[40:43], s61 offen lds\n"
"s_add_u32 s60, s60, 128\n"
"s_add_u32 s61, s61, 524288\n"
"ds_read_b64_tr_b16 v[188:189], v11 offset:0\n"
"v_mfma_f32_16x16x32_bf16 a[152:155], v[248:251], v[208:211], a[152:155]\n"
"ds_read_b64_tr_b16 v[190:191], v11 offset:2048\n"
"v_mfma_f32_16x16x32_bf16 a[156:159], v[252:255], v[208:211], a[156:159]\n"
"v_mfma_f32_16x16x32_bf16 a[184:187], v[248:251], v[212:215], a[184:187]\n"
"v_mfma_f32_16x16x32_bf16 a[188:191], v[252:255], v[212:215], a[188:191]\n"
"v_mfma_f32_16x16x32_bf16 a[216:219], v[248:251], v[216:219], a[216:219]\n"
"v_mfma_f32_16x16x32_bf16 a[220:223], v[252:255], v[216:219], a[220:223]\n"
"v_mfma_f32_16x16x32_bf16 a[248:251], v[248:251], v[220:223], a[248:251]\n"
"s_sub_u32 s62, s62, 1\n"
"s_cmp_eq_u32 s62, 0\n"
"v_mfma_f32_16x16x32_bf16 a[252:255], v[252:255], v[220:223], a[252:255]\n"
"s_waitcnt lgkmcnt(0)\n"
"s_cbranch_scc0 L_top%=\n"
"L_epi%=:\n"
"s_nop 7\n"
"s_cmp_eq_u32 s63, 0\n"
"s_cbranch_scc1 L_nr%=\n"
"s_lshr_b32 s68, s76, 1\n"
"s_mul_i32 s68, s68, 131072\n"
"s_and_b32 s69, s76, 1\n"
"s_lshl_b32 s69, s69, 12\n"
"s_add_u32 s68, s68, s69\n"
"s_add_u32 s69, s68, 0\n"
"v_accvgpr_read_b32 v40, a0\n"
"v_accvgpr_read_b32 v41, a1\n"
"v_accvgpr_read_b32 v42, a2\n"
"v_accvgpr_read_b32 v43, a3\n"
"v_accvgpr_read_b32 v44, a4\n"
"v_accvgpr_read_b32 v45, a5\n"
"v_accvgpr_read_b32 v46, a6\n"
"v_accvgpr_read_b32 v47, a7\n"
"v_cvt_pk_bf16_f32 v40, v40, v41\n"
"v_cvt_pk_bf16_f32 v41, v42, v43\n"
"v_cvt_pk_bf16_f32 v42, v44, v45\n"
"v_cvt_pk_bf16_f32 v43, v46, v47\n"
"buffer_store_dwordx4 v[40:43], v36, s[44:47], s69 offen offset:0\n"
"v_accvgpr_read_b32 v48, a8\n"
"v_accvgpr_read_b32 v49, a9\n"
"v_accvgpr_read_b32 v50, a10\n"
"v_accvgpr_read_b32 v51, a11\n"
"v_accvgpr_read_b32 v52, a12\n"
"v_accvgpr_read_b32 v53, a13\n"
"v_accvgpr_read_b32 v54, a14\n"
"v_accvgpr_read_b32 v55, a15\n"
"v_cvt_pk_bf16_f32 v48, v48, v49\n"
"v_cvt_pk_bf16_f32 v49, v50, v51\n"
"v_cvt_pk_bf16_f32 v50, v52, v53\n"
"v_cvt_pk_bf16_f32 v51, v54, v55\n"
"buffer_store_dwordx4 v[48:51], v36, s[44:47], s69 offen offset:1024\n"
"v_accvgpr_read_b32 v56, a16\n"
"v_accvgpr_read_b32 v57, a17\n"
"v_accvgpr_read_b32 v58, a18\n"
"v_accvgpr_read_b32 v59, a19\n"
"v_accvgpr_read_b32 v60, a20\n"
"v_accvgpr_read_b32 v61, a21\n"
"v_accvgpr_read_b32 v62, a22\n"
"v_accvgpr_read_b32 v63, a23\n"
"v_cvt_pk_bf16_f32 v56, v56, v57\n"
"v_cvt_pk_bf16_f32 v57, v58, v59\n"
"v_cvt_pk_bf16_f32 v58, v60, v61\n"
"v_cvt_pk_bf16_f32 v59, v62, v63\n"
"buffer_store_dwordx4 v[56:59], v36, s[44:47], s69 offen offset:2048\n"
"v_accvgpr_read_b32 v64, a24\n"
"v_accvgpr_read_b32 v65, a25\n"
"v_accvgpr_read_b32 v66, a26\n"
"v_accvgpr_read_b32 v67, a27\n"
"v_accvgpr_read_b32 v68, a28\n"
"v_accvgpr_read_b32 v69, a29\n"
"v_accvgpr_read_b32 v70, a30\n"
"v_accvgpr_read_b32 v71, a31\n"
"v_cvt_pk_bf16_f32 v64, v64, v65\n"
"v_cvt_pk_bf16_f32 v65, v66, v67\n"
"v_cvt_pk_bf16_f32 v66, v68, v69\n"
"v_cvt_pk_bf16_f32 v67, v70, v71\n"
"buffer_store_dwordx4 v[64:67], v36, s[44:47], s69 offen offset:3072\n"
"s_add_u32 s69, s68, 8192\n"
"v_accvgpr_read_b32 v40, a32\n"
"v_accvgpr_read_b32 v41, a33\n"
"v_accvgpr_read_b32 v42, a34\n"
"v_accvgpr_read_b32 v43, a35\n"
"v_accvgpr_read_b32 v44, a36\n"
"v_accvgpr_read_b32 v45, a37\n"
"v_accvgpr_read_b32 v46, a38\n"
"v_accvgpr_read_b32 v47, a39\n"
"v_cvt_pk_bf16_f32 v40, v40, v41\n"
"v_cvt_pk_bf16_f32 v41, v42, v43\n"
"v_cvt_pk_bf16_f32 v42, v44, v45\n"
"v_cvt_pk_bf16_f32 v43, v46, v47\n"
"buffer_store_dwordx4 v[40:43], v36, s[44:47], s69 offen offset:0\n"
"v_accvgpr_read_b32 v48, a40\n"
"v_accvgpr_read_b32 v49, a41\n"
"v_accvgpr_read_b32 v50, a42\n"
"v_accvgpr_read_b32 v51, a43\n"
"v_accvgpr_read_b32 v52, a44\n"
"v_accvgpr_read_b32 v53, a45\n"
"v_accvgpr_read_b32 v54, a46\n"
"v_accvgpr_read_b32 v55, a47\n"
"v_cvt_pk_bf16_f32 v48, v48, v49\n"
"v_cvt_pk_bf16_f32 v49, v50, v51\n"
"v_cvt_pk_bf16_f32 v50, v52, v53\n"
"v_cvt_pk_bf16_f32 v51, v54, v55\n"
"buffer_store_dwordx4 v[48:51], v36, s[44:47], s69 offen offset:1024\n"
"v_accvgpr_read_b32 v56, a48\n"
"v_accvgpr_read_b32 v57, a49\n"
"v_accvgpr_read_b32 v58, a50\n"
"v_accvgpr_read_b32 v59, a51\n"
"v_accvgpr_read_b32 v60, a52\n"
"v_accvgpr_read_b32 v61, a53\n"
"v_accvgpr_read_b32 v62, a54\n"
"v_accvgpr_read_b32 v63, a55\n"
"v_cvt_pk_bf16_f32 v56, v56, v57\n"
"v_cvt_pk_bf16_f32 v57, v58, v59\n"
"v_cvt_pk_bf16_f32 v58, v60, v61\n"
"v_cvt_pk_bf16_f32 v59, v62, v63\n"
"buffer_store_dwordx4 v[56:59], v36, s[44:47], s69 offen offset:2048\n"
"v_accvgpr_read_b32 v64, a56\n"
"v_accvgpr_read_b32 v65, a57\n"
"v_accvgpr_read_b32 v66, a58\n"
"v_accvgpr_read_b32 v67, a59\n"
"v_accvgpr_read_b32 v68, a60\n"
"v_accvgpr_read_b32 v69, a61\n"
"v_accvgpr_read_b32 v70, a62\n"
"v_accvgpr_read_b32 v71, a63\n"
"v_cvt_pk_bf16_f32 v64, v64, v65\n"
"v_cvt_pk_bf16_f32 v65, v66, v67\n"
"v_cvt_pk_bf16_f32 v66, v68, v69\n"
"v_cvt_pk_bf16_f32 v67, v70, v71\n"
"buffer_store_dwordx4 v[64:67], v36, s[44:47], s69 offen offset:3072\n"
"s_add_u32 s69, s68, 16384\n"
"v_accvgpr_read_b32 v40, a64\n"
"v_accvgpr_read_b32 v41, a65\n"
"v_accvgpr_read_b32 v42, a66\n"
"v_accvgpr_read_b32 v43, a67\n"
"v_accvgpr_read_b32 v44, a68\n"
"v_accvgpr_read_b32 v45, a69\n"
"v_accvgpr_read_b32 v46, a70\n"
"v_accvgpr_read_b32 v47, a71\n"
"v_cvt_pk_bf16_f32 v40, v40, v41\n"
"v_cvt_pk_bf16_f32 v41, v42, v43\n"
"v_cvt_pk_bf16_f32 v42, v44, v45\n"
"v_cvt_pk_bf16_f32 v43, v46, v47\n"
"buffer_store_dwordx4 v[40:43], v36, s[44:47], s69 offen offset:0\n"
"v_accvgpr_read_b32 v48, a72\n"
"v_accvgpr_read_b32 v49, a73\n"
"v_accvgpr_read_b32 v50, a74\n"
"v_accvgpr_read_b32 v51, a75\n"
"v_accvgpr_read_b32 v52, a76\n"
"v_accvgpr_read_b32 v53, a77\n"
"v_accvgpr_read_b32 v54, a78\n"
"v_accvgpr_read_b32 v55, a79\n"
"v_cvt_pk_bf16_f32 v48, v48, v49\n"
"v_cvt_pk_bf16_f32 v49, v50, v51\n"
"v_cvt_pk_bf16_f32 v50, v52, v53\n"
"v_cvt_pk_bf16_f32 v51, v54, v55\n"
"buffer_store_dwordx4 v[48:51], v36, s[44:47], s69 offen offset:1024\n"
"v_accvgpr_read_b32 v56, a80\n"
"v_accvgpr_read_b32 v57, a81\n"
"v_accvgpr_read_b32 v58, a82\n"
"v_accvgpr_read_b32 v59, a83\n"
"v_accvgpr_read_b32 v60, a84\n"
"v_accvgpr_read_b32 v61, a85\n"
"v_accvgpr_read_b32 v62, a86\n"
"v_accvgpr_read_b32 v63, a87\n"
"v_cvt_pk_bf16_f32 v56, v56, v57\n"
"v_cvt_pk_bf16_f32 v57, v58, v59\n"
"v_cvt_pk_bf16_f32 v58, v60, v61\n"
"v_cvt_pk_bf16_f32 v59, v62, v63\n"
"buffer_store_dwordx4 v[56:59], v36, s[44:47], s69 offen offset:2048\n"
"v_accvgpr_read_b32 v64, a88\n"
"v_accvgpr_read_b32 v65, a89\n"
"v_accvgpr_read_b32 v66, a90\n"
"v_accvgpr_read_b32 v67, a91\n"
"v_accvgpr_read_b32 v68, a92\n"
"v_accvgpr_read_b32 v69, a93\n"
"v_accvgpr_read_b32 v70, a94\n"
"v_accvgpr_read_b32 v71, a95\n"
"v_cvt_pk_bf16_f32 v64, v64, v65\n"
"v_cvt_pk_bf16_f32 v65, v66, v67\n"
"v_cvt_pk_bf16_f32 v66, v68, v69\n"
"v_cvt_pk_bf16_f32 v67, v70, v71\n"
"buffer_store_dwordx4 v[64:67], v36, s[44:47], s69 offen offset:3072\n"
"s_add_u32 s69, s68, 24576\n"
"v_accvgpr_read_b32 v40, a96\n"
"v_accvgpr_read_b32 v41, a97\n"
"v_accvgpr_read_b32 v42, a98\n"
"v_accvgpr_read_b32 v43, a99\n"
"v_accvgpr_read_b32 v44, a100\n"
"v_accvgpr_read_b32 v45, a101\n"
"v_accvgpr_read_b32 v46, a102\n"
"v_accvgpr_read_b32 v47, a103\n"
"v_cvt_pk_bf16_f32 v40, v40, v41\n"
"v_cvt_pk_bf16_f32 v41, v42, v43\n"
"v_cvt_pk_bf16_f32 v42, v44, v45\n"
"v_cvt_pk_bf16_f32 v43, v46, v47\n"
"buffer_store_dwordx4 v[40:43], v36, s[44:47], s69 offen offset:0\n"
"v_accvgpr_read_b32 v48, a104\n"
"v_accvgpr_read_b32 v49, a105\n"
"v_accvgpr_read_b32 v50, a106\n"
"v_accvgpr_read_b32 v51, a107\n"
"v_accvgpr_read_b32 v52, a108\n"
"v_accvgpr_read_b32 v53, a109\n"
"v_accvgpr_read_b32 v54, a110\n"
"v_accvgpr_read_b32 v55, a111\n"
"v_cvt_pk_bf16_f32 v48, v48, v49\n"
"v_cvt_pk_bf16_f32 v49, v50, v51\n"
"v_cvt_pk_bf16_f32 v50, v52, v53\n"
"v_cvt_pk_bf16_f32 v51, v54, v55\n"
"buffer_store_dwordx4 v[48:51], v36, s[44:47], s69 offen offset:1024\n"
"v_accvgpr_read_b32 v56, a112\n"
"v_accvgpr_read_b32 v57, a113\n"
"v_accvgpr_read_b32 v58, a114\n"
"v_accvgpr_read_b32 v59, a115\n"
"v_accvgpr_read_b32 v60, a116\n"
"v_accvgpr_read_b32 v61, a117\n"
"v_accvgpr_read_b32 v62, a118\n"
"v_accvgpr_read_b32 v63, a119\n"
"v_cvt_pk_bf16_f32 v56, v56, v57\n"
"v_cvt_pk_bf16_f32 v57, v58, v59\n"
"v_cvt_pk_bf16_f32 v58, v60, v61\n"
"v_cvt_pk_bf16_f32 v59, v62, v63\n"
"buffer_store_dwordx4 v[56:59], v36, s[44:47], s69 offen offset:2048\n"
"v_accvgpr_read_b32 v64, a120\n"
"v_accvgpr_read_b32 v65, a121\n"
"v_accvgpr_read_b32 v66, a122\n"
"v_accvgpr_read_b32 v67, a123\n"
"v_accvgpr_read_b32 v68, a124\n"
"v_accvgpr_read_b32 v69, a125\n"
"v_accvgpr_read_b32 v70, a126\n"
"v_accvgpr_read_b32 v71, a127\n"
"v_cvt_pk_bf16_f32 v64, v64, v65\n"
"v_cvt_pk_bf16_f32 v65, v66, v67\n"
"v_cvt_pk_bf16_f32 v66, v68, v69\n"
"v_cvt_pk_bf16_f32 v67, v70, v71\n"
"buffer_store_dwordx4 v[64:67], v36, s[44:47], s69 offen offset:3072\n"
"s_add_u32 s69, s68, 65536\n"
"v_accvgpr_read_b32 v40, a128\n"
"v_accvgpr_read_b32 v41, a129\n"
"v_accvgpr_read_b32 v42, a130\n"
"v_accvgpr_read_b32 v43, a131\n"
"v_accvgpr_read_b32 v44, a132\n"
"v_accvgpr_read_b32 v45, a133\n"
"v_accvgpr_read_b32 v46, a134\n"
"v_accvgpr_read_b32 v47, a135\n"
"v_cvt_pk_bf16_f32 v40, v40, v41\n"
"v_cvt_pk_bf16_f32 v41, v42, v43\n"
"v_cvt_pk_bf16_f32 v42, v44, v45\n"
"v_cvt_pk_bf16_f32 v43, v46, v47\n"
"buffer_store_dwordx4 v[40:43], v36, s[44:47], s69 offen offset:0\n"
"v_accvgpr_read_b32 v48, a136\n"
"v_accvgpr_read_b32 v49, a137\n"
"v_accvgpr_read_b32 v50, a138\n"
"v_accvgpr_read_b32 v51, a139\n"
"v_accvgpr_read_b32 v52, a140\n"
"v_accvgpr_read_b32 v53, a141\n"
"v_accvgpr_read_b32 v54, a142\n"
"v_accvgpr_read_b32 v55, a143\n"
"v_cvt_pk_bf16_f32 v48, v48, v49\n"
"v_cvt_pk_bf16_f32 v49, v50, v51\n"
"v_cvt_pk_bf16_f32 v50, v52, v53\n"
"v_cvt_pk_bf16_f32 v51, v54, v55\n"
"buffer_store_dwordx4 v[48:51], v36, s[44:47], s69 offen offset:1024\n"
"v_accvgpr_read_b32 v56, a144\n"
"v_accvgpr_read_b32 v57, a145\n"
"v_accvgpr_read_b32 v58, a146\n"
"v_accvgpr_read_b32 v59, a147\n"
"v_accvgpr_read_b32 v60, a148\n"
"v_accvgpr_read_b32 v61, a149\n"
"v_accvgpr_read_b32 v62, a150\n"
"v_accvgpr_read_b32 v63, a151\n"
"v_cvt_pk_bf16_f32 v56, v56, v57\n"
"v_cvt_pk_bf16_f32 v57, v58, v59\n"
"v_cvt_pk_bf16_f32 v58, v60, v61\n"
"v_cvt_pk_bf16_f32 v59, v62, v63\n"
"buffer_store_dwordx4 v[56:59], v36, s[44:47], s69 offen offset:2048\n"
"v_accvgpr_read_b32 v64, a152\n"
"v_accvgpr_read_b32 v65, a153\n"
"v_accvgpr_read_b32 v66, a154\n"
"v_accvgpr_read_b32 v67, a155\n"
"v_accvgpr_read_b32 v68, a156\n"
"v_accvgpr_read_b32 v69, a157\n"
"v_accvgpr_read_b32 v70, a158\n"
"v_accvgpr_read_b32 v71, a159\n"
"v_cvt_pk_bf16_f32 v64, v64, v65\n"
"v_cvt_pk_bf16_f32 v65, v66, v67\n"
"v_cvt_pk_bf16_f32 v66, v68, v69\n"
"v_cvt_pk_bf16_f32 v67, v70, v71\n"
"buffer_store_dwordx4 v[64:67], v36, s[44:47], s69 offen offset:3072\n"
"s_add_u32 s69, s68, 73728\n"
"v_accvgpr_read_b32 v40, a160\n"
"v_accvgpr_read_b32 v41, a161\n"
"v_accvgpr_read_b32 v42, a162\n"
"v_accvgpr_read_b32 v43, a163\n"
"v_accvgpr_read_b32 v44, a164\n"
"v_accvgpr_read_b32 v45, a165\n"
"v_accvgpr_read_b32 v46, a166\n"
"v_accvgpr_read_b32 v47, a167\n"
"v_cvt_pk_bf16_f32 v40, v40, v41\n"
"v_cvt_pk_bf16_f32 v41, v42, v43\n"
"v_cvt_pk_bf16_f32 v42, v44, v45\n"
"v_cvt_pk_bf16_f32 v43, v46, v47\n"
"buffer_store_dwordx4 v[40:43], v36, s[44:47], s69 offen offset:0\n"
"v_accvgpr_read_b32 v48, a168\n"
"v_accvgpr_read_b32 v49, a169\n"
"v_accvgpr_read_b32 v50, a170\n"
"v_accvgpr_read_b32 v51, a171\n"
"v_accvgpr_read_b32 v52, a172\n"
"v_accvgpr_read_b32 v53, a173\n"
"v_accvgpr_read_b32 v54, a174\n"
"v_accvgpr_read_b32 v55, a175\n"
"v_cvt_pk_bf16_f32 v48, v48, v49\n"
"v_cvt_pk_bf16_f32 v49, v50, v51\n"
"v_cvt_pk_bf16_f32 v50, v52, v53\n"
"v_cvt_pk_bf16_f32 v51, v54, v55\n"
"buffer_store_dwordx4 v[48:51], v36, s[44:47], s69 offen offset:1024\n"
"v_accvgpr_read_b32 v56, a176\n"
"v_accvgpr_read_b32 v57, a177\n"
"v_accvgpr_read_b32 v58, a178\n"
"v_accvgpr_read_b32 v59, a179\n"
"v_accvgpr_read_b32 v60, a180\n"
"v_accvgpr_read_b32 v61, a181\n"
"v_accvgpr_read_b32 v62, a182\n"
"v_accvgpr_read_b32 v63, a183\n"
"v_cvt_pk_bf16_f32 v56, v56, v57\n"
"v_cvt_pk_bf16_f32 v57, v58, v59\n"
"v_cvt_pk_bf16_f32 v58, v60, v61\n"
"v_cvt_pk_bf16_f32 v59, v62, v63\n"
"buffer_store_dwordx4 v[56:59], v36, s[44:47], s69 offen offset:2048\n"
"v_accvgpr_read_b32 v64, a184\n"
"v_accvgpr_read_b32 v65, a185\n"
"v_accvgpr_read_b32 v66, a186\n"
"v_accvgpr_read_b32 v67, a187\n"
"v_accvgpr_read_b32 v68, a188\n"
"v_accvgpr_read_b32 v69, a189\n"
"v_accvgpr_read_b32 v70, a190\n"
"v_accvgpr_read_b32 v71, a191\n"
"v_cvt_pk_bf16_f32 v64, v64, v65\n"
"v_cvt_pk_bf16_f32 v65, v66, v67\n"
"v_cvt_pk_bf16_f32 v66, v68, v69\n"
"v_cvt_pk_bf16_f32 v67, v70, v71\n"
"buffer_store_dwordx4 v[64:67], v36, s[44:47], s69 offen offset:3072\n"
"s_add_u32 s69, s68, 81920\n"
"v_accvgpr_read_b32 v40, a192\n"
"v_accvgpr_read_b32 v41, a193\n"
"v_accvgpr_read_b32 v42, a194\n"
"v_accvgpr_read_b32 v43, a195\n"
"v_accvgpr_read_b32 v44, a196\n"
"v_accvgpr_read_b32 v45, a197\n"
"v_accvgpr_read_b32 v46, a198\n"
"v_accvgpr_read_b32 v47, a199\n"
"v_cvt_pk_bf16_f32 v40, v40, v41\n"
"v_cvt_pk_bf16_f32 v41, v42, v43\n"
"v_cvt_pk_bf16_f32 v42, v44, v45\n"
"v_cvt_pk_bf16_f32 v43, v46, v47\n"
"buffer_store_dwordx4 v[40:43], v36, s[44:47], s69 offen offset:0\n"
"v_accvgpr_read_b32 v48, a200\n"
"v_accvgpr_read_b32 v49, a201\n"
"v_accvgpr_read_b32 v50, a202\n"
"v_accvgpr_read_b32 v51, a203\n"
"v_accvgpr_read_b32 v52, a204\n"
"v_accvgpr_read_b32 v53, a205\n"
"v_accvgpr_read_b32 v54, a206\n"
"v_accvgpr_read_b32 v55, a207\n"
"v_cvt_pk_bf16_f32 v48, v48, v49\n"
"v_cvt_pk_bf16_f32 v49, v50, v51\n"
"v_cvt_pk_bf16_f32 v50, v52, v53\n"
"v_cvt_pk_bf16_f32 v51, v54, v55\n"
"buffer_store_dwordx4 v[48:51], v36, s[44:47], s69 offen offset:1024\n"
"v_accvgpr_read_b32 v56, a208\n"
"v_accvgpr_read_b32 v57, a209\n"
"v_accvgpr_read_b32 v58, a210\n"
"v_accvgpr_read_b32 v59, a211\n"
"v_accvgpr_read_b32 v60, a212\n"
"v_accvgpr_read_b32 v61, a213\n"
"v_accvgpr_read_b32 v62, a214\n"
"v_accvgpr_read_b32 v63, a215\n"
"v_cvt_pk_bf16_f32 v56, v56, v57\n"
"v_cvt_pk_bf16_f32 v57, v58, v59\n"
"v_cvt_pk_bf16_f32 v58, v60, v61\n"
"v_cvt_pk_bf16_f32 v59, v62, v63\n"
"buffer_store_dwordx4 v[56:59], v36, s[44:47], s69 offen offset:2048\n"
"v_accvgpr_read_b32 v64, a216\n"
"v_accvgpr_read_b32 v65, a217\n"
"v_accvgpr_read_b32 v66, a218\n"
"v_accvgpr_read_b32 v67, a219\n"
"v_accvgpr_read_b32 v68, a220\n"
"v_accvgpr_read_b32 v69, a221\n"
"v_accvgpr_read_b32 v70, a222\n"
"v_accvgpr_read_b32 v71, a223\n"
"v_cvt_pk_bf16_f32 v64, v64, v65\n"
"v_cvt_pk_bf16_f32 v65, v66, v67\n"
"v_cvt_pk_bf16_f32 v66, v68, v69\n"
"v_cvt_pk_bf16_f32 v67, v70, v71\n"
"buffer_store_dwordx4 v[64:67], v36, s[44:47], s69 offen offset:3072\n"
"s_add_u32 s69, s68, 90112\n"
"v_accvgpr_read_b32 v40, a224\n"
"v_accvgpr_read_b32 v41, a225\n"
"v_accvgpr_read_b32 v42, a226\n"
"v_accvgpr_read_b32 v43, a227\n"
"v_accvgpr_read_b32 v44, a228\n"
"v_accvgpr_read_b32 v45, a229\n"
"v_accvgpr_read_b32 v46, a230\n"
"v_accvgpr_read_b32 v47, a231\n"
"v_cvt_pk_bf16_f32 v40, v40, v41\n"
"v_cvt_pk_bf16_f32 v41, v42, v43\n"
"v_cvt_pk_bf16_f32 v42, v44, v45\n"
"v_cvt_pk_bf16_f32 v43, v46, v47\n"
"buffer_store_dwordx4 v[40:43], v36, s[44:47], s69 offen offset:0\n"
"v_accvgpr_read_b32 v48, a232\n"
"v_accvgpr_read_b32 v49, a233\n"
"v_accvgpr_read_b32 v50, a234\n"
"v_accvgpr_read_b32 v51, a235\n"
"v_accvgpr_read_b32 v52, a236\n"
"v_accvgpr_read_b32 v53, a237\n"
"v_accvgpr_read_b32 v54, a238\n"
"v_accvgpr_read_b32 v55, a239\n"
"v_cvt_pk_bf16_f32 v48, v48, v49\n"
"v_cvt_pk_bf16_f32 v49, v50, v51\n"
"v_cvt_pk_bf16_f32 v50, v52, v53\n"
"v_cvt_pk_bf16_f32 v51, v54, v55\n"
"buffer_store_dwordx4 v[48:51], v36, s[44:47], s69 offen offset:1024\n"
"v_accvgpr_read_b32 v56, a240\n"
"v_accvgpr_read_b32 v57, a241\n"
"v_accvgpr_read_b32 v58, a242\n"
"v_accvgpr_read_b32 v59, a243\n"
"v_accvgpr_read_b32 v60, a244\n"
"v_accvgpr_read_b32 v61, a245\n"
"v_accvgpr_read_b32 v62, a246\n"
"v_accvgpr_read_b32 v63, a247\n"
"v_cvt_pk_bf16_f32 v56, v56, v57\n"
"v_cvt_pk_bf16_f32 v57, v58, v59\n"
"v_cvt_pk_bf16_f32 v58, v60, v61\n"
"v_cvt_pk_bf16_f32 v59, v62, v63\n"
"buffer_store_dwordx4 v[56:59], v36, s[44:47], s69 offen offset:2048\n"
"v_accvgpr_read_b32 v64, a248\n"
"v_accvgpr_read_b32 v65, a249\n"
"v_accvgpr_read_b32 v66, a250\n"
"v_accvgpr_read_b32 v67, a251\n"
"v_accvgpr_read_b32 v68, a252\n"
"v_accvgpr_read_b32 v69, a253\n"
"v_accvgpr_read_b32 v70, a254\n"
"v_accvgpr_read_b32 v71, a255\n"
"v_cvt_pk_bf16_f32 v64, v64, v65\n"
"v_cvt_pk_bf16_f32 v65, v66, v67\n"
"v_cvt_pk_bf16_f32 v66, v68, v69\n"
"v_cvt_pk_bf16_f32 v67, v70, v71\n"
"buffer_store_dwordx4 v[64:67], v36, s[44:47], s69 offen offset:3072\n"
"s_mov_b32 s62, 7\n"
"s_mov_b32 s63, 0\n"
"s_nop 1\n"
"s_branch L_peel%=\n"
"L_nr%=:\n"
"s_lshr_b32 s68, s76, 1\n"
"s_mul_i32 s68, s68, 65536\n"
"s_and_b32 s69, s76, 1\n"
"s_lshl_b32 s69, s69, 12\n"
"s_add_u32 s68, s68, s69\n"
"s_add_u32 s69, s68, 0\n"
"v_accvgpr_read_b32 v40, a0\n"
"v_accvgpr_read_b32 v41, a1\n"
"v_accvgpr_read_b32 v42, a2\n"
"v_accvgpr_read_b32 v43, a3\n"
"v_accvgpr_read_b32 v44, a4\n"
"v_accvgpr_read_b32 v45, a5\n"
"v_accvgpr_read_b32 v46, a6\n"
"v_accvgpr_read_b32 v47, a7\n"
"v_pk_mul_f32 v[40:41], v[40:41], s[74:75]\n"
"v_pk_mul_f32 v[42:43], v[42:43], s[74:75]\n"
"v_pk_mul_f32 v[44:45], v[44:45], s[74:75]\n"
"v_pk_mul_f32 v[46:47], v[46:47], s[74:75]\n"
"v_cvt_pk_bf16_f32 v40, v40, v41\n"
"v_cvt_pk_bf16_f32 v41, v42, v43\n"
"v_cvt_pk_bf16_f32 v42, v44, v45\n"
"v_cvt_pk_bf16_f32 v43, v46, v47\n"
"buffer_store_dwordx4 v[40:43], v36, s[56:59], s69 offen offset:0\n"
"v_accvgpr_read_b32 v48, a8\n"
"v_accvgpr_read_b32 v49, a9\n"
"v_accvgpr_read_b32 v50, a10\n"
"v_accvgpr_read_b32 v51, a11\n"
"v_accvgpr_read_b32 v52, a12\n"
"v_accvgpr_read_b32 v53, a13\n"
"v_accvgpr_read_b32 v54, a14\n"
"v_accvgpr_read_b32 v55, a15\n"
"v_pk_mul_f32 v[48:49], v[48:49], s[74:75]\n"
"v_pk_mul_f32 v[50:51], v[50:51], s[74:75]\n"
"v_pk_mul_f32 v[52:53], v[52:53], s[74:75]\n"
"v_pk_mul_f32 v[54:55], v[54:55], s[74:75]\n"
"v_cvt_pk_bf16_f32 v48, v48, v49\n"
"v_cvt_pk_bf16_f32 v49, v50, v51\n"
"v_cvt_pk_bf16_f32 v50, v52, v53\n"
"v_cvt_pk_bf16_f32 v51, v54, v55\n"
"buffer_store_dwordx4 v[48:51], v36, s[56:59], s69 offen offset:1024\n"
"v_accvgpr_read_b32 v56, a16\n"
"v_accvgpr_read_b32 v57, a17\n"
"v_accvgpr_read_b32 v58, a18\n"
"v_accvgpr_read_b32 v59, a19\n"
"v_accvgpr_read_b32 v60, a20\n"
"v_accvgpr_read_b32 v61, a21\n"
"v_accvgpr_read_b32 v62, a22\n"
"v_accvgpr_read_b32 v63, a23\n"
"v_pk_mul_f32 v[56:57], v[56:57], s[74:75]\n"
"v_pk_mul_f32 v[58:59], v[58:59], s[74:75]\n"
"v_pk_mul_f32 v[60:61], v[60:61], s[74:75]\n"
"v_pk_mul_f32 v[62:63], v[62:63], s[74:75]\n"
"v_cvt_pk_bf16_f32 v56, v56, v57\n"
"v_cvt_pk_bf16_f32 v57, v58, v59\n"
"v_cvt_pk_bf16_f32 v58, v60, v61\n"
"v_cvt_pk_bf16_f32 v59, v62, v63\n"
"buffer_store_dwordx4 v[56:59], v36, s[56:59], s69 offen offset:2048\n"
"v_accvgpr_read_b32 v64, a24\n"
"v_accvgpr_read_b32 v65, a25\n"
"v_accvgpr_read_b32 v66, a26\n"
"v_accvgpr_read_b32 v67, a27\n"
"v_accvgpr_read_b32 v68, a28\n"
"v_accvgpr_read_b32 v69, a29\n"
"v_accvgpr_read_b32 v70, a30\n"
"v_accvgpr_read_b32 v71, a31\n"
"v_pk_mul_f32 v[64:65], v[64:65], s[74:75]\n"
"v_pk_mul_f32 v[66:67], v[66:67], s[74:75]\n"
"v_pk_mul_f32 v[68:69], v[68:69], s[74:75]\n"
"v_pk_mul_f32 v[70:71], v[70:71], s[74:75]\n"
"v_cvt_pk_bf16_f32 v64, v64, v65\n"
"v_cvt_pk_bf16_f32 v65, v66, v67\n"
"v_cvt_pk_bf16_f32 v66, v68, v69\n"
"v_cvt_pk_bf16_f32 v67, v70, v71\n"
"buffer_store_dwordx4 v[64:67], v36, s[56:59], s69 offen offset:3072\n"
"s_add_u32 s69, s68, 8192\n"
"v_accvgpr_read_b32 v40, a32\n"
"v_accvgpr_read_b32 v41, a33\n"
"v_accvgpr_read_b32 v42, a34\n"
"v_accvgpr_read_b32 v43, a35\n"
"v_accvgpr_read_b32 v44, a36\n"
"v_accvgpr_read_b32 v45, a37\n"
"v_accvgpr_read_b32 v46, a38\n"
"v_accvgpr_read_b32 v47, a39\n"
"v_pk_mul_f32 v[40:41], v[40:41], s[74:75]\n"
"v_pk_mul_f32 v[42:43], v[42:43], s[74:75]\n"
"v_pk_mul_f32 v[44:45], v[44:45], s[74:75]\n"
"v_pk_mul_f32 v[46:47], v[46:47], s[74:75]\n"
"v_cvt_pk_bf16_f32 v40, v40, v41\n"
"v_cvt_pk_bf16_f32 v41, v42, v43\n"
"v_cvt_pk_bf16_f32 v42, v44, v45\n"
"v_cvt_pk_bf16_f32 v43, v46, v47\n"
"buffer_store_dwordx4 v[40:43], v36, s[56:59], s69 offen offset:0\n"
"v_accvgpr_read_b32 v48, a40\n"
"v_accvgpr_read_b32 v49, a41\n"
"v_accvgpr_read_b32 v50, a42\n"
"v_accvgpr_read_b32 v51, a43\n"
"v_accvgpr_read_b32 v52, a44\n"
"v_accvgpr_read_b32 v53, a45\n"
"v_accvgpr_read_b32 v54, a46\n"
"v_accvgpr_read_b32 v55, a47\n"
"v_pk_mul_f32 v[48:49], v[48:49], s[74:75]\n"
"v_pk_mul_f32 v[50:51], v[50:51], s[74:75]\n"
"v_pk_mul_f32 v[52:53], v[52:53], s[74:75]\n"
"v_pk_mul_f32 v[54:55], v[54:55], s[74:75]\n"
"v_cvt_pk_bf16_f32 v48, v48, v49\n"
"v_cvt_pk_bf16_f32 v49, v50, v51\n"
"v_cvt_pk_bf16_f32 v50, v52, v53\n"
"v_cvt_pk_bf16_f32 v51, v54, v55\n"
"buffer_store_dwordx4 v[48:51], v36, s[56:59], s69 offen offset:1024\n"
"v_accvgpr_read_b32 v56, a48\n"
"v_accvgpr_read_b32 v57, a49\n"
"v_accvgpr_read_b32 v58, a50\n"
"v_accvgpr_read_b32 v59, a51\n"
"v_accvgpr_read_b32 v60, a52\n"
"v_accvgpr_read_b32 v61, a53\n"
"v_accvgpr_read_b32 v62, a54\n"
"v_accvgpr_read_b32 v63, a55\n"
"v_pk_mul_f32 v[56:57], v[56:57], s[74:75]\n"
"v_pk_mul_f32 v[58:59], v[58:59], s[74:75]\n"
"v_pk_mul_f32 v[60:61], v[60:61], s[74:75]\n"
"v_pk_mul_f32 v[62:63], v[62:63], s[74:75]\n"
"v_cvt_pk_bf16_f32 v56, v56, v57\n"
"v_cvt_pk_bf16_f32 v57, v58, v59\n"
"v_cvt_pk_bf16_f32 v58, v60, v61\n"
"v_cvt_pk_bf16_f32 v59, v62, v63\n"
"buffer_store_dwordx4 v[56:59], v36, s[56:59], s69 offen offset:2048\n"
"v_accvgpr_read_b32 v64, a56\n"
"v_accvgpr_read_b32 v65, a57\n"
"v_accvgpr_read_b32 v66, a58\n"
"v_accvgpr_read_b32 v67, a59\n"
"v_accvgpr_read_b32 v68, a60\n"
"v_accvgpr_read_b32 v69, a61\n"
"v_accvgpr_read_b32 v70, a62\n"
"v_accvgpr_read_b32 v71, a63\n"
"v_pk_mul_f32 v[64:65], v[64:65], s[74:75]\n"
"v_pk_mul_f32 v[66:67], v[66:67], s[74:75]\n"
"v_pk_mul_f32 v[68:69], v[68:69], s[74:75]\n"
"v_pk_mul_f32 v[70:71], v[70:71], s[74:75]\n"
"v_cvt_pk_bf16_f32 v64, v64, v65\n"
"v_cvt_pk_bf16_f32 v65, v66, v67\n"
"v_cvt_pk_bf16_f32 v66, v68, v69\n"
"v_cvt_pk_bf16_f32 v67, v70, v71\n"
"buffer_store_dwordx4 v[64:67], v36, s[56:59], s69 offen offset:3072\n"
"s_add_u32 s69, s68, 16384\n"
"v_accvgpr_read_b32 v40, a64\n"
"v_accvgpr_read_b32 v41, a65\n"
"v_accvgpr_read_b32 v42, a66\n"
"v_accvgpr_read_b32 v43, a67\n"
"v_accvgpr_read_b32 v44, a68\n"
"v_accvgpr_read_b32 v45, a69\n"
"v_accvgpr_read_b32 v46, a70\n"
"v_accvgpr_read_b32 v47, a71\n"
"v_pk_mul_f32 v[40:41], v[40:41], s[74:75]\n"
"v_pk_mul_f32 v[42:43], v[42:43], s[74:75]\n"
"v_pk_mul_f32 v[44:45], v[44:45], s[74:75]\n"
"v_pk_mul_f32 v[46:47], v[46:47], s[74:75]\n"
"v_cvt_pk_bf16_f32 v40, v40, v41\n"
"v_cvt_pk_bf16_f32 v41, v42, v43\n"
"v_cvt_pk_bf16_f32 v42, v44, v45\n"
"v_cvt_pk_bf16_f32 v43, v46, v47\n"
"buffer_store_dwordx4 v[40:43], v36, s[56:59], s69 offen offset:0\n"
"v_accvgpr_read_b32 v48, a72\n"
"v_accvgpr_read_b32 v49, a73\n"
"v_accvgpr_read_b32 v50, a74\n"
"v_accvgpr_read_b32 v51, a75\n"
"v_accvgpr_read_b32 v52, a76\n"
"v_accvgpr_read_b32 v53, a77\n"
"v_accvgpr_read_b32 v54, a78\n"
"v_accvgpr_read_b32 v55, a79\n"
"v_pk_mul_f32 v[48:49], v[48:49], s[74:75]\n"
"v_pk_mul_f32 v[50:51], v[50:51], s[74:75]\n"
"v_pk_mul_f32 v[52:53], v[52:53], s[74:75]\n"
"v_pk_mul_f32 v[54:55], v[54:55], s[74:75]\n"
"v_cvt_pk_bf16_f32 v48, v48, v49\n"
"v_cvt_pk_bf16_f32 v49, v50, v51\n"
"v_cvt_pk_bf16_f32 v50, v52, v53\n"
"v_cvt_pk_bf16_f32 v51, v54, v55\n"
"buffer_store_dwordx4 v[48:51], v36, s[56:59], s69 offen offset:1024\n"
"v_accvgpr_read_b32 v56, a80\n"
"v_accvgpr_read_b32 v57, a81\n"
"v_accvgpr_read_b32 v58, a82\n"
"v_accvgpr_read_b32 v59, a83\n"
"v_accvgpr_read_b32 v60, a84\n"
"v_accvgpr_read_b32 v61, a85\n"
"v_accvgpr_read_b32 v62, a86\n"
"v_accvgpr_read_b32 v63, a87\n"
"v_pk_mul_f32 v[56:57], v[56:57], s[74:75]\n"
"v_pk_mul_f32 v[58:59], v[58:59], s[74:75]\n"
"v_pk_mul_f32 v[60:61], v[60:61], s[74:75]\n"
"v_pk_mul_f32 v[62:63], v[62:63], s[74:75]\n"
"v_cvt_pk_bf16_f32 v56, v56, v57\n"
"v_cvt_pk_bf16_f32 v57, v58, v59\n"
"v_cvt_pk_bf16_f32 v58, v60, v61\n"
"v_cvt_pk_bf16_f32 v59, v62, v63\n"
"buffer_store_dwordx4 v[56:59], v36, s[56:59], s69 offen offset:2048\n"
"v_accvgpr_read_b32 v64, a88\n"
"v_accvgpr_read_b32 v65, a89\n"
"v_accvgpr_read_b32 v66, a90\n"
"v_accvgpr_read_b32 v67, a91\n"
"v_accvgpr_read_b32 v68, a92\n"
"v_accvgpr_read_b32 v69, a93\n"
"v_accvgpr_read_b32 v70, a94\n"
"v_accvgpr_read_b32 v71, a95\n"
"v_pk_mul_f32 v[64:65], v[64:65], s[74:75]\n"
"v_pk_mul_f32 v[66:67], v[66:67], s[74:75]\n"
"v_pk_mul_f32 v[68:69], v[68:69], s[74:75]\n"
"v_pk_mul_f32 v[70:71], v[70:71], s[74:75]\n"
"v_cvt_pk_bf16_f32 v64, v64, v65\n"
"v_cvt_pk_bf16_f32 v65, v66, v67\n"
"v_cvt_pk_bf16_f32 v66, v68, v69\n"
"v_cvt_pk_bf16_f32 v67, v70, v71\n"
"buffer_store_dwordx4 v[64:67], v36, s[56:59], s69 offen offset:3072\n"
"s_add_u32 s69, s68, 24576\n"
"v_accvgpr_read_b32 v40, a96\n"
"v_accvgpr_read_b32 v41, a97\n"
"v_accvgpr_read_b32 v42, a98\n"
"v_accvgpr_read_b32 v43, a99\n"
"v_accvgpr_read_b32 v44, a100\n"
"v_accvgpr_read_b32 v45, a101\n"
"v_accvgpr_read_b32 v46, a102\n"
"v_accvgpr_read_b32 v47, a103\n"
"v_pk_mul_f32 v[40:41], v[40:41], s[74:75]\n"
"v_pk_mul_f32 v[42:43], v[42:43], s[74:75]\n"
"v_pk_mul_f32 v[44:45], v[44:45], s[74:75]\n"
"v_pk_mul_f32 v[46:47], v[46:47], s[74:75]\n"
"v_cvt_pk_bf16_f32 v40, v40, v41\n"
"v_cvt_pk_bf16_f32 v41, v42, v43\n"
"v_cvt_pk_bf16_f32 v42, v44, v45\n"
"v_cvt_pk_bf16_f32 v43, v46, v47\n"
"buffer_store_dwordx4 v[40:43], v36, s[56:59], s69 offen offset:0\n"
"v_accvgpr_read_b32 v48, a104\n"
"v_accvgpr_read_b32 v49, a105\n"
"v_accvgpr_read_b32 v50, a106\n"
"v_accvgpr_read_b32 v51, a107\n"
"v_accvgpr_read_b32 v52, a108\n"
"v_accvgpr_read_b32 v53, a109\n"
"v_accvgpr_read_b32 v54, a110\n"
"v_accvgpr_read_b32 v55, a111\n"
"v_pk_mul_f32 v[48:49], v[48:49], s[74:75]\n"
"v_pk_mul_f32 v[50:51], v[50:51], s[74:75]\n"
"v_pk_mul_f32 v[52:53], v[52:53], s[74:75]\n"
"v_pk_mul_f32 v[54:55], v[54:55], s[74:75]\n"
"v_cvt_pk_bf16_f32 v48, v48, v49\n"
"v_cvt_pk_bf16_f32 v49, v50, v51\n"
"v_cvt_pk_bf16_f32 v50, v52, v53\n"
"v_cvt_pk_bf16_f32 v51, v54, v55\n"
"buffer_store_dwordx4 v[48:51], v36, s[56:59], s69 offen offset:1024\n"
"v_accvgpr_read_b32 v56, a112\n"
"v_accvgpr_read_b32 v57, a113\n"
"v_accvgpr_read_b32 v58, a114\n"
"v_accvgpr_read_b32 v59, a115\n"
"v_accvgpr_read_b32 v60, a116\n"
"v_accvgpr_read_b32 v61, a117\n"
"v_accvgpr_read_b32 v62, a118\n"
"v_accvgpr_read_b32 v63, a119\n"
"v_pk_mul_f32 v[56:57], v[56:57], s[74:75]\n"
"v_pk_mul_f32 v[58:59], v[58:59], s[74:75]\n"
"v_pk_mul_f32 v[60:61], v[60:61], s[74:75]\n"
"v_pk_mul_f32 v[62:63], v[62:63], s[74:75]\n"
"v_cvt_pk_bf16_f32 v56, v56, v57\n"
"v_cvt_pk_bf16_f32 v57, v58, v59\n"
"v_cvt_pk_bf16_f32 v58, v60, v61\n"
"v_cvt_pk_bf16_f32 v59, v62, v63\n"
"buffer_store_dwordx4 v[56:59], v36, s[56:59], s69 offen offset:2048\n"
"v_accvgpr_read_b32 v64, a120\n"
"v_accvgpr_read_b32 v65, a121\n"
"v_accvgpr_read_b32 v66, a122\n"
"v_accvgpr_read_b32 v67, a123\n"
"v_accvgpr_read_b32 v68, a124\n"
"v_accvgpr_read_b32 v69, a125\n"
"v_accvgpr_read_b32 v70, a126\n"
"v_accvgpr_read_b32 v71, a127\n"
"v_pk_mul_f32 v[64:65], v[64:65], s[74:75]\n"
"v_pk_mul_f32 v[66:67], v[66:67], s[74:75]\n"
"v_pk_mul_f32 v[68:69], v[68:69], s[74:75]\n"
"v_pk_mul_f32 v[70:71], v[70:71], s[74:75]\n"
"v_cvt_pk_bf16_f32 v64, v64, v65\n"
"v_cvt_pk_bf16_f32 v65, v66, v67\n"
"v_cvt_pk_bf16_f32 v66, v68, v69\n"
"v_cvt_pk_bf16_f32 v67, v70, v71\n"
"buffer_store_dwordx4 v[64:67], v36, s[56:59], s69 offen offset:3072\n"
"s_add_u32 s69, s68, 32768\n"
"v_accvgpr_read_b32 v40, a128\n"
"v_accvgpr_read_b32 v41, a129\n"
"v_accvgpr_read_b32 v42, a130\n"
"v_accvgpr_read_b32 v43, a131\n"
"v_accvgpr_read_b32 v44, a132\n"
"v_accvgpr_read_b32 v45, a133\n"
"v_accvgpr_read_b32 v46, a134\n"
"v_accvgpr_read_b32 v47, a135\n"
"v_pk_mul_f32 v[40:41], v[40:41], s[74:75]\n"
"v_pk_mul_f32 v[42:43], v[42:43], s[74:75]\n"
"v_pk_mul_f32 v[44:45], v[44:45], s[74:75]\n"
"v_pk_mul_f32 v[46:47], v[46:47], s[74:75]\n"
"v_cvt_pk_bf16_f32 v40, v40, v41\n"
"v_cvt_pk_bf16_f32 v41, v42, v43\n"
"v_cvt_pk_bf16_f32 v42, v44, v45\n"
"v_cvt_pk_bf16_f32 v43, v46, v47\n"
"buffer_store_dwordx4 v[40:43], v36, s[56:59], s69 offen offset:0\n"
"v_accvgpr_read_b32 v48, a136\n"
"v_accvgpr_read_b32 v49, a137\n"
"v_accvgpr_read_b32 v50, a138\n"
"v_accvgpr_read_b32 v51, a139\n"
"v_accvgpr_read_b32 v52, a140\n"
"v_accvgpr_read_b32 v53, a141\n"
"v_accvgpr_read_b32 v54, a142\n"
"v_accvgpr_read_b32 v55, a143\n"
"v_pk_mul_f32 v[48:49], v[48:49], s[74:75]\n"
"v_pk_mul_f32 v[50:51], v[50:51], s[74:75]\n"
"v_pk_mul_f32 v[52:53], v[52:53], s[74:75]\n"
"v_pk_mul_f32 v[54:55], v[54:55], s[74:75]\n"
"v_cvt_pk_bf16_f32 v48, v48, v49\n"
"v_cvt_pk_bf16_f32 v49, v50, v51\n"
"v_cvt_pk_bf16_f32 v50, v52, v53\n"
"v_cvt_pk_bf16_f32 v51, v54, v55\n"
"buffer_store_dwordx4 v[48:51], v36, s[56:59], s69 offen offset:1024\n"
"v_accvgpr_read_b32 v56, a144\n"
"v_accvgpr_read_b32 v57, a145\n"
"v_accvgpr_read_b32 v58, a146\n"
"v_accvgpr_read_b32 v59, a147\n"
"v_accvgpr_read_b32 v60, a148\n"
"v_accvgpr_read_b32 v61, a149\n"
"v_accvgpr_read_b32 v62, a150\n"
"v_accvgpr_read_b32 v63, a151\n"
"v_pk_mul_f32 v[56:57], v[56:57], s[74:75]\n"
"v_pk_mul_f32 v[58:59], v[58:59], s[74:75]\n"
"v_pk_mul_f32 v[60:61], v[60:61], s[74:75]\n"
"v_pk_mul_f32 v[62:63], v[62:63], s[74:75]\n"
"v_cvt_pk_bf16_f32 v56, v56, v57\n"
"v_cvt_pk_bf16_f32 v57, v58, v59\n"
"v_cvt_pk_bf16_f32 v58, v60, v61\n"
"v_cvt_pk_bf16_f32 v59, v62, v63\n"
"buffer_store_dwordx4 v[56:59], v36, s[56:59], s69 offen offset:2048\n"
"v_accvgpr_read_b32 v64, a152\n"
"v_accvgpr_read_b32 v65, a153\n"
"v_accvgpr_read_b32 v66, a154\n"
"v_accvgpr_read_b32 v67, a155\n"
"v_accvgpr_read_b32 v68, a156\n"
"v_accvgpr_read_b32 v69, a157\n"
"v_accvgpr_read_b32 v70, a158\n"
"v_accvgpr_read_b32 v71, a159\n"
"v_pk_mul_f32 v[64:65], v[64:65], s[74:75]\n"
"v_pk_mul_f32 v[66:67], v[66:67], s[74:75]\n"
"v_pk_mul_f32 v[68:69], v[68:69], s[74:75]\n"
"v_pk_mul_f32 v[70:71], v[70:71], s[74:75]\n"
"v_cvt_pk_bf16_f32 v64, v64, v65\n"
"v_cvt_pk_bf16_f32 v65, v66, v67\n"
"v_cvt_pk_bf16_f32 v66, v68, v69\n"
"v_cvt_pk_bf16_f32 v67, v70, v71\n"
"buffer_store_dwordx4 v[64:67], v36, s[56:59], s69 offen offset:3072\n"
"s_add_u32 s69, s68, 40960\n"
"v_accvgpr_read_b32 v40, a160\n"
"v_accvgpr_read_b32 v41, a161\n"
"v_accvgpr_read_b32 v42, a162\n"
"v_accvgpr_read_b32 v43, a163\n"
"v_accvgpr_read_b32 v44, a164\n"
"v_accvgpr_read_b32 v45, a165\n"
"v_accvgpr_read_b32 v46, a166\n"
"v_accvgpr_read_b32 v47, a167\n"
"v_pk_mul_f32 v[40:41], v[40:41], s[74:75]\n"
"v_pk_mul_f32 v[42:43], v[42:43], s[74:75]\n"
"v_pk_mul_f32 v[44:45], v[44:45], s[74:75]\n"
"v_pk_mul_f32 v[46:47], v[46:47], s[74:75]\n"
"v_cvt_pk_bf16_f32 v40, v40, v41\n"
"v_cvt_pk_bf16_f32 v41, v42, v43\n"
"v_cvt_pk_bf16_f32 v42, v44, v45\n"
"v_cvt_pk_bf16_f32 v43, v46, v47\n"
"buffer_store_dwordx4 v[40:43], v36, s[56:59], s69 offen offset:0\n"
"v_accvgpr_read_b32 v48, a168\n"
"v_accvgpr_read_b32 v49, a169\n"
"v_accvgpr_read_b32 v50, a170\n"
"v_accvgpr_read_b32 v51, a171\n"
"v_accvgpr_read_b32 v52, a172\n"
"v_accvgpr_read_b32 v53, a173\n"
"v_accvgpr_read_b32 v54, a174\n"
"v_accvgpr_read_b32 v55, a175\n"
"v_pk_mul_f32 v[48:49], v[48:49], s[74:75]\n"
"v_pk_mul_f32 v[50:51], v[50:51], s[74:75]\n"
"v_pk_mul_f32 v[52:53], v[52:53], s[74:75]\n"
"v_pk_mul_f32 v[54:55], v[54:55], s[74:75]\n"
"v_cvt_pk_bf16_f32 v48, v48, v49\n"
"v_cvt_pk_bf16_f32 v49, v50, v51\n"
"v_cvt_pk_bf16_f32 v50, v52, v53\n"
"v_cvt_pk_bf16_f32 v51, v54, v55\n"
"buffer_store_dwordx4 v[48:51], v36, s[56:59], s69 offen offset:1024\n"
"v_accvgpr_read_b32 v56, a176\n"
"v_accvgpr_read_b32 v57, a177\n"
"v_accvgpr_read_b32 v58, a178\n"
"v_accvgpr_read_b32 v59, a179\n"
"v_accvgpr_read_b32 v60, a180\n"
"v_accvgpr_read_b32 v61, a181\n"
"v_accvgpr_read_b32 v62, a182\n"
"v_accvgpr_read_b32 v63, a183\n"
"v_pk_mul_f32 v[56:57], v[56:57], s[74:75]\n"
"v_pk_mul_f32 v[58:59], v[58:59], s[74:75]\n"
"v_pk_mul_f32 v[60:61], v[60:61], s[74:75]\n"
"v_pk_mul_f32 v[62:63], v[62:63], s[74:75]\n"
"v_cvt_pk_bf16_f32 v56, v56, v57\n"
"v_cvt_pk_bf16_f32 v57, v58, v59\n"
"v_cvt_pk_bf16_f32 v58, v60, v61\n"
"v_cvt_pk_bf16_f32 v59, v62, v63\n"
"buffer_store_dwordx4 v[56:59], v36, s[56:59], s69 offen offset:2048\n"
"v_accvgpr_read_b32 v64, a184\n"
"v_accvgpr_read_b32 v65, a185\n"
"v_accvgpr_read_b32 v66, a186\n"
"v_accvgpr_read_b32 v67, a187\n"
"v_accvgpr_read_b32 v68, a188\n"
"v_accvgpr_read_b32 v69, a189\n"
"v_accvgpr_read_b32 v70, a190\n"
"v_accvgpr_read_b32 v71, a191\n"
"v_pk_mul_f32 v[64:65], v[64:65], s[74:75]\n"
"v_pk_mul_f32 v[66:67], v[66:67], s[74:75]\n"
"v_pk_mul_f32 v[68:69], v[68:69], s[74:75]\n"
"v_pk_mul_f32 v[70:71], v[70:71], s[74:75]\n"
"v_cvt_pk_bf16_f32 v64, v64, v65\n"
"v_cvt_pk_bf16_f32 v65, v66, v67\n"
"v_cvt_pk_bf16_f32 v66, v68, v69\n"
"v_cvt_pk_bf16_f32 v67, v70, v71\n"
"buffer_store_dwordx4 v[64:67], v36, s[56:59], s69 offen offset:3072\n"
"s_add_u32 s69, s68, 49152\n"
"v_accvgpr_read_b32 v40, a192\n"
"v_accvgpr_read_b32 v41, a193\n"
"v_accvgpr_read_b32 v42, a194\n"
"v_accvgpr_read_b32 v43, a195\n"
"v_accvgpr_read_b32 v44, a196\n"
"v_accvgpr_read_b32 v45, a197\n"
"v_accvgpr_read_b32 v46, a198\n"
"v_accvgpr_read_b32 v47, a199\n"
"v_pk_mul_f32 v[40:41], v[40:41], s[74:75]\n"
"v_pk_mul_f32 v[42:43], v[42:43], s[74:75]\n"
"v_pk_mul_f32 v[44:45], v[44:45], s[74:75]\n"
"v_pk_mul_f32 v[46:47], v[46:47], s[74:75]\n"
"v_cvt_pk_bf16_f32 v40, v40, v41\n"
"v_cvt_pk_bf16_f32 v41, v42, v43\n"
"v_cvt_pk_bf16_f32 v42, v44, v45\n"
"v_cvt_pk_bf16_f32 v43, v46, v47\n"
"buffer_store_dwordx4 v[40:43], v36, s[56:59], s69 offen offset:0\n"
"v_accvgpr_read_b32 v48, a200\n"
"v_accvgpr_read_b32 v49, a201\n"
"v_accvgpr_read_b32 v50, a202\n"
"v_accvgpr_read_b32 v51, a203\n"
"v_accvgpr_read_b32 v52, a204\n"
"v_accvgpr_read_b32 v53, a205\n"
"v_accvgpr_read_b32 v54, a206\n"
"v_accvgpr_read_b32 v55, a207\n"
"v_pk_mul_f32 v[48:49], v[48:49], s[74:75]\n"
"v_pk_mul_f32 v[50:51], v[50:51], s[74:75]\n"
"v_pk_mul_f32 v[52:53], v[52:53], s[74:75]\n"
"v_pk_mul_f32 v[54:55], v[54:55], s[74:75]\n"
"v_cvt_pk_bf16_f32 v48, v48, v49\n"
"v_cvt_pk_bf16_f32 v49, v50, v51\n"
"v_cvt_pk_bf16_f32 v50, v52, v53\n"
"v_cvt_pk_bf16_f32 v51, v54, v55\n"
"buffer_store_dwordx4 v[48:51], v36, s[56:59], s69 offen offset:1024\n"
"v_accvgpr_read_b32 v56, a208\n"
"v_accvgpr_read_b32 v57, a209\n"
"v_accvgpr_read_b32 v58, a210\n"
"v_accvgpr_read_b32 v59, a211\n"
"v_accvgpr_read_b32 v60, a212\n"
"v_accvgpr_read_b32 v61, a213\n"
"v_accvgpr_read_b32 v62, a214\n"
"v_accvgpr_read_b32 v63, a215\n"
"v_pk_mul_f32 v[56:57], v[56:57], s[74:75]\n"
"v_pk_mul_f32 v[58:59], v[58:59], s[74:75]\n"
"v_pk_mul_f32 v[60:61], v[60:61], s[74:75]\n"
"v_pk_mul_f32 v[62:63], v[62:63], s[74:75]\n"
"v_cvt_pk_bf16_f32 v56, v56, v57\n"
"v_cvt_pk_bf16_f32 v57, v58, v59\n"
"v_cvt_pk_bf16_f32 v58, v60, v61\n"
"v_cvt_pk_bf16_f32 v59, v62, v63\n"
"buffer_store_dwordx4 v[56:59], v36, s[56:59], s69 offen offset:2048\n"
"v_accvgpr_read_b32 v64, a216\n"
"v_accvgpr_read_b32 v65, a217\n"
"v_accvgpr_read_b32 v66, a218\n"
"v_accvgpr_read_b32 v67, a219\n"
"v_accvgpr_read_b32 v68, a220\n"
"v_accvgpr_read_b32 v69, a221\n"
"v_accvgpr_read_b32 v70, a222\n"
"v_accvgpr_read_b32 v71, a223\n"
"v_pk_mul_f32 v[64:65], v[64:65], s[74:75]\n"
"v_pk_mul_f32 v[66:67], v[66:67], s[74:75]\n"
"v_pk_mul_f32 v[68:69], v[68:69], s[74:75]\n"
"v_pk_mul_f32 v[70:71], v[70:71], s[74:75]\n"
"v_cvt_pk_bf16_f32 v64, v64, v65\n"
"v_cvt_pk_bf16_f32 v65, v66, v67\n"
"v_cvt_pk_bf16_f32 v66, v68, v69\n"
"v_cvt_pk_bf16_f32 v67, v70, v71\n"
"buffer_store_dwordx4 v[64:67], v36, s[56:59], s69 offen offset:3072\n"
"s_add_u32 s69, s68, 57344\n"
"v_accvgpr_read_b32 v40, a224\n"
"v_accvgpr_read_b32 v41, a225\n"
"v_accvgpr_read_b32 v42, a226\n"
"v_accvgpr_read_b32 v43, a227\n"
"v_accvgpr_read_b32 v44, a228\n"
"v_accvgpr_read_b32 v45, a229\n"
"v_accvgpr_read_b32 v46, a230\n"
"v_accvgpr_read_b32 v47, a231\n"
"v_pk_mul_f32 v[40:41], v[40:41], s[74:75]\n"
"v_pk_mul_f32 v[42:43], v[42:43], s[74:75]\n"
"v_pk_mul_f32 v[44:45], v[44:45], s[74:75]\n"
"v_pk_mul_f32 v[46:47], v[46:47], s[74:75]\n"
"v_cvt_pk_bf16_f32 v40, v40, v41\n"
"v_cvt_pk_bf16_f32 v41, v42, v43\n"
"v_cvt_pk_bf16_f32 v42, v44, v45\n"
"v_cvt_pk_bf16_f32 v43, v46, v47\n"
"buffer_store_dwordx4 v[40:43], v36, s[56:59], s69 offen offset:0\n"
"v_accvgpr_read_b32 v48, a232\n"
"v_accvgpr_read_b32 v49, a233\n"
"v_accvgpr_read_b32 v50, a234\n"
"v_accvgpr_read_b32 v51, a235\n"
"v_accvgpr_read_b32 v52, a236\n"
"v_accvgpr_read_b32 v53, a237\n"
"v_accvgpr_read_b32 v54, a238\n"
"v_accvgpr_read_b32 v55, a239\n"
"v_pk_mul_f32 v[48:49], v[48:49], s[74:75]\n"
"v_pk_mul_f32 v[50:51], v[50:51], s[74:75]\n"
"v_pk_mul_f32 v[52:53], v[52:53], s[74:75]\n"
"v_pk_mul_f32 v[54:55], v[54:55], s[74:75]\n"
"v_cvt_pk_bf16_f32 v48, v48, v49\n"
"v_cvt_pk_bf16_f32 v49, v50, v51\n"
"v_cvt_pk_bf16_f32 v50, v52, v53\n"
"v_cvt_pk_bf16_f32 v51, v54, v55\n"
"buffer_store_dwordx4 v[48:51], v36, s[56:59], s69 offen offset:1024\n"
"v_accvgpr_read_b32 v56, a240\n"
"v_accvgpr_read_b32 v57, a241\n"
"v_accvgpr_read_b32 v58, a242\n"
"v_accvgpr_read_b32 v59, a243\n"
"v_accvgpr_read_b32 v60, a244\n"
"v_accvgpr_read_b32 v61, a245\n"
"v_accvgpr_read_b32 v62, a246\n"
"v_accvgpr_read_b32 v63, a247\n"
"v_pk_mul_f32 v[56:57], v[56:57], s[74:75]\n"
"v_pk_mul_f32 v[58:59], v[58:59], s[74:75]\n"
"v_pk_mul_f32 v[60:61], v[60:61], s[74:75]\n"
"v_pk_mul_f32 v[62:63], v[62:63], s[74:75]\n"
"v_cvt_pk_bf16_f32 v56, v56, v57\n"
"v_cvt_pk_bf16_f32 v57, v58, v59\n"
"v_cvt_pk_bf16_f32 v58, v60, v61\n"
"v_cvt_pk_bf16_f32 v59, v62, v63\n"
"buffer_store_dwordx4 v[56:59], v36, s[56:59], s69 offen offset:2048\n"
"v_accvgpr_read_b32 v64, a248\n"
"v_accvgpr_read_b32 v65, a249\n"
"v_accvgpr_read_b32 v66, a250\n"
"v_accvgpr_read_b32 v67, a251\n"
"v_accvgpr_read_b32 v68, a252\n"
"v_accvgpr_read_b32 v69, a253\n"
"v_accvgpr_read_b32 v70, a254\n"
"v_accvgpr_read_b32 v71, a255\n"
"v_pk_mul_f32 v[64:65], v[64:65], s[74:75]\n"
"v_pk_mul_f32 v[66:67], v[66:67], s[74:75]\n"
"v_pk_mul_f32 v[68:69], v[68:69], s[74:75]\n"
"v_pk_mul_f32 v[70:71], v[70:71], s[74:75]\n"
"v_cvt_pk_bf16_f32 v64, v64, v65\n"
"v_cvt_pk_bf16_f32 v65, v66, v67\n"
"v_cvt_pk_bf16_f32 v66, v68, v69\n"
"v_cvt_pk_bf16_f32 v67, v70, v71\n"
"buffer_store_dwordx4 v[64:67], v36, s[56:59], s69 offen offset:3072\n"
"s_waitcnt vmcnt(32)\n"
        ::[c] "s"(cp), [w] "s"(wp), [o] "s"(op), [nc] "s"(ncp), [nw] "s"(nwp), [no] "s"(nop), [wm] "s"(wm), [wn] "s"(wn), [sm] "s"(smo), [wv] "s"(wvi)
        : "v1","v2","v3","v4","v5","v6","v7","v8","v9","v10","v11","v12","v13","v14","v15","v16","v17","v18","v19","v20","v21","v22","v23","v24","v25","v26","v27","v28","v29","v30","v31","v32","v33","v34","v35","v36","v37","v38","v39","v40","v41","v42","v43","v44","v45","v46","v47","v48","v49","v50","v51","v52","v53","v54","v55","v56","v57","v58","v59","v60","v61","v62","v63","v64","v65","v66","v67","v68","v69","v70","v71","v72","v73","v74","v75","v76","v128","v129","v130","v131","v132","v133","v134","v135","v136","v137","v138","v139","v140","v141","v142","v143","v144","v145","v146","v147","v148","v149","v150","v151","v152","v153","v154","v155","v156","v157","v158","v159","v160","v161","v162","v163","v164","v165","v166","v167","v168","v169","v170","v171","v172","v173","v174","v175","v176","v177","v178","v179","v180","v181","v182","v183","v184","v185","v186","v187","v188","v189","v190","v191","v192","v193","v194","v195","v196","v197","v198","v199","v200","v201","v202","v203","v204","v205","v206","v207","v208","v209","v210","v211","v212","v213","v214","v215","v216","v217","v218","v219","v220","v221","v222","v223","v224","v225","v226","v227","v228","v229","v230","v231","v232","v233","v234","v235","v236","v237","v238","v239","v240","v241","v242","v243","v244","v245","v246","v247","v248","v249","v250","v251","v252","v253","v254","v255","a0","a1","a2","a3","a4","a5","a6","a7","a8","a9","a10","a11","a12","a13","a14","a15","a16","a17","a18","a19","a20","a21","a22","a23","a24","a25","a26","a27","a28","a29","a30","a31","a32","a33","a34","a35","a36","a37","a38","a39","a40","a41","a42","a43","a44","a45","a46","a47","a48","a49","a50","a51","a52","a53","a54","a55","a56","a57","a58","a59","a60","a61","a62","a63","a64","a65","a66","a67","a68","a69","a70","a71","a72","a73","a74","a75","a76","a77","a78","a79","a80","a81","a82","a83","a84","a85","a86","a87","a88","a89","a90","a91","a92","a93","a94","a95","a96","a97","a98","a99","a100","a101","a102","a103","a104","a105","a106","a107","a108","a109","a110","a111","a112","a113","a114","a115","a116","a117","a118","a119","a120","a121","a122","a123","a124","a125","a126","a127","a128","a129","a130","a131","a132","a133","a134","a135","a136","a137","a138","a139","a140","a141","a142","a143","a144","a145","a146","a147","a148","a149","a150","a151","a152","a153","a154","a155","a156","a157","a158","a159","a160","a161","a162","a163","a164","a165","a166","a167","a168","a169","a170","a171","a172","a173","a174","a175","a176","a177","a178","a179","a180","a181","a182","a183","a184","a185","a186","a187","a188","a189","a190","a191","a192","a193","a194","a195","a196","a197","a198","a199","a200","a201","a202","a203","a204","a205","a206","a207","a208","a209","a210","a211","a212","a213","a214","a215","a216","a217","a218","a219","a220","a221","a222","a223","a224","a225","a226","a227","a228","a229","a230","a231","a232","a233","a234","a235","a236","a237","a238","a239","a240","a241","a242","a243","a244","a245","a246","a247","a248","a249","a250","a251","a252","a253","a254","a255","s36","s37","s38","s39","s40","s41","s42","s43","s44","s45","s46","s47","s48","s49","s50","s51","s52","s53","s54","s55","s56","s57","s58","s59","s60","s61","s62","s63","s64","s65","s66","s67","s68","s69","s70","s71","s72","s73","s74","s75","s76","s77","s78","s79","s80","s81","s82","s83","memory","vcc","scc");
  } else {
    const char* wp = wv + h * 512;
    const int wg = xc * 16 + kb * 2 + (id & 1), u0 = wg * 4 + wvi;
    auto touch = [&](const char* p) {
      asm volatile("s_mov_b32 m0, %0\n s_nop 0\n global_load_lds_dword %1, %2" ::"s"(smo + 118784), "v"((threadIdx.x & 63) * 128), "s"(p + u0 * 8192) : "memory");
    };
    if (1 & 1) touch(c);
    if (1 & 2) touch(wk + 128 * 8192);
    b1([&]() {
      relay<0>(u0, wvi, smo, wo, w2, w3, w4, ws);
      if (1 & 4) touch(wk + 512 * 8192);
      touch(wv);
      touch(wv + 512 * 8192);
    }, [&]() {
      asm volatile("s_waitcnt vmcnt(0)" ::: "memory");
      relay<1>(u0, wvi, smo, wo, w2, w3, w4, ws);
      relay<0>(u0 + 1024, wvi, smo, wo, w2, w3, w4, ws);
    }, sm, threadIdx.x, wg, x, w1, gw, ws);
    __builtin_amdgcn_s_barrier();
    asm volatile(
"v_mbcnt_lo_u32_b32 v1, -1, 0\n"
"v_mbcnt_hi_u32_b32 v1, -1, v1\n"
"v_lshl_add_u32 v1, %[wv], 6, v1\n"
"s_mov_b64 s[36:37], %[c]\n"
"s_mov_b32 s39, 0x20000\n"
"s_mov_b64 s[40:41], %[w]\n"
"s_mov_b32 s43, 0x20000\n"
"s_mov_b64 s[44:45], %[o]\n"
"s_mov_b32 s47, 0x20000\n"
"s_mov_b64 s[48:49], s[36:37]\n"
"s_mov_b32 s50, 0\n"
"s_mov_b32 s51, 0x20000\n"
"s_mov_b64 s[52:53], s[40:41]\n"
"s_mov_b32 s54, 0\n"
"s_mov_b32 s55, 0x20000\n"
"s_mov_b64 s[56:57], s[44:45]\n"
"s_mov_b32 s58, 0\n"
"s_mov_b32 s59, 0x20000\n"
"s_mov_b32 s38, 524288\n"
"s_mov_b32 s42, %[wn]\n"
"s_mov_b32 s46, 262144\n"
"s_mov_b32 s62, 16\n"
"s_mov_b32 s63, 16\n"
"s_mov_b32 s74, 0x3db8aa3b\n"
"s_mov_b32 s75, 0x3db8aa3b\n"
"s_mov_b32 s60, 0\n"
"s_mov_b32 s61, 0\n"
"v_lshrrev_b32 v41, 6, v1\n"
"v_and_b32 v40, 63, v1\n"
"s_nop 0\n"
"v_readfirstlane_b32 s76, v41\n"
"s_lshl_b32 s68, s76, 13\n"
"s_add_u32 s64, s68, %[sm]\n"
"s_add_u32 s65, s64, 32768\n"
"s_add_u32 s66, s64, 65536\n"
"s_add_u32 s67, s64, 98304\n"
"v_lshrrev_b32 v41, 4, v40\n"
"v_add_u32 v41, 0, v41\n"
"v_xor_b32 v41, v41, v40\n"
"v_and_b32 v41, 7, v41\n"
"v_lshlrev_b32 v41, 4, v41\n"
"s_lshl_b32 s68, s76, 6\n"
"s_add_u32 s68, s68, 0\n"
"v_lshrrev_b32 v42, 3, v40\n"
"v_add_u32 v42, s68, v42\n"
"v_lshl_add_u32 v12, v42, 11, v41\n"
"s_lshl_b32 s68, s76, 4\n"
"s_add_u32 s68, s68, 0\n"
"v_lshrrev_b32 v42, 5, v40\n"
"v_add_u32 v42, s68, v42\n"
"v_and_b32 v41, 3, v42\n"
"v_or_b32 v41, 0, v41\n"
"v_lshlrev_b32 v41, 1, v41\n"
"v_xor_b32 v41, v41, v40\n"
"v_and_b32 v41, 31, v41\n"
"v_lshlrev_b32 v41, 4, v41\n"
"v_lshl_add_u32 v20, v42, 13, v41\n"
"v_lshrrev_b32 v41, 4, v40\n"
"v_add_u32 v41, 4, v41\n"
"v_xor_b32 v41, v41, v40\n"
"v_and_b32 v41, 7, v41\n"
"v_lshlrev_b32 v41, 4, v41\n"
"s_lshl_b32 s68, s76, 6\n"
"s_add_u32 s68, s68, 8\n"
"v_lshrrev_b32 v42, 3, v40\n"
"v_add_u32 v42, s68, v42\n"
"v_lshl_add_u32 v13, v42, 11, v41\n"
"s_lshl_b32 s68, s76, 4\n"
"s_add_u32 s68, s68, 2\n"
"v_lshrrev_b32 v42, 5, v40\n"
"v_add_u32 v42, s68, v42\n"
"v_and_b32 v41, 3, v42\n"
"v_or_b32 v41, 0, v41\n"
"v_lshlrev_b32 v41, 1, v41\n"
"v_xor_b32 v41, v41, v40\n"
"v_and_b32 v41, 31, v41\n"
"v_lshlrev_b32 v41, 4, v41\n"
"v_lshl_add_u32 v21, v42, 13, v41\n"
"v_add_u32 v14, 32768, v12\n"
"v_add_u32 v22, 32768, v20\n"
"v_add_u32 v15, 32768, v13\n"
"v_add_u32 v23, 32768, v21\n"
"v_add_u32 v16, 32768, v14\n"
"v_xor_b32 v41, 128, v22\n"
"v_add_u32 v24, 32768, v41\n"
"v_add_u32 v17, 32768, v15\n"
"v_xor_b32 v41, 128, v23\n"
"v_add_u32 v25, 32768, v41\n"
"v_add_u32 v18, 32768, v16\n"
"v_add_u32 v26, 32768, v24\n"
"v_add_u32 v19, 32768, v17\n"
"v_add_u32 v27, 32768, v25\n"
"s_mov_b32 m0, s64\n"
"s_lshr_b32 s68, s76, 1\n"
"s_lshl_b32 s68, s68, 14\n"
"s_add_u32 s68, s68, %[sm]\n"
"buffer_load_dwordx4 v12, s[36:39], s60 offen lds\n"
"s_add_u32 m0, m0, 1024\n"
"v_and_b32 v41, 15, v40\n"
"v_lshrrev_b32 v42, 4, v40\n"
"v_lshrrev_b32 v43, 1, v41\n"
"buffer_load_dwordx4 v13, s[36:39], s60 offen lds\n"
"s_add_u32 m0, m0, 1024\n"
"v_add_u32 v44, 0, v42\n"
"v_xor_b32 v44, v44, v43\n"
"v_lshlrev_b32 v44, 4, v44\n"
"buffer_load_dwordx4 v14, s[36:39], s60 offen lds\n"
"s_add_u32 m0, m0, 1024\n"
"v_lshl_add_u32 v44, v41, 7, v44\n"
"v_add_u32 v2, s68, v44\n"
"v_add_u32 v44, 4, v42\n"
"buffer_load_dwordx4 v15, s[36:39], s60 offen lds\n"
"s_add_u32 m0, m0, 1024\n"
"v_xor_b32 v44, v44, v43\n"
"v_lshlrev_b32 v44, 4, v44\n"
"v_lshl_add_u32 v44, v41, 7, v44\n"
"buffer_load_dwordx4 v16, s[36:39], s60 offen lds\n"
"s_add_u32 m0, m0, 1024\n"
"v_add_u32 v3, s68, v44\n"
"s_and_b32 s68, s76, 1\n"
"s_lshl_b32 s68, s68, 8\n"
"buffer_load_dwordx4 v17, s[36:39], s60 offen lds\n"
"s_add_u32 m0, m0, 1024\n"
"s_add_u32 s68, s68, %[sm]\n"
"s_add_u32 s68, s68, 65536\n"
"v_lshrrev_b32 v41, 2, v41\n"
"buffer_load_dwordx4 v18, s[36:39], s60 offen lds\n"
"s_add_u32 m0, m0, 1024\n"
"v_lshl_add_u32 v43, v42, 3, v41\n"
"v_lshlrev_b32 v43, 9, v43\n"
"v_and_b32 v44, 3, v40\n"
"buffer_load_dwordx4 v19, s[36:39], s60 offen lds\n"
"s_mov_b32 m0, s66\n"
"v_lshl_add_u32 v43, v44, 3, v43\n"
"v_add_u32 v43, s68, v43\n"
"v_and_b32 v42, 1, v42\n"
"buffer_load_dwordx4 v20, s[40:43], s61 offen lds\n"
"s_add_u32 m0, m0, 1024\n"
"v_lshl_add_u32 v41, v42, 2, v41\n"
"v_xor_b32 v44, 0, v41\n"
"v_lshl_add_u32 v4, v44, 5, v43\n"
"buffer_load_dwordx4 v21, s[40:43], s61 offen lds\n"
"s_add_u32 m0, m0, 1024\n"
"v_xor_b32 v44, 1, v41\n"
"v_lshl_add_u32 v5, v44, 5, v43\n"
"v_xor_b32 v44, 2, v41\n"
"buffer_load_dwordx4 v22, s[40:43], s61 offen lds\n"
"s_add_u32 m0, m0, 1024\n"
"v_lshl_add_u32 v6, v44, 5, v43\n"
"v_xor_b32 v44, 3, v41\n"
"v_lshl_add_u32 v7, v44, 5, v43\n"
"buffer_load_dwordx4 v23, s[40:43], s61 offen lds\n"
"s_add_u32 m0, m0, 1024\n"
"v_xor_b32 v44, 4, v41\n"
"v_lshl_add_u32 v8, v44, 5, v43\n"
"v_xor_b32 v44, 5, v41\n"
"buffer_load_dwordx4 v24, s[40:43], s61 offen lds\n"
"s_add_u32 m0, m0, 1024\n"
"v_lshl_add_u32 v9, v44, 5, v43\n"
"v_xor_b32 v44, 6, v41\n"
"v_lshl_add_u32 v10, v44, 5, v43\n"
"buffer_load_dwordx4 v25, s[40:43], s61 offen lds\n"
"s_add_u32 m0, m0, 1024\n"
"v_xor_b32 v44, 7, v41\n"
"v_lshl_add_u32 v11, v44, 5, v43\n"
"v_lshlrev_b32 v36, 4, v40\n"
"buffer_load_dwordx4 v26, s[40:43], s61 offen lds\n"
"s_add_u32 m0, m0, 1024\n"
"s_nop 0\n"
"buffer_load_dwordx4 v27, s[40:43], s61 offen lds\n"
"s_add_u32 s60, s60, 128\n"
"s_add_u32 s61, s61, 524288\n"
"s_mov_b32 m0, s65\n"
"s_nop 0\n"
"buffer_load_dwordx4 v12, s[36:39], s60 offen lds\n"
"s_add_u32 m0, m0, 1024\n"
"s_nop 0\n"
"buffer_load_dwordx4 v13, s[36:39], s60 offen lds\n"
"s_add_u32 m0, m0, 1024\n"
"s_nop 0\n"
"buffer_load_dwordx4 v14, s[36:39], s60 offen lds\n"
"s_add_u32 m0, m0, 1024\n"
"s_nop 0\n"
"buffer_load_dwordx4 v15, s[36:39], s60 offen lds\n"
"s_add_u32 m0, m0, 1024\n"
"s_nop 0\n"
"buffer_load_dwordx4 v16, s[36:39], s60 offen lds\n"
"s_add_u32 m0, m0, 1024\n"
"s_nop 0\n"
"buffer_load_dwordx4 v17, s[36:39], s60 offen lds\n"
"s_add_u32 m0, m0, 1024\n"
"s_nop 0\n"
"buffer_load_dwordx4 v18, s[36:39], s60 offen lds\n"
"s_add_u32 m0, m0, 1024\n"
"s_nop 0\n"
"buffer_load_dwordx4 v19, s[36:39], s60 offen lds\n"
"s_mov_b32 m0, s67\n"
"s_nop 0\n"
"buffer_load_dwordx4 v20, s[40:43], s61 offen lds\n"
"s_add_u32 m0, m0, 1024\n"
"s_nop 0\n"
"buffer_load_dwordx4 v21, s[40:43], s61 offen lds\n"
"s_add_u32 m0, m0, 1024\n"
"s_nop 0\n"
"buffer_load_dwordx4 v22, s[40:43], s61 offen lds\n"
"s_add_u32 m0, m0, 1024\n"
"s_nop 0\n"
"buffer_load_dwordx4 v23, s[40:43], s61 offen lds\n"
"s_add_u32 m0, m0, 1024\n"
"s_nop 0\n"
"buffer_load_dwordx4 v24, s[40:43], s61 offen lds\n"
"s_add_u32 m0, m0, 1024\n"
"s_nop 0\n"
"buffer_load_dwordx4 v25, s[40:43], s61 offen lds\n"
"s_add_u32 m0, m0, 1024\n"
"s_nop 0\n"
"buffer_load_dwordx4 v26, s[40:43], s61 offen lds\n"
"s_add_u32 m0, m0, 1024\n"
"s_nop 0\n"
"buffer_load_dwordx4 v27, s[40:43], s61 offen lds\n"
"s_add_u32 s60, s60, 128\n"
"s_add_u32 s61, s61, 524288\n"
"s_waitcnt vmcnt(16)\n"
"s_barrier\n"
"ds_read_b128 v[128:131], v2 offset:0\n"
"ds_read_b128 v[132:135], v2 offset:2048\n"
"ds_read_b64_tr_b16 v[160:161], v4 offset:0\n"
"ds_read_b64_tr_b16 v[162:163], v4 offset:2048\n"
"ds_read_b64_tr_b16 v[164:165], v5 offset:0\n"
"ds_read_b64_tr_b16 v[166:167], v5 offset:2048\n"
"ds_read_b64_tr_b16 v[168:169], v6 offset:0\n"
"ds_read_b64_tr_b16 v[170:171], v6 offset:2048\n"
"ds_read_b64_tr_b16 v[172:173], v7 offset:0\n"
"ds_read_b64_tr_b16 v[174:175], v7 offset:2048\n"
"ds_read_b64_tr_b16 v[176:177], v8 offset:0\n"
"ds_read_b64_tr_b16 v[178:179], v8 offset:2048\n"
"ds_read_b64_tr_b16 v[180:181], v9 offset:0\n"
"ds_read_b64_tr_b16 v[182:183], v9 offset:2048\n"
"ds_read_b64_tr_b16 v[184:185], v10 offset:0\n"
"ds_read_b64_tr_b16 v[186:187], v10 offset:2048\n"
"ds_read_b64_tr_b16 v[188:189], v11 offset:0\n"
"ds_read_b64_tr_b16 v[190:191], v11 offset:2048\n"
"ds_read_b128 v[136:139], v2 offset:4096\n"
"ds_read_b128 v[140:143], v2 offset:6144\n"
"ds_read_b128 v[144:147], v2 offset:8192\n"
"ds_read_b128 v[148:151], v2 offset:10240\n"
"ds_read_b128 v[152:155], v2 offset:12288\n"
"ds_read_b128 v[156:159], v2 offset:14336\n"
"s_waitcnt lgkmcnt(0)\n"
"L_peel%=:\n"
"v_mfma_f32_16x16x32_bf16 a[0:3], v[128:131], v[160:163], 0\n"
"ds_read_b128 v[192:195], v3 offset:0\n"
"v_mfma_f32_16x16x32_bf16 a[4:7], v[132:135], v[160:163], 0\n"
"ds_read_b128 v[196:199], v3 offset:2048\n"
"v_mfma_f32_16x16x32_bf16 a[32:35], v[128:131], v[164:167], 0\n"
"ds_read_b64_tr_b16 v[224:225], v4 offset:16384\n"
"v_mfma_f32_16x16x32_bf16 a[36:39], v[132:135], v[164:167], 0\n"
"ds_read_b64_tr_b16 v[226:227], v4 offset:18432\n"
"v_mfma_f32_16x16x32_bf16 a[64:67], v[128:131], v[168:171], 0\n"
"ds_read_b64_tr_b16 v[228:229], v5 offset:16384\n"
"v_mfma_f32_16x16x32_bf16 a[68:71], v[132:135], v[168:171], 0\n"
"ds_read_b64_tr_b16 v[230:231], v5 offset:18432\n"
"v_mfma_f32_16x16x32_bf16 a[96:99], v[128:131], v[172:175], 0\n"
"ds_read_b64_tr_b16 v[232:233], v6 offset:16384\n"
"v_mfma_f32_16x16x32_bf16 a[100:103], v[132:135], v[172:175], 0\n"
"ds_read_b64_tr_b16 v[234:235], v6 offset:18432\n"
"v_mfma_f32_16x16x32_bf16 a[128:131], v[128:131], v[176:179], 0\n"
"ds_read_b64_tr_b16 v[236:237], v7 offset:16384\n"
"v_mfma_f32_16x16x32_bf16 a[132:135], v[132:135], v[176:179], 0\n"
"ds_read_b64_tr_b16 v[238:239], v7 offset:18432\n"
"v_mfma_f32_16x16x32_bf16 a[160:163], v[128:131], v[180:183], 0\n"
"ds_read_b64_tr_b16 v[240:241], v8 offset:16384\n"
"v_mfma_f32_16x16x32_bf16 a[164:167], v[132:135], v[180:183], 0\n"
"ds_read_b64_tr_b16 v[242:243], v8 offset:18432\n"
"v_mfma_f32_16x16x32_bf16 a[192:195], v[128:131], v[184:187], 0\n"
"ds_read_b64_tr_b16 v[244:245], v9 offset:16384\n"
"v_mfma_f32_16x16x32_bf16 a[196:199], v[132:135], v[184:187], 0\n"
"ds_read_b64_tr_b16 v[246:247], v9 offset:18432\n"
"v_mfma_f32_16x16x32_bf16 a[224:227], v[128:131], v[188:191], 0\n"
"ds_read_b64_tr_b16 v[248:249], v10 offset:16384\n"
"v_mfma_f32_16x16x32_bf16 a[228:231], v[132:135], v[188:191], 0\n"
"ds_read_b64_tr_b16 v[250:251], v10 offset:18432\n"
"v_mfma_f32_16x16x32_bf16 a[8:11], v[136:139], v[160:163], 0\n"
"ds_read_b64_tr_b16 v[252:253], v11 offset:16384\n"
"v_mfma_f32_16x16x32_bf16 a[12:15], v[140:143], v[160:163], 0\n"
"ds_read_b64_tr_b16 v[254:255], v11 offset:18432\n"
"v_mfma_f32_16x16x32_bf16 a[40:43], v[136:139], v[164:167], 0\n"
"ds_read_b128 v[200:203], v3 offset:4096\n"
"v_mfma_f32_16x16x32_bf16 a[44:47], v[140:143], v[164:167], 0\n"
"ds_read_b128 v[204:207], v3 offset:6144\n"
"v_mfma_f32_16x16x32_bf16 a[72:75], v[136:139], v[168:171], 0\n"
"ds_read_b128 v[208:211], v3 offset:8192\n"
"v_mfma_f32_16x16x32_bf16 a[76:79], v[140:143], v[168:171], 0\n"
"ds_read_b128 v[212:215], v3 offset:10240\n"
"v_mfma_f32_16x16x32_bf16 a[104:107], v[136:139], v[172:175], 0\n"
"ds_read_b128 v[216:219], v3 offset:12288\n"
"v_mfma_f32_16x16x32_bf16 a[108:111], v[140:143], v[172:175], 0\n"
"ds_read_b128 v[220:223], v3 offset:14336\n"
"v_mfma_f32_16x16x32_bf16 a[136:139], v[136:139], v[176:179], 0\n"
"v_mfma_f32_16x16x32_bf16 a[140:143], v[140:143], v[176:179], 0\n"
"v_mfma_f32_16x16x32_bf16 a[168:171], v[136:139], v[180:183], 0\n"
"v_mfma_f32_16x16x32_bf16 a[172:175], v[140:143], v[180:183], 0\n"
"s_waitcnt lgkmcnt(0)\n"
"s_barrier\n"
"v_mfma_f32_16x16x32_bf16 a[200:203], v[136:139], v[184:187], 0\n"
"s_mov_b32 m0, s64\n"
"v_mfma_f32_16x16x32_bf16 a[204:207], v[140:143], v[184:187], 0\n"
"buffer_load_dwordx4 v12, s[36:39], s60 offen lds\n"
"v_mfma_f32_16x16x32_bf16 a[232:235], v[136:139], v[188:191], 0\n"
"v_mfma_f32_16x16x32_bf16 a[236:239], v[140:143], v[188:191], 0\n"
"v_mfma_f32_16x16x32_bf16 a[16:19], v[144:147], v[160:163], 0\n"
"v_mfma_f32_16x16x32_bf16 a[20:23], v[148:151], v[160:163], 0\n"
"v_mfma_f32_16x16x32_bf16 a[48:51], v[144:147], v[164:167], 0\n"
"s_add_u32 m0, m0, 1024\n"
"v_mfma_f32_16x16x32_bf16 a[52:55], v[148:151], v[164:167], 0\n"
"buffer_load_dwordx4 v13, s[36:39], s60 offen lds\n"
"v_mfma_f32_16x16x32_bf16 a[80:83], v[144:147], v[168:171], 0\n"
"v_mfma_f32_16x16x32_bf16 a[84:87], v[148:151], v[168:171], 0\n"
"v_mfma_f32_16x16x32_bf16 a[112:115], v[144:147], v[172:175], 0\n"
"v_mfma_f32_16x16x32_bf16 a[116:119], v[148:151], v[172:175], 0\n"
"v_mfma_f32_16x16x32_bf16 a[144:147], v[144:147], v[176:179], 0\n"
"s_add_u32 m0, m0, 1024\n"
"v_mfma_f32_16x16x32_bf16 a[148:151], v[148:151], v[176:179], 0\n"
"buffer_load_dwordx4 v14, s[36:39], s60 offen lds\n"
"v_mfma_f32_16x16x32_bf16 a[176:179], v[144:147], v[180:183], 0\n"
"v_mfma_f32_16x16x32_bf16 a[180:183], v[148:151], v[180:183], 0\n"
"v_mfma_f32_16x16x32_bf16 a[208:211], v[144:147], v[184:187], 0\n"
"v_mfma_f32_16x16x32_bf16 a[212:215], v[148:151], v[184:187], 0\n"
"v_mfma_f32_16x16x32_bf16 a[240:243], v[144:147], v[188:191], 0\n"
"s_add_u32 m0, m0, 1024\n"
"v_mfma_f32_16x16x32_bf16 a[244:247], v[148:151], v[188:191], 0\n"
"buffer_load_dwordx4 v15, s[36:39], s60 offen lds\n"
"v_mfma_f32_16x16x32_bf16 a[24:27], v[152:155], v[160:163], 0\n"
"v_mfma_f32_16x16x32_bf16 a[28:31], v[156:159], v[160:163], 0\n"
"v_mfma_f32_16x16x32_bf16 a[56:59], v[152:155], v[164:167], 0\n"
"v_mfma_f32_16x16x32_bf16 a[60:63], v[156:159], v[164:167], 0\n"
"v_mfma_f32_16x16x32_bf16 a[88:91], v[152:155], v[168:171], 0\n"
"s_add_u32 m0, m0, 1024\n"
"v_mfma_f32_16x16x32_bf16 a[92:95], v[156:159], v[168:171], 0\n"
"buffer_load_dwordx4 v16, s[36:39], s60 offen lds\n"
"v_mfma_f32_16x16x32_bf16 a[120:123], v[152:155], v[172:175], 0\n"
"v_mfma_f32_16x16x32_bf16 a[124:127], v[156:159], v[172:175], 0\n"
"v_mfma_f32_16x16x32_bf16 a[152:155], v[152:155], v[176:179], 0\n"
"v_mfma_f32_16x16x32_bf16 a[156:159], v[156:159], v[176:179], 0\n"
"v_mfma_f32_16x16x32_bf16 a[184:187], v[152:155], v[180:183], 0\n"
"s_add_u32 m0, m0, 1024\n"
"v_mfma_f32_16x16x32_bf16 a[188:191], v[156:159], v[180:183], 0\n"
"buffer_load_dwordx4 v17, s[36:39], s60 offen lds\n"
"v_mfma_f32_16x16x32_bf16 a[216:219], v[152:155], v[184:187], 0\n"
"v_mfma_f32_16x16x32_bf16 a[220:223], v[156:159], v[184:187], 0\n"
"v_mfma_f32_16x16x32_bf16 a[248:251], v[152:155], v[188:191], 0\n"
"v_mfma_f32_16x16x32_bf16 a[252:255], v[156:159], v[188:191], 0\n"
"v_mfma_f32_16x16x32_bf16 a[0:3], v[192:195], v[224:227], a[0:3]\n"
"s_add_u32 m0, m0, 1024\n"
"v_mfma_f32_16x16x32_bf16 a[4:7], v[196:199], v[224:227], a[4:7]\n"
"buffer_load_dwordx4 v18, s[36:39], s60 offen lds\n"
"v_mfma_f32_16x16x32_bf16 a[32:35], v[192:195], v[228:231], a[32:35]\n"
"v_mfma_f32_16x16x32_bf16 a[36:39], v[196:199], v[228:231], a[36:39]\n"
"v_mfma_f32_16x16x32_bf16 a[64:67], v[192:195], v[232:235], a[64:67]\n"
"v_mfma_f32_16x16x32_bf16 a[68:71], v[196:199], v[232:235], a[68:71]\n"
"v_mfma_f32_16x16x32_bf16 a[96:99], v[192:195], v[236:239], a[96:99]\n"
"s_add_u32 m0, m0, 1024\n"
"v_mfma_f32_16x16x32_bf16 a[100:103], v[196:199], v[236:239], a[100:103]\n"
"buffer_load_dwordx4 v19, s[36:39], s60 offen lds\n"
"v_mfma_f32_16x16x32_bf16 a[128:131], v[192:195], v[240:243], a[128:131]\n"
"v_mfma_f32_16x16x32_bf16 a[132:135], v[196:199], v[240:243], a[132:135]\n"
"v_mfma_f32_16x16x32_bf16 a[160:163], v[192:195], v[244:247], a[160:163]\n"
"v_mfma_f32_16x16x32_bf16 a[164:167], v[196:199], v[244:247], a[164:167]\n"
"v_mfma_f32_16x16x32_bf16 a[192:195], v[192:195], v[248:251], a[192:195]\n"
"s_mov_b32 m0, s66\n"
"v_mfma_f32_16x16x32_bf16 a[196:199], v[196:199], v[248:251], a[196:199]\n"
"buffer_load_dwordx4 v20, s[40:43], s61 offen lds\n"
"v_mfma_f32_16x16x32_bf16 a[224:227], v[192:195], v[252:255], a[224:227]\n"
"v_mfma_f32_16x16x32_bf16 a[228:231], v[196:199], v[252:255], a[228:231]\n"
"v_mfma_f32_16x16x32_bf16 a[8:11], v[200:203], v[224:227], a[8:11]\n"
"v_mfma_f32_16x16x32_bf16 a[12:15], v[204:207], v[224:227], a[12:15]\n"
"v_mfma_f32_16x16x32_bf16 a[40:43], v[200:203], v[228:231], a[40:43]\n"
"s_add_u32 m0, m0, 1024\n"
"v_mfma_f32_16x16x32_bf16 a[44:47], v[204:207], v[228:231], a[44:47]\n"
"buffer_load_dwordx4 v21, s[40:43], s61 offen lds\n"
"v_mfma_f32_16x16x32_bf16 a[72:75], v[200:203], v[232:235], a[72:75]\n"
"v_mfma_f32_16x16x32_bf16 a[76:79], v[204:207], v[232:235], a[76:79]\n"
"v_mfma_f32_16x16x32_bf16 a[104:107], v[200:203], v[236:239], a[104:107]\n"
"v_mfma_f32_16x16x32_bf16 a[108:111], v[204:207], v[236:239], a[108:111]\n"
"v_mfma_f32_16x16x32_bf16 a[136:139], v[200:203], v[240:243], a[136:139]\n"
"s_add_u32 m0, m0, 1024\n"
"v_mfma_f32_16x16x32_bf16 a[140:143], v[204:207], v[240:243], a[140:143]\n"
"buffer_load_dwordx4 v22, s[40:43], s61 offen lds\n"
"v_mfma_f32_16x16x32_bf16 a[168:171], v[200:203], v[244:247], a[168:171]\n"
"v_mfma_f32_16x16x32_bf16 a[172:175], v[204:207], v[244:247], a[172:175]\n"
"v_mfma_f32_16x16x32_bf16 a[200:203], v[200:203], v[248:251], a[200:203]\n"
"v_mfma_f32_16x16x32_bf16 a[204:207], v[204:207], v[248:251], a[204:207]\n"
"v_mfma_f32_16x16x32_bf16 a[232:235], v[200:203], v[252:255], a[232:235]\n"
"s_add_u32 m0, m0, 1024\n"
"v_mfma_f32_16x16x32_bf16 a[236:239], v[204:207], v[252:255], a[236:239]\n"
"buffer_load_dwordx4 v23, s[40:43], s61 offen lds\n"
"v_mfma_f32_16x16x32_bf16 a[16:19], v[208:211], v[224:227], a[16:19]\n"
"s_waitcnt vmcnt(12)\n"
"s_barrier\n"
"v_mfma_f32_16x16x32_bf16 a[20:23], v[212:215], v[224:227], a[20:23]\n"
"ds_read_b128 v[128:131], v2 offset:32768\n"
"v_mfma_f32_16x16x32_bf16 a[48:51], v[208:211], v[228:231], a[48:51]\n"
"ds_read_b128 v[132:135], v2 offset:34816\n"
"v_mfma_f32_16x16x32_bf16 a[52:55], v[212:215], v[228:231], a[52:55]\n"
"ds_read_b64_tr_b16 v[160:161], v4 offset:32768\n"
"v_mfma_f32_16x16x32_bf16 a[80:83], v[208:211], v[232:235], a[80:83]\n"
"s_add_u32 m0, m0, 1024\n"
"ds_read_b64_tr_b16 v[162:163], v4 offset:34816\n"
"v_mfma_f32_16x16x32_bf16 a[84:87], v[212:215], v[232:235], a[84:87]\n"
"buffer_load_dwordx4 v24, s[40:43], s61 offen lds\n"
"ds_read_b64_tr_b16 v[164:165], v5 offset:32768\n"
"v_mfma_f32_16x16x32_bf16 a[112:115], v[208:211], v[236:239], a[112:115]\n"
"ds_read_b64_tr_b16 v[166:167], v5 offset:34816\n"
"v_mfma_f32_16x16x32_bf16 a[116:119], v[212:215], v[236:239], a[116:119]\n"
"ds_read_b64_tr_b16 v[168:169], v6 offset:32768\n"
"v_mfma_f32_16x16x32_bf16 a[144:147], v[208:211], v[240:243], a[144:147]\n"
"ds_read_b64_tr_b16 v[170:171], v6 offset:34816\n"
"v_mfma_f32_16x16x32_bf16 a[148:151], v[212:215], v[240:243], a[148:151]\n"
"ds_read_b64_tr_b16 v[172:173], v7 offset:32768\n"
"v_mfma_f32_16x16x32_bf16 a[176:179], v[208:211], v[244:247], a[176:179]\n"
"s_add_u32 m0, m0, 1024\n"
"ds_read_b64_tr_b16 v[174:175], v7 offset:34816\n"
"v_mfma_f32_16x16x32_bf16 a[180:183], v[212:215], v[244:247], a[180:183]\n"
"buffer_load_dwordx4 v25, s[40:43], s61 offen lds\n"
"ds_read_b64_tr_b16 v[176:177], v8 offset:32768\n"
"v_mfma_f32_16x16x32_bf16 a[208:211], v[208:211], v[248:251], a[208:211]\n"
"ds_read_b64_tr_b16 v[178:179], v8 offset:34816\n"
"v_mfma_f32_16x16x32_bf16 a[212:215], v[212:215], v[248:251], a[212:215]\n"
"ds_read_b64_tr_b16 v[180:181], v9 offset:32768\n"
"v_mfma_f32_16x16x32_bf16 a[240:243], v[208:211], v[252:255], a[240:243]\n"
"ds_read_b64_tr_b16 v[182:183], v9 offset:34816\n"
"v_mfma_f32_16x16x32_bf16 a[244:247], v[212:215], v[252:255], a[244:247]\n"
"ds_read_b64_tr_b16 v[184:185], v10 offset:32768\n"
"v_mfma_f32_16x16x32_bf16 a[24:27], v[216:219], v[224:227], a[24:27]\n"
"s_add_u32 m0, m0, 1024\n"
"ds_read_b64_tr_b16 v[186:187], v10 offset:34816\n"
"v_mfma_f32_16x16x32_bf16 a[28:31], v[220:223], v[224:227], a[28:31]\n"
"buffer_load_dwordx4 v26, s[40:43], s61 offen lds\n"
"ds_read_b64_tr_b16 v[188:189], v11 offset:32768\n"
"v_mfma_f32_16x16x32_bf16 a[56:59], v[216:219], v[228:231], a[56:59]\n"
"ds_read_b64_tr_b16 v[190:191], v11 offset:34816\n"
"v_mfma_f32_16x16x32_bf16 a[60:63], v[220:223], v[228:231], a[60:63]\n"
"ds_read_b128 v[136:139], v2 offset:36864\n"
"v_mfma_f32_16x16x32_bf16 a[88:91], v[216:219], v[232:235], a[88:91]\n"
"ds_read_b128 v[140:143], v2 offset:38912\n"
"v_mfma_f32_16x16x32_bf16 a[92:95], v[220:223], v[232:235], a[92:95]\n"
"ds_read_b128 v[144:147], v2 offset:40960\n"
"v_mfma_f32_16x16x32_bf16 a[120:123], v[216:219], v[236:239], a[120:123]\n"
"s_add_u32 m0, m0, 1024\n"
"ds_read_b128 v[148:151], v2 offset:43008\n"
"v_mfma_f32_16x16x32_bf16 a[124:127], v[220:223], v[236:239], a[124:127]\n"
"buffer_load_dwordx4 v27, s[40:43], s61 offen lds\n"
"s_add_u32 s60, s60, 128\n"
"s_add_u32 s61, s61, 524288\n"
"ds_read_b128 v[152:155], v2 offset:45056\n"
"v_mfma_f32_16x16x32_bf16 a[152:155], v[216:219], v[240:243], a[152:155]\n"
"ds_read_b128 v[156:159], v2 offset:47104\n"
"v_mfma_f32_16x16x32_bf16 a[156:159], v[220:223], v[240:243], a[156:159]\n"
"v_mfma_f32_16x16x32_bf16 a[184:187], v[216:219], v[244:247], a[184:187]\n"
"v_mfma_f32_16x16x32_bf16 a[188:191], v[220:223], v[244:247], a[188:191]\n"
"v_mfma_f32_16x16x32_bf16 a[216:219], v[216:219], v[248:251], a[216:219]\n"
"v_mfma_f32_16x16x32_bf16 a[220:223], v[220:223], v[248:251], a[220:223]\n"
"v_mfma_f32_16x16x32_bf16 a[248:251], v[216:219], v[252:255], a[248:251]\n"
"s_sub_u32 s62, s62, 1\n"
"s_cmp_eq_u32 s62, 0\n"
"v_mfma_f32_16x16x32_bf16 a[252:255], v[220:223], v[252:255], a[252:255]\n"
"s_waitcnt lgkmcnt(0)\n"
"s_branch L_mid%=\n"
"L_top%=:\n"
"s_cmp_lg_u32 s62, 2\n"
"s_cbranch_scc1 L_ns0%=\n"
"s_mov_b64 s[36:37], s[48:49]\n"
"s_mov_b64 s[38:39], s[50:51]\n"
"s_mov_b64 s[40:41], s[52:53]\n"
"s_mov_b64 s[42:43], s[54:55]\n"
"s_mov_b32 s50, 0\n"
"s_mov_b32 s54, 0\n"
"s_mov_b32 s60, 0\n"
"s_mov_b32 s61, 0\n"
"L_ns0%=:\n"
"v_mfma_f32_16x16x32_bf16 a[0:3], v[128:131], v[160:163], a[0:3]\n"
"ds_read_b128 v[192:195], v3 offset:0\n"
"v_mfma_f32_16x16x32_bf16 a[4:7], v[132:135], v[160:163], a[4:7]\n"
"ds_read_b128 v[196:199], v3 offset:2048\n"
"v_mfma_f32_16x16x32_bf16 a[32:35], v[128:131], v[164:167], a[32:35]\n"
"ds_read_b64_tr_b16 v[224:225], v4 offset:16384\n"
"v_mfma_f32_16x16x32_bf16 a[36:39], v[132:135], v[164:167], a[36:39]\n"
"ds_read_b64_tr_b16 v[226:227], v4 offset:18432\n"
"v_mfma_f32_16x16x32_bf16 a[64:67], v[128:131], v[168:171], a[64:67]\n"
"ds_read_b64_tr_b16 v[228:229], v5 offset:16384\n"
"v_mfma_f32_16x16x32_bf16 a[68:71], v[132:135], v[168:171], a[68:71]\n"
"ds_read_b64_tr_b16 v[230:231], v5 offset:18432\n"
"v_mfma_f32_16x16x32_bf16 a[96:99], v[128:131], v[172:175], a[96:99]\n"
"ds_read_b64_tr_b16 v[232:233], v6 offset:16384\n"
"v_mfma_f32_16x16x32_bf16 a[100:103], v[132:135], v[172:175], a[100:103]\n"
"ds_read_b64_tr_b16 v[234:235], v6 offset:18432\n"
"v_mfma_f32_16x16x32_bf16 a[128:131], v[128:131], v[176:179], a[128:131]\n"
"ds_read_b64_tr_b16 v[236:237], v7 offset:16384\n"
"v_mfma_f32_16x16x32_bf16 a[132:135], v[132:135], v[176:179], a[132:135]\n"
"ds_read_b64_tr_b16 v[238:239], v7 offset:18432\n"
"v_mfma_f32_16x16x32_bf16 a[160:163], v[128:131], v[180:183], a[160:163]\n"
"ds_read_b64_tr_b16 v[240:241], v8 offset:16384\n"
"v_mfma_f32_16x16x32_bf16 a[164:167], v[132:135], v[180:183], a[164:167]\n"
"ds_read_b64_tr_b16 v[242:243], v8 offset:18432\n"
"v_mfma_f32_16x16x32_bf16 a[192:195], v[128:131], v[184:187], a[192:195]\n"
"ds_read_b64_tr_b16 v[244:245], v9 offset:16384\n"
"v_mfma_f32_16x16x32_bf16 a[196:199], v[132:135], v[184:187], a[196:199]\n"
"ds_read_b64_tr_b16 v[246:247], v9 offset:18432\n"
"v_mfma_f32_16x16x32_bf16 a[224:227], v[128:131], v[188:191], a[224:227]\n"
"ds_read_b64_tr_b16 v[248:249], v10 offset:16384\n"
"v_mfma_f32_16x16x32_bf16 a[228:231], v[132:135], v[188:191], a[228:231]\n"
"ds_read_b64_tr_b16 v[250:251], v10 offset:18432\n"
"v_mfma_f32_16x16x32_bf16 a[8:11], v[136:139], v[160:163], a[8:11]\n"
"ds_read_b64_tr_b16 v[252:253], v11 offset:16384\n"
"v_mfma_f32_16x16x32_bf16 a[12:15], v[140:143], v[160:163], a[12:15]\n"
"ds_read_b64_tr_b16 v[254:255], v11 offset:18432\n"
"v_mfma_f32_16x16x32_bf16 a[40:43], v[136:139], v[164:167], a[40:43]\n"
"ds_read_b128 v[200:203], v3 offset:4096\n"
"v_mfma_f32_16x16x32_bf16 a[44:47], v[140:143], v[164:167], a[44:47]\n"
"ds_read_b128 v[204:207], v3 offset:6144\n"
"v_mfma_f32_16x16x32_bf16 a[72:75], v[136:139], v[168:171], a[72:75]\n"
"ds_read_b128 v[208:211], v3 offset:8192\n"
"v_mfma_f32_16x16x32_bf16 a[76:79], v[140:143], v[168:171], a[76:79]\n"
"ds_read_b128 v[212:215], v3 offset:10240\n"
"v_mfma_f32_16x16x32_bf16 a[104:107], v[136:139], v[172:175], a[104:107]\n"
"ds_read_b128 v[216:219], v3 offset:12288\n"
"v_mfma_f32_16x16x32_bf16 a[108:111], v[140:143], v[172:175], a[108:111]\n"
"ds_read_b128 v[220:223], v3 offset:14336\n"
"v_mfma_f32_16x16x32_bf16 a[136:139], v[136:139], v[176:179], a[136:139]\n"
"v_mfma_f32_16x16x32_bf16 a[140:143], v[140:143], v[176:179], a[140:143]\n"
"v_mfma_f32_16x16x32_bf16 a[168:171], v[136:139], v[180:183], a[168:171]\n"
"v_mfma_f32_16x16x32_bf16 a[172:175], v[140:143], v[180:183], a[172:175]\n"
"s_waitcnt lgkmcnt(0)\n"
"s_barrier\n"
"v_mfma_f32_16x16x32_bf16 a[200:203], v[136:139], v[184:187], a[200:203]\n"
"s_mov_b32 m0, s64\n"
"v_mfma_f32_16x16x32_bf16 a[204:207], v[140:143], v[184:187], a[204:207]\n"
"buffer_load_dwordx4 v12, s[36:39], s60 offen lds\n"
"v_mfma_f32_16x16x32_bf16 a[232:235], v[136:139], v[188:191], a[232:235]\n"
"v_mfma_f32_16x16x32_bf16 a[236:239], v[140:143], v[188:191], a[236:239]\n"
"v_mfma_f32_16x16x32_bf16 a[16:19], v[144:147], v[160:163], a[16:19]\n"
"v_mfma_f32_16x16x32_bf16 a[20:23], v[148:151], v[160:163], a[20:23]\n"
"v_mfma_f32_16x16x32_bf16 a[48:51], v[144:147], v[164:167], a[48:51]\n"
"s_add_u32 m0, m0, 1024\n"
"v_mfma_f32_16x16x32_bf16 a[52:55], v[148:151], v[164:167], a[52:55]\n"
"buffer_load_dwordx4 v13, s[36:39], s60 offen lds\n"
"v_mfma_f32_16x16x32_bf16 a[80:83], v[144:147], v[168:171], a[80:83]\n"
"v_mfma_f32_16x16x32_bf16 a[84:87], v[148:151], v[168:171], a[84:87]\n"
"v_mfma_f32_16x16x32_bf16 a[112:115], v[144:147], v[172:175], a[112:115]\n"
"v_mfma_f32_16x16x32_bf16 a[116:119], v[148:151], v[172:175], a[116:119]\n"
"v_mfma_f32_16x16x32_bf16 a[144:147], v[144:147], v[176:179], a[144:147]\n"
"s_add_u32 m0, m0, 1024\n"
"v_mfma_f32_16x16x32_bf16 a[148:151], v[148:151], v[176:179], a[148:151]\n"
"buffer_load_dwordx4 v14, s[36:39], s60 offen lds\n"
"v_mfma_f32_16x16x32_bf16 a[176:179], v[144:147], v[180:183], a[176:179]\n"
"v_mfma_f32_16x16x32_bf16 a[180:183], v[148:151], v[180:183], a[180:183]\n"
"v_mfma_f32_16x16x32_bf16 a[208:211], v[144:147], v[184:187], a[208:211]\n"
"v_mfma_f32_16x16x32_bf16 a[212:215], v[148:151], v[184:187], a[212:215]\n"
"v_mfma_f32_16x16x32_bf16 a[240:243], v[144:147], v[188:191], a[240:243]\n"
"s_add_u32 m0, m0, 1024\n"
"v_mfma_f32_16x16x32_bf16 a[244:247], v[148:151], v[188:191], a[244:247]\n"
"buffer_load_dwordx4 v15, s[36:39], s60 offen lds\n"
"v_mfma_f32_16x16x32_bf16 a[24:27], v[152:155], v[160:163], a[24:27]\n"
"v_mfma_f32_16x16x32_bf16 a[28:31], v[156:159], v[160:163], a[28:31]\n"
"v_mfma_f32_16x16x32_bf16 a[56:59], v[152:155], v[164:167], a[56:59]\n"
"v_mfma_f32_16x16x32_bf16 a[60:63], v[156:159], v[164:167], a[60:63]\n"
"v_mfma_f32_16x16x32_bf16 a[88:91], v[152:155], v[168:171], a[88:91]\n"
"s_add_u32 m0, m0, 1024\n"
"v_mfma_f32_16x16x32_bf16 a[92:95], v[156:159], v[168:171], a[92:95]\n"
"buffer_load_dwordx4 v16, s[36:39], s60 offen lds\n"
"v_mfma_f32_16x16x32_bf16 a[120:123], v[152:155], v[172:175], a[120:123]\n"
"v_mfma_f32_16x16x32_bf16 a[124:127], v[156:159], v[172:175], a[124:127]\n"
"v_mfma_f32_16x16x32_bf16 a[152:155], v[152:155], v[176:179], a[152:155]\n"
"v_mfma_f32_16x16x32_bf16 a[156:159], v[156:159], v[176:179], a[156:159]\n"
"v_mfma_f32_16x16x32_bf16 a[184:187], v[152:155], v[180:183], a[184:187]\n"
"s_add_u32 m0, m0, 1024\n"
"v_mfma_f32_16x16x32_bf16 a[188:191], v[156:159], v[180:183], a[188:191]\n"
"buffer_load_dwordx4 v17, s[36:39], s60 offen lds\n"
"v_mfma_f32_16x16x32_bf16 a[216:219], v[152:155], v[184:187], a[216:219]\n"
"v_mfma_f32_16x16x32_bf16 a[220:223], v[156:159], v[184:187], a[220:223]\n"
"v_mfma_f32_16x16x32_bf16 a[248:251], v[152:155], v[188:191], a[248:251]\n"
"v_mfma_f32_16x16x32_bf16 a[252:255], v[156:159], v[188:191], a[252:255]\n"
"v_mfma_f32_16x16x32_bf16 a[0:3], v[192:195], v[224:227], a[0:3]\n"
"s_add_u32 m0, m0, 1024\n"
"v_mfma_f32_16x16x32_bf16 a[4:7], v[196:199], v[224:227], a[4:7]\n"
"buffer_load_dwordx4 v18, s[36:39], s60 offen lds\n"
"v_mfma_f32_16x16x32_bf16 a[32:35], v[192:195], v[228:231], a[32:35]\n"
"v_mfma_f32_16x16x32_bf16 a[36:39], v[196:199], v[228:231], a[36:39]\n"
"v_mfma_f32_16x16x32_bf16 a[64:67], v[192:195], v[232:235], a[64:67]\n"
"v_mfma_f32_16x16x32_bf16 a[68:71], v[196:199], v[232:235], a[68:71]\n"
"v_mfma_f32_16x16x32_bf16 a[96:99], v[192:195], v[236:239], a[96:99]\n"
"s_add_u32 m0, m0, 1024\n"
"v_mfma_f32_16x16x32_bf16 a[100:103], v[196:199], v[236:239], a[100:103]\n"
"buffer_load_dwordx4 v19, s[36:39], s60 offen lds\n"
"v_mfma_f32_16x16x32_bf16 a[128:131], v[192:195], v[240:243], a[128:131]\n"
"v_mfma_f32_16x16x32_bf16 a[132:135], v[196:199], v[240:243], a[132:135]\n"
"v_mfma_f32_16x16x32_bf16 a[160:163], v[192:195], v[244:247], a[160:163]\n"
"v_mfma_f32_16x16x32_bf16 a[164:167], v[196:199], v[244:247], a[164:167]\n"
"v_mfma_f32_16x16x32_bf16 a[192:195], v[192:195], v[248:251], a[192:195]\n"
"s_mov_b32 m0, s66\n"
"v_mfma_f32_16x16x32_bf16 a[196:199], v[196:199], v[248:251], a[196:199]\n"
"buffer_load_dwordx4 v20, s[40:43], s61 offen lds\n"
"v_mfma_f32_16x16x32_bf16 a[224:227], v[192:195], v[252:255], a[224:227]\n"
"v_mfma_f32_16x16x32_bf16 a[228:231], v[196:199], v[252:255], a[228:231]\n"
"v_mfma_f32_16x16x32_bf16 a[8:11], v[200:203], v[224:227], a[8:11]\n"
"v_mfma_f32_16x16x32_bf16 a[12:15], v[204:207], v[224:227], a[12:15]\n"
"v_mfma_f32_16x16x32_bf16 a[40:43], v[200:203], v[228:231], a[40:43]\n"
"s_add_u32 m0, m0, 1024\n"
"v_mfma_f32_16x16x32_bf16 a[44:47], v[204:207], v[228:231], a[44:47]\n"
"buffer_load_dwordx4 v21, s[40:43], s61 offen lds\n"
"v_mfma_f32_16x16x32_bf16 a[72:75], v[200:203], v[232:235], a[72:75]\n"
"v_mfma_f32_16x16x32_bf16 a[76:79], v[204:207], v[232:235], a[76:79]\n"
"v_mfma_f32_16x16x32_bf16 a[104:107], v[200:203], v[236:239], a[104:107]\n"
"v_mfma_f32_16x16x32_bf16 a[108:111], v[204:207], v[236:239], a[108:111]\n"
"v_mfma_f32_16x16x32_bf16 a[136:139], v[200:203], v[240:243], a[136:139]\n"
"s_add_u32 m0, m0, 1024\n"
"v_mfma_f32_16x16x32_bf16 a[140:143], v[204:207], v[240:243], a[140:143]\n"
"buffer_load_dwordx4 v22, s[40:43], s61 offen lds\n"
"v_mfma_f32_16x16x32_bf16 a[168:171], v[200:203], v[244:247], a[168:171]\n"
"v_mfma_f32_16x16x32_bf16 a[172:175], v[204:207], v[244:247], a[172:175]\n"
"v_mfma_f32_16x16x32_bf16 a[200:203], v[200:203], v[248:251], a[200:203]\n"
"v_mfma_f32_16x16x32_bf16 a[204:207], v[204:207], v[248:251], a[204:207]\n"
"v_mfma_f32_16x16x32_bf16 a[232:235], v[200:203], v[252:255], a[232:235]\n"
"s_add_u32 m0, m0, 1024\n"
"v_mfma_f32_16x16x32_bf16 a[236:239], v[204:207], v[252:255], a[236:239]\n"
"buffer_load_dwordx4 v23, s[40:43], s61 offen lds\n"
"v_mfma_f32_16x16x32_bf16 a[16:19], v[208:211], v[224:227], a[16:19]\n"
"s_waitcnt vmcnt(12)\n"
"s_barrier\n"
"v_mfma_f32_16x16x32_bf16 a[20:23], v[212:215], v[224:227], a[20:23]\n"
"ds_read_b128 v[128:131], v2 offset:32768\n"
"v_mfma_f32_16x16x32_bf16 a[48:51], v[208:211], v[228:231], a[48:51]\n"
"ds_read_b128 v[132:135], v2 offset:34816\n"
"v_mfma_f32_16x16x32_bf16 a[52:55], v[212:215], v[228:231], a[52:55]\n"
"ds_read_b64_tr_b16 v[160:161], v4 offset:32768\n"
"v_mfma_f32_16x16x32_bf16 a[80:83], v[208:211], v[232:235], a[80:83]\n"
"s_add_u32 m0, m0, 1024\n"
"ds_read_b64_tr_b16 v[162:163], v4 offset:34816\n"
"v_mfma_f32_16x16x32_bf16 a[84:87], v[212:215], v[232:235], a[84:87]\n"
"buffer_load_dwordx4 v24, s[40:43], s61 offen lds\n"
"ds_read_b64_tr_b16 v[164:165], v5 offset:32768\n"
"v_mfma_f32_16x16x32_bf16 a[112:115], v[208:211], v[236:239], a[112:115]\n"
"ds_read_b64_tr_b16 v[166:167], v5 offset:34816\n"
"v_mfma_f32_16x16x32_bf16 a[116:119], v[212:215], v[236:239], a[116:119]\n"
"ds_read_b64_tr_b16 v[168:169], v6 offset:32768\n"
"v_mfma_f32_16x16x32_bf16 a[144:147], v[208:211], v[240:243], a[144:147]\n"
"ds_read_b64_tr_b16 v[170:171], v6 offset:34816\n"
"v_mfma_f32_16x16x32_bf16 a[148:151], v[212:215], v[240:243], a[148:151]\n"
"ds_read_b64_tr_b16 v[172:173], v7 offset:32768\n"
"v_mfma_f32_16x16x32_bf16 a[176:179], v[208:211], v[244:247], a[176:179]\n"
"s_add_u32 m0, m0, 1024\n"
"ds_read_b64_tr_b16 v[174:175], v7 offset:34816\n"
"v_mfma_f32_16x16x32_bf16 a[180:183], v[212:215], v[244:247], a[180:183]\n"
"buffer_load_dwordx4 v25, s[40:43], s61 offen lds\n"
"ds_read_b64_tr_b16 v[176:177], v8 offset:32768\n"
"v_mfma_f32_16x16x32_bf16 a[208:211], v[208:211], v[248:251], a[208:211]\n"
"ds_read_b64_tr_b16 v[178:179], v8 offset:34816\n"
"v_mfma_f32_16x16x32_bf16 a[212:215], v[212:215], v[248:251], a[212:215]\n"
"ds_read_b64_tr_b16 v[180:181], v9 offset:32768\n"
"v_mfma_f32_16x16x32_bf16 a[240:243], v[208:211], v[252:255], a[240:243]\n"
"ds_read_b64_tr_b16 v[182:183], v9 offset:34816\n"
"v_mfma_f32_16x16x32_bf16 a[244:247], v[212:215], v[252:255], a[244:247]\n"
"ds_read_b64_tr_b16 v[184:185], v10 offset:32768\n"
"v_mfma_f32_16x16x32_bf16 a[24:27], v[216:219], v[224:227], a[24:27]\n"
"s_add_u32 m0, m0, 1024\n"
"ds_read_b64_tr_b16 v[186:187], v10 offset:34816\n"
"v_mfma_f32_16x16x32_bf16 a[28:31], v[220:223], v[224:227], a[28:31]\n"
"buffer_load_dwordx4 v26, s[40:43], s61 offen lds\n"
"ds_read_b64_tr_b16 v[188:189], v11 offset:32768\n"
"v_mfma_f32_16x16x32_bf16 a[56:59], v[216:219], v[228:231], a[56:59]\n"
"ds_read_b64_tr_b16 v[190:191], v11 offset:34816\n"
"v_mfma_f32_16x16x32_bf16 a[60:63], v[220:223], v[228:231], a[60:63]\n"
"ds_read_b128 v[136:139], v2 offset:36864\n"
"v_mfma_f32_16x16x32_bf16 a[88:91], v[216:219], v[232:235], a[88:91]\n"
"ds_read_b128 v[140:143], v2 offset:38912\n"
"v_mfma_f32_16x16x32_bf16 a[92:95], v[220:223], v[232:235], a[92:95]\n"
"ds_read_b128 v[144:147], v2 offset:40960\n"
"v_mfma_f32_16x16x32_bf16 a[120:123], v[216:219], v[236:239], a[120:123]\n"
"s_add_u32 m0, m0, 1024\n"
"ds_read_b128 v[148:151], v2 offset:43008\n"
"v_mfma_f32_16x16x32_bf16 a[124:127], v[220:223], v[236:239], a[124:127]\n"
"buffer_load_dwordx4 v27, s[40:43], s61 offen lds\n"
"s_add_u32 s60, s60, 128\n"
"s_add_u32 s61, s61, 524288\n"
"ds_read_b128 v[152:155], v2 offset:45056\n"
"v_mfma_f32_16x16x32_bf16 a[152:155], v[216:219], v[240:243], a[152:155]\n"
"ds_read_b128 v[156:159], v2 offset:47104\n"
"v_mfma_f32_16x16x32_bf16 a[156:159], v[220:223], v[240:243], a[156:159]\n"
"v_mfma_f32_16x16x32_bf16 a[184:187], v[216:219], v[244:247], a[184:187]\n"
"v_mfma_f32_16x16x32_bf16 a[188:191], v[220:223], v[244:247], a[188:191]\n"
"v_mfma_f32_16x16x32_bf16 a[216:219], v[216:219], v[248:251], a[216:219]\n"
"v_mfma_f32_16x16x32_bf16 a[220:223], v[220:223], v[248:251], a[220:223]\n"
"v_mfma_f32_16x16x32_bf16 a[248:251], v[216:219], v[252:255], a[248:251]\n"
"s_sub_u32 s62, s62, 1\n"
"s_cmp_eq_u32 s62, 0\n"
"v_mfma_f32_16x16x32_bf16 a[252:255], v[220:223], v[252:255], a[252:255]\n"
"s_waitcnt lgkmcnt(0)\n"
"s_cbranch_scc1 L_epi%=\n"
"L_mid%=:\n"
"s_cmp_lg_u32 s62, 2\n"
"s_cbranch_scc1 L_ns1%=\n"
"s_mov_b64 s[36:37], s[48:49]\n"
"s_mov_b64 s[38:39], s[50:51]\n"
"s_mov_b64 s[40:41], s[52:53]\n"
"s_mov_b64 s[42:43], s[54:55]\n"
"s_mov_b32 s50, 0\n"
"s_mov_b32 s54, 0\n"
"s_mov_b32 s60, 0\n"
"s_mov_b32 s61, 0\n"
"L_ns1%=:\n"
"v_mfma_f32_16x16x32_bf16 a[0:3], v[128:131], v[160:163], a[0:3]\n"
"ds_read_b128 v[192:195], v3 offset:32768\n"
"v_mfma_f32_16x16x32_bf16 a[4:7], v[132:135], v[160:163], a[4:7]\n"
"ds_read_b128 v[196:199], v3 offset:34816\n"
"v_mfma_f32_16x16x32_bf16 a[32:35], v[128:131], v[164:167], a[32:35]\n"
"ds_read_b64_tr_b16 v[224:225], v4 offset:49152\n"
"v_mfma_f32_16x16x32_bf16 a[36:39], v[132:135], v[164:167], a[36:39]\n"
"ds_read_b64_tr_b16 v[226:227], v4 offset:51200\n"
"v_mfma_f32_16x16x32_bf16 a[64:67], v[128:131], v[168:171], a[64:67]\n"
"ds_read_b64_tr_b16 v[228:229], v5 offset:49152\n"
"v_mfma_f32_16x16x32_bf16 a[68:71], v[132:135], v[168:171], a[68:71]\n"
"ds_read_b64_tr_b16 v[230:231], v5 offset:51200\n"
"v_mfma_f32_16x16x32_bf16 a[96:99], v[128:131], v[172:175], a[96:99]\n"
"ds_read_b64_tr_b16 v[232:233], v6 offset:49152\n"
"v_mfma_f32_16x16x32_bf16 a[100:103], v[132:135], v[172:175], a[100:103]\n"
"ds_read_b64_tr_b16 v[234:235], v6 offset:51200\n"
"v_mfma_f32_16x16x32_bf16 a[128:131], v[128:131], v[176:179], a[128:131]\n"
"ds_read_b64_tr_b16 v[236:237], v7 offset:49152\n"
"v_mfma_f32_16x16x32_bf16 a[132:135], v[132:135], v[176:179], a[132:135]\n"
"ds_read_b64_tr_b16 v[238:239], v7 offset:51200\n"
"v_mfma_f32_16x16x32_bf16 a[160:163], v[128:131], v[180:183], a[160:163]\n"
"ds_read_b64_tr_b16 v[240:241], v8 offset:49152\n"
"v_mfma_f32_16x16x32_bf16 a[164:167], v[132:135], v[180:183], a[164:167]\n"
"ds_read_b64_tr_b16 v[242:243], v8 offset:51200\n"
"v_mfma_f32_16x16x32_bf16 a[192:195], v[128:131], v[184:187], a[192:195]\n"
"ds_read_b64_tr_b16 v[244:245], v9 offset:49152\n"
"v_mfma_f32_16x16x32_bf16 a[196:199], v[132:135], v[184:187], a[196:199]\n"
"ds_read_b64_tr_b16 v[246:247], v9 offset:51200\n"
"v_mfma_f32_16x16x32_bf16 a[224:227], v[128:131], v[188:191], a[224:227]\n"
"ds_read_b64_tr_b16 v[248:249], v10 offset:49152\n"
"v_mfma_f32_16x16x32_bf16 a[228:231], v[132:135], v[188:191], a[228:231]\n"
"ds_read_b64_tr_b16 v[250:251], v10 offset:51200\n"
"v_mfma_f32_16x16x32_bf16 a[8:11], v[136:139], v[160:163], a[8:11]\n"
"ds_read_b64_tr_b16 v[252:253], v11 offset:49152\n"
"v_mfma_f32_16x16x32_bf16 a[12:15], v[140:143], v[160:163], a[12:15]\n"
"ds_read_b64_tr_b16 v[254:255], v11 offset:51200\n"
"v_mfma_f32_16x16x32_bf16 a[40:43], v[136:139], v[164:167], a[40:43]\n"
"ds_read_b128 v[200:203], v3 offset:36864\n"
"v_mfma_f32_16x16x32_bf16 a[44:47], v[140:143], v[164:167], a[44:47]\n"
"ds_read_b128 v[204:207], v3 offset:38912\n"
"v_mfma_f32_16x16x32_bf16 a[72:75], v[136:139], v[168:171], a[72:75]\n"
"ds_read_b128 v[208:211], v3 offset:40960\n"
"v_mfma_f32_16x16x32_bf16 a[76:79], v[140:143], v[168:171], a[76:79]\n"
"ds_read_b128 v[212:215], v3 offset:43008\n"
"v_mfma_f32_16x16x32_bf16 a[104:107], v[136:139], v[172:175], a[104:107]\n"
"ds_read_b128 v[216:219], v3 offset:45056\n"
"v_mfma_f32_16x16x32_bf16 a[108:111], v[140:143], v[172:175], a[108:111]\n"
"ds_read_b128 v[220:223], v3 offset:47104\n"
"v_mfma_f32_16x16x32_bf16 a[136:139], v[136:139], v[176:179], a[136:139]\n"
"v_mfma_f32_16x16x32_bf16 a[140:143], v[140:143], v[176:179], a[140:143]\n"
"v_mfma_f32_16x16x32_bf16 a[168:171], v[136:139], v[180:183], a[168:171]\n"
"v_mfma_f32_16x16x32_bf16 a[172:175], v[140:143], v[180:183], a[172:175]\n"
"s_waitcnt lgkmcnt(0)\n"
"s_barrier\n"
"v_mfma_f32_16x16x32_bf16 a[200:203], v[136:139], v[184:187], a[200:203]\n"
"s_mov_b32 m0, s65\n"
"v_mfma_f32_16x16x32_bf16 a[204:207], v[140:143], v[184:187], a[204:207]\n"
"buffer_load_dwordx4 v12, s[36:39], s60 offen lds\n"
"v_mfma_f32_16x16x32_bf16 a[232:235], v[136:139], v[188:191], a[232:235]\n"
"v_mfma_f32_16x16x32_bf16 a[236:239], v[140:143], v[188:191], a[236:239]\n"
"v_mfma_f32_16x16x32_bf16 a[16:19], v[144:147], v[160:163], a[16:19]\n"
"v_mfma_f32_16x16x32_bf16 a[20:23], v[148:151], v[160:163], a[20:23]\n"
"v_mfma_f32_16x16x32_bf16 a[48:51], v[144:147], v[164:167], a[48:51]\n"
"s_add_u32 m0, m0, 1024\n"
"v_mfma_f32_16x16x32_bf16 a[52:55], v[148:151], v[164:167], a[52:55]\n"
"buffer_load_dwordx4 v13, s[36:39], s60 offen lds\n"
"v_mfma_f32_16x16x32_bf16 a[80:83], v[144:147], v[168:171], a[80:83]\n"
"v_mfma_f32_16x16x32_bf16 a[84:87], v[148:151], v[168:171], a[84:87]\n"
"v_mfma_f32_16x16x32_bf16 a[112:115], v[144:147], v[172:175], a[112:115]\n"
"v_mfma_f32_16x16x32_bf16 a[116:119], v[148:151], v[172:175], a[116:119]\n"
"v_mfma_f32_16x16x32_bf16 a[144:147], v[144:147], v[176:179], a[144:147]\n"
"s_add_u32 m0, m0, 1024\n"
"v_mfma_f32_16x16x32_bf16 a[148:151], v[148:151], v[176:179], a[148:151]\n"
"buffer_load_dwordx4 v14, s[36:39], s60 offen lds\n"
"v_mfma_f32_16x16x32_bf16 a[176:179], v[144:147], v[180:183], a[176:179]\n"
"v_mfma_f32_16x16x32_bf16 a[180:183], v[148:151], v[180:183], a[180:183]\n"
"v_mfma_f32_16x16x32_bf16 a[208:211], v[144:147], v[184:187], a[208:211]\n"
"v_mfma_f32_16x16x32_bf16 a[212:215], v[148:151], v[184:187], a[212:215]\n"
"v_mfma_f32_16x16x32_bf16 a[240:243], v[144:147], v[188:191], a[240:243]\n"
"s_add_u32 m0, m0, 1024\n"
"v_mfma_f32_16x16x32_bf16 a[244:247], v[148:151], v[188:191], a[244:247]\n"
"buffer_load_dwordx4 v15, s[36:39], s60 offen lds\n"
"v_mfma_f32_16x16x32_bf16 a[24:27], v[152:155], v[160:163], a[24:27]\n"
"v_mfma_f32_16x16x32_bf16 a[28:31], v[156:159], v[160:163], a[28:31]\n"
"v_mfma_f32_16x16x32_bf16 a[56:59], v[152:155], v[164:167], a[56:59]\n"
"v_mfma_f32_16x16x32_bf16 a[60:63], v[156:159], v[164:167], a[60:63]\n"
"v_mfma_f32_16x16x32_bf16 a[88:91], v[152:155], v[168:171], a[88:91]\n"
"s_add_u32 m0, m0, 1024\n"
"v_mfma_f32_16x16x32_bf16 a[92:95], v[156:159], v[168:171], a[92:95]\n"
"buffer_load_dwordx4 v16, s[36:39], s60 offen lds\n"
"v_mfma_f32_16x16x32_bf16 a[120:123], v[152:155], v[172:175], a[120:123]\n"
"v_mfma_f32_16x16x32_bf16 a[124:127], v[156:159], v[172:175], a[124:127]\n"
"v_mfma_f32_16x16x32_bf16 a[152:155], v[152:155], v[176:179], a[152:155]\n"
"v_mfma_f32_16x16x32_bf16 a[156:159], v[156:159], v[176:179], a[156:159]\n"
"v_mfma_f32_16x16x32_bf16 a[184:187], v[152:155], v[180:183], a[184:187]\n"
"s_add_u32 m0, m0, 1024\n"
"v_mfma_f32_16x16x32_bf16 a[188:191], v[156:159], v[180:183], a[188:191]\n"
"buffer_load_dwordx4 v17, s[36:39], s60 offen lds\n"
"v_mfma_f32_16x16x32_bf16 a[216:219], v[152:155], v[184:187], a[216:219]\n"
"v_mfma_f32_16x16x32_bf16 a[220:223], v[156:159], v[184:187], a[220:223]\n"
"v_mfma_f32_16x16x32_bf16 a[248:251], v[152:155], v[188:191], a[248:251]\n"
"v_mfma_f32_16x16x32_bf16 a[252:255], v[156:159], v[188:191], a[252:255]\n"
"v_mfma_f32_16x16x32_bf16 a[0:3], v[192:195], v[224:227], a[0:3]\n"
"s_add_u32 m0, m0, 1024\n"
"v_mfma_f32_16x16x32_bf16 a[4:7], v[196:199], v[224:227], a[4:7]\n"
"buffer_load_dwordx4 v18, s[36:39], s60 offen lds\n"
"v_mfma_f32_16x16x32_bf16 a[32:35], v[192:195], v[228:231], a[32:35]\n"
"v_mfma_f32_16x16x32_bf16 a[36:39], v[196:199], v[228:231], a[36:39]\n"
"v_mfma_f32_16x16x32_bf16 a[64:67], v[192:195], v[232:235], a[64:67]\n"
"v_mfma_f32_16x16x32_bf16 a[68:71], v[196:199], v[232:235], a[68:71]\n"
"v_mfma_f32_16x16x32_bf16 a[96:99], v[192:195], v[236:239], a[96:99]\n"
"s_add_u32 m0, m0, 1024\n"
"v_mfma_f32_16x16x32_bf16 a[100:103], v[196:199], v[236:239], a[100:103]\n"
"buffer_load_dwordx4 v19, s[36:39], s60 offen lds\n"
"v_mfma_f32_16x16x32_bf16 a[128:131], v[192:195], v[240:243], a[128:131]\n"
"v_mfma_f32_16x16x32_bf16 a[132:135], v[196:199], v[240:243], a[132:135]\n"
"v_mfma_f32_16x16x32_bf16 a[160:163], v[192:195], v[244:247], a[160:163]\n"
"v_mfma_f32_16x16x32_bf16 a[164:167], v[196:199], v[244:247], a[164:167]\n"
"v_mfma_f32_16x16x32_bf16 a[192:195], v[192:195], v[248:251], a[192:195]\n"
"s_mov_b32 m0, s67\n"
"v_mfma_f32_16x16x32_bf16 a[196:199], v[196:199], v[248:251], a[196:199]\n"
"buffer_load_dwordx4 v20, s[40:43], s61 offen lds\n"
"v_mfma_f32_16x16x32_bf16 a[224:227], v[192:195], v[252:255], a[224:227]\n"
"v_mfma_f32_16x16x32_bf16 a[228:231], v[196:199], v[252:255], a[228:231]\n"
"v_mfma_f32_16x16x32_bf16 a[8:11], v[200:203], v[224:227], a[8:11]\n"
"v_mfma_f32_16x16x32_bf16 a[12:15], v[204:207], v[224:227], a[12:15]\n"
"v_mfma_f32_16x16x32_bf16 a[40:43], v[200:203], v[228:231], a[40:43]\n"
"s_add_u32 m0, m0, 1024\n"
"v_mfma_f32_16x16x32_bf16 a[44:47], v[204:207], v[228:231], a[44:47]\n"
"buffer_load_dwordx4 v21, s[40:43], s61 offen lds\n"
"v_mfma_f32_16x16x32_bf16 a[72:75], v[200:203], v[232:235], a[72:75]\n"
"v_mfma_f32_16x16x32_bf16 a[76:79], v[204:207], v[232:235], a[76:79]\n"
"v_mfma_f32_16x16x32_bf16 a[104:107], v[200:203], v[236:239], a[104:107]\n"
"v_mfma_f32_16x16x32_bf16 a[108:111], v[204:207], v[236:239], a[108:111]\n"
"v_mfma_f32_16x16x32_bf16 a[136:139], v[200:203], v[240:243], a[136:139]\n"
"s_add_u32 m0, m0, 1024\n"
"v_mfma_f32_16x16x32_bf16 a[140:143], v[204:207], v[240:243], a[140:143]\n"
"buffer_load_dwordx4 v22, s[40:43], s61 offen lds\n"
"v_mfma_f32_16x16x32_bf16 a[168:171], v[200:203], v[244:247], a[168:171]\n"
"v_mfma_f32_16x16x32_bf16 a[172:175], v[204:207], v[244:247], a[172:175]\n"
"v_mfma_f32_16x16x32_bf16 a[200:203], v[200:203], v[248:251], a[200:203]\n"
"v_mfma_f32_16x16x32_bf16 a[204:207], v[204:207], v[248:251], a[204:207]\n"
"v_mfma_f32_16x16x32_bf16 a[232:235], v[200:203], v[252:255], a[232:235]\n"
"s_add_u32 m0, m0, 1024\n"
"v_mfma_f32_16x16x32_bf16 a[236:239], v[204:207], v[252:255], a[236:239]\n"
"buffer_load_dwordx4 v23, s[40:43], s61 offen lds\n"
"v_mfma_f32_16x16x32_bf16 a[16:19], v[208:211], v[224:227], a[16:19]\n"
"s_waitcnt vmcnt(12)\n"
"s_barrier\n"
"v_mfma_f32_16x16x32_bf16 a[20:23], v[212:215], v[224:227], a[20:23]\n"
"ds_read_b128 v[128:131], v2 offset:0\n"
"v_mfma_f32_16x16x32_bf16 a[48:51], v[208:211], v[228:231], a[48:51]\n"
"ds_read_b128 v[132:135], v2 offset:2048\n"
"v_mfma_f32_16x16x32_bf16 a[52:55], v[212:215], v[228:231], a[52:55]\n"
"ds_read_b64_tr_b16 v[160:161], v4 offset:0\n"
"v_mfma_f32_16x16x32_bf16 a[80:83], v[208:211], v[232:235], a[80:83]\n"
"s_add_u32 m0, m0, 1024\n"
"ds_read_b64_tr_b16 v[162:163], v4 offset:2048\n"
"v_mfma_f32_16x16x32_bf16 a[84:87], v[212:215], v[232:235], a[84:87]\n"
"buffer_load_dwordx4 v24, s[40:43], s61 offen lds\n"
"ds_read_b64_tr_b16 v[164:165], v5 offset:0\n"
"v_mfma_f32_16x16x32_bf16 a[112:115], v[208:211], v[236:239], a[112:115]\n"
"ds_read_b64_tr_b16 v[166:167], v5 offset:2048\n"
"v_mfma_f32_16x16x32_bf16 a[116:119], v[212:215], v[236:239], a[116:119]\n"
"ds_read_b64_tr_b16 v[168:169], v6 offset:0\n"
"v_mfma_f32_16x16x32_bf16 a[144:147], v[208:211], v[240:243], a[144:147]\n"
"ds_read_b64_tr_b16 v[170:171], v6 offset:2048\n"
"v_mfma_f32_16x16x32_bf16 a[148:151], v[212:215], v[240:243], a[148:151]\n"
"ds_read_b64_tr_b16 v[172:173], v7 offset:0\n"
"v_mfma_f32_16x16x32_bf16 a[176:179], v[208:211], v[244:247], a[176:179]\n"
"s_add_u32 m0, m0, 1024\n"
"ds_read_b64_tr_b16 v[174:175], v7 offset:2048\n"
"v_mfma_f32_16x16x32_bf16 a[180:183], v[212:215], v[244:247], a[180:183]\n"
"buffer_load_dwordx4 v25, s[40:43], s61 offen lds\n"
"ds_read_b64_tr_b16 v[176:177], v8 offset:0\n"
"v_mfma_f32_16x16x32_bf16 a[208:211], v[208:211], v[248:251], a[208:211]\n"
"ds_read_b64_tr_b16 v[178:179], v8 offset:2048\n"
"v_mfma_f32_16x16x32_bf16 a[212:215], v[212:215], v[248:251], a[212:215]\n"
"ds_read_b64_tr_b16 v[180:181], v9 offset:0\n"
"v_mfma_f32_16x16x32_bf16 a[240:243], v[208:211], v[252:255], a[240:243]\n"
"ds_read_b64_tr_b16 v[182:183], v9 offset:2048\n"
"v_mfma_f32_16x16x32_bf16 a[244:247], v[212:215], v[252:255], a[244:247]\n"
"ds_read_b64_tr_b16 v[184:185], v10 offset:0\n"
"v_mfma_f32_16x16x32_bf16 a[24:27], v[216:219], v[224:227], a[24:27]\n"
"s_add_u32 m0, m0, 1024\n"
"ds_read_b64_tr_b16 v[186:187], v10 offset:2048\n"
"v_mfma_f32_16x16x32_bf16 a[28:31], v[220:223], v[224:227], a[28:31]\n"
"buffer_load_dwordx4 v26, s[40:43], s61 offen lds\n"
"ds_read_b64_tr_b16 v[188:189], v11 offset:0\n"
"v_mfma_f32_16x16x32_bf16 a[56:59], v[216:219], v[228:231], a[56:59]\n"
"ds_read_b64_tr_b16 v[190:191], v11 offset:2048\n"
"v_mfma_f32_16x16x32_bf16 a[60:63], v[220:223], v[228:231], a[60:63]\n"
"ds_read_b128 v[136:139], v2 offset:4096\n"
"v_mfma_f32_16x16x32_bf16 a[88:91], v[216:219], v[232:235], a[88:91]\n"
"ds_read_b128 v[140:143], v2 offset:6144\n"
"v_mfma_f32_16x16x32_bf16 a[92:95], v[220:223], v[232:235], a[92:95]\n"
"ds_read_b128 v[144:147], v2 offset:8192\n"
"v_mfma_f32_16x16x32_bf16 a[120:123], v[216:219], v[236:239], a[120:123]\n"
"s_add_u32 m0, m0, 1024\n"
"ds_read_b128 v[148:151], v2 offset:10240\n"
"v_mfma_f32_16x16x32_bf16 a[124:127], v[220:223], v[236:239], a[124:127]\n"
"buffer_load_dwordx4 v27, s[40:43], s61 offen lds\n"
"s_add_u32 s60, s60, 128\n"
"s_add_u32 s61, s61, 524288\n"
"ds_read_b128 v[152:155], v2 offset:12288\n"
"v_mfma_f32_16x16x32_bf16 a[152:155], v[216:219], v[240:243], a[152:155]\n"
"ds_read_b128 v[156:159], v2 offset:14336\n"
"v_mfma_f32_16x16x32_bf16 a[156:159], v[220:223], v[240:243], a[156:159]\n"
"v_mfma_f32_16x16x32_bf16 a[184:187], v[216:219], v[244:247], a[184:187]\n"
"v_mfma_f32_16x16x32_bf16 a[188:191], v[220:223], v[244:247], a[188:191]\n"
"v_mfma_f32_16x16x32_bf16 a[216:219], v[216:219], v[248:251], a[216:219]\n"
"v_mfma_f32_16x16x32_bf16 a[220:223], v[220:223], v[248:251], a[220:223]\n"
"v_mfma_f32_16x16x32_bf16 a[248:251], v[216:219], v[252:255], a[248:251]\n"
"s_sub_u32 s62, s62, 1\n"
"s_cmp_eq_u32 s62, 0\n"
"v_mfma_f32_16x16x32_bf16 a[252:255], v[220:223], v[252:255], a[252:255]\n"
"s_waitcnt lgkmcnt(0)\n"
"s_cbranch_scc0 L_top%=\n"
"L_epi%=:\n"
"s_nop 7\n"
"s_lshr_b32 s68, s76, 1\n"
"s_mul_i32 s68, s68, 131072\n"
"s_and_b32 s69, s76, 1\n"
"s_lshl_b32 s69, s69, 13\n"
"s_add_u32 s68, s68, s69\n"
"s_add_u32 s69, s68, 32768\n"
"v_accvgpr_read_b32 v40, a0\n"
"v_accvgpr_read_b32 v41, a1\n"
"v_accvgpr_read_b32 v42, a2\n"
"v_accvgpr_read_b32 v43, a3\n"
"v_accvgpr_read_b32 v44, a4\n"
"v_accvgpr_read_b32 v45, a5\n"
"v_accvgpr_read_b32 v46, a6\n"
"v_accvgpr_read_b32 v47, a7\n"
"v_cvt_pk_bf16_f32 v40, v40, v41\n"
"v_cvt_pk_bf16_f32 v41, v42, v43\n"
"v_cvt_pk_bf16_f32 v42, v44, v45\n"
"v_cvt_pk_bf16_f32 v43, v46, v47\n"
"buffer_store_dwordx4 v[40:43], v36, s[44:47], s69 offen offset:0\n"
"s_add_u32 s69, s68, 49152\n"
"v_accvgpr_read_b32 v48, a8\n"
"v_accvgpr_read_b32 v49, a9\n"
"v_accvgpr_read_b32 v50, a10\n"
"v_accvgpr_read_b32 v51, a11\n"
"v_accvgpr_read_b32 v52, a12\n"
"v_accvgpr_read_b32 v53, a13\n"
"v_accvgpr_read_b32 v54, a14\n"
"v_accvgpr_read_b32 v55, a15\n"
"v_cvt_pk_bf16_f32 v48, v48, v49\n"
"v_cvt_pk_bf16_f32 v49, v50, v51\n"
"v_cvt_pk_bf16_f32 v50, v52, v53\n"
"v_cvt_pk_bf16_f32 v51, v54, v55\n"
"buffer_store_dwordx4 v[48:51], v36, s[44:47], s69 offen offset:0\n"
"s_add_u32 s69, s68, 98304\n"
"v_accvgpr_read_b32 v56, a16\n"
"v_accvgpr_read_b32 v57, a17\n"
"v_accvgpr_read_b32 v58, a18\n"
"v_accvgpr_read_b32 v59, a19\n"
"v_accvgpr_read_b32 v60, a20\n"
"v_accvgpr_read_b32 v61, a21\n"
"v_accvgpr_read_b32 v62, a22\n"
"v_accvgpr_read_b32 v63, a23\n"
"v_cvt_pk_bf16_f32 v56, v56, v57\n"
"v_cvt_pk_bf16_f32 v57, v58, v59\n"
"v_cvt_pk_bf16_f32 v58, v60, v61\n"
"v_cvt_pk_bf16_f32 v59, v62, v63\n"
"buffer_store_dwordx4 v[56:59], v36, s[44:47], s69 offen offset:0\n"
"s_add_u32 s69, s68, 114688\n"
"v_accvgpr_read_b32 v64, a24\n"
"v_accvgpr_read_b32 v65, a25\n"
"v_accvgpr_read_b32 v66, a26\n"
"v_accvgpr_read_b32 v67, a27\n"
"v_accvgpr_read_b32 v68, a28\n"
"v_accvgpr_read_b32 v69, a29\n"
"v_accvgpr_read_b32 v70, a30\n"
"v_accvgpr_read_b32 v71, a31\n"
"v_cvt_pk_bf16_f32 v64, v64, v65\n"
"v_cvt_pk_bf16_f32 v65, v66, v67\n"
"v_cvt_pk_bf16_f32 v66, v68, v69\n"
"v_cvt_pk_bf16_f32 v67, v70, v71\n"
"buffer_store_dwordx4 v[64:67], v36, s[44:47], s69 offen offset:0\n"
"s_add_u32 s69, s68, 33792\n"
"v_accvgpr_read_b32 v40, a32\n"
"v_accvgpr_read_b32 v41, a33\n"
"v_accvgpr_read_b32 v42, a34\n"
"v_accvgpr_read_b32 v43, a35\n"
"v_accvgpr_read_b32 v44, a36\n"
"v_accvgpr_read_b32 v45, a37\n"
"v_accvgpr_read_b32 v46, a38\n"
"v_accvgpr_read_b32 v47, a39\n"
"v_cvt_pk_bf16_f32 v40, v40, v41\n"
"v_cvt_pk_bf16_f32 v41, v42, v43\n"
"v_cvt_pk_bf16_f32 v42, v44, v45\n"
"v_cvt_pk_bf16_f32 v43, v46, v47\n"
"buffer_store_dwordx4 v[40:43], v36, s[44:47], s69 offen offset:0\n"
"s_add_u32 s69, s68, 50176\n"
"v_accvgpr_read_b32 v48, a40\n"
"v_accvgpr_read_b32 v49, a41\n"
"v_accvgpr_read_b32 v50, a42\n"
"v_accvgpr_read_b32 v51, a43\n"
"v_accvgpr_read_b32 v52, a44\n"
"v_accvgpr_read_b32 v53, a45\n"
"v_accvgpr_read_b32 v54, a46\n"
"v_accvgpr_read_b32 v55, a47\n"
"v_cvt_pk_bf16_f32 v48, v48, v49\n"
"v_cvt_pk_bf16_f32 v49, v50, v51\n"
"v_cvt_pk_bf16_f32 v50, v52, v53\n"
"v_cvt_pk_bf16_f32 v51, v54, v55\n"
"buffer_store_dwordx4 v[48:51], v36, s[44:47], s69 offen offset:0\n"
"s_add_u32 s69, s68, 99328\n"
"v_accvgpr_read_b32 v56, a48\n"
"v_accvgpr_read_b32 v57, a49\n"
"v_accvgpr_read_b32 v58, a50\n"
"v_accvgpr_read_b32 v59, a51\n"
"v_accvgpr_read_b32 v60, a52\n"
"v_accvgpr_read_b32 v61, a53\n"
"v_accvgpr_read_b32 v62, a54\n"
"v_accvgpr_read_b32 v63, a55\n"
"v_cvt_pk_bf16_f32 v56, v56, v57\n"
"v_cvt_pk_bf16_f32 v57, v58, v59\n"
"v_cvt_pk_bf16_f32 v58, v60, v61\n"
"v_cvt_pk_bf16_f32 v59, v62, v63\n"
"buffer_store_dwordx4 v[56:59], v36, s[44:47], s69 offen offset:0\n"
"s_add_u32 s69, s68, 115712\n"
"v_accvgpr_read_b32 v64, a56\n"
"v_accvgpr_read_b32 v65, a57\n"
"v_accvgpr_read_b32 v66, a58\n"
"v_accvgpr_read_b32 v67, a59\n"
"v_accvgpr_read_b32 v68, a60\n"
"v_accvgpr_read_b32 v69, a61\n"
"v_accvgpr_read_b32 v70, a62\n"
"v_accvgpr_read_b32 v71, a63\n"
"v_cvt_pk_bf16_f32 v64, v64, v65\n"
"v_cvt_pk_bf16_f32 v65, v66, v67\n"
"v_cvt_pk_bf16_f32 v66, v68, v69\n"
"v_cvt_pk_bf16_f32 v67, v70, v71\n"
"buffer_store_dwordx4 v[64:67], v36, s[44:47], s69 offen offset:0\n"
"s_add_u32 s69, s68, 34816\n"
"v_accvgpr_read_b32 v40, a64\n"
"v_accvgpr_read_b32 v41, a65\n"
"v_accvgpr_read_b32 v42, a66\n"
"v_accvgpr_read_b32 v43, a67\n"
"v_accvgpr_read_b32 v44, a68\n"
"v_accvgpr_read_b32 v45, a69\n"
"v_accvgpr_read_b32 v46, a70\n"
"v_accvgpr_read_b32 v47, a71\n"
"v_cvt_pk_bf16_f32 v40, v40, v41\n"
"v_cvt_pk_bf16_f32 v41, v42, v43\n"
"v_cvt_pk_bf16_f32 v42, v44, v45\n"
"v_cvt_pk_bf16_f32 v43, v46, v47\n"
"buffer_store_dwordx4 v[40:43], v36, s[44:47], s69 offen offset:0\n"
"s_add_u32 s69, s68, 51200\n"
"v_accvgpr_read_b32 v48, a72\n"
"v_accvgpr_read_b32 v49, a73\n"
"v_accvgpr_read_b32 v50, a74\n"
"v_accvgpr_read_b32 v51, a75\n"
"v_accvgpr_read_b32 v52, a76\n"
"v_accvgpr_read_b32 v53, a77\n"
"v_accvgpr_read_b32 v54, a78\n"
"v_accvgpr_read_b32 v55, a79\n"
"v_cvt_pk_bf16_f32 v48, v48, v49\n"
"v_cvt_pk_bf16_f32 v49, v50, v51\n"
"v_cvt_pk_bf16_f32 v50, v52, v53\n"
"v_cvt_pk_bf16_f32 v51, v54, v55\n"
"buffer_store_dwordx4 v[48:51], v36, s[44:47], s69 offen offset:0\n"
"s_add_u32 s69, s68, 100352\n"
"v_accvgpr_read_b32 v56, a80\n"
"v_accvgpr_read_b32 v57, a81\n"
"v_accvgpr_read_b32 v58, a82\n"
"v_accvgpr_read_b32 v59, a83\n"
"v_accvgpr_read_b32 v60, a84\n"
"v_accvgpr_read_b32 v61, a85\n"
"v_accvgpr_read_b32 v62, a86\n"
"v_accvgpr_read_b32 v63, a87\n"
"v_cvt_pk_bf16_f32 v56, v56, v57\n"
"v_cvt_pk_bf16_f32 v57, v58, v59\n"
"v_cvt_pk_bf16_f32 v58, v60, v61\n"
"v_cvt_pk_bf16_f32 v59, v62, v63\n"
"buffer_store_dwordx4 v[56:59], v36, s[44:47], s69 offen offset:0\n"
"s_add_u32 s69, s68, 116736\n"
"v_accvgpr_read_b32 v64, a88\n"
"v_accvgpr_read_b32 v65, a89\n"
"v_accvgpr_read_b32 v66, a90\n"
"v_accvgpr_read_b32 v67, a91\n"
"v_accvgpr_read_b32 v68, a92\n"
"v_accvgpr_read_b32 v69, a93\n"
"v_accvgpr_read_b32 v70, a94\n"
"v_accvgpr_read_b32 v71, a95\n"
"v_cvt_pk_bf16_f32 v64, v64, v65\n"
"v_cvt_pk_bf16_f32 v65, v66, v67\n"
"v_cvt_pk_bf16_f32 v66, v68, v69\n"
"v_cvt_pk_bf16_f32 v67, v70, v71\n"
"buffer_store_dwordx4 v[64:67], v36, s[44:47], s69 offen offset:0\n"
"s_add_u32 s69, s68, 35840\n"
"v_accvgpr_read_b32 v40, a96\n"
"v_accvgpr_read_b32 v41, a97\n"
"v_accvgpr_read_b32 v42, a98\n"
"v_accvgpr_read_b32 v43, a99\n"
"v_accvgpr_read_b32 v44, a100\n"
"v_accvgpr_read_b32 v45, a101\n"
"v_accvgpr_read_b32 v46, a102\n"
"v_accvgpr_read_b32 v47, a103\n"
"v_cvt_pk_bf16_f32 v40, v40, v41\n"
"v_cvt_pk_bf16_f32 v41, v42, v43\n"
"v_cvt_pk_bf16_f32 v42, v44, v45\n"
"v_cvt_pk_bf16_f32 v43, v46, v47\n"
"buffer_store_dwordx4 v[40:43], v36, s[44:47], s69 offen offset:0\n"
"s_add_u32 s69, s68, 52224\n"
"v_accvgpr_read_b32 v48, a104\n"
"v_accvgpr_read_b32 v49, a105\n"
"v_accvgpr_read_b32 v50, a106\n"
"v_accvgpr_read_b32 v51, a107\n"
"v_accvgpr_read_b32 v52, a108\n"
"v_accvgpr_read_b32 v53, a109\n"
"v_accvgpr_read_b32 v54, a110\n"
"v_accvgpr_read_b32 v55, a111\n"
"v_cvt_pk_bf16_f32 v48, v48, v49\n"
"v_cvt_pk_bf16_f32 v49, v50, v51\n"
"v_cvt_pk_bf16_f32 v50, v52, v53\n"
"v_cvt_pk_bf16_f32 v51, v54, v55\n"
"buffer_store_dwordx4 v[48:51], v36, s[44:47], s69 offen offset:0\n"
"s_add_u32 s69, s68, 101376\n"
"v_accvgpr_read_b32 v56, a112\n"
"v_accvgpr_read_b32 v57, a113\n"
"v_accvgpr_read_b32 v58, a114\n"
"v_accvgpr_read_b32 v59, a115\n"
"v_accvgpr_read_b32 v60, a116\n"
"v_accvgpr_read_b32 v61, a117\n"
"v_accvgpr_read_b32 v62, a118\n"
"v_accvgpr_read_b32 v63, a119\n"
"v_cvt_pk_bf16_f32 v56, v56, v57\n"
"v_cvt_pk_bf16_f32 v57, v58, v59\n"
"v_cvt_pk_bf16_f32 v58, v60, v61\n"
"v_cvt_pk_bf16_f32 v59, v62, v63\n"
"buffer_store_dwordx4 v[56:59], v36, s[44:47], s69 offen offset:0\n"
"s_add_u32 s69, s68, 117760\n"
"v_accvgpr_read_b32 v64, a120\n"
"v_accvgpr_read_b32 v65, a121\n"
"v_accvgpr_read_b32 v66, a122\n"
"v_accvgpr_read_b32 v67, a123\n"
"v_accvgpr_read_b32 v68, a124\n"
"v_accvgpr_read_b32 v69, a125\n"
"v_accvgpr_read_b32 v70, a126\n"
"v_accvgpr_read_b32 v71, a127\n"
"v_cvt_pk_bf16_f32 v64, v64, v65\n"
"v_cvt_pk_bf16_f32 v65, v66, v67\n"
"v_cvt_pk_bf16_f32 v66, v68, v69\n"
"v_cvt_pk_bf16_f32 v67, v70, v71\n"
"buffer_store_dwordx4 v[64:67], v36, s[44:47], s69 offen offset:0\n"
"s_add_u32 s69, s68, 36864\n"
"v_accvgpr_read_b32 v40, a128\n"
"v_accvgpr_read_b32 v41, a129\n"
"v_accvgpr_read_b32 v42, a130\n"
"v_accvgpr_read_b32 v43, a131\n"
"v_accvgpr_read_b32 v44, a132\n"
"v_accvgpr_read_b32 v45, a133\n"
"v_accvgpr_read_b32 v46, a134\n"
"v_accvgpr_read_b32 v47, a135\n"
"v_cvt_pk_bf16_f32 v40, v40, v41\n"
"v_cvt_pk_bf16_f32 v41, v42, v43\n"
"v_cvt_pk_bf16_f32 v42, v44, v45\n"
"v_cvt_pk_bf16_f32 v43, v46, v47\n"
"buffer_store_dwordx4 v[40:43], v36, s[44:47], s69 offen offset:0\n"
"s_add_u32 s69, s68, 53248\n"
"v_accvgpr_read_b32 v48, a136\n"
"v_accvgpr_read_b32 v49, a137\n"
"v_accvgpr_read_b32 v50, a138\n"
"v_accvgpr_read_b32 v51, a139\n"
"v_accvgpr_read_b32 v52, a140\n"
"v_accvgpr_read_b32 v53, a141\n"
"v_accvgpr_read_b32 v54, a142\n"
"v_accvgpr_read_b32 v55, a143\n"
"v_cvt_pk_bf16_f32 v48, v48, v49\n"
"v_cvt_pk_bf16_f32 v49, v50, v51\n"
"v_cvt_pk_bf16_f32 v50, v52, v53\n"
"v_cvt_pk_bf16_f32 v51, v54, v55\n"
"buffer_store_dwordx4 v[48:51], v36, s[44:47], s69 offen offset:0\n"
"s_add_u32 s69, s68, 102400\n"
"v_accvgpr_read_b32 v56, a144\n"
"v_accvgpr_read_b32 v57, a145\n"
"v_accvgpr_read_b32 v58, a146\n"
"v_accvgpr_read_b32 v59, a147\n"
"v_accvgpr_read_b32 v60, a148\n"
"v_accvgpr_read_b32 v61, a149\n"
"v_accvgpr_read_b32 v62, a150\n"
"v_accvgpr_read_b32 v63, a151\n"
"v_cvt_pk_bf16_f32 v56, v56, v57\n"
"v_cvt_pk_bf16_f32 v57, v58, v59\n"
"v_cvt_pk_bf16_f32 v58, v60, v61\n"
"v_cvt_pk_bf16_f32 v59, v62, v63\n"
"buffer_store_dwordx4 v[56:59], v36, s[44:47], s69 offen offset:0\n"
"s_add_u32 s69, s68, 118784\n"
"v_accvgpr_read_b32 v64, a152\n"
"v_accvgpr_read_b32 v65, a153\n"
"v_accvgpr_read_b32 v66, a154\n"
"v_accvgpr_read_b32 v67, a155\n"
"v_accvgpr_read_b32 v68, a156\n"
"v_accvgpr_read_b32 v69, a157\n"
"v_accvgpr_read_b32 v70, a158\n"
"v_accvgpr_read_b32 v71, a159\n"
"v_cvt_pk_bf16_f32 v64, v64, v65\n"
"v_cvt_pk_bf16_f32 v65, v66, v67\n"
"v_cvt_pk_bf16_f32 v66, v68, v69\n"
"v_cvt_pk_bf16_f32 v67, v70, v71\n"
"buffer_store_dwordx4 v[64:67], v36, s[44:47], s69 offen offset:0\n"
"s_add_u32 s69, s68, 37888\n"
"v_accvgpr_read_b32 v40, a160\n"
"v_accvgpr_read_b32 v41, a161\n"
"v_accvgpr_read_b32 v42, a162\n"
"v_accvgpr_read_b32 v43, a163\n"
"v_accvgpr_read_b32 v44, a164\n"
"v_accvgpr_read_b32 v45, a165\n"
"v_accvgpr_read_b32 v46, a166\n"
"v_accvgpr_read_b32 v47, a167\n"
"v_cvt_pk_bf16_f32 v40, v40, v41\n"
"v_cvt_pk_bf16_f32 v41, v42, v43\n"
"v_cvt_pk_bf16_f32 v42, v44, v45\n"
"v_cvt_pk_bf16_f32 v43, v46, v47\n"
"buffer_store_dwordx4 v[40:43], v36, s[44:47], s69 offen offset:0\n"
"s_add_u32 s69, s68, 54272\n"
"v_accvgpr_read_b32 v48, a168\n"
"v_accvgpr_read_b32 v49, a169\n"
"v_accvgpr_read_b32 v50, a170\n"
"v_accvgpr_read_b32 v51, a171\n"
"v_accvgpr_read_b32 v52, a172\n"
"v_accvgpr_read_b32 v53, a173\n"
"v_accvgpr_read_b32 v54, a174\n"
"v_accvgpr_read_b32 v55, a175\n"
"v_cvt_pk_bf16_f32 v48, v48, v49\n"
"v_cvt_pk_bf16_f32 v49, v50, v51\n"
"v_cvt_pk_bf16_f32 v50, v52, v53\n"
"v_cvt_pk_bf16_f32 v51, v54, v55\n"
"buffer_store_dwordx4 v[48:51], v36, s[44:47], s69 offen offset:0\n"
"s_add_u32 s69, s68, 103424\n"
"v_accvgpr_read_b32 v56, a176\n"
"v_accvgpr_read_b32 v57, a177\n"
"v_accvgpr_read_b32 v58, a178\n"
"v_accvgpr_read_b32 v59, a179\n"
"v_accvgpr_read_b32 v60, a180\n"
"v_accvgpr_read_b32 v61, a181\n"
"v_accvgpr_read_b32 v62, a182\n"
"v_accvgpr_read_b32 v63, a183\n"
"v_cvt_pk_bf16_f32 v56, v56, v57\n"
"v_cvt_pk_bf16_f32 v57, v58, v59\n"
"v_cvt_pk_bf16_f32 v58, v60, v61\n"
"v_cvt_pk_bf16_f32 v59, v62, v63\n"
"buffer_store_dwordx4 v[56:59], v36, s[44:47], s69 offen offset:0\n"
"s_add_u32 s69, s68, 119808\n"
"v_accvgpr_read_b32 v64, a184\n"
"v_accvgpr_read_b32 v65, a185\n"
"v_accvgpr_read_b32 v66, a186\n"
"v_accvgpr_read_b32 v67, a187\n"
"v_accvgpr_read_b32 v68, a188\n"
"v_accvgpr_read_b32 v69, a189\n"
"v_accvgpr_read_b32 v70, a190\n"
"v_accvgpr_read_b32 v71, a191\n"
"v_cvt_pk_bf16_f32 v64, v64, v65\n"
"v_cvt_pk_bf16_f32 v65, v66, v67\n"
"v_cvt_pk_bf16_f32 v66, v68, v69\n"
"v_cvt_pk_bf16_f32 v67, v70, v71\n"
"buffer_store_dwordx4 v[64:67], v36, s[44:47], s69 offen offset:0\n"
"s_add_u32 s69, s68, 38912\n"
"v_accvgpr_read_b32 v40, a192\n"
"v_accvgpr_read_b32 v41, a193\n"
"v_accvgpr_read_b32 v42, a194\n"
"v_accvgpr_read_b32 v43, a195\n"
"v_accvgpr_read_b32 v44, a196\n"
"v_accvgpr_read_b32 v45, a197\n"
"v_accvgpr_read_b32 v46, a198\n"
"v_accvgpr_read_b32 v47, a199\n"
"v_cvt_pk_bf16_f32 v40, v40, v41\n"
"v_cvt_pk_bf16_f32 v41, v42, v43\n"
"v_cvt_pk_bf16_f32 v42, v44, v45\n"
"v_cvt_pk_bf16_f32 v43, v46, v47\n"
"buffer_store_dwordx4 v[40:43], v36, s[44:47], s69 offen offset:0\n"
"s_add_u32 s69, s68, 55296\n"
"v_accvgpr_read_b32 v48, a200\n"
"v_accvgpr_read_b32 v49, a201\n"
"v_accvgpr_read_b32 v50, a202\n"
"v_accvgpr_read_b32 v51, a203\n"
"v_accvgpr_read_b32 v52, a204\n"
"v_accvgpr_read_b32 v53, a205\n"
"v_accvgpr_read_b32 v54, a206\n"
"v_accvgpr_read_b32 v55, a207\n"
"v_cvt_pk_bf16_f32 v48, v48, v49\n"
"v_cvt_pk_bf16_f32 v49, v50, v51\n"
"v_cvt_pk_bf16_f32 v50, v52, v53\n"
"v_cvt_pk_bf16_f32 v51, v54, v55\n"
"buffer_store_dwordx4 v[48:51], v36, s[44:47], s69 offen offset:0\n"
"s_add_u32 s69, s68, 104448\n"
"v_accvgpr_read_b32 v56, a208\n"
"v_accvgpr_read_b32 v57, a209\n"
"v_accvgpr_read_b32 v58, a210\n"
"v_accvgpr_read_b32 v59, a211\n"
"v_accvgpr_read_b32 v60, a212\n"
"v_accvgpr_read_b32 v61, a213\n"
"v_accvgpr_read_b32 v62, a214\n"
"v_accvgpr_read_b32 v63, a215\n"
"v_cvt_pk_bf16_f32 v56, v56, v57\n"
"v_cvt_pk_bf16_f32 v57, v58, v59\n"
"v_cvt_pk_bf16_f32 v58, v60, v61\n"
"v_cvt_pk_bf16_f32 v59, v62, v63\n"
"buffer_store_dwordx4 v[56:59], v36, s[44:47], s69 offen offset:0\n"
"s_add_u32 s69, s68, 120832\n"
"v_accvgpr_read_b32 v64, a216\n"
"v_accvgpr_read_b32 v65, a217\n"
"v_accvgpr_read_b32 v66, a218\n"
"v_accvgpr_read_b32 v67, a219\n"
"v_accvgpr_read_b32 v68, a220\n"
"v_accvgpr_read_b32 v69, a221\n"
"v_accvgpr_read_b32 v70, a222\n"
"v_accvgpr_read_b32 v71, a223\n"
"v_cvt_pk_bf16_f32 v64, v64, v65\n"
"v_cvt_pk_bf16_f32 v65, v66, v67\n"
"v_cvt_pk_bf16_f32 v66, v68, v69\n"
"v_cvt_pk_bf16_f32 v67, v70, v71\n"
"buffer_store_dwordx4 v[64:67], v36, s[44:47], s69 offen offset:0\n"
"s_add_u32 s69, s68, 39936\n"
"v_accvgpr_read_b32 v40, a224\n"
"v_accvgpr_read_b32 v41, a225\n"
"v_accvgpr_read_b32 v42, a226\n"
"v_accvgpr_read_b32 v43, a227\n"
"v_accvgpr_read_b32 v44, a228\n"
"v_accvgpr_read_b32 v45, a229\n"
"v_accvgpr_read_b32 v46, a230\n"
"v_accvgpr_read_b32 v47, a231\n"
"v_cvt_pk_bf16_f32 v40, v40, v41\n"
"v_cvt_pk_bf16_f32 v41, v42, v43\n"
"v_cvt_pk_bf16_f32 v42, v44, v45\n"
"v_cvt_pk_bf16_f32 v43, v46, v47\n"
"buffer_store_dwordx4 v[40:43], v36, s[44:47], s69 offen offset:0\n"
"s_add_u32 s69, s68, 56320\n"
"v_accvgpr_read_b32 v48, a232\n"
"v_accvgpr_read_b32 v49, a233\n"
"v_accvgpr_read_b32 v50, a234\n"
"v_accvgpr_read_b32 v51, a235\n"
"v_accvgpr_read_b32 v52, a236\n"
"v_accvgpr_read_b32 v53, a237\n"
"v_accvgpr_read_b32 v54, a238\n"
"v_accvgpr_read_b32 v55, a239\n"
"v_cvt_pk_bf16_f32 v48, v48, v49\n"
"v_cvt_pk_bf16_f32 v49, v50, v51\n"
"v_cvt_pk_bf16_f32 v50, v52, v53\n"
"v_cvt_pk_bf16_f32 v51, v54, v55\n"
"buffer_store_dwordx4 v[48:51], v36, s[44:47], s69 offen offset:0\n"
"s_add_u32 s69, s68, 105472\n"
"v_accvgpr_read_b32 v56, a240\n"
"v_accvgpr_read_b32 v57, a241\n"
"v_accvgpr_read_b32 v58, a242\n"
"v_accvgpr_read_b32 v59, a243\n"
"v_accvgpr_read_b32 v60, a244\n"
"v_accvgpr_read_b32 v61, a245\n"
"v_accvgpr_read_b32 v62, a246\n"
"v_accvgpr_read_b32 v63, a247\n"
"v_cvt_pk_bf16_f32 v56, v56, v57\n"
"v_cvt_pk_bf16_f32 v57, v58, v59\n"
"v_cvt_pk_bf16_f32 v58, v60, v61\n"
"v_cvt_pk_bf16_f32 v59, v62, v63\n"
"buffer_store_dwordx4 v[56:59], v36, s[44:47], s69 offen offset:0\n"
"s_add_u32 s69, s68, 121856\n"
"v_accvgpr_read_b32 v64, a248\n"
"v_accvgpr_read_b32 v65, a249\n"
"v_accvgpr_read_b32 v66, a250\n"
"v_accvgpr_read_b32 v67, a251\n"
"v_accvgpr_read_b32 v68, a252\n"
"v_accvgpr_read_b32 v69, a253\n"
"v_accvgpr_read_b32 v70, a254\n"
"v_accvgpr_read_b32 v71, a255\n"
"v_cvt_pk_bf16_f32 v64, v64, v65\n"
"v_cvt_pk_bf16_f32 v65, v66, v67\n"
"v_cvt_pk_bf16_f32 v66, v68, v69\n"
"v_cvt_pk_bf16_f32 v67, v70, v71\n"
"buffer_store_dwordx4 v[64:67], v36, s[44:47], s69 offen offset:0\n"
"s_waitcnt vmcnt(32)\n"
        ::[c] "s"(cp), [w] "s"(wp), [o] "s"(op), [wn] "s"(wn), [sm] "s"(smo), [wv] "s"(wvi)
        : "v1","v2","v3","v4","v5","v6","v7","v8","v9","v10","v11","v12","v13","v14","v15","v16","v17","v18","v19","v20","v21","v22","v23","v24","v25","v26","v27","v28","v29","v30","v31","v32","v33","v34","v35","v36","v37","v38","v39","v40","v41","v42","v43","v44","v45","v46","v47","v48","v49","v50","v51","v52","v53","v54","v55","v56","v57","v58","v59","v60","v61","v62","v63","v64","v65","v66","v67","v68","v69","v70","v71","v72","v73","v74","v75","v76","v128","v129","v130","v131","v132","v133","v134","v135","v136","v137","v138","v139","v140","v141","v142","v143","v144","v145","v146","v147","v148","v149","v150","v151","v152","v153","v154","v155","v156","v157","v158","v159","v160","v161","v162","v163","v164","v165","v166","v167","v168","v169","v170","v171","v172","v173","v174","v175","v176","v177","v178","v179","v180","v181","v182","v183","v184","v185","v186","v187","v188","v189","v190","v191","v192","v193","v194","v195","v196","v197","v198","v199","v200","v201","v202","v203","v204","v205","v206","v207","v208","v209","v210","v211","v212","v213","v214","v215","v216","v217","v218","v219","v220","v221","v222","v223","v224","v225","v226","v227","v228","v229","v230","v231","v232","v233","v234","v235","v236","v237","v238","v239","v240","v241","v242","v243","v244","v245","v246","v247","v248","v249","v250","v251","v252","v253","v254","v255","a0","a1","a2","a3","a4","a5","a6","a7","a8","a9","a10","a11","a12","a13","a14","a15","a16","a17","a18","a19","a20","a21","a22","a23","a24","a25","a26","a27","a28","a29","a30","a31","a32","a33","a34","a35","a36","a37","a38","a39","a40","a41","a42","a43","a44","a45","a46","a47","a48","a49","a50","a51","a52","a53","a54","a55","a56","a57","a58","a59","a60","a61","a62","a63","a64","a65","a66","a67","a68","a69","a70","a71","a72","a73","a74","a75","a76","a77","a78","a79","a80","a81","a82","a83","a84","a85","a86","a87","a88","a89","a90","a91","a92","a93","a94","a95","a96","a97","a98","a99","a100","a101","a102","a103","a104","a105","a106","a107","a108","a109","a110","a111","a112","a113","a114","a115","a116","a117","a118","a119","a120","a121","a122","a123","a124","a125","a126","a127","a128","a129","a130","a131","a132","a133","a134","a135","a136","a137","a138","a139","a140","a141","a142","a143","a144","a145","a146","a147","a148","a149","a150","a151","a152","a153","a154","a155","a156","a157","a158","a159","a160","a161","a162","a163","a164","a165","a166","a167","a168","a169","a170","a171","a172","a173","a174","a175","a176","a177","a178","a179","a180","a181","a182","a183","a184","a185","a186","a187","a188","a189","a190","a191","a192","a193","a194","a195","a196","a197","a198","a199","a200","a201","a202","a203","a204","a205","a206","a207","a208","a209","a210","a211","a212","a213","a214","a215","a216","a217","a218","a219","a220","a221","a222","a223","a224","a225","a226","a227","a228","a229","a230","a231","a232","a233","a234","a235","a236","a237","a238","a239","a240","a241","a242","a243","a244","a245","a246","a247","a248","a249","a250","a251","a252","a253","a254","a255","s36","s37","s38","s39","s40","s41","s42","s43","s44","s45","s46","s47","s48","s49","s50","s51","s52","s53","s54","s55","s56","s57","s58","s59","s60","s61","s62","s63","s64","s65","s66","s67","s68","s69","s70","s71","s72","s73","s74","s75","s76","s77","s78","s79","s80","s81","s82","s83","memory","vcc","scc");
    relay<1>(u0 + 1024, wvi, smo, wo, w2, w3, w4, ws);
  }
}

__constant__ const u32 segtab[14 * 4] = {151594,0,0,0,413771,0,0,0,675948,0,0,0,938125,0,0,0,1200302,0,0,0,1462479,0,0,0,20489,0,0,0,67594,18440,0,0,331787,16391,0,0,595980,14342,0,0,860173,12293,0,0,1124366,10244,0,0,1388559,8195,0,0,6146,4097,2048,0};
__global__ __launch_bounds__(256, 1) void k2(const char* __restrict__ x, char* __restrict__ ws, char* __restrict__ out) {
  __shared__ __attribute__((aligned(1024))) char sm[143360];
  const int xc = blockIdx.x & 7, id = blockIdx.x >> 3, wvi = __builtin_amdgcn_readfirstlane(threadIdx.x >> 6);
  if (id < 2 * 14) {
    const int h = 2 * xc + (id & 1), cu = id >> 1;
    const u32 smo = (u32)(size_t)sm, so = (u32)h << 21;
    u32 s0 = segtab[cu * 4] + so, s1 = segtab[cu * 4 + 1], s2 = segtab[cu * 4 + 2], s3 = segtab[cu * 4 + 3];
    s1 = s1 ? s1 + so : 0; s2 = s2 ? s2 + so : 0; s3 = s3 ? s3 + so : 0;
    const char *kv = ws + h * (2 * MB), *q = ws + OFF_Q + h * MB, *cx = ws + OFF_CTX + h * 8192, *ax = ws + OFF_AUX;
    const int pw = id * 4 + wvi;
    const char* pf = pw < 49 ? ws + OFF_W2 + pw * 8192 : pw < 77 ? ws + OFF_H + (xc * 28 + pw - 49) * 8192 : x + (xc * 28 + (pw < 105 ? pw - 77 : 0)) * 8192;
    asm volatile(
"v_mbcnt_lo_u32_b32 v254, -1, 0\n"
"v_mbcnt_hi_u32_b32 v254, -1, v254\n"
"v_lshl_add_u32 v254, %[wv], 6, v254\n"
"s_mov_b64 s[36:37], %[kv]\n"
"s_mov_b32 s38, 2097152\n"
"s_mov_b32 s39, 0x20000\n"
"s_mov_b64 s[40:41], %[q]\n"
"s_mov_b32 s42, 1048576\n"
"s_mov_b32 s43, 0x20000\n"
"s_mov_b64 s[44:45], %[x]\n"
"s_mov_b32 s46, 23994368\n"
"s_mov_b32 s47, 0x20000\n"
"s_mov_b64 s[48:49], %[a]\n"
"s_mov_b32 s50, 9043968\n"
"s_mov_b32 s51, 0x20000\n"
"s_mov_b32 s52, %[s0]\n"
"s_mov_b32 s53, %[s1]\n"
"s_mov_b32 s54, %[s2]\n"
"s_mov_b32 s55, %[s3]\n"
"s_mov_b32 s56, 0\n"
"s_mov_b32 s74, 0\n"
"s_mov_b32 s73, 0x41c00000\n"
"v_lshrrev_b32 v8, 6, v254\n"
"v_and_b32 v9, 63, v254\n"
"s_nop 0\n"
"v_readfirstlane_b32 s57, v8\n"
"v_lshlrev_b32 v6, 4, v9\n"
"v_lshl_add_u32 v1, v8, 13, v6\n"
"v_add_u32 v2, 4096, v1\n"
"v_add_u32 v3, %[sm], v6\n"
"v_add_u32 v4, 65536, v3\n"
"s_lshl_b32 s58, s57, 13\n"
"s_add_u32 s69, s58, %[sm]\n"
"s_add_u32 s70, s69, 32768\n"
"s_add_u32 s71, s69, 65536\n"
"s_add_u32 s72, s69, 98304\n"
"s_bfe_u32 s64, s52, 0x60004\n"
"s_lshl_b32 s64, s64, 16\n"
"s_or_b32 s66, s64, 32768\n"
"s_mov_b32 s65, 0\n"
"s_mov_b32 s67, 0\n"
"v_lshlrev_b32 v255, 3, v6\n"
"global_load_dword v255, v255, %[pf]\n"
"s_and_b32 s58, s52, 15\n"
"s_lshl_b32 s58, s58, 2\n"
"s_add_u32 s58, s58, s57\n"
"s_lshl_b32 s58, s58, 14\n"
"buffer_load_dwordx4 a[136:139], v6, s[40:43], s58 offen offset:0\n"
"buffer_load_dwordx4 a[140:143], v6, s[40:43], s58 offen offset:1024\n"
"buffer_load_dwordx4 a[144:147], v6, s[40:43], s58 offen offset:2048\n"
"buffer_load_dwordx4 a[148:151], v6, s[40:43], s58 offen offset:3072\n"
"s_add_u32 s58, s58, 4096\n"
"buffer_load_dwordx4 a[152:155], v6, s[40:43], s58 offen offset:0\n"
"buffer_load_dwordx4 a[156:159], v6, s[40:43], s58 offen offset:1024\n"
"buffer_load_dwordx4 a[160:163], v6, s[40:43], s58 offen offset:2048\n"
"buffer_load_dwordx4 a[164:167], v6, s[40:43], s58 offen offset:3072\n"
"s_add_u32 s58, s58, 4096\n"
"buffer_load_dwordx4 a[168:171], v6, s[40:43], s58 offen offset:0\n"
"buffer_load_dwordx4 a[172:175], v6, s[40:43], s58 offen offset:1024\n"
"buffer_load_dwordx4 a[176:179], v6, s[40:43], s58 offen offset:2048\n"
"buffer_load_dwordx4 a[180:183], v6, s[40:43], s58 offen offset:3072\n"
"s_add_u32 s58, s58, 4096\n"
"buffer_load_dwordx4 a[184:187], v6, s[40:43], s58 offen offset:0\n"
"buffer_load_dwordx4 a[188:191], v6, s[40:43], s58 offen offset:1024\n"
"buffer_load_dwordx4 a[192:195], v6, s[40:43], s58 offen offset:2048\n"
"buffer_load_dwordx4 a[196:199], v6, s[40:43], s58 offen offset:3072\n"
"s_add_u32 s58, s58, 4096\n"
"s_mov_b32 m0, s69\n"
"s_nop 0\n"
"buffer_load_dwordx4 v1, s[36:39], s64 offen offset:0 lds\n"
"buffer_load_dwordx4 v1, s[36:39], s64 offen offset:1024 lds\n"
"buffer_load_dwordx4 v1, s[36:39], s64 offen offset:2048 lds\n"
"buffer_load_dwordx4 v1, s[36:39], s64 offen offset:3072 lds\n"
"s_add_u32 m0, m0, 4096\n"
"s_nop 0\n"
"buffer_load_dwordx4 v2, s[36:39], s64 offen offset:0 lds\n"
"buffer_load_dwordx4 v2, s[36:39], s64 offen offset:1024 lds\n"
"buffer_load_dwordx4 v2, s[36:39], s64 offen offset:2048 lds\n"
"buffer_load_dwordx4 v2, s[36:39], s64 offen offset:3072 lds\n"
"s_add_u32 s64, s64, 65536\n"
"s_sub_u32 s65, s65, 1\n"
"s_cmp_eq_u32 s65, 0\n"
"s_cselect_b32 s64, s68, s64\n"
"s_mov_b32 m0, s70\n"
"s_nop 0\n"
"buffer_load_dwordx4 v1, s[36:39], s64 offen offset:0 lds\n"
"buffer_load_dwordx4 v1, s[36:39], s64 offen offset:1024 lds\n"
"buffer_load_dwordx4 v1, s[36:39], s64 offen offset:2048 lds\n"
"buffer_load_dwordx4 v1, s[36:39], s64 offen offset:3072 lds\n"
"s_add_u32 m0, m0, 4096\n"
"s_nop 0\n"
"buffer_load_dwordx4 v2, s[36:39], s64 offen offset:0 lds\n"
"buffer_load_dwordx4 v2, s[36:39], s64 offen offset:1024 lds\n"
"buffer_load_dwordx4 v2, s[36:39], s64 offen offset:2048 lds\n"
"buffer_load_dwordx4 v2, s[36:39], s64 offen offset:3072 lds\n"
"s_add_u32 s64, s64, 65536\n"
"s_sub_u32 s65, s65, 1\n"
"s_cmp_eq_u32 s65, 0\n"
"s_cselect_b32 s64, s68, s64\n"
"s_mov_b32 m0, s71\n"
"s_nop 0\n"
"buffer_load_dwordx4 v1, s[36:39], s66 offen offset:0 lds\n"
"buffer_load_dwordx4 v1, s[36:39], s66 offen offset:1024 lds\n"
"buffer_load_dwordx4 v1, s[36:39], s66 offen offset:2048 lds\n"
"buffer_load_dwordx4 v1, s[36:39], s66 offen offset:3072 lds\n"
"s_add_u32 m0, m0, 4096\n"
"s_nop 0\n"
"buffer_load_dwordx4 v2, s[36:39], s66 offen offset:0 lds\n"
"buffer_load_dwordx4 v2, s[36:39], s66 offen offset:1024 lds\n"
"buffer_load_dwordx4 v2, s[36:39], s66 offen offset:2048 lds\n"
"buffer_load_dwordx4 v2, s[36:39], s66 offen offset:3072 lds\n"
"s_add_u32 s66, s66, 65536\n"
"s_sub_u32 s67, s67, 1\n"
"s_cmp_eq_u32 s67, 0\n"
"s_cselect_b32 s66, s68, s66\n"
"s_or_b32 s66, s66, 32768\n"
"v_and_b32 v10, 15, v9\n"
"v_lshrrev_b32 v11, 4, v9\n"
"v_lshlrev_b32 v11, 2, v11\n"
"v_sub_u32 v5, v10, v11\n"
"v_lshl_add_u32 v5, v8, 5, v5\n"
"v_lshlrev_b32 v7, 2, v10\n"
"v_mov_b32 v30, 0xff800000\n"
"v_mov_b32 v24, 0x3f803f80\n"
"v_mov_b32 v25, 0x3f803f80\n"
"v_mov_b32 v26, 0x3f803f80\n"
"v_mov_b32 v27, 0x3f803f80\n"
"s_waitcnt vmcnt(16)\n"
"s_barrier\n"
"s_mov_b64 vcc, -1\n"
"L_seg%=:\n"
"s_and_b32 s60, s52, 15\n"
"s_bfe_u32 s61, s52, 0x6000a\n"
"s_bfe_u32 s62, s52, 0x20010\n"
"s_lshr_b32 s63, s52, 18\n"
"s_bfe_u32 s58, s52, 0x60004\n"
"s_lshl_b32 s58, s58, 6\n"
"s_lshl_b32 s75, s60, 7\n"
"s_sub_u32 s75, s75, s58\n"
"s_bfe_u32 s68, s53, 0x60004\n"
"s_lshl_b32 s68, s68, 16\n"
"s_add_u32 s65, s65, s61\n"
"s_add_u32 s67, s67, s61\n"
"s_cmp_eq_u32 s65, 0\n"
"s_cselect_b32 s64, s68, s64\n"
"ds_read_b128 v[112:115], v3 offset:0\n"
"ds_read_b128 v[116:119], v3 offset:1024\n"
"ds_read_b128 v[120:123], v3 offset:2048\n"
"ds_read_b128 v[124:127], v3 offset:3072\n"
"ds_read_b128 v[128:131], v3 offset:4096\n"
"ds_read_b128 v[132:135], v3 offset:5120\n"
"ds_read_b128 v[136:139], v3 offset:6144\n"
"ds_read_b128 v[140:143], v3 offset:7168\n"
"s_waitcnt lgkmcnt(7)\n"
"v_mfma_f32_16x16x32_bf16 v[48:51], v[112:115], a[136:139], 0\n"
"ds_read_b128 v[144:147], v3 offset:8192\n"
"v_accvgpr_write_b32 a0, 0\n"
"v_accvgpr_write_b32 a1, 0\n"
"v_accvgpr_write_b32 a2, 0\n"
"v_mfma_f32_16x16x32_bf16 v[52:55], v[112:115], a[168:171], 0\n"
"v_accvgpr_write_b32 a3, 0\n"
"v_accvgpr_write_b32 a4, 0\n"
"s_lshl_b32 s58, s60, 2\n"
"s_add_u32 s58, s58, s57\n"
"s_lshl_b32 s59, s58, 7\n"
"s_waitcnt lgkmcnt(0)\n"
"v_mfma_f32_16x16x32_bf16 v[48:51], v[116:119], a[140:143], v[48:51]\n"
"ds_read_b128 v[148:151], v3 offset:9216\n"
"v_accvgpr_write_b32 a5, 0\n"
"v_accvgpr_write_b32 a6, 0\n"
"v_mfma_f32_16x16x32_bf16 v[52:55], v[116:119], a[172:175], v[52:55]\n"
"v_accvgpr_write_b32 a7, 0\n"
"v_accvgpr_write_b32 a8, 0\n"
"v_accvgpr_write_b32 a9, 0\n"
"s_mul_i32 s60, s58, 376832\n"
"s_mul_i32 s76, s63, 69632\n"
"s_mul_i32 s58, s57, 17408\n"
"s_add_u32 s76, s76, s58\n"
"s_add_u32 s76, s76, 131072\n"
"s_lshl_b32 s63, s63, 7\n"
"s_add_u32 s63, s63, 8192\n"
"v_mfma_f32_16x16x32_bf16 v[48:51], v[120:123], a[144:147], v[48:51]\n"
"ds_read_b128 v[152:155], v3 offset:10240\n"
"v_accvgpr_write_b32 a10, 0\n"
"v_accvgpr_write_b32 a11, 0\n"
"v_mfma_f32_16x16x32_bf16 v[52:55], v[120:123], a[176:179], v[52:55]\n"
"v_accvgpr_write_b32 a12, 0\n"
"v_accvgpr_write_b32 a13, 0\n"
"v_mfma_f32_16x16x32_bf16 v[48:51], v[124:127], a[148:151], v[48:51]\n"
"ds_read_b128 v[156:159], v3 offset:11264\n"
"v_accvgpr_write_b32 a14, 0\n"
"v_accvgpr_write_b32 a15, 0\n"
"v_mfma_f32_16x16x32_bf16 v[52:55], v[124:127], a[180:183], v[52:55]\n"
"v_accvgpr_write_b32 a16, 0\n"
"v_accvgpr_write_b32 a17, 0\n"
"v_accvgpr_write_b32 a18, 0\n"
"v_mfma_f32_16x16x32_bf16 v[48:51], v[128:131], a[152:155], v[48:51]\n"
"ds_read_b128 v[160:163], v3 offset:12288\n"
"v_accvgpr_write_b32 a19, 0\n"
"v_accvgpr_write_b32 a20, 0\n"
"v_mfma_f32_16x16x32_bf16 v[52:55], v[128:131], a[184:187], v[52:55]\n"
"v_accvgpr_write_b32 a21, 0\n"
"v_accvgpr_write_b32 a22, 0\n"
"v_mfma_f32_16x16x32_bf16 v[48:51], v[132:135], a[156:159], v[48:51]\n"
"ds_read_b128 v[164:167], v3 offset:13312\n"
"v_accvgpr_write_b32 a23, 0\n"
"v_accvgpr_write_b32 a24, 0\n"
"v_mfma_f32_16x16x32_bf16 v[52:55], v[132:135], a[188:191], v[52:55]\n"
"v_accvgpr_write_b32 a25, 0\n"
"v_accvgpr_write_b32 a26, 0\n"
"v_accvgpr_write_b32 a27, 0\n"
"v_mfma_f32_16x16x32_bf16 v[48:51], v[136:139], a[160:163], v[48:51]\n"
"ds_read_b128 v[168:171], v3 offset:14336\n"
"v_accvgpr_write_b32 a28, 0\n"
"v_accvgpr_write_b32 a29, 0\n"
"v_mfma_f32_16x16x32_bf16 v[52:55], v[136:139], a[192:195], v[52:55]\n"
"v_accvgpr_write_b32 a30, 0\n"
"v_accvgpr_write_b32 a31, 0\n"
"v_mfma_f32_16x16x32_bf16 v[48:51], v[140:143], a[164:167], v[48:51]\n"
"ds_read_b128 v[172:175], v3 offset:15360\n"
"v_accvgpr_write_b32 a32, 0\n"
"v_accvgpr_write_b32 a33, 0\n"
"v_mfma_f32_16x16x32_bf16 v[52:55], v[140:143], a[196:199], v[52:55]\n"
"v_accvgpr_write_b32 a34, 0\n"
"v_accvgpr_write_b32 a35, 0\n"
"v_accvgpr_write_b32 a36, 0\n"
"v_mfma_f32_16x16x32_bf16 v[56:59], v[144:147], a[136:139], 0\n"
"ds_read_b128 v[112:115], v3 offset:16384\n"
"v_accvgpr_write_b32 a37, 0\n"
"v_accvgpr_write_b32 a38, 0\n"
"v_mfma_f32_16x16x32_bf16 v[60:63], v[144:147], a[168:171], 0\n"
"v_accvgpr_write_b32 a39, 0\n"
"v_accvgpr_write_b32 a40, 0\n"
"s_waitcnt lgkmcnt(1)\n"
"v_mfma_f32_16x16x32_bf16 v[56:59], v[148:151], a[140:143], v[56:59]\n"
"ds_read_b128 v[116:119], v3 offset:17408\n"
"v_accvgpr_write_b32 a41, 0\n"
"v_accvgpr_write_b32 a42, 0\n"
"v_accvgpr_write_b32 a43, 0\n"
"v_mfma_f32_16x16x32_bf16 v[60:63], v[148:151], a[172:175], v[60:63]\n"
"v_accvgpr_write_b32 a44, 0\n"
"v_accvgpr_write_b32 a45, 0\n"
"v_mfma_f32_16x16x32_bf16 v[56:59], v[152:155], a[144:147], v[56:59]\n"
"ds_read_b128 v[120:123], v3 offset:18432\n"
"v_accvgpr_write_b32 a46, 0\n"
"v_accvgpr_write_b32 a47, 0\n"
"v_mfma_f32_16x16x32_bf16 v[60:63], v[152:155], a[176:179], v[60:63]\n"
"v_accvgpr_write_b32 a48, 0\n"
"v_accvgpr_write_b32 a49, 0\n"
"v_mfma_f32_16x16x32_bf16 v[56:59], v[156:159], a[148:151], v[56:59]\n"
"ds_read_b128 v[124:127], v3 offset:19456\n"
"v_accvgpr_write_b32 a50, 0\n"
"v_accvgpr_write_b32 a51, 0\n"
"v_accvgpr_write_b32 a52, 0\n"
"v_mfma_f32_16x16x32_bf16 v[60:63], v[156:159], a[180:183], v[60:63]\n"
"v_accvgpr_write_b32 a53, 0\n"
"v_accvgpr_write_b32 a54, 0\n"
"v_mfma_f32_16x16x32_bf16 v[56:59], v[160:163], a[152:155], v[56:59]\n"
"ds_read_b128 v[128:131], v3 offset:20480\n"
"v_accvgpr_write_b32 a55, 0\n"
"v_accvgpr_write_b32 a56, 0\n"
"v_mfma_f32_16x16x32_bf16 v[60:63], v[160:163], a[184:187], v[60:63]\n"
"v_accvgpr_write_b32 a57, 0\n"
"v_accvgpr_write_b32 a58, 0\n"
"v_mfma_f32_16x16x32_bf16 v[56:59], v[164:167], a[156:159], v[56:59]\n"
"ds_read_b128 v[132:135], v3 offset:21504\n"
"v_accvgpr_write_b32 a59, 0\n"
"v_accvgpr_write_b32 a60, 0\n"
"v_accvgpr_write_b32 a61, 0\n"
"v_mfma_f32_16x16x32_bf16 v[60:63], v[164:167], a[188:191], v[60:63]\n"
"v_accvgpr_write_b32 a62, 0\n"
"v_accvgpr_write_b32 a63, 0\n"
"v_mfma_f32_16x16x32_bf16 v[56:59], v[168:171], a[160:163], v[56:59]\n"
"ds_read_b128 v[136:139], v3 offset:22528\n"
"v_accvgpr_write_b32 a64, 0\n"
"v_accvgpr_write_b32 a65, 0\n"
"v_mfma_f32_16x16x32_bf16 v[60:63], v[168:171], a[192:195], v[60:63]\n"
"v_accvgpr_write_b32 a66, 0\n"
"v_accvgpr_write_b32 a67, 0\n"
"v_mfma_f32_16x16x32_bf16 v[56:59], v[172:175], a[164:167], v[56:59]\n"
"ds_read_b128 v[140:143], v3 offset:23552\n"
"v_accvgpr_write_b32 a68, 0\n"
"v_accvgpr_write_b32 a69, 0\n"
"v_accvgpr_write_b32 a70, 0\n"
"v_mfma_f32_16x16x32_bf16 v[60:63], v[172:175], a[196:199], v[60:63]\n"
"v_accvgpr_write_b32 a71, 0\n"
"v_accvgpr_write_b32 a72, 0\n"
"s_waitcnt lgkmcnt(1)\n"
"v_mfma_f32_16x16x32_bf16 v[64:67], v[112:115], a[136:139], 0\n"
"ds_read_b128 v[144:147], v3 offset:24576\n"
"v_accvgpr_write_b32 a73, 0\n"
"v_accvgpr_write_b32 a74, 0\n"
"v_mfma_f32_16x16x32_bf16 v[68:71], v[112:115], a[168:171], 0\n"
"v_accvgpr_write_b32 a75, 0\n"
"v_accvgpr_write_b32 a76, 0\n"
"v_accvgpr_write_b32 a77, 0\n"
"v_mfma_f32_16x16x32_bf16 v[64:67], v[116:119], a[140:143], v[64:67]\n"
"ds_read_b128 v[148:151], v3 offset:25600\n"
"v_accvgpr_write_b32 a78, 0\n"
"v_accvgpr_write_b32 a79, 0\n"
"v_mfma_f32_16x16x32_bf16 v[68:71], v[116:119], a[172:175], v[68:71]\n"
"v_accvgpr_write_b32 a80, 0\n"
"v_accvgpr_write_b32 a81, 0\n"
"v_mfma_f32_16x16x32_bf16 v[64:67], v[120:123], a[144:147], v[64:67]\n"
"ds_read_b128 v[152:155], v3 offset:26624\n"
"v_accvgpr_write_b32 a82, 0\n"
"v_accvgpr_write_b32 a83, 0\n"
"v_mfma_f32_16x16x32_bf16 v[68:71], v[120:123], a[176:179], v[68:71]\n"
"v_accvgpr_write_b32 a84, 0\n"
"v_accvgpr_write_b32 a85, 0\n"
"v_accvgpr_write_b32 a86, 0\n"
"v_mfma_f32_16x16x32_bf16 v[64:67], v[124:127], a[148:151], v[64:67]\n"
"ds_read_b128 v[156:159], v3 offset:27648\n"
"v_accvgpr_write_b32 a87, 0\n"
"v_accvgpr_write_b32 a88, 0\n"
"v_mfma_f32_16x16x32_bf16 v[68:71], v[124:127], a[180:183], v[68:71]\n"
"v_accvgpr_write_b32 a89, 0\n"
"v_accvgpr_write_b32 a90, 0\n"
"v_mfma_f32_16x16x32_bf16 v[64:67], v[128:131], a[152:155], v[64:67]\n"
"ds_read_b128 v[160:163], v3 offset:28672\n"
"v_accvgpr_write_b32 a91, 0\n"
"v_accvgpr_write_b32 a92, 0\n"
"v_mfma_f32_16x16x32_bf16 v[68:71], v[128:131], a[184:187], v[68:71]\n"
"v_accvgpr_write_b32 a93, 0\n"
"v_accvgpr_write_b32 a94, 0\n"
"v_accvgpr_write_b32 a95, 0\n"
"v_mfma_f32_16x16x32_bf16 v[64:67], v[132:135], a[156:159], v[64:67]\n"
"ds_read_b128 v[164:167], v3 offset:29696\n"
"v_accvgpr_write_b32 a96, 0\n"
"v_accvgpr_write_b32 a97, 0\n"
"v_mfma_f32_16x16x32_bf16 v[68:71], v[132:135], a[188:191], v[68:71]\n"
"v_accvgpr_write_b32 a98, 0\n"
"v_accvgpr_write_b32 a99, 0\n"
"v_mfma_f32_16x16x32_bf16 v[64:67], v[136:139], a[160:163], v[64:67]\n"
"ds_read_b128 v[168:171], v3 offset:30720\n"
"v_accvgpr_write_b32 a100, 0\n"
"v_accvgpr_write_b32 a101, 0\n"
"v_mfma_f32_16x16x32_bf16 v[68:71], v[136:139], a[192:195], v[68:71]\n"
"v_accvgpr_write_b32 a102, 0\n"
"v_accvgpr_write_b32 a103, 0\n"
"v_accvgpr_write_b32 a104, 0\n"
"s_waitcnt lgkmcnt(1)\n"
"v_mfma_f32_16x16x32_bf16 v[64:67], v[140:143], a[164:167], v[64:67]\n"
"ds_read_b128 v[172:175], v3 offset:31744\n"
"v_accvgpr_write_b32 a105, 0\n"
"v_accvgpr_write_b32 a106, 0\n"
"v_mfma_f32_16x16x32_bf16 v[68:71], v[140:143], a[196:199], v[68:71]\n"
"v_accvgpr_write_b32 a107, 0\n"
"v_accvgpr_write_b32 a108, 0\n"
"v_mfma_f32_16x16x32_bf16 v[72:75], v[144:147], a[136:139], 0\n"
"v_accvgpr_write_b32 a109, 0\n"
"v_accvgpr_write_b32 a110, 0\n"
"v_accvgpr_write_b32 a111, 0\n"
"v_mfma_f32_16x16x32_bf16 v[76:79], v[144:147], a[168:171], 0\n"
"v_accvgpr_write_b32 a112, 0\n"
"v_accvgpr_write_b32 a113, 0\n"
"v_mfma_f32_16x16x32_bf16 v[72:75], v[148:151], a[140:143], v[72:75]\n"
"v_accvgpr_write_b32 a114, 0\n"
"v_accvgpr_write_b32 a115, 0\n"
"v_mfma_f32_16x16x32_bf16 v[76:79], v[148:151], a[172:175], v[76:79]\n"
"v_accvgpr_write_b32 a116, 0\n"
"v_accvgpr_write_b32 a117, 0\n"
"v_mfma_f32_16x16x32_bf16 v[72:75], v[152:155], a[144:147], v[72:75]\n"
"v_accvgpr_write_b32 a118, 0\n"
"v_accvgpr_write_b32 a119, 0\n"
"v_accvgpr_write_b32 a120, 0\n"
"v_mfma_f32_16x16x32_bf16 v[76:79], v[152:155], a[176:179], v[76:79]\n"
"v_accvgpr_write_b32 a121, 0\n"
"v_accvgpr_write_b32 a122, 0\n"
"v_mfma_f32_16x16x32_bf16 v[72:75], v[156:159], a[148:151], v[72:75]\n"
"v_accvgpr_write_b32 a123, 0\n"
"v_accvgpr_write_b32 a124, 0\n"
"v_mfma_f32_16x16x32_bf16 v[76:79], v[156:159], a[180:183], v[76:79]\n"
"v_accvgpr_write_b32 a125, 0\n"
"v_accvgpr_write_b32 a126, 0\n"
"v_mfma_f32_16x16x32_bf16 v[72:75], v[160:163], a[152:155], v[72:75]\n"
"v_accvgpr_write_b32 a127, 0\n"
"v_accvgpr_write_b32 a128, 0\n"
"v_accvgpr_write_b32 a129, 0\n"
"v_mfma_f32_16x16x32_bf16 v[76:79], v[160:163], a[184:187], v[76:79]\n"
"v_accvgpr_write_b32 a130, 0\n"
"v_accvgpr_write_b32 a131, 0\n"
"v_mfma_f32_16x16x32_bf16 v[72:75], v[164:167], a[156:159], v[72:75]\n"
"v_accvgpr_write_b32 a132, 0\n"
"v_accvgpr_write_b32 a133, 0\n"
"v_mfma_f32_16x16x32_bf16 v[76:79], v[164:167], a[188:191], v[76:79]\n"
"v_accvgpr_write_b32 a134, 0\n"
"v_accvgpr_write_b32 a135, 0\n"
"s_waitcnt lgkmcnt(0)\n"
"v_mfma_f32_16x16x32_bf16 v[72:75], v[168:171], a[160:163], v[72:75]\n"
"v_mfma_f32_16x16x32_bf16 v[76:79], v[168:171], a[192:195], v[76:79]\n"
"v_mfma_f32_16x16x32_bf16 v[72:75], v[172:175], a[164:167], v[72:75]\n"
"v_mfma_f32_16x16x32_bf16 v[76:79], v[172:175], a[196:199], v[76:79]\n"
"s_cbranch_vccz L_nw%=\n"
"s_waitcnt vmcnt(8)\n"
"L_nw%=:\n"
"s_barrier\n"
"ds_read_b128 v[112:115], v3 offset:32768\n"
"ds_read_b128 v[116:119], v3 offset:33792\n"
"ds_read_b128 v[120:123], v3 offset:34816\n"
"ds_read_b128 v[124:127], v3 offset:35840\n"
"ds_read_b128 v[128:131], v3 offset:36864\n"
"ds_read_b128 v[132:135], v3 offset:37888\n"
"ds_read_b128 v[136:139], v3 offset:38912\n"
"ds_read_b128 v[140:143], v3 offset:39936\n"
"buffer_load_dword v248, v7, s[48:51], s59 offen offset:0\n"
"buffer_load_dword v249, v7, s[48:51], s59 offen offset:64\n"
"s_mov_b32 m0, s69\n"
"s_nop 0\n"
"buffer_load_dwordx4 v1, s[36:39], s64 offen offset:0 lds\n"
"buffer_load_dwordx4 v1, s[36:39], s64 offen offset:1024 lds\n"
"buffer_load_dwordx4 v1, s[36:39], s64 offen offset:2048 lds\n"
"s_cmp_gt_i32 s75, 0\n"
"s_cbranch_scc1 L_nmp%=\n"
"s_nop 7\n"
"v_add_u32 v8, s75, v5\n"
"v_cmp_gt_i32_e64 s[78:79], -16, v8\n"
"v_cmp_gt_i32_e64 s[80:81], -15, v8\n"
"v_cmp_gt_i32_e64 s[82:83], -14, v8\n"
"v_cmp_gt_i32_e64 s[84:85], -13, v8\n"
"v_cndmask_b32_e64 v52, v52, v30, s[78:79]\n"
"v_cndmask_b32_e64 v53, v53, v30, s[80:81]\n"
"v_cndmask_b32_e64 v54, v54, v30, s[82:83]\n"
"v_cndmask_b32_e64 v55, v55, v30, s[84:85]\n"
"v_cmp_gt_i32_e64 s[78:79], 0, v8\n"
"v_cmp_gt_i32_e64 s[80:81], 1, v8\n"
"v_cmp_gt_i32_e64 s[82:83], 2, v8\n"
"v_cmp_gt_i32_e64 s[84:85], 3, v8\n"
"v_cndmask_b32_e64 v48, v48, v30, s[78:79]\n"
"v_cndmask_b32_e64 v49, v49, v30, s[80:81]\n"
"v_cndmask_b32_e64 v50, v50, v30, s[82:83]\n"
"v_cndmask_b32_e64 v51, v51, v30, s[84:85]\n"
"v_cndmask_b32_e64 v60, v60, v30, s[78:79]\n"
"v_cndmask_b32_e64 v61, v61, v30, s[80:81]\n"
"v_cndmask_b32_e64 v62, v62, v30, s[82:83]\n"
"v_cndmask_b32_e64 v63, v63, v30, s[84:85]\n"
"v_cmp_gt_i32_e64 s[78:79], 16, v8\n"
"v_cmp_gt_i32_e64 s[80:81], 17, v8\n"
"v_cmp_gt_i32_e64 s[82:83], 18, v8\n"
"v_cmp_gt_i32_e64 s[84:85], 19, v8\n"
"v_cndmask_b32_e64 v56, v56, v30, s[78:79]\n"
"v_cndmask_b32_e64 v57, v57, v30, s[80:81]\n"
"v_cndmask_b32_e64 v58, v58, v30, s[82:83]\n"
"v_cndmask_b32_e64 v59, v59, v30, s[84:85]\n"
"v_cndmask_b32_e64 v68, v68, v30, s[78:79]\n"
"v_cndmask_b32_e64 v69, v69, v30, s[80:81]\n"
"v_cndmask_b32_e64 v70, v70, v30, s[82:83]\n"
"v_cndmask_b32_e64 v71, v71, v30, s[84:85]\n"
"v_cmp_gt_i32_e64 s[78:79], 32, v8\n"
"v_cmp_gt_i32_e64 s[80:81], 33, v8\n"
"v_cmp_gt_i32_e64 s[82:83], 34, v8\n"
"v_cmp_gt_i32_e64 s[84:85], 35, v8\n"
"v_cndmask_b32_e64 v64, v64, v30, s[78:79]\n"
"v_cndmask_b32_e64 v65, v65, v30, s[80:81]\n"
"v_cndmask_b32_e64 v66, v66, v30, s[82:83]\n"
"v_cndmask_b32_e64 v67, v67, v30, s[84:85]\n"
"v_cndmask_b32_e64 v76, v76, v30, s[78:79]\n"
"v_cndmask_b32_e64 v77, v77, v30, s[80:81]\n"
"v_cndmask_b32_e64 v78, v78, v30, s[82:83]\n"
"v_cndmask_b32_e64 v79, v79, v30, s[84:85]\n"
"v_cmp_gt_i32_e64 s[78:79], 48, v8\n"
"v_cmp_gt_i32_e64 s[80:81], 49, v8\n"
"v_cmp_gt_i32_e64 s[82:83], 50, v8\n"
"v_cmp_gt_i32_e64 s[84:85], 51, v8\n"
"v_cndmask_b32_e64 v72, v72, v30, s[78:79]\n"
"v_cndmask_b32_e64 v73, v73, v30, s[80:81]\n"
"v_cndmask_b32_e64 v74, v74, v30, s[82:83]\n"
"v_cndmask_b32_e64 v75, v75, v30, s[84:85]\n"
"L_nmp%=:\n"
"s_sub_u32 s75, s75, 64\n"
"v_max3_f32 v28, v48, v49, v50\n"
"v_max_f32 v28, v28, v51\n"
"v_max3_f32 v29, v52, v53, v54\n"
"v_max_f32 v29, v29, v55\n"
"v_max3_f32 v28, v28, v56, v57\n"
"v_max3_f32 v28, v28, v58, v59\n"
"v_max3_f32 v29, v29, v60, v61\n"
"v_max3_f32 v29, v29, v62, v63\n"
"v_max3_f32 v28, v28, v64, v65\n"
"v_max3_f32 v28, v28, v66, v67\n"
"v_max3_f32 v29, v29, v68, v69\n"
"v_max3_f32 v29, v29, v70, v71\n"
"v_max3_f32 v28, v28, v72, v73\n"
"v_max3_f32 v28, v28, v74, v75\n"
"v_max3_f32 v29, v29, v76, v77\n"
"v_max3_f32 v29, v29, v78, v79\n"
"v_mov_b32 v8, v28\n"
"v_mov_b32 v9, v29\n"
"s_nop 0\n"
"v_permlane32_swap_b32 v8, v28\n"
"v_permlane32_swap_b32 v9, v29\n"
"v_max_f32 v28, v28, v8\n"
"v_max_f32 v29, v29, v9\n"
"v_mov_b32 v8, v28\n"
"v_mov_b32 v9, v29\n"
"s_nop 0\n"
"v_permlane16_swap_b32 v8, v28\n"
"v_permlane16_swap_b32 v9, v29\n"
"v_max_f32 v28, v28, v8\n"
"v_max_f32 v29, v29, v9\n"
"v_sub_f32 v48, v48, v28\n"
"v_sub_f32 v49, v49, v28\n"
"v_sub_f32 v50, v50, v28\n"
"v_sub_f32 v51, v51, v28\n"
"v_sub_f32 v52, v52, v29\n"
"v_sub_f32 v53, v53, v29\n"
"v_sub_f32 v54, v54, v29\n"
"v_sub_f32 v55, v55, v29\n"
"v_sub_f32 v56, v56, v28\n"
"v_sub_f32 v57, v57, v28\n"
"v_sub_f32 v58, v58, v28\n"
"v_sub_f32 v59, v59, v28\n"
"v_sub_f32 v60, v60, v29\n"
"v_sub_f32 v61, v61, v29\n"
"v_sub_f32 v62, v62, v29\n"
"v_sub_f32 v63, v63, v29\n"
"v_sub_f32 v64, v64, v28\n"
"v_sub_f32 v65, v65, v28\n"
"v_sub_f32 v66, v66, v28\n"
"v_sub_f32 v67, v67, v28\n"
"v_sub_f32 v68, v68, v29\n"
"v_sub_f32 v69, v69, v29\n"
"v_sub_f32 v70, v70, v29\n"
"v_sub_f32 v71, v71, v29\n"
"v_sub_f32 v72, v72, v28\n"
"v_sub_f32 v73, v73, v28\n"
"v_sub_f32 v74, v74, v28\n"
"v_sub_f32 v75, v75, v28\n"
"v_sub_f32 v76, v76, v29\n"
"v_sub_f32 v77, v77, v29\n"
"v_sub_f32 v78, v78, v29\n"
"v_sub_f32 v79, v79, v29\n"
"v_sub_f32 v16, 0, v28\n"
"v_sub_f32 v17, 0, v28\n"
"v_sub_f32 v18, 0, v28\n"
"v_sub_f32 v19, 0, v28\n"
"v_sub_f32 v20, 0, v29\n"
"v_sub_f32 v21, 0, v29\n"
"v_sub_f32 v22, 0, v29\n"
"v_sub_f32 v23, 0, v29\n"
"s_nop 1\n"
"L_top%=:\n"
"s_waitcnt lgkmcnt(0)\n"
"v_mfma_f32_16x16x32_bf16 v[80:83], v[112:115], a[136:139], v[16:19]\n"
"ds_read_b128 v[144:147], v3 offset:40960\n"
"v_exp_f32 v48, v48\n"
"v_mfma_f32_16x16x32_bf16 v[84:87], v[112:115], a[168:171], v[20:23]\n"
"v_exp_f32 v49, v49\n"
"v_mfma_f32_16x16x32_bf16 v[80:83], v[116:119], a[140:143], v[80:83]\n"
"ds_read_b128 v[148:151], v3 offset:41984\n"
"v_exp_f32 v50, v50\n"
"v_mfma_f32_16x16x32_bf16 v[84:87], v[116:119], a[172:175], v[84:87]\n"
"buffer_load_dwordx4 v1, s[36:39], s64 offen offset:3072 lds\n"
"v_exp_f32 v51, v51\n"
"v_mfma_f32_16x16x32_bf16 v[80:83], v[120:123], a[144:147], v[80:83]\n"
"ds_read_b128 v[152:155], v3 offset:43008\n"
"v_cvt_pk_bf16_f32 v32, v48, v49\n"
"v_mfma_f32_16x16x32_bf16 v[84:87], v[120:123], a[176:179], v[84:87]\n"
"v_cvt_pk_bf16_f32 v33, v50, v51\n"
"v_mfma_f32_16x16x32_bf16 v[80:83], v[124:127], a[148:151], v[80:83]\n"
"ds_read_b128 v[156:159], v3 offset:44032\n"
"v_exp_f32 v52, v52\n"
"v_mfma_f32_16x16x32_bf16 v[84:87], v[124:127], a[180:183], v[84:87]\n"
"v_exp_f32 v53, v53\n"
"v_mfma_f32_16x16x32_bf16 v[80:83], v[128:131], a[152:155], v[80:83]\n"
"ds_read_b128 v[160:163], v3 offset:45056\n"
"s_add_u32 m0, m0, 4096\n"
"v_exp_f32 v54, v54\n"
"v_mfma_f32_16x16x32_bf16 v[84:87], v[128:131], a[184:187], v[84:87]\n"
"buffer_load_dwordx4 v2, s[36:39], s64 offen offset:0 lds\n"
"v_exp_f32 v55, v55\n"
"v_mfma_f32_16x16x32_bf16 v[80:83], v[132:135], a[156:159], v[80:83]\n"
"ds_read_b128 v[164:167], v3 offset:46080\n"
"v_cvt_pk_bf16_f32 v36, v52, v53\n"
"v_mfma_f32_16x16x32_bf16 v[84:87], v[132:135], a[188:191], v[84:87]\n"
"v_cvt_pk_bf16_f32 v37, v54, v55\n"
"v_mfma_f32_16x16x32_bf16 v[80:83], v[136:139], a[160:163], v[80:83]\n"
"ds_read_b128 v[168:171], v3 offset:47104\n"
"v_exp_f32 v56, v56\n"
"v_mfma_f32_16x16x32_bf16 v[84:87], v[136:139], a[192:195], v[84:87]\n"
"v_exp_f32 v57, v57\n"
"v_mfma_f32_16x16x32_bf16 v[80:83], v[140:143], a[164:167], v[80:83]\n"
"ds_read_b128 v[172:175], v3 offset:48128\n"
"v_exp_f32 v58, v58\n"
"v_mfma_f32_16x16x32_bf16 v[84:87], v[140:143], a[196:199], v[84:87]\n"
"buffer_load_dwordx4 v2, s[36:39], s64 offen offset:1024 lds\n"
"v_exp_f32 v59, v59\n"
"s_waitcnt lgkmcnt(1)\n"
"v_mfma_f32_16x16x32_bf16 v[88:91], v[144:147], a[136:139], v[16:19]\n"
"ds_read_b128 v[112:115], v3 offset:49152\n"
"v_cvt_pk_bf16_f32 v34, v56, v57\n"
"v_mfma_f32_16x16x32_bf16 v[92:95], v[144:147], a[168:171], v[20:23]\n"
"v_cvt_pk_bf16_f32 v35, v58, v59\n"
"v_mfma_f32_16x16x32_bf16 v[88:91], v[148:151], a[140:143], v[88:91]\n"
"ds_read_b128 v[116:119], v3 offset:50176\n"
"v_exp_f32 v60, v60\n"
"v_mfma_f32_16x16x32_bf16 v[92:95], v[148:151], a[172:175], v[92:95]\n"
"v_exp_f32 v61, v61\n"
"v_mfma_f32_16x16x32_bf16 v[88:91], v[152:155], a[144:147], v[88:91]\n"
"ds_read_b128 v[120:123], v3 offset:51200\n"
"v_exp_f32 v62, v62\n"
"v_mfma_f32_16x16x32_bf16 v[92:95], v[152:155], a[176:179], v[92:95]\n"
"buffer_load_dwordx4 v2, s[36:39], s64 offen offset:2048 lds\n"
"v_exp_f32 v63, v63\n"
"v_mfma_f32_16x16x32_bf16 v[88:91], v[156:159], a[148:151], v[88:91]\n"
"ds_read_b128 v[124:127], v3 offset:52224\n"
"v_cvt_pk_bf16_f32 v38, v60, v61\n"
"v_mfma_f32_16x16x32_bf16 v[92:95], v[156:159], a[180:183], v[92:95]\n"
"v_cvt_pk_bf16_f32 v39, v62, v63\n"
"v_mfma_f32_16x16x32_bf16 v[88:91], v[160:163], a[152:155], v[88:91]\n"
"ds_read_b128 v[128:131], v3 offset:53248\n"
"v_exp_f32 v64, v64\n"
"v_mfma_f32_16x16x32_bf16 v[92:95], v[160:163], a[184:187], v[92:95]\n"
"v_exp_f32 v65, v65\n"
"v_mfma_f32_16x16x32_bf16 v[88:91], v[164:167], a[156:159], v[88:91]\n"
"ds_read_b128 v[132:135], v3 offset:54272\n"
"v_exp_f32 v66, v66\n"
"v_mfma_f32_16x16x32_bf16 v[92:95], v[164:167], a[188:191], v[92:95]\n"
"buffer_load_dwordx4 v2, s[36:39], s64 offen offset:3072 lds\n"
"v_exp_f32 v67, v67\n"
"v_mfma_f32_16x16x32_bf16 v[88:91], v[168:171], a[160:163], v[88:91]\n"
"ds_read_b128 v[136:139], v3 offset:55296\n"
"v_cvt_pk_bf16_f32 v40, v64, v65\n"
"v_mfma_f32_16x16x32_bf16 v[92:95], v[168:171], a[192:195], v[92:95]\n"
"s_add_u32 s64, s64, 65536\n"
"s_sub_u32 s65, s65, 1\n"
"s_cmp_eq_u32 s65, 0\n"
"s_cselect_b32 s64, s68, s64\n"
"v_cvt_pk_bf16_f32 v41, v66, v67\n"
"s_waitcnt lgkmcnt(1)\n"
"v_mfma_f32_16x16x32_bf16 v[88:91], v[172:175], a[164:167], v[88:91]\n"
"ds_read_b128 v[140:143], v3 offset:56320\n"
"v_exp_f32 v68, v68\n"
"v_mfma_f32_16x16x32_bf16 v[92:95], v[172:175], a[196:199], v[92:95]\n"
"v_exp_f32 v69, v69\n"
"v_mfma_f32_16x16x32_bf16 v[96:99], v[112:115], a[136:139], v[16:19]\n"
"ds_read_b128 v[144:147], v3 offset:57344\n"
"v_exp_f32 v70, v70\n"
"v_mfma_f32_16x16x32_bf16 v[100:103], v[112:115], a[168:171], v[20:23]\n"
"v_exp_f32 v71, v71\n"
"v_mfma_f32_16x16x32_bf16 v[96:99], v[116:119], a[140:143], v[96:99]\n"
"ds_read_b128 v[148:151], v3 offset:58368\n"
"v_cvt_pk_bf16_f32 v44, v68, v69\n"
"v_mfma_f32_16x16x32_bf16 v[100:103], v[116:119], a[172:175], v[100:103]\n"
"v_cvt_pk_bf16_f32 v45, v70, v71\n"
"v_mfma_f32_16x16x32_bf16 v[96:99], v[120:123], a[144:147], v[96:99]\n"
"ds_read_b128 v[152:155], v3 offset:59392\n"
"v_exp_f32 v72, v72\n"
"v_mfma_f32_16x16x32_bf16 v[100:103], v[120:123], a[176:179], v[100:103]\n"
"v_exp_f32 v73, v73\n"
"v_mfma_f32_16x16x32_bf16 v[96:99], v[124:127], a[148:151], v[96:99]\n"
"ds_read_b128 v[156:159], v3 offset:60416\n"
"v_exp_f32 v74, v74\n"
"v_mfma_f32_16x16x32_bf16 v[100:103], v[124:127], a[180:183], v[100:103]\n"
"v_exp_f32 v75, v75\n"
"v_mfma_f32_16x16x32_bf16 v[96:99], v[128:131], a[152:155], v[96:99]\n"
"ds_read_b128 v[160:163], v3 offset:61440\n"
"v_cvt_pk_bf16_f32 v42, v72, v73\n"
"v_mfma_f32_16x16x32_bf16 v[100:103], v[128:131], a[184:187], v[100:103]\n"
"v_cvt_pk_bf16_f32 v43, v74, v75\n"
"v_mfma_f32_16x16x32_bf16 v[96:99], v[132:135], a[156:159], v[96:99]\n"
"ds_read_b128 v[164:167], v3 offset:62464\n"
"v_exp_f32 v76, v76\n"
"v_mfma_f32_16x16x32_bf16 v[100:103], v[132:135], a[188:191], v[100:103]\n"
"v_exp_f32 v77, v77\n"
"s_waitcnt lgkmcnt(2)\n"
"v_mfma_f32_16x16x32_bf16 v[96:99], v[136:139], a[160:163], v[96:99]\n"
"ds_read_b128 v[168:171], v3 offset:63488\n"
"v_exp_f32 v78, v78\n"
"v_mfma_f32_16x16x32_bf16 v[100:103], v[136:139], a[192:195], v[100:103]\n"
"v_exp_f32 v79, v79\n"
"v_mfma_f32_16x16x32_bf16 v[96:99], v[140:143], a[164:167], v[96:99]\n"
"ds_read_b128 v[172:175], v3 offset:64512\n"
"v_cvt_pk_bf16_f32 v46, v76, v77\n"
"v_mfma_f32_16x16x32_bf16 v[100:103], v[140:143], a[196:199], v[100:103]\n"
"s_waitcnt vmcnt(8)\n"
"s_barrier\n"
"s_cmp_eq_u32 s74, 0\n"
"s_cbranch_scc1 L_nfl%=\n"
"v_mov_b32 v12, 1\n"
"s_mov_b64 exec, 1\n"
"buffer_atomic_add v12, off, s[48:51], s74 sc1\n"
"s_mov_b64 exec, -1\n"
"s_mov_b32 s74, 0\n"
"L_nfl%=:\n"
"v_cvt_pk_bf16_f32 v47, v78, v79\n"
"v_mfma_f32_16x16x32_bf16 v[104:107], v[144:147], a[136:139], v[16:19]\n"
"ds_read_b128 v[112:115], v4 offset:0\n"
"s_mov_b32 m0, s72\n"
"v_mfma_f32_16x16x32_bf16 v[108:111], v[144:147], a[168:171], v[20:23]\n"
"buffer_load_dwordx4 v1, s[36:39], s66 offen offset:0 lds\n"
"v_mfma_f32_16x16x32_bf16 v[104:107], v[148:151], a[140:143], v[104:107]\n"
"ds_read_b128 v[116:119], v4 offset:1024\n"
"v_mfma_f32_16x16x32_bf16 v[108:111], v[148:151], a[172:175], v[108:111]\n"
"v_mfma_f32_16x16x32_bf16 v[104:107], v[152:155], a[144:147], v[104:107]\n"
"ds_read_b128 v[120:123], v4 offset:2048\n"
"v_mfma_f32_16x16x32_bf16 v[108:111], v[152:155], a[176:179], v[108:111]\n"
"v_mfma_f32_16x16x32_bf16 v[104:107], v[156:159], a[148:151], v[104:107]\n"
"ds_read_b128 v[124:127], v4 offset:3072\n"
"v_mfma_f32_16x16x32_bf16 v[108:111], v[156:159], a[180:183], v[108:111]\n"
"buffer_load_dwordx4 v1, s[36:39], s66 offen offset:1024 lds\n"
"s_waitcnt lgkmcnt(3)\n"
"v_mfma_f32_16x16x32_bf16 v[104:107], v[160:163], a[152:155], v[104:107]\n"
"ds_read_b128 v[128:131], v4 offset:4096\n"
"v_mfma_f32_16x16x32_bf16 v[108:111], v[160:163], a[184:187], v[108:111]\n"
"v_mfma_f32_16x16x32_bf16 v[104:107], v[164:167], a[156:159], v[104:107]\n"
"ds_read_b128 v[132:135], v4 offset:5120\n"
"v_mfma_f32_16x16x32_bf16 v[108:111], v[164:167], a[188:191], v[108:111]\n"
"v_mfma_f32_16x16x32_bf16 v[104:107], v[168:171], a[160:163], v[104:107]\n"
"ds_read_b128 v[136:139], v4 offset:6144\n"
"v_mfma_f32_16x16x32_bf16 v[108:111], v[168:171], a[192:195], v[108:111]\n"
"buffer_load_dwordx4 v1, s[36:39], s66 offen offset:2048 lds\n"
"v_mfma_f32_16x16x32_bf16 v[104:107], v[172:175], a[164:167], v[104:107]\n"
"ds_read_b128 v[140:143], v4 offset:7168\n"
"v_mfma_f32_16x16x32_bf16 v[108:111], v[172:175], a[196:199], v[108:111]\n"
"s_cmp_gt_i32 s75, 0\n"
"s_cbranch_scc1 L_nmm0%=\n"
"s_nop 7\n"
"v_add_u32 v8, s75, v5\n"
"v_cmp_gt_i32_e64 s[78:79], -16, v8\n"
"v_cmp_gt_i32_e64 s[80:81], -15, v8\n"
"v_cmp_gt_i32_e64 s[82:83], -14, v8\n"
"v_cmp_gt_i32_e64 s[84:85], -13, v8\n"
"v_cndmask_b32_e64 v84, v84, v30, s[78:79]\n"
"v_cndmask_b32_e64 v85, v85, v30, s[80:81]\n"
"v_cndmask_b32_e64 v86, v86, v30, s[82:83]\n"
"v_cndmask_b32_e64 v87, v87, v30, s[84:85]\n"
"v_cmp_gt_i32_e64 s[78:79], 0, v8\n"
"v_cmp_gt_i32_e64 s[80:81], 1, v8\n"
"v_cmp_gt_i32_e64 s[82:83], 2, v8\n"
"v_cmp_gt_i32_e64 s[84:85], 3, v8\n"
"v_cndmask_b32_e64 v80, v80, v30, s[78:79]\n"
"v_cndmask_b32_e64 v81, v81, v30, s[80:81]\n"
"v_cndmask_b32_e64 v82, v82, v30, s[82:83]\n"
"v_cndmask_b32_e64 v83, v83, v30, s[84:85]\n"
"v_cndmask_b32_e64 v92, v92, v30, s[78:79]\n"
"v_cndmask_b32_e64 v93, v93, v30, s[80:81]\n"
"v_cndmask_b32_e64 v94, v94, v30, s[82:83]\n"
"v_cndmask_b32_e64 v95, v95, v30, s[84:85]\n"
"v_cmp_gt_i32_e64 s[78:79], 16, v8\n"
"v_cmp_gt_i32_e64 s[80:81], 17, v8\n"
"v_cmp_gt_i32_e64 s[82:83], 18, v8\n"
"v_cmp_gt_i32_e64 s[84:85], 19, v8\n"
"v_cndmask_b32_e64 v88, v88, v30, s[78:79]\n"
"v_cndmask_b32_e64 v89, v89, v30, s[80:81]\n"
"v_cndmask_b32_e64 v90, v90, v30, s[82:83]\n"
"v_cndmask_b32_e64 v91, v91, v30, s[84:85]\n"
"v_cndmask_b32_e64 v100, v100, v30, s[78:79]\n"
"v_cndmask_b32_e64 v101, v101, v30, s[80:81]\n"
"v_cndmask_b32_e64 v102, v102, v30, s[82:83]\n"
"v_cndmask_b32_e64 v103, v103, v30, s[84:85]\n"
"v_cmp_gt_i32_e64 s[78:79], 32, v8\n"
"v_cmp_gt_i32_e64 s[80:81], 33, v8\n"
"v_cmp_gt_i32_e64 s[82:83], 34, v8\n"
"v_cmp_gt_i32_e64 s[84:85], 35, v8\n"
"v_cndmask_b32_e64 v96, v96, v30, s[78:79]\n"
"v_cndmask_b32_e64 v97, v97, v30, s[80:81]\n"
"v_cndmask_b32_e64 v98, v98, v30, s[82:83]\n"
"v_cndmask_b32_e64 v99, v99, v30, s[84:85]\n"
"v_cndmask_b32_e64 v108, v108, v30, s[78:79]\n"
"v_cndmask_b32_e64 v109, v109, v30, s[80:81]\n"
"v_cndmask_b32_e64 v110, v110, v30, s[82:83]\n"
"v_cndmask_b32_e64 v111, v111, v30, s[84:85]\n"
"v_cmp_gt_i32_e64 s[78:79], 48, v8\n"
"v_cmp_gt_i32_e64 s[80:81], 49, v8\n"
"v_cmp_gt_i32_e64 s[82:83], 50, v8\n"
"v_cmp_gt_i32_e64 s[84:85], 51, v8\n"
"v_cndmask_b32_e64 v104, v104, v30, s[78:79]\n"
"v_cndmask_b32_e64 v105, v105, v30, s[80:81]\n"
"v_cndmask_b32_e64 v106, v106, v30, s[82:83]\n"
"v_cndmask_b32_e64 v107, v107, v30, s[84:85]\n"
"L_nmm0%=:\n"
"s_sub_u32 s75, s75, 64\n"
"s_waitcnt lgkmcnt(0)\n"
"v_mfma_f32_16x16x32_bf16 a[0:3], v[112:115], v[32:35], a[0:3]\n"
"ds_read_b128 v[144:147], v4 offset:8192\n"
"v_mfma_f32_16x16x32_bf16 a[64:67], v[112:115], v[36:39], a[64:67]\n"
"v_mfma_f32_16x16x32_bf16 a[4:7], v[116:119], v[32:35], a[4:7]\n"
"ds_read_b128 v[148:151], v4 offset:9216\n"
"v_mfma_f32_16x16x32_bf16 a[68:71], v[116:119], v[36:39], a[68:71]\n"
"buffer_load_dwordx4 v1, s[36:39], s66 offen offset:3072 lds\n"
"v_max3_f32 v28, v80, v81, v82\n"
"v_mfma_f32_16x16x32_bf16 a[8:11], v[120:123], v[32:35], a[8:11]\n"
"ds_read_b128 v[152:155], v4 offset:10240\n"
"v_mfma_f32_16x16x32_bf16 a[72:75], v[120:123], v[36:39], a[72:75]\n"
"v_max_f32 v28, v28, v83\n"
"v_mfma_f32_16x16x32_bf16 a[12:15], v[124:127], v[32:35], a[12:15]\n"
"ds_read_b128 v[156:159], v4 offset:11264\n"
"v_mfma_f32_16x16x32_bf16 a[76:79], v[124:127], v[36:39], a[76:79]\n"
"v_max3_f32 v29, v84, v85, v86\n"
"v_mfma_f32_16x16x32_bf16 a[128:131], v[24:27], v[32:35], a[128:131]\n"
"s_add_u32 m0, m0, 4096\n"
"v_mfma_f32_16x16x32_bf16 a[132:135], v[24:27], v[36:39], a[132:135]\n"
"buffer_load_dwordx4 v2, s[36:39], s66 offen offset:0 lds\n"
"v_max_f32 v29, v29, v87\n"
"v_mfma_f32_16x16x32_bf16 a[16:19], v[128:131], v[32:35], a[16:19]\n"
"ds_read_b128 v[160:163], v4 offset:12288\n"
"v_mfma_f32_16x16x32_bf16 a[80:83], v[128:131], v[36:39], a[80:83]\n"
"v_max3_f32 v28, v28, v88, v89\n"
"v_mfma_f32_16x16x32_bf16 a[20:23], v[132:135], v[32:35], a[20:23]\n"
"ds_read_b128 v[164:167], v4 offset:13312\n"
"v_mfma_f32_16x16x32_bf16 a[84:87], v[132:135], v[36:39], a[84:87]\n"
"v_max3_f32 v28, v28, v90, v91\n"
"v_mfma_f32_16x16x32_bf16 a[24:27], v[136:139], v[32:35], a[24:27]\n"
"ds_read_b128 v[168:171], v4 offset:14336\n"
"v_mfma_f32_16x16x32_bf16 a[88:91], v[136:139], v[36:39], a[88:91]\n"
"buffer_load_dwordx4 v2, s[36:39], s66 offen offset:1024 lds\n"
"v_max3_f32 v29, v29, v92, v93\n"
"v_mfma_f32_16x16x32_bf16 a[28:31], v[140:143], v[32:35], a[28:31]\n"
"ds_read_b128 v[172:175], v4 offset:15360\n"
"v_mfma_f32_16x16x32_bf16 a[92:95], v[140:143], v[36:39], a[92:95]\n"
"v_max3_f32 v29, v29, v94, v95\n"
"s_waitcnt lgkmcnt(2)\n"
"v_mfma_f32_16x16x32_bf16 a[32:35], v[144:147], v[32:35], a[32:35]\n"
"ds_read_b128 v[112:115], v4 offset:16384\n"
"v_mfma_f32_16x16x32_bf16 a[96:99], v[144:147], v[36:39], a[96:99]\n"
"v_max3_f32 v28, v28, v96, v97\n"
"v_mfma_f32_16x16x32_bf16 a[36:39], v[148:151], v[32:35], a[36:39]\n"
"ds_read_b128 v[116:119], v4 offset:17408\n"
"v_mfma_f32_16x16x32_bf16 a[100:103], v[148:151], v[36:39], a[100:103]\n"
"buffer_load_dwordx4 v2, s[36:39], s66 offen offset:2048 lds\n"
"v_max3_f32 v28, v28, v98, v99\n"
"v_mfma_f32_16x16x32_bf16 a[40:43], v[152:155], v[32:35], a[40:43]\n"
"ds_read_b128 v[120:123], v4 offset:18432\n"
"v_mfma_f32_16x16x32_bf16 a[104:107], v[152:155], v[36:39], a[104:107]\n"
"v_max3_f32 v29, v29, v100, v101\n"
"v_mfma_f32_16x16x32_bf16 a[44:47], v[156:159], v[32:35], a[44:47]\n"
"ds_read_b128 v[124:127], v4 offset:19456\n"
"v_mfma_f32_16x16x32_bf16 a[108:111], v[156:159], v[36:39], a[108:111]\n"
"v_max3_f32 v29, v29, v102, v103\n"
"v_mfma_f32_16x16x32_bf16 a[48:51], v[160:163], v[32:35], a[48:51]\n"
"ds_read_b128 v[128:131], v4 offset:20480\n"
"v_mfma_f32_16x16x32_bf16 a[112:115], v[160:163], v[36:39], a[112:115]\n"
"buffer_load_dwordx4 v2, s[36:39], s66 offen offset:3072 lds\n"
"v_max3_f32 v28, v28, v104, v105\n"
"v_mfma_f32_16x16x32_bf16 a[52:55], v[164:167], v[32:35], a[52:55]\n"
"ds_read_b128 v[132:135], v4 offset:21504\n"
"v_mfma_f32_16x16x32_bf16 a[116:119], v[164:167], v[36:39], a[116:119]\n"
"s_add_u32 s66, s66, 65536\n"
"s_sub_u32 s67, s67, 1\n"
"s_cmp_eq_u32 s67, 0\n"
"s_cselect_b32 s66, s68, s66\n"
"s_or_b32 s66, s66, 32768\n"
"v_max3_f32 v28, v28, v106, v107\n"
"s_waitcnt lgkmcnt(1)\n"
"v_mfma_f32_16x16x32_bf16 a[56:59], v[168:171], v[32:35], a[56:59]\n"
"ds_read_b128 v[136:139], v4 offset:22528\n"
"v_mfma_f32_16x16x32_bf16 a[120:123], v[168:171], v[36:39], a[120:123]\n"
"v_max3_f32 v29, v29, v108, v109\n"
"v_mfma_f32_16x16x32_bf16 a[60:63], v[172:175], v[32:35], a[60:63]\n"
"ds_read_b128 v[140:143], v4 offset:23552\n"
"v_mfma_f32_16x16x32_bf16 a[124:127], v[172:175], v[36:39], a[124:127]\n"
"v_max3_f32 v29, v29, v110, v111\n"
"v_mfma_f32_16x16x32_bf16 a[0:3], v[112:115], v[40:43], a[0:3]\n"
"ds_read_b128 v[144:147], v4 offset:24576\n"
"v_mfma_f32_16x16x32_bf16 a[64:67], v[112:115], v[44:47], a[64:67]\n"
"v_mfma_f32_16x16x32_bf16 a[4:7], v[116:119], v[40:43], a[4:7]\n"
"ds_read_b128 v[148:151], v4 offset:25600\n"
"v_mfma_f32_16x16x32_bf16 a[68:71], v[116:119], v[44:47], a[68:71]\n"
"v_max_f32 v8, v28, v29\n"
"v_mfma_f32_16x16x32_bf16 a[8:11], v[120:123], v[40:43], a[8:11]\n"
"ds_read_b128 v[152:155], v4 offset:26624\n"
"v_mfma_f32_16x16x32_bf16 a[72:75], v[120:123], v[44:47], a[72:75]\n"
"v_cmp_lt_f32 vcc, s73, v8\n"
"v_mfma_f32_16x16x32_bf16 a[12:15], v[124:127], v[40:43], a[12:15]\n"
"ds_read_b128 v[156:159], v4 offset:27648\n"
"v_mfma_f32_16x16x32_bf16 a[76:79], v[124:127], v[44:47], a[76:79]\n"
"v_mfma_f32_16x16x32_bf16 a[128:131], v[24:27], v[40:43], a[128:131]\n"
"v_mfma_f32_16x16x32_bf16 a[132:135], v[24:27], v[44:47], a[132:135]\n"
"v_mfma_f32_16x16x32_bf16 a[16:19], v[128:131], v[40:43], a[16:19]\n"
"ds_read_b128 v[160:163], v4 offset:28672\n"
"v_mfma_f32_16x16x32_bf16 a[80:83], v[128:131], v[44:47], a[80:83]\n"
"s_waitcnt lgkmcnt(2)\n"
"v_mfma_f32_16x16x32_bf16 a[20:23], v[132:135], v[40:43], a[20:23]\n"
"ds_read_b128 v[164:167], v4 offset:29696\n"
"v_mfma_f32_16x16x32_bf16 a[84:87], v[132:135], v[44:47], a[84:87]\n"
"v_mfma_f32_16x16x32_bf16 a[24:27], v[136:139], v[40:43], a[24:27]\n"
"ds_read_b128 v[168:171], v4 offset:30720\n"
"v_mfma_f32_16x16x32_bf16 a[88:91], v[136:139], v[44:47], a[88:91]\n"
"v_mfma_f32_16x16x32_bf16 a[28:31], v[140:143], v[40:43], a[28:31]\n"
"ds_read_b128 v[172:175], v4 offset:31744\n"
"v_mfma_f32_16x16x32_bf16 a[92:95], v[140:143], v[44:47], a[92:95]\n"
"s_waitcnt vmcnt(8)\n"
"s_barrier\n"
"v_mfma_f32_16x16x32_bf16 a[32:35], v[144:147], v[40:43], a[32:35]\n"
"ds_read_b128 v[112:115], v3 offset:0\n"
"s_mov_b32 m0, s70\n"
"v_mfma_f32_16x16x32_bf16 a[96:99], v[144:147], v[44:47], a[96:99]\n"
"buffer_load_dwordx4 v1, s[36:39], s64 offen offset:0 lds\n"
"v_mfma_f32_16x16x32_bf16 a[36:39], v[148:151], v[40:43], a[36:39]\n"
"ds_read_b128 v[116:119], v3 offset:1024\n"
"v_mfma_f32_16x16x32_bf16 a[100:103], v[148:151], v[44:47], a[100:103]\n"
"v_mfma_f32_16x16x32_bf16 a[40:43], v[152:155], v[40:43], a[40:43]\n"
"ds_read_b128 v[120:123], v3 offset:2048\n"
"v_mfma_f32_16x16x32_bf16 a[104:107], v[152:155], v[44:47], a[104:107]\n"
"s_waitcnt lgkmcnt(2)\n"
"v_mfma_f32_16x16x32_bf16 a[44:47], v[156:159], v[40:43], a[44:47]\n"
"ds_read_b128 v[124:127], v3 offset:3072\n"
"v_mfma_f32_16x16x32_bf16 a[108:111], v[156:159], v[44:47], a[108:111]\n"
"buffer_load_dwordx4 v1, s[36:39], s64 offen offset:1024 lds\n"
"v_mfma_f32_16x16x32_bf16 a[48:51], v[160:163], v[40:43], a[48:51]\n"
"ds_read_b128 v[128:131], v3 offset:4096\n"
"v_mfma_f32_16x16x32_bf16 a[112:115], v[160:163], v[44:47], a[112:115]\n"
"v_mfma_f32_16x16x32_bf16 a[52:55], v[164:167], v[40:43], a[52:55]\n"
"ds_read_b128 v[132:135], v3 offset:5120\n"
"v_mfma_f32_16x16x32_bf16 a[116:119], v[164:167], v[44:47], a[116:119]\n"
"v_mfma_f32_16x16x32_bf16 a[56:59], v[168:171], v[40:43], a[56:59]\n"
"ds_read_b128 v[136:139], v3 offset:6144\n"
"v_mfma_f32_16x16x32_bf16 a[120:123], v[168:171], v[44:47], a[120:123]\n"
"buffer_load_dwordx4 v1, s[36:39], s64 offen offset:2048 lds\n"
"v_mfma_f32_16x16x32_bf16 a[60:63], v[172:175], v[40:43], a[60:63]\n"
"ds_read_b128 v[140:143], v3 offset:7168\n"
"v_mfma_f32_16x16x32_bf16 a[124:127], v[172:175], v[44:47], a[124:127]\n"
"s_cbranch_vccnz L_sl0%=\n"
"L_r0%=:\n"
"s_sub_u32 s61, s61, 2\n"
"s_cmp_eq_u32 s61, 0\n"
"s_cbranch_scc1 L_tail%=\n"
"s_waitcnt lgkmcnt(0)\n"
"v_mfma_f32_16x16x32_bf16 v[48:51], v[112:115], a[136:139], v[16:19]\n"
"ds_read_b128 v[144:147], v3 offset:8192\n"
"v_exp_f32 v80, v80\n"
"v_mfma_f32_16x16x32_bf16 v[52:55], v[112:115], a[168:171], v[20:23]\n"
"v_exp_f32 v81, v81\n"
"v_mfma_f32_16x16x32_bf16 v[48:51], v[116:119], a[140:143], v[48:51]\n"
"ds_read_b128 v[148:151], v3 offset:9216\n"
"v_exp_f32 v82, v82\n"
"v_mfma_f32_16x16x32_bf16 v[52:55], v[116:119], a[172:175], v[52:55]\n"
"buffer_load_dwordx4 v1, s[36:39], s64 offen offset:3072 lds\n"
"v_exp_f32 v83, v83\n"
"v_mfma_f32_16x16x32_bf16 v[48:51], v[120:123], a[144:147], v[48:51]\n"
"ds_read_b128 v[152:155], v3 offset:10240\n"
"v_cvt_pk_bf16_f32 v32, v80, v81\n"
"v_mfma_f32_16x16x32_bf16 v[52:55], v[120:123], a[176:179], v[52:55]\n"
"v_cvt_pk_bf16_f32 v33, v82, v83\n"
"v_mfma_f32_16x16x32_bf16 v[48:51], v[124:127], a[148:151], v[48:51]\n"
"ds_read_b128 v[156:159], v3 offset:11264\n"
"v_exp_f32 v84, v84\n"
"v_mfma_f32_16x16x32_bf16 v[52:55], v[124:127], a[180:183], v[52:55]\n"
"v_exp_f32 v85, v85\n"
"v_mfma_f32_16x16x32_bf16 v[48:51], v[128:131], a[152:155], v[48:51]\n"
"ds_read_b128 v[160:163], v3 offset:12288\n"
"s_add_u32 m0, m0, 4096\n"
"v_exp_f32 v86, v86\n"
"v_mfma_f32_16x16x32_bf16 v[52:55], v[128:131], a[184:187], v[52:55]\n"
"buffer_load_dwordx4 v2, s[36:39], s64 offen offset:0 lds\n"
"v_exp_f32 v87, v87\n"
"v_mfma_f32_16x16x32_bf16 v[48:51], v[132:135], a[156:159], v[48:51]\n"
"ds_read_b128 v[164:167], v3 offset:13312\n"
"v_cvt_pk_bf16_f32 v36, v84, v85\n"
"v_mfma_f32_16x16x32_bf16 v[52:55], v[132:135], a[188:191], v[52:55]\n"
"v_cvt_pk_bf16_f32 v37, v86, v87\n"
"v_mfma_f32_16x16x32_bf16 v[48:51], v[136:139], a[160:163], v[48:51]\n"
"ds_read_b128 v[168:171], v3 offset:14336\n"
"v_exp_f32 v88, v88\n"
"v_mfma_f32_16x16x32_bf16 v[52:55], v[136:139], a[192:195], v[52:55]\n"
"v_exp_f32 v89, v89\n"
"v_mfma_f32_16x16x32_bf16 v[48:51], v[140:143], a[164:167], v[48:51]\n"
"ds_read_b128 v[172:175], v3 offset:15360\n"
"v_exp_f32 v90, v90\n"
"v_mfma_f32_16x16x32_bf16 v[52:55], v[140:143], a[196:199], v[52:55]\n"
"buffer_load_dwordx4 v2, s[36:39], s64 offen offset:1024 lds\n"
"v_exp_f32 v91, v91\n"
"s_waitcnt lgkmcnt(1)\n"
"v_mfma_f32_16x16x32_bf16 v[56:59], v[144:147], a[136:139], v[16:19]\n"
"ds_read_b128 v[112:115], v3 offset:16384\n"
"v_cvt_pk_bf16_f32 v34, v88, v89\n"
"v_mfma_f32_16x16x32_bf16 v[60:63], v[144:147], a[168:171], v[20:23]\n"
"v_cvt_pk_bf16_f32 v35, v90, v91\n"
"v_mfma_f32_16x16x32_bf16 v[56:59], v[148:151], a[140:143], v[56:59]\n"
"ds_read_b128 v[116:119], v3 offset:17408\n"
"v_exp_f32 v92, v92\n"
"v_mfma_f32_16x16x32_bf16 v[60:63], v[148:151], a[172:175], v[60:63]\n"
"v_exp_f32 v93, v93\n"
"v_mfma_f32_16x16x32_bf16 v[56:59], v[152:155], a[144:147], v[56:59]\n"
"ds_read_b128 v[120:123], v3 offset:18432\n"
"v_exp_f32 v94, v94\n"
"v_mfma_f32_16x16x32_bf16 v[60:63], v[152:155], a[176:179], v[60:63]\n"
"buffer_load_dwordx4 v2, s[36:39], s64 offen offset:2048 lds\n"
"v_exp_f32 v95, v95\n"
"v_mfma_f32_16x16x32_bf16 v[56:59], v[156:159], a[148:151], v[56:59]\n"
"ds_read_b128 v[124:127], v3 offset:19456\n"
"v_cvt_pk_bf16_f32 v38, v92, v93\n"
"v_mfma_f32_16x16x32_bf16 v[60:63], v[156:159], a[180:183], v[60:63]\n"
"v_cvt_pk_bf16_f32 v39, v94, v95\n"
"v_mfma_f32_16x16x32_bf16 v[56:59], v[160:163], a[152:155], v[56:59]\n"
"ds_read_b128 v[128:131], v3 offset:20480\n"
"v_exp_f32 v96, v96\n"
"v_mfma_f32_16x16x32_bf16 v[60:63], v[160:163], a[184:187], v[60:63]\n"
"v_exp_f32 v97, v97\n"
"v_mfma_f32_16x16x32_bf16 v[56:59], v[164:167], a[156:159], v[56:59]\n"
"ds_read_b128 v[132:135], v3 offset:21504\n"
"v_exp_f32 v98, v98\n"
"v_mfma_f32_16x16x32_bf16 v[60:63], v[164:167], a[188:191], v[60:63]\n"
"buffer_load_dwordx4 v2, s[36:39], s64 offen offset:3072 lds\n"
"v_exp_f32 v99, v99\n"
"v_mfma_f32_16x16x32_bf16 v[56:59], v[168:171], a[160:163], v[56:59]\n"
"ds_read_b128 v[136:139], v3 offset:22528\n"
"v_cvt_pk_bf16_f32 v40, v96, v97\n"
"v_mfma_f32_16x16x32_bf16 v[60:63], v[168:171], a[192:195], v[60:63]\n"
"s_add_u32 s64, s64, 65536\n"
"s_sub_u32 s65, s65, 1\n"
"s_cmp_eq_u32 s65, 0\n"
"s_cselect_b32 s64, s68, s64\n"
"v_cvt_pk_bf16_f32 v41, v98, v99\n"
"s_waitcnt lgkmcnt(1)\n"
"v_mfma_f32_16x16x32_bf16 v[56:59], v[172:175], a[164:167], v[56:59]\n"
"ds_read_b128 v[140:143], v3 offset:23552\n"
"v_exp_f32 v100, v100\n"
"v_mfma_f32_16x16x32_bf16 v[60:63], v[172:175], a[196:199], v[60:63]\n"
"v_exp_f32 v101, v101\n"
"v_mfma_f32_16x16x32_bf16 v[64:67], v[112:115], a[136:139], v[16:19]\n"
"ds_read_b128 v[144:147], v3 offset:24576\n"
"v_exp_f32 v102, v102\n"
"v_mfma_f32_16x16x32_bf16 v[68:71], v[112:115], a[168:171], v[20:23]\n"
"v_exp_f32 v103, v103\n"
"v_mfma_f32_16x16x32_bf16 v[64:67], v[116:119], a[140:143], v[64:67]\n"
"ds_read_b128 v[148:151], v3 offset:25600\n"
"v_cvt_pk_bf16_f32 v44, v100, v101\n"
"v_mfma_f32_16x16x32_bf16 v[68:71], v[116:119], a[172:175], v[68:71]\n"
"v_cvt_pk_bf16_f32 v45, v102, v103\n"
"v_mfma_f32_16x16x32_bf16 v[64:67], v[120:123], a[144:147], v[64:67]\n"
"ds_read_b128 v[152:155], v3 offset:26624\n"
"v_exp_f32 v104, v104\n"
"v_mfma_f32_16x16x32_bf16 v[68:71], v[120:123], a[176:179], v[68:71]\n"
"v_exp_f32 v105, v105\n"
"v_mfma_f32_16x16x32_bf16 v[64:67], v[124:127], a[148:151], v[64:67]\n"
"ds_read_b128 v[156:159], v3 offset:27648\n"
"v_exp_f32 v106, v106\n"
"v_mfma_f32_16x16x32_bf16 v[68:71], v[124:127], a[180:183], v[68:71]\n"
"v_exp_f32 v107, v107\n"
"v_mfma_f32_16x16x32_bf16 v[64:67], v[128:131], a[152:155], v[64:67]\n"
"ds_read_b128 v[160:163], v3 offset:28672\n"
"v_cvt_pk_bf16_f32 v42, v104, v105\n"
"v_mfma_f32_16x16x32_bf16 v[68:71], v[128:131], a[184:187], v[68:71]\n"
"v_cvt_pk_bf16_f32 v43, v106, v107\n"
"v_mfma_f32_16x16x32_bf16 v[64:67], v[132:135], a[156:159], v[64:67]\n"
"ds_read_b128 v[164:167], v3 offset:29696\n"
"v_exp_f32 v108, v108\n"
"v_mfma_f32_16x16x32_bf16 v[68:71], v[132:135], a[188:191], v[68:71]\n"
"v_exp_f32 v109, v109\n"
"s_waitcnt lgkmcnt(2)\n"
"v_mfma_f32_16x16x32_bf16 v[64:67], v[136:139], a[160:163], v[64:67]\n"
"ds_read_b128 v[168:171], v3 offset:30720\n"
"v_exp_f32 v110, v110\n"
"v_mfma_f32_16x16x32_bf16 v[68:71], v[136:139], a[192:195], v[68:71]\n"
"v_exp_f32 v111, v111\n"
"v_mfma_f32_16x16x32_bf16 v[64:67], v[140:143], a[164:167], v[64:67]\n"
"ds_read_b128 v[172:175], v3 offset:31744\n"
"v_cvt_pk_bf16_f32 v46, v108, v109\n"
"v_mfma_f32_16x16x32_bf16 v[68:71], v[140:143], a[196:199], v[68:71]\n"
"s_waitcnt vmcnt(8)\n"
"s_barrier\n"
"v_cvt_pk_bf16_f32 v47, v110, v111\n"
"v_mfma_f32_16x16x32_bf16 v[72:75], v[144:147], a[136:139], v[16:19]\n"
"ds_read_b128 v[112:115], v4 offset:32768\n"
"s_mov_b32 m0, s71\n"
"v_mfma_f32_16x16x32_bf16 v[76:79], v[144:147], a[168:171], v[20:23]\n"
"buffer_load_dwordx4 v1, s[36:39], s66 offen offset:0 lds\n"
"v_mfma_f32_16x16x32_bf16 v[72:75], v[148:151], a[140:143], v[72:75]\n"
"ds_read_b128 v[116:119], v4 offset:33792\n"
"v_mfma_f32_16x16x32_bf16 v[76:79], v[148:151], a[172:175], v[76:79]\n"
"v_mfma_f32_16x16x32_bf16 v[72:75], v[152:155], a[144:147], v[72:75]\n"
"ds_read_b128 v[120:123], v4 offset:34816\n"
"v_mfma_f32_16x16x32_bf16 v[76:79], v[152:155], a[176:179], v[76:79]\n"
"v_mfma_f32_16x16x32_bf16 v[72:75], v[156:159], a[148:151], v[72:75]\n"
"ds_read_b128 v[124:127], v4 offset:35840\n"
"v_mfma_f32_16x16x32_bf16 v[76:79], v[156:159], a[180:183], v[76:79]\n"
"buffer_load_dwordx4 v1, s[36:39], s66 offen offset:1024 lds\n"
"s_waitcnt lgkmcnt(3)\n"
"v_mfma_f32_16x16x32_bf16 v[72:75], v[160:163], a[152:155], v[72:75]\n"
"ds_read_b128 v[128:131], v4 offset:36864\n"
"v_mfma_f32_16x16x32_bf16 v[76:79], v[160:163], a[184:187], v[76:79]\n"
"v_mfma_f32_16x16x32_bf16 v[72:75], v[164:167], a[156:159], v[72:75]\n"
"ds_read_b128 v[132:135], v4 offset:37888\n"
"v_mfma_f32_16x16x32_bf16 v[76:79], v[164:167], a[188:191], v[76:79]\n"
"v_mfma_f32_16x16x32_bf16 v[72:75], v[168:171], a[160:163], v[72:75]\n"
"ds_read_b128 v[136:139], v4 offset:38912\n"
"v_mfma_f32_16x16x32_bf16 v[76:79], v[168:171], a[192:195], v[76:79]\n"
"buffer_load_dwordx4 v1, s[36:39], s66 offen offset:2048 lds\n"
"v_mfma_f32_16x16x32_bf16 v[72:75], v[172:175], a[164:167], v[72:75]\n"
"ds_read_b128 v[140:143], v4 offset:39936\n"
"v_mfma_f32_16x16x32_bf16 v[76:79], v[172:175], a[196:199], v[76:79]\n"
"s_cmp_gt_i32 s75, 0\n"
"s_cbranch_scc1 L_nmm1%=\n"
"s_nop 7\n"
"v_add_u32 v8, s75, v5\n"
"v_cmp_gt_i32_e64 s[78:79], -16, v8\n"
"v_cmp_gt_i32_e64 s[80:81], -15, v8\n"
"v_cmp_gt_i32_e64 s[82:83], -14, v8\n"
"v_cmp_gt_i32_e64 s[84:85], -13, v8\n"
"v_cndmask_b32_e64 v52, v52, v30, s[78:79]\n"
"v_cndmask_b32_e64 v53, v53, v30, s[80:81]\n"
"v_cndmask_b32_e64 v54, v54, v30, s[82:83]\n"
"v_cndmask_b32_e64 v55, v55, v30, s[84:85]\n"
"v_cmp_gt_i32_e64 s[78:79], 0, v8\n"
"v_cmp_gt_i32_e64 s[80:81], 1, v8\n"
"v_cmp_gt_i32_e64 s[82:83], 2, v8\n"
"v_cmp_gt_i32_e64 s[84:85], 3, v8\n"
"v_cndmask_b32_e64 v48, v48, v30, s[78:79]\n"
"v_cndmask_b32_e64 v49, v49, v30, s[80:81]\n"
"v_cndmask_b32_e64 v50, v50, v30, s[82:83]\n"
"v_cndmask_b32_e64 v51, v51, v30, s[84:85]\n"
"v_cndmask_b32_e64 v60, v60, v30, s[78:79]\n"
"v_cndmask_b32_e64 v61, v61, v30, s[80:81]\n"
"v_cndmask_b32_e64 v62, v62, v30, s[82:83]\n"
"v_cndmask_b32_e64 v63, v63, v30, s[84:85]\n"
"v_cmp_gt_i32_e64 s[78:79], 16, v8\n"
"v_cmp_gt_i32_e64 s[80:81], 17, v8\n"
"v_cmp_gt_i32_e64 s[82:83], 18, v8\n"
"v_cmp_gt_i32_e64 s[84:85], 19, v8\n"
"v_cndmask_b32_e64 v56, v56, v30, s[78:79]\n"
"v_cndmask_b32_e64 v57, v57, v30, s[80:81]\n"
"v_cndmask_b32_e64 v58, v58, v30, s[82:83]\n"
"v_cndmask_b32_e64 v59, v59, v30, s[84:85]\n"
"v_cndmask_b32_e64 v68, v68, v30, s[78:79]\n"
"v_cndmask_b32_e64 v69, v69, v30, s[80:81]\n"
"v_cndmask_b32_e64 v70, v70, v30, s[82:83]\n"
"v_cndmask_b32_e64 v71, v71, v30, s[84:85]\n"
"v_cmp_gt_i32_e64 s[78:79], 32, v8\n"
"v_cmp_gt_i32_e64 s[80:81], 33, v8\n"
"v_cmp_gt_i32_e64 s[82:83], 34, v8\n"
"v_cmp_gt_i32_e64 s[84:85], 35, v8\n"
"v_cndmask_b32_e64 v64, v64, v30, s[78:79]\n"
"v_cndmask_b32_e64 v65, v65, v30, s[80:81]\n"
"v_cndmask_b32_e64 v66, v66, v30, s[82:83]\n"
"v_cndmask_b32_e64 v67, v67, v30, s[84:85]\n"
"v_cndmask_b32_e64 v76, v76, v30, s[78:79]\n"
"v_cndmask_b32_e64 v77, v77, v30, s[80:81]\n"
"v_cndmask_b32_e64 v78, v78, v30, s[82:83]\n"
"v_cndmask_b32_e64 v79, v79, v30, s[84:85]\n"
"v_cmp_gt_i32_e64 s[78:79], 48, v8\n"
"v_cmp_gt_i32_e64 s[80:81], 49, v8\n"
"v_cmp_gt_i32_e64 s[82:83], 50, v8\n"
"v_cmp_gt_i32_e64 s[84:85], 51, v8\n"
"v_cndmask_b32_e64 v72, v72, v30, s[78:79]\n"
"v_cndmask_b32_e64 v73, v73, v30, s[80:81]\n"
"v_cndmask_b32_e64 v74, v74, v30, s[82:83]\n"
"v_cndmask_b32_e64 v75, v75, v30, s[84:85]\n"
"L_nmm1%=:\n"
"s_sub_u32 s75, s75, 64\n"
"s_waitcnt lgkmcnt(0)\n"
"v_mfma_f32_16x16x32_bf16 a[0:3], v[112:115], v[32:35], a[0:3]\n"
"ds_read_b128 v[144:147], v4 offset:40960\n"
"v_mfma_f32_16x16x32_bf16 a[64:67], v[112:115], v[36:39], a[64:67]\n"
"v_mfma_f32_16x16x32_bf16 a[4:7], v[116:119], v[32:35], a[4:7]\n"
"ds_read_b128 v[148:151], v4 offset:41984\n"
"v_mfma_f32_16x16x32_bf16 a[68:71], v[116:119], v[36:39], a[68:71]\n"
"buffer_load_dwordx4 v1, s[36:39], s66 offen offset:3072 lds\n"
"v_max3_f32 v28, v48, v49, v50\n"
"v_mfma_f32_16x16x32_bf16 a[8:11], v[120:123], v[32:35], a[8:11]\n"
"ds_read_b128 v[152:155], v4 offset:43008\n"
"v_mfma_f32_16x16x32_bf16 a[72:75], v[120:123], v[36:39], a[72:75]\n"
"v_max_f32 v28, v28, v51\n"
"v_mfma_f32_16x16x32_bf16 a[12:15], v[124:127], v[32:35], a[12:15]\n"
"ds_read_b128 v[156:159], v4 offset:44032\n"
"v_mfma_f32_16x16x32_bf16 a[76:79], v[124:127], v[36:39], a[76:79]\n"
"v_max3_f32 v29, v52, v53, v54\n"
"v_mfma_f32_16x16x32_bf16 a[128:131], v[24:27], v[32:35], a[128:131]\n"
"s_add_u32 m0, m0, 4096\n"
"v_mfma_f32_16x16x32_bf16 a[132:135], v[24:27], v[36:39], a[132:135]\n"
"buffer_load_dwordx4 v2, s[36:39], s66 offen offset:0 lds\n"
"v_max_f32 v29, v29, v55\n"
"v_mfma_f32_16x16x32_bf16 a[16:19], v[128:131], v[32:35], a[16:19]\n"
"ds_read_b128 v[160:163], v4 offset:45056\n"
"v_mfma_f32_16x16x32_bf16 a[80:83], v[128:131], v[36:39], a[80:83]\n"
"v_max3_f32 v28, v28, v56, v57\n"
"v_mfma_f32_16x16x32_bf16 a[20:23], v[132:135], v[32:35], a[20:23]\n"
"ds_read_b128 v[164:167], v4 offset:46080\n"
"v_mfma_f32_16x16x32_bf16 a[84:87], v[132:135], v[36:39], a[84:87]\n"
"v_max3_f32 v28, v28, v58, v59\n"
"v_mfma_f32_16x16x32_bf16 a[24:27], v[136:139], v[32:35], a[24:27]\n"
"ds_read_b128 v[168:171], v4 offset:47104\n"
"v_mfma_f32_16x16x32_bf16 a[88:91], v[136:139], v[36:39], a[88:91]\n"
"buffer_load_dwordx4 v2, s[36:39], s66 offen offset:1024 lds\n"
"v_max3_f32 v29, v29, v60, v61\n"
"v_mfma_f32_16x16x32_bf16 a[28:31], v[140:143], v[32:35], a[28:31]\n"
"ds_read_b128 v[172:175], v4 offset:48128\n"
"v_mfma_f32_16x16x32_bf16 a[92:95], v[140:143], v[36:39], a[92:95]\n"
"v_max3_f32 v29, v29, v62, v63\n"
"s_waitcnt lgkmcnt(2)\n"
"v_mfma_f32_16x16x32_bf16 a[32:35], v[144:147], v[32:35], a[32:35]\n"
"ds_read_b128 v[112:115], v4 offset:49152\n"
"v_mfma_f32_16x16x32_bf16 a[96:99], v[144:147], v[36:39], a[96:99]\n"
"v_max3_f32 v28, v28, v64, v65\n"
"v_mfma_f32_16x16x32_bf16 a[36:39], v[148:151], v[32:35], a[36:39]\n"
"ds_read_b128 v[116:119], v4 offset:50176\n"
"v_mfma_f32_16x16x32_bf16 a[100:103], v[148:151], v[36:39], a[100:103]\n"
"buffer_load_dwordx4 v2, s[36:39], s66 offen offset:2048 lds\n"
"v_max3_f32 v28, v28, v66, v67\n"
"v_mfma_f32_16x16x32_bf16 a[40:43], v[152:155], v[32:35], a[40:43]\n"
"ds_read_b128 v[120:123], v4 offset:51200\n"
"v_mfma_f32_16x16x32_bf16 a[104:107], v[152:155], v[36:39], a[104:107]\n"
"v_max3_f32 v29, v29, v68, v69\n"
"v_mfma_f32_16x16x32_bf16 a[44:47], v[156:159], v[32:35], a[44:47]\n"
"ds_read_b128 v[124:127], v4 offset:52224\n"
"v_mfma_f32_16x16x32_bf16 a[108:111], v[156:159], v[36:39], a[108:111]\n"
"v_max3_f32 v29, v29, v70, v71\n"
"v_mfma_f32_16x16x32_bf16 a[48:51], v[160:163], v[32:35], a[48:51]\n"
"ds_read_b128 v[128:131], v4 offset:53248\n"
"v_mfma_f32_16x16x32_bf16 a[112:115], v[160:163], v[36:39], a[112:115]\n"
"buffer_load_dwordx4 v2, s[36:39], s66 offen offset:3072 lds\n"
"v_max3_f32 v28, v28, v72, v73\n"
"v_mfma_f32_16x16x32_bf16 a[52:55], v[164:167], v[32:35], a[52:55]\n"
"ds_read_b128 v[132:135], v4 offset:54272\n"
"v_mfma_f32_16x16x32_bf16 a[116:119], v[164:167], v[36:39], a[116:119]\n"
"s_add_u32 s66, s66, 65536\n"
"s_sub_u32 s67, s67, 1\n"
"s_cmp_eq_u32 s67, 0\n"
"s_cselect_b32 s66, s68, s66\n"
"s_or_b32 s66, s66, 32768\n"
"v_max3_f32 v28, v28, v74, v75\n"
"s_waitcnt lgkmcnt(1)\n"
"v_mfma_f32_16x16x32_bf16 a[56:59], v[168:171], v[32:35], a[56:59]\n"
"ds_read_b128 v[136:139], v4 offset:55296\n"
"v_mfma_f32_16x16x32_bf16 a[120:123], v[168:171], v[36:39], a[120:123]\n"
"v_max3_f32 v29, v29, v76, v77\n"
"v_mfma_f32_16x16x32_bf16 a[60:63], v[172:175], v[32:35], a[60:63]\n"
"ds_read_b128 v[140:143], v4 offset:56320\n"
"v_mfma_f32_16x16x32_bf16 a[124:127], v[172:175], v[36:39], a[124:127]\n"
"v_max3_f32 v29, v29, v78, v79\n"
"v_mfma_f32_16x16x32_bf16 a[0:3], v[112:115], v[40:43], a[0:3]\n"
"ds_read_b128 v[144:147], v4 offset:57344\n"
"v_mfma_f32_16x16x32_bf16 a[64:67], v[112:115], v[44:47], a[64:67]\n"
"v_mfma_f32_16x16x32_bf16 a[4:7], v[116:119], v[40:43], a[4:7]\n"
"ds_read_b128 v[148:151], v4 offset:58368\n"
"v_mfma_f32_16x16x32_bf16 a[68:71], v[116:119], v[44:47], a[68:71]\n"
"v_max_f32 v8, v28, v29\n"
"v_mfma_f32_16x16x32_bf16 a[8:11], v[120:123], v[40:43], a[8:11]\n"
"ds_read_b128 v[152:155], v4 offset:59392\n"
"v_mfma_f32_16x16x32_bf16 a[72:75], v[120:123], v[44:47], a[72:75]\n"
"v_cmp_lt_f32 vcc, s73, v8\n"
"v_mfma_f32_16x16x32_bf16 a[12:15], v[124:127], v[40:43], a[12:15]\n"
"ds_read_b128 v[156:159], v4 offset:60416\n"
"v_mfma_f32_16x16x32_bf16 a[76:79], v[124:127], v[44:47], a[76:79]\n"
"v_mfma_f32_16x16x32_bf16 a[128:131], v[24:27], v[40:43], a[128:131]\n"
"v_mfma_f32_16x16x32_bf16 a[132:135], v[24:27], v[44:47], a[132:135]\n"
"v_mfma_f32_16x16x32_bf16 a[16:19], v[128:131], v[40:43], a[16:19]\n"
"ds_read_b128 v[160:163], v4 offset:61440\n"
"v_mfma_f32_16x16x32_bf16 a[80:83], v[128:131], v[44:47], a[80:83]\n"
"s_waitcnt lgkmcnt(2)\n"
"v_mfma_f32_16x16x32_bf16 a[20:23], v[132:135], v[40:43], a[20:23]\n"
"ds_read_b128 v[164:167], v4 offset:62464\n"
"v_mfma_f32_16x16x32_bf16 a[84:87], v[132:135], v[44:47], a[84:87]\n"
"v_mfma_f32_16x16x32_bf16 a[24:27], v[136:139], v[40:43], a[24:27]\n"
"ds_read_b128 v[168:171], v4 offset:63488\n"
"v_mfma_f32_16x16x32_bf16 a[88:91], v[136:139], v[44:47], a[88:91]\n"
"v_mfma_f32_16x16x32_bf16 a[28:31], v[140:143], v[40:43], a[28:31]\n"
"ds_read_b128 v[172:175], v4 offset:64512\n"
"v_mfma_f32_16x16x32_bf16 a[92:95], v[140:143], v[44:47], a[92:95]\n"
"s_waitcnt vmcnt(8)\n"
"s_barrier\n"
"v_mfma_f32_16x16x32_bf16 a[32:35], v[144:147], v[40:43], a[32:35]\n"
"ds_read_b128 v[112:115], v3 offset:32768\n"
"s_mov_b32 m0, s69\n"
"v_mfma_f32_16x16x32_bf16 a[96:99], v[144:147], v[44:47], a[96:99]\n"
"buffer_load_dwordx4 v1, s[36:39], s64 offen offset:0 lds\n"
"v_mfma_f32_16x16x32_bf16 a[36:39], v[148:151], v[40:43], a[36:39]\n"
"ds_read_b128 v[116:119], v3 offset:33792\n"
"v_mfma_f32_16x16x32_bf16 a[100:103], v[148:151], v[44:47], a[100:103]\n"
"v_mfma_f32_16x16x32_bf16 a[40:43], v[152:155], v[40:43], a[40:43]\n"
"ds_read_b128 v[120:123], v3 offset:34816\n"
"v_mfma_f32_16x16x32_bf16 a[104:107], v[152:155], v[44:47], a[104:107]\n"
"s_waitcnt lgkmcnt(2)\n"
"v_mfma_f32_16x16x32_bf16 a[44:47], v[156:159], v[40:43], a[44:47]\n"
"ds_read_b128 v[124:127], v3 offset:35840\n"
"v_mfma_f32_16x16x32_bf16 a[108:111], v[156:159], v[44:47], a[108:111]\n"
"buffer_load_dwordx4 v1, s[36:39], s64 offen offset:1024 lds\n"
"v_mfma_f32_16x16x32_bf16 a[48:51], v[160:163], v[40:43], a[48:51]\n"
"ds_read_b128 v[128:131], v3 offset:36864\n"
"v_mfma_f32_16x16x32_bf16 a[112:115], v[160:163], v[44:47], a[112:115]\n"
"v_mfma_f32_16x16x32_bf16 a[52:55], v[164:167], v[40:43], a[52:55]\n"
"ds_read_b128 v[132:135], v3 offset:37888\n"
"v_mfma_f32_16x16x32_bf16 a[116:119], v[164:167], v[44:47], a[116:119]\n"
"v_mfma_f32_16x16x32_bf16 a[56:59], v[168:171], v[40:43], a[56:59]\n"
"ds_read_b128 v[136:139], v3 offset:38912\n"
"v_mfma_f32_16x16x32_bf16 a[120:123], v[168:171], v[44:47], a[120:123]\n"
"buffer_load_dwordx4 v1, s[36:39], s64 offen offset:2048 lds\n"
"v_mfma_f32_16x16x32_bf16 a[60:63], v[172:175], v[40:43], a[60:63]\n"
"ds_read_b128 v[140:143], v3 offset:39936\n"
"v_mfma_f32_16x16x32_bf16 a[124:127], v[172:175], v[44:47], a[124:127]\n"
"s_cbranch_vccz L_top%=\n"
"s_nop 7\n"
"v_mov_b32 v8, v28\n"
"v_mov_b32 v9, v29\n"
"s_nop 0\n"
"v_permlane32_swap_b32 v8, v28\n"
"v_permlane32_swap_b32 v9, v29\n"
"v_max_f32 v28, v28, v8\n"
"v_max_f32 v29, v29, v9\n"
"v_mov_b32 v8, v28\n"
"v_mov_b32 v9, v29\n"
"s_nop 0\n"
"v_permlane16_swap_b32 v8, v28\n"
"v_permlane16_swap_b32 v9, v29\n"
"v_max_f32 v28, v28, v8\n"
"v_max_f32 v29, v29, v9\n"
"v_max_f32 v28, 0, v28\n"
"v_exp_f32 v10, -v28\n"
"v_max_f32 v29, 0, v29\n"
"v_exp_f32 v11, -v29\n"
"v_sub_f32 v48, v48, v28\n"
"v_sub_f32 v49, v49, v28\n"
"v_sub_f32 v50, v50, v28\n"
"v_sub_f32 v51, v51, v28\n"
"v_sub_f32 v52, v52, v29\n"
"v_sub_f32 v53, v53, v29\n"
"v_sub_f32 v54, v54, v29\n"
"v_sub_f32 v55, v55, v29\n"
"v_sub_f32 v56, v56, v28\n"
"v_sub_f32 v57, v57, v28\n"
"v_sub_f32 v58, v58, v28\n"
"v_sub_f32 v59, v59, v28\n"
"v_sub_f32 v60, v60, v29\n"
"v_sub_f32 v61, v61, v29\n"
"v_sub_f32 v62, v62, v29\n"
"v_sub_f32 v63, v63, v29\n"
"v_sub_f32 v64, v64, v28\n"
"v_sub_f32 v65, v65, v28\n"
"v_sub_f32 v66, v66, v28\n"
"v_sub_f32 v67, v67, v28\n"
"v_sub_f32 v68, v68, v29\n"
"v_sub_f32 v69, v69, v29\n"
"v_sub_f32 v70, v70, v29\n"
"v_sub_f32 v71, v71, v29\n"
"v_sub_f32 v72, v72, v28\n"
"v_sub_f32 v73, v73, v28\n"
"v_sub_f32 v74, v74, v28\n"
"v_sub_f32 v75, v75, v28\n"
"v_sub_f32 v76, v76, v29\n"
"v_sub_f32 v77, v77, v29\n"
"v_sub_f32 v78, v78, v29\n"
"v_sub_f32 v79, v79, v29\n"
"v_sub_f32 v16, v16, v28\n"
"v_sub_f32 v17, v17, v28\n"
"v_sub_f32 v18, v18, v28\n"
"v_sub_f32 v19, v19, v28\n"
"v_sub_f32 v20, v20, v29\n"
"v_sub_f32 v21, v21, v29\n"
"v_sub_f32 v22, v22, v29\n"
"v_sub_f32 v23, v23, v29\n"
"v_accvgpr_read_b32 v240, a0\n"
"v_accvgpr_read_b32 v241, a1\n"
"v_accvgpr_read_b32 v242, a2\n"
"v_accvgpr_read_b32 v243, a3\n"
"v_mul_f32 v240, v240, v10\n"
"v_mul_f32 v241, v241, v10\n"
"v_mul_f32 v242, v242, v10\n"
"v_mul_f32 v243, v243, v10\n"
"v_accvgpr_write_b32 a0, v240\n"
"v_accvgpr_write_b32 a1, v241\n"
"v_accvgpr_write_b32 a2, v242\n"
"v_accvgpr_write_b32 a3, v243\n"
"v_accvgpr_read_b32 v240, a4\n"
"v_accvgpr_read_b32 v241, a5\n"
"v_accvgpr_read_b32 v242, a6\n"
"v_accvgpr_read_b32 v243, a7\n"
"v_mul_f32 v240, v240, v10\n"
"v_mul_f32 v241, v241, v10\n"
"v_mul_f32 v242, v242, v10\n"
"v_mul_f32 v243, v243, v10\n"
"v_accvgpr_write_b32 a4, v240\n"
"v_accvgpr_write_b32 a5, v241\n"
"v_accvgpr_write_b32 a6, v242\n"
"v_accvgpr_write_b32 a7, v243\n"
"v_accvgpr_read_b32 v240, a8\n"
"v_accvgpr_read_b32 v241, a9\n"
"v_accvgpr_read_b32 v242, a10\n"
"v_accvgpr_read_b32 v243, a11\n"
"v_mul_f32 v240, v240, v10\n"
"v_mul_f32 v241, v241, v10\n"
"v_mul_f32 v242, v242, v10\n"
"v_mul_f32 v243, v243, v10\n"
"v_accvgpr_write_b32 a8, v240\n"
"v_accvgpr_write_b32 a9, v241\n"
"v_accvgpr_write_b32 a10, v242\n"
"v_accvgpr_write_b32 a11, v243\n"
"v_accvgpr_read_b32 v240, a12\n"
"v_accvgpr_read_b32 v241, a13\n"
"v_accvgpr_read_b32 v242, a14\n"
"v_accvgpr_read_b32 v243, a15\n"
"v_mul_f32 v240, v240, v10\n"
"v_mul_f32 v241, v241, v10\n"
"v_mul_f32 v242, v242, v10\n"
"v_mul_f32 v243, v243, v10\n"
"v_accvgpr_write_b32 a12, v240\n"
"v_accvgpr_write_b32 a13, v241\n"
"v_accvgpr_write_b32 a14, v242\n"
"v_accvgpr_write_b32 a15, v243\n"
"v_accvgpr_read_b32 v240, a16\n"
"v_accvgpr_read_b32 v241, a17\n"
"v_accvgpr_read_b32 v242, a18\n"
"v_accvgpr_read_b32 v243, a19\n"
"v_mul_f32 v240, v240, v10\n"
"v_mul_f32 v241, v241, v10\n"
"v_mul_f32 v242, v242, v10\n"
"v_mul_f32 v243, v243, v10\n"
"v_accvgpr_write_b32 a16, v240\n"
"v_accvgpr_write_b32 a17, v241\n"
"v_accvgpr_write_b32 a18, v242\n"
"v_accvgpr_write_b32 a19, v243\n"
"v_accvgpr_read_b32 v240, a20\n"
"v_accvgpr_read_b32 v241, a21\n"
"v_accvgpr_read_b32 v242, a22\n"
"v_accvgpr_read_b32 v243, a23\n"
"v_mul_f32 v240, v240, v10\n"
"v_mul_f32 v241, v241, v10\n"
"v_mul_f32 v242, v242, v10\n"
"v_mul_f32 v243, v243, v10\n"
"v_accvgpr_write_b32 a20, v240\n"
"v_accvgpr_write_b32 a21, v241\n"
"v_accvgpr_write_b32 a22, v242\n"
"v_accvgpr_write_b32 a23, v243\n"
"v_accvgpr_read_b32 v240, a24\n"
"v_accvgpr_read_b32 v241, a25\n"
"v_accvgpr_read_b32 v242, a26\n"
"v_accvgpr_read_b32 v243, a27\n"
"v_mul_f32 v240, v240, v10\n"
"v_mul_f32 v241, v241, v10\n"
"v_mul_f32 v242, v242, v10\n"
"v_mul_f32 v243, v243, v10\n"
"v_accvgpr_write_b32 a24, v240\n"
"v_accvgpr_write_b32 a25, v241\n"
"v_accvgpr_write_b32 a26, v242\n"
"v_accvgpr_write_b32 a27, v243\n"
"v_accvgpr_read_b32 v240, a28\n"
"v_accvgpr_read_b32 v241, a29\n"
"v_accvgpr_read_b32 v242, a30\n"
"v_accvgpr_read_b32 v243, a31\n"
"v_mul_f32 v240, v240, v10\n"
"v_mul_f32 v241, v241, v10\n"
"v_mul_f32 v242, v242, v10\n"
"v_mul_f32 v243, v243, v10\n"
"v_accvgpr_write_b32 a28, v240\n"
"v_accvgpr_write_b32 a29, v241\n"
"v_accvgpr_write_b32 a30, v242\n"
"v_accvgpr_write_b32 a31, v243\n"
"v_accvgpr_read_b32 v240, a32\n"
"v_accvgpr_read_b32 v241, a33\n"
"v_accvgpr_read_b32 v242, a34\n"
"v_accvgpr_read_b32 v243, a35\n"
"v_mul_f32 v240, v240, v10\n"
"v_mul_f32 v241, v241, v10\n"
"v_mul_f32 v242, v242, v10\n"
"v_mul_f32 v243, v243, v10\n"
"v_accvgpr_write_b32 a32, v240\n"
"v_accvgpr_write_b32 a33, v241\n"
"v_accvgpr_write_b32 a34, v242\n"
"v_accvgpr_write_b32 a35, v243\n"
"v_accvgpr_read_b32 v240, a36\n"
"v_accvgpr_read_b32 v241, a37\n"
"v_accvgpr_read_b32 v242, a38\n"
"v_accvgpr_read_b32 v243, a39\n"
"v_mul_f32 v240, v240, v10\n"
"v_mul_f32 v241, v241, v10\n"
"v_mul_f32 v242, v242, v10\n"
"v_mul_f32 v243, v243, v10\n"
"v_accvgpr_write_b32 a36, v240\n"
"v_accvgpr_write_b32 a37, v241\n"
"v_accvgpr_write_b32 a38, v242\n"
"v_accvgpr_write_b32 a39, v243\n"
"v_accvgpr_read_b32 v240, a40\n"
"v_accvgpr_read_b32 v241, a41\n"
"v_accvgpr_read_b32 v242, a42\n"
"v_accvgpr_read_b32 v243, a43\n"
"v_mul_f32 v240, v240, v10\n"
"v_mul_f32 v241, v241, v10\n"
"v_mul_f32 v242, v242, v10\n"
"v_mul_f32 v243, v243, v10\n"
"v_accvgpr_write_b32 a40, v240\n"
"v_accvgpr_write_b32 a41, v241\n"
"v_accvgpr_write_b32 a42, v242\n"
"v_accvgpr_write_b32 a43, v243\n"
"v_accvgpr_read_b32 v240, a44\n"
"v_accvgpr_read_b32 v241, a45\n"
"v_accvgpr_read_b32 v242, a46\n"
"v_accvgpr_read_b32 v243, a47\n"
"v_mul_f32 v240, v240, v10\n"
"v_mul_f32 v241, v241, v10\n"
"v_mul_f32 v242, v242, v10\n"
"v_mul_f32 v243, v243, v10\n"
"v_accvgpr_write_b32 a44, v240\n"
"v_accvgpr_write_b32 a45, v241\n"
"v_accvgpr_write_b32 a46, v242\n"
"v_accvgpr_write_b32 a47, v243\n"
"v_accvgpr_read_b32 v240, a48\n"
"v_accvgpr_read_b32 v241, a49\n"
"v_accvgpr_read_b32 v242, a50\n"
"v_accvgpr_read_b32 v243, a51\n"
"v_mul_f32 v240, v240, v10\n"
"v_mul_f32 v241, v241, v10\n"
"v_mul_f32 v242, v242, v10\n"
"v_mul_f32 v243, v243, v10\n"
"v_accvgpr_write_b32 a48, v240\n"
"v_accvgpr_write_b32 a49, v241\n"
"v_accvgpr_write_b32 a50, v242\n"
"v_accvgpr_write_b32 a51, v243\n"
"v_accvgpr_read_b32 v240, a52\n"
"v_accvgpr_read_b32 v241, a53\n"
"v_accvgpr_read_b32 v242, a54\n"
"v_accvgpr_read_b32 v243, a55\n"
"v_mul_f32 v240, v240, v10\n"
"v_mul_f32 v241, v241, v10\n"
"v_mul_f32 v242, v242, v10\n"
"v_mul_f32 v243, v243, v10\n"
"v_accvgpr_write_b32 a52, v240\n"
"v_accvgpr_write_b32 a53, v241\n"
"v_accvgpr_write_b32 a54, v242\n"
"v_accvgpr_write_b32 a55, v243\n"
"v_accvgpr_read_b32 v240, a56\n"
"v_accvgpr_read_b32 v241, a57\n"
"v_accvgpr_read_b32 v242, a58\n"
"v_accvgpr_read_b32 v243, a59\n"
"v_mul_f32 v240, v240, v10\n"
"v_mul_f32 v241, v241, v10\n"
"v_mul_f32 v242, v242, v10\n"
"v_mul_f32 v243, v243, v10\n"
"v_accvgpr_write_b32 a56, v240\n"
"v_accvgpr_write_b32 a57, v241\n"
"v_accvgpr_write_b32 a58, v242\n"
"v_accvgpr_write_b32 a59, v243\n"
"v_accvgpr_read_b32 v240, a60\n"
"v_accvgpr_read_b32 v241, a61\n"
"v_accvgpr_read_b32 v242, a62\n"
"v_accvgpr_read_b32 v243, a63\n"
"v_mul_f32 v240, v240, v10\n"
"v_mul_f32 v241, v241, v10\n"
"v_mul_f32 v242, v242, v10\n"
"v_mul_f32 v243, v243, v10\n"
"v_accvgpr_write_b32 a60, v240\n"
"v_accvgpr_write_b32 a61, v241\n"
"v_accvgpr_write_b32 a62, v242\n"
"v_accvgpr_write_b32 a63, v243\n"
"v_accvgpr_read_b32 v240, a64\n"
"v_accvgpr_read_b32 v241, a65\n"
"v_accvgpr_read_b32 v242, a66\n"
"v_accvgpr_read_b32 v243, a67\n"
"v_mul_f32 v240, v240, v11\n"
"v_mul_f32 v241, v241, v11\n"
"v_mul_f32 v242, v242, v11\n"
"v_mul_f32 v243, v243, v11\n"
"v_accvgpr_write_b32 a64, v240\n"
"v_accvgpr_write_b32 a65, v241\n"
"v_accvgpr_write_b32 a66, v242\n"
"v_accvgpr_write_b32 a67, v243\n"
"v_accvgpr_read_b32 v240, a68\n"
"v_accvgpr_read_b32 v241, a69\n"
"v_accvgpr_read_b32 v242, a70\n"
"v_accvgpr_read_b32 v243, a71\n"
"v_mul_f32 v240, v240, v11\n"
"v_mul_f32 v241, v241, v11\n"
"v_mul_f32 v242, v242, v11\n"
"v_mul_f32 v243, v243, v11\n"
"v_accvgpr_write_b32 a68, v240\n"
"v_accvgpr_write_b32 a69, v241\n"
"v_accvgpr_write_b32 a70, v242\n"
"v_accvgpr_write_b32 a71, v243\n"
"v_accvgpr_read_b32 v240, a72\n"
"v_accvgpr_read_b32 v241, a73\n"
"v_accvgpr_read_b32 v242, a74\n"
"v_accvgpr_read_b32 v243, a75\n"
"v_mul_f32 v240, v240, v11\n"
"v_mul_f32 v241, v241, v11\n"
"v_mul_f32 v242, v242, v11\n"
"v_mul_f32 v243, v243, v11\n"
"v_accvgpr_write_b32 a72, v240\n"
"v_accvgpr_write_b32 a73, v241\n"
"v_accvgpr_write_b32 a74, v242\n"
"v_accvgpr_write_b32 a75, v243\n"
"v_accvgpr_read_b32 v240, a76\n"
"v_accvgpr_read_b32 v241, a77\n"
"v_accvgpr_read_b32 v242, a78\n"
"v_accvgpr_read_b32 v243, a79\n"
"v_mul_f32 v240, v240, v11\n"
"v_mul_f32 v241, v241, v11\n"
"v_mul_f32 v242, v242, v11\n"
"v_mul_f32 v243, v243, v11\n"
"v_accvgpr_write_b32 a76, v240\n"
"v_accvgpr_write_b32 a77, v241\n"
"v_accvgpr_write_b32 a78, v242\n"
"v_accvgpr_write_b32 a79, v243\n"
"v_accvgpr_read_b32 v240, a80\n"
"v_accvgpr_read_b32 v241, a81\n"
"v_accvgpr_read_b32 v242, a82\n"
"v_accvgpr_read_b32 v243, a83\n"
"v_mul_f32 v240, v240, v11\n"
"v_mul_f32 v241, v241, v11\n"
"v_mul_f32 v242, v242, v11\n"
"v_mul_f32 v243, v243, v11\n"
"v_accvgpr_write_b32 a80, v240\n"
"v_accvgpr_write_b32 a81, v241\n"
"v_accvgpr_write_b32 a82, v242\n"
"v_accvgpr_write_b32 a83, v243\n"
"v_accvgpr_read_b32 v240, a84\n"
"v_accvgpr_read_b32 v241, a85\n"
"v_accvgpr_read_b32 v242, a86\n"
"v_accvgpr_read_b32 v243, a87\n"
"v_mul_f32 v240, v240, v11\n"
"v_mul_f32 v241, v241, v11\n"
"v_mul_f32 v242, v242, v11\n"
"v_mul_f32 v243, v243, v11\n"
"v_accvgpr_write_b32 a84, v240\n"
"v_accvgpr_write_b32 a85, v241\n"
"v_accvgpr_write_b32 a86, v242\n"
"v_accvgpr_write_b32 a87, v243\n"
"v_accvgpr_read_b32 v240, a88\n"
"v_accvgpr_read_b32 v241, a89\n"
"v_accvgpr_read_b32 v242, a90\n"
"v_accvgpr_read_b32 v243, a91\n"
"v_mul_f32 v240, v240, v11\n"
"v_mul_f32 v241, v241, v11\n"
"v_mul_f32 v242, v242, v11\n"
"v_mul_f32 v243, v243, v11\n"
"v_accvgpr_write_b32 a88, v240\n"
"v_accvgpr_write_b32 a89, v241\n"
"v_accvgpr_write_b32 a90, v242\n"
"v_accvgpr_write_b32 a91, v243\n"
"v_accvgpr_read_b32 v240, a92\n"
"v_accvgpr_read_b32 v241, a93\n"
"v_accvgpr_read_b32 v242, a94\n"
"v_accvgpr_read_b32 v243, a95\n"
"v_mul_f32 v240, v240, v11\n"
"v_mul_f32 v241, v241, v11\n"
"v_mul_f32 v242, v242, v11\n"
"v_mul_f32 v243, v243, v11\n"
"v_accvgpr_write_b32 a92, v240\n"
"v_accvgpr_write_b32 a93, v241\n"
"v_accvgpr_write_b32 a94, v242\n"
"v_accvgpr_write_b32 a95, v243\n"
"v_accvgpr_read_b32 v240, a96\n"
"v_accvgpr_read_b32 v241, a97\n"
"v_accvgpr_read_b32 v242, a98\n"
"v_accvgpr_read_b32 v243, a99\n"
"v_mul_f32 v240, v240, v11\n"
"v_mul_f32 v241, v241, v11\n"
"v_mul_f32 v242, v242, v11\n"
"v_mul_f32 v243, v243, v11\n"
"v_accvgpr_write_b32 a96, v240\n"
"v_accvgpr_write_b32 a97, v241\n"
"v_accvgpr_write_b32 a98, v242\n"
"v_accvgpr_write_b32 a99, v243\n"
"v_accvgpr_read_b32 v240, a100\n"
"v_accvgpr_read_b32 v241, a101\n"
"v_accvgpr_read_b32 v242, a102\n"
"v_accvgpr_read_b32 v243, a103\n"
"v_mul_f32 v240, v240, v11\n"
"v_mul_f32 v241, v241, v11\n"
"v_mul_f32 v242, v242, v11\n"
"v_mul_f32 v243, v243, v11\n"
"v_accvgpr_write_b32 a100, v240\n"
"v_accvgpr_write_b32 a101, v241\n"
"v_accvgpr_write_b32 a102, v242\n"
"v_accvgpr_write_b32 a103, v243\n"
"v_accvgpr_read_b32 v240, a104\n"
"v_accvgpr_read_b32 v241, a105\n"
"v_accvgpr_read_b32 v242, a106\n"
"v_accvgpr_read_b32 v243, a107\n"
"v_mul_f32 v240, v240, v11\n"
"v_mul_f32 v241, v241, v11\n"
"v_mul_f32 v242, v242, v11\n"
"v_mul_f32 v243, v243, v11\n"
"v_accvgpr_write_b32 a104, v240\n"
"v_accvgpr_write_b32 a105, v241\n"
"v_accvgpr_write_b32 a106, v242\n"
"v_accvgpr_write_b32 a107, v243\n"
"v_accvgpr_read_b32 v240, a108\n"
"v_accvgpr_read_b32 v241, a109\n"
"v_accvgpr_read_b32 v242, a110\n"
"v_accvgpr_read_b32 v243, a111\n"
"v_mul_f32 v240, v240, v11\n"
"v_mul_f32 v241, v241, v11\n"
"v_mul_f32 v242, v242, v11\n"
"v_mul_f32 v243, v243, v11\n"
"v_accvgpr_write_b32 a108, v240\n"
"v_accvgpr_write_b32 a109, v241\n"
"v_accvgpr_write_b32 a110, v242\n"
"v_accvgpr_write_b32 a111, v243\n"
"v_accvgpr_read_b32 v240, a112\n"
"v_accvgpr_read_b32 v241, a113\n"
"v_accvgpr_read_b32 v242, a114\n"
"v_accvgpr_read_b32 v243, a115\n"
"v_mul_f32 v240, v240, v11\n"
"v_mul_f32 v241, v241, v11\n"
"v_mul_f32 v242, v242, v11\n"
"v_mul_f32 v243, v243, v11\n"
"v_accvgpr_write_b32 a112, v240\n"
"v_accvgpr_write_b32 a113, v241\n"
"v_accvgpr_write_b32 a114, v242\n"
"v_accvgpr_write_b32 a115, v243\n"
"v_accvgpr_read_b32 v240, a116\n"
"v_accvgpr_read_b32 v241, a117\n"
"v_accvgpr_read_b32 v242, a118\n"
"v_accvgpr_read_b32 v243, a119\n"
"v_mul_f32 v240, v240, v11\n"
"v_mul_f32 v241, v241, v11\n"
"v_mul_f32 v242, v242, v11\n"
"v_mul_f32 v243, v243, v11\n"
"v_accvgpr_write_b32 a116, v240\n"
"v_accvgpr_write_b32 a117, v241\n"
"v_accvgpr_write_b32 a118, v242\n"
"v_accvgpr_write_b32 a119, v243\n"
"v_accvgpr_read_b32 v240, a120\n"
"v_accvgpr_read_b32 v241, a121\n"
"v_accvgpr_read_b32 v242, a122\n"
"v_accvgpr_read_b32 v243, a123\n"
"v_mul_f32 v240, v240, v11\n"
"v_mul_f32 v241, v241, v11\n"
"v_mul_f32 v242, v242, v11\n"
"v_mul_f32 v243, v243, v11\n"
"v_accvgpr_write_b32 a120, v240\n"
"v_accvgpr_write_b32 a121, v241\n"
"v_accvgpr_write_b32 a122, v242\n"
"v_accvgpr_write_b32 a123, v243\n"
"v_accvgpr_read_b32 v240, a124\n"
"v_accvgpr_read_b32 v241, a125\n"
"v_accvgpr_read_b32 v242, a126\n"
"v_accvgpr_read_b32 v243, a127\n"
"v_mul_f32 v240, v240, v11\n"
"v_mul_f32 v241, v241, v11\n"
"v_mul_f32 v242, v242, v11\n"
"v_mul_f32 v243, v243, v11\n"
"v_accvgpr_write_b32 a124, v240\n"
"v_accvgpr_write_b32 a125, v241\n"
"v_accvgpr_write_b32 a126, v242\n"
"v_accvgpr_write_b32 a127, v243\n"
"v_accvgpr_read_b32 v240, a128\n"
"v_accvgpr_read_b32 v241, a129\n"
"v_accvgpr_read_b32 v242, a130\n"
"v_accvgpr_read_b32 v243, a131\n"
"v_mul_f32 v240, v240, v10\n"
"v_mul_f32 v241, v241, v10\n"
"v_mul_f32 v242, v242, v10\n"
"v_mul_f32 v243, v243, v10\n"
"v_accvgpr_write_b32 a128, v240\n"
"v_accvgpr_write_b32 a129, v241\n"
"v_accvgpr_write_b32 a130, v242\n"
"v_accvgpr_write_b32 a131, v243\n"
"v_accvgpr_read_b32 v240, a132\n"
"v_accvgpr_read_b32 v241, a133\n"
"v_accvgpr_read_b32 v242, a134\n"
"v_accvgpr_read_b32 v243, a135\n"
"v_mul_f32 v240, v240, v11\n"
"v_mul_f32 v241, v241, v11\n"
"v_mul_f32 v242, v242, v11\n"
"v_mul_f32 v243, v243, v11\n"
"v_accvgpr_write_b32 a132, v240\n"
"v_accvgpr_write_b32 a133, v241\n"
"v_accvgpr_write_b32 a134, v242\n"
"v_accvgpr_write_b32 a135, v243\n"
"s_nop 1\n"
"s_branch L_top%=\n"
"L_sl0%=:\n"
"s_nop 7\n"
"v_mov_b32 v8, v28\n"
"v_mov_b32 v9, v29\n"
"s_nop 0\n"
"v_permlane32_swap_b32 v8, v28\n"
"v_permlane32_swap_b32 v9, v29\n"
"v_max_f32 v28, v28, v8\n"
"v_max_f32 v29, v29, v9\n"
"v_mov_b32 v8, v28\n"
"v_mov_b32 v9, v29\n"
"s_nop 0\n"
"v_permlane16_swap_b32 v8, v28\n"
"v_permlane16_swap_b32 v9, v29\n"
"v_max_f32 v28, v28, v8\n"
"v_max_f32 v29, v29, v9\n"
"v_max_f32 v28, 0, v28\n"
"v_exp_f32 v10, -v28\n"
"v_max_f32 v29, 0, v29\n"
"v_exp_f32 v11, -v29\n"
"v_sub_f32 v80, v80, v28\n"
"v_sub_f32 v81, v81, v28\n"
"v_sub_f32 v82, v82, v28\n"
"v_sub_f32 v83, v83, v28\n"
"v_sub_f32 v84, v84, v29\n"
"v_sub_f32 v85, v85, v29\n"
"v_sub_f32 v86, v86, v29\n"
"v_sub_f32 v87, v87, v29\n"
"v_sub_f32 v88, v88, v28\n"
"v_sub_f32 v89, v89, v28\n"
"v_sub_f32 v90, v90, v28\n"
"v_sub_f32 v91, v91, v28\n"
"v_sub_f32 v92, v92, v29\n"
"v_sub_f32 v93, v93, v29\n"
"v_sub_f32 v94, v94, v29\n"
"v_sub_f32 v95, v95, v29\n"
"v_sub_f32 v96, v96, v28\n"
"v_sub_f32 v97, v97, v28\n"
"v_sub_f32 v98, v98, v28\n"
"v_sub_f32 v99, v99, v28\n"
"v_sub_f32 v100, v100, v29\n"
"v_sub_f32 v101, v101, v29\n"
"v_sub_f32 v102, v102, v29\n"
"v_sub_f32 v103, v103, v29\n"
"v_sub_f32 v104, v104, v28\n"
"v_sub_f32 v105, v105, v28\n"
"v_sub_f32 v106, v106, v28\n"
"v_sub_f32 v107, v107, v28\n"
"v_sub_f32 v108, v108, v29\n"
"v_sub_f32 v109, v109, v29\n"
"v_sub_f32 v110, v110, v29\n"
"v_sub_f32 v111, v111, v29\n"
"v_sub_f32 v16, v16, v28\n"
"v_sub_f32 v17, v17, v28\n"
"v_sub_f32 v18, v18, v28\n"
"v_sub_f32 v19, v19, v28\n"
"v_sub_f32 v20, v20, v29\n"
"v_sub_f32 v21, v21, v29\n"
"v_sub_f32 v22, v22, v29\n"
"v_sub_f32 v23, v23, v29\n"
"v_accvgpr_read_b32 v240, a0\n"
"v_accvgpr_read_b32 v241, a1\n"
"v_accvgpr_read_b32 v242, a2\n"
"v_accvgpr_read_b32 v243, a3\n"
"v_mul_f32 v240, v240, v10\n"
"v_mul_f32 v241, v241, v10\n"
"v_mul_f32 v242, v242, v10\n"
"v_mul_f32 v243, v243, v10\n"
"v_accvgpr_write_b32 a0, v240\n"
"v_accvgpr_write_b32 a1, v241\n"
"v_accvgpr_write_b32 a2, v242\n"
"v_accvgpr_write_b32 a3, v243\n"
"v_accvgpr_read_b32 v240, a4\n"
"v_accvgpr_read_b32 v241, a5\n"
"v_accvgpr_read_b32 v242, a6\n"
"v_accvgpr_read_b32 v243, a7\n"
"v_mul_f32 v240, v240, v10\n"
"v_mul_f32 v241, v241, v10\n"
"v_mul_f32 v242, v242, v10\n"
"v_mul_f32 v243, v243, v10\n"
"v_accvgpr_write_b32 a4, v240\n"
"v_accvgpr_write_b32 a5, v241\n"
"v_accvgpr_write_b32 a6, v242\n"
"v_accvgpr_write_b32 a7, v243\n"
"v_accvgpr_read_b32 v240, a8\n"
"v_accvgpr_read_b32 v241, a9\n"
"v_accvgpr_read_b32 v242, a10\n"
"v_accvgpr_read_b32 v243, a11\n"
"v_mul_f32 v240, v240, v10\n"
"v_mul_f32 v241, v241, v10\n"
"v_mul_f32 v242, v242, v10\n"
"v_mul_f32 v243, v243, v10\n"
"v_accvgpr_write_b32 a8, v240\n"
"v_accvgpr_write_b32 a9, v241\n"
"v_accvgpr_write_b32 a10, v242\n"
"v_accvgpr_write_b32 a11, v243\n"
"v_accvgpr_read_b32 v240, a12\n"
"v_accvgpr_read_b32 v241, a13\n"
"v_accvgpr_read_b32 v242, a14\n"
"v_accvgpr_read_b32 v243, a15\n"
"v_mul_f32 v240, v240, v10\n"
"v_mul_f32 v241, v241, v10\n"
"v_mul_f32 v242, v242, v10\n"
"v_mul_f32 v243, v243, v10\n"
"v_accvgpr_write_b32 a12, v240\n"
"v_accvgpr_write_b32 a13, v241\n"
"v_accvgpr_write_b32 a14, v242\n"
"v_accvgpr_write_b32 a15, v243\n"
"v_accvgpr_read_b32 v240, a16\n"
"v_accvgpr_read_b32 v241, a17\n"
"v_accvgpr_read_b32 v242, a18\n"
"v_accvgpr_read_b32 v243, a19\n"
"v_mul_f32 v240, v240, v10\n"
"v_mul_f32 v241, v241, v10\n"
"v_mul_f32 v242, v242, v10\n"
"v_mul_f32 v243, v243, v10\n"
"v_accvgpr_write_b32 a16, v240\n"
"v_accvgpr_write_b32 a17, v241\n"
"v_accvgpr_write_b32 a18, v242\n"
"v_accvgpr_write_b32 a19, v243\n"
"v_accvgpr_read_b32 v240, a20\n"
"v_accvgpr_read_b32 v241, a21\n"
"v_accvgpr_read_b32 v242, a22\n"
"v_accvgpr_read_b32 v243, a23\n"
"v_mul_f32 v240, v240, v10\n"
"v_mul_f32 v241, v241, v10\n"
"v_mul_f32 v242, v242, v10\n"
"v_mul_f32 v243, v243, v10\n"
"v_accvgpr_write_b32 a20, v240\n"
"v_accvgpr_write_b32 a21, v241\n"
"v_accvgpr_write_b32 a22, v242\n"
"v_accvgpr_write_b32 a23, v243\n"
"v_accvgpr_read_b32 v240, a24\n"
"v_accvgpr_read_b32 v241, a25\n"
"v_accvgpr_read_b32 v242, a26\n"
"v_accvgpr_read_b32 v243, a27\n"
"v_mul_f32 v240, v240, v10\n"
"v_mul_f32 v241, v241, v10\n"
"v_mul_f32 v242, v242, v10\n"
"v_mul_f32 v243, v243, v10\n"
"v_accvgpr_write_b32 a24, v240\n"
"v_accvgpr_write_b32 a25, v241\n"
"v_accvgpr_write_b32 a26, v242\n"
"v_accvgpr_write_b32 a27, v243\n"
"v_accvgpr_read_b32 v240, a28\n"
"v_accvgpr_read_b32 v241, a29\n"
"v_accvgpr_read_b32 v242, a30\n"
"v_accvgpr_read_b32 v243, a31\n"
"v_mul_f32 v240, v240, v10\n"
"v_mul_f32 v241, v241, v10\n"
"v_mul_f32 v242, v242, v10\n"
"v_mul_f32 v243, v243, v10\n"
"v_accvgpr_write_b32 a28, v240\n"
"v_accvgpr_write_b32 a29, v241\n"
"v_accvgpr_write_b32 a30, v242\n"
"v_accvgpr_write_b32 a31, v243\n"
"v_accvgpr_read_b32 v240, a32\n"
"v_accvgpr_read_b32 v241, a33\n"
"v_accvgpr_read_b32 v242, a34\n"
"v_accvgpr_read_b32 v243, a35\n"
"v_mul_f32 v240, v240, v10\n"
"v_mul_f32 v241, v241, v10\n"
"v_mul_f32 v242, v242, v10\n"
"v_mul_f32 v243, v243, v10\n"
"v_accvgpr_write_b32 a32, v240\n"
"v_accvgpr_write_b32 a33, v241\n"
"v_accvgpr_write_b32 a34, v242\n"
"v_accvgpr_write_b32 a35, v243\n"
"v_accvgpr_read_b32 v240, a36\n"
"v_accvgpr_read_b32 v241, a37\n"
"v_accvgpr_read_b32 v242, a38\n"
"v_accvgpr_read_b32 v243, a39\n"
"v_mul_f32 v240, v240, v10\n"
"v_mul_f32 v241, v241, v10\n"
"v_mul_f32 v242, v242, v10\n"
"v_mul_f32 v243, v243, v10\n"
"v_accvgpr_write_b32 a36, v240\n"
"v_accvgpr_write_b32 a37, v241\n"
"v_accvgpr_write_b32 a38, v242\n"
"v_accvgpr_write_b32 a39, v243\n"
"v_accvgpr_read_b32 v240, a40\n"
"v_accvgpr_read_b32 v241, a41\n"
"v_accvgpr_read_b32 v242, a42\n"
"v_accvgpr_read_b32 v243, a43\n"
"v_mul_f32 v240, v240, v10\n"
"v_mul_f32 v241, v241, v10\n"
"v_mul_f32 v242, v242, v10\n"
"v_mul_f32 v243, v243, v10\n"
"v_accvgpr_write_b32 a40, v240\n"
"v_accvgpr_write_b32 a41, v241\n"
"v_accvgpr_write_b32 a42, v242\n"
"v_accvgpr_write_b32 a43, v243\n"
"v_accvgpr_read_b32 v240, a44\n"
"v_accvgpr_read_b32 v241, a45\n"
"v_accvgpr_read_b32 v242, a46\n"
"v_accvgpr_read_b32 v243, a47\n"
"v_mul_f32 v240, v240, v10\n"
"v_mul_f32 v241, v241, v10\n"
"v_mul_f32 v242, v242, v10\n"
"v_mul_f32 v243, v243, v10\n"
"v_accvgpr_write_b32 a44, v240\n"
"v_accvgpr_write_b32 a45, v241\n"
"v_accvgpr_write_b32 a46, v242\n"
"v_accvgpr_write_b32 a47, v243\n"
"v_accvgpr_read_b32 v240, a48\n"
"v_accvgpr_read_b32 v241, a49\n"
"v_accvgpr_read_b32 v242, a50\n"
"v_accvgpr_read_b32 v243, a51\n"
"v_mul_f32 v240, v240, v10\n"
"v_mul_f32 v241, v241, v10\n"
"v_mul_f32 v242, v242, v10\n"
"v_mul_f32 v243, v243, v10\n"
"v_accvgpr_write_b32 a48, v240\n"
"v_accvgpr_write_b32 a49, v241\n"
"v_accvgpr_write_b32 a50, v242\n"
"v_accvgpr_write_b32 a51, v243\n"
"v_accvgpr_read_b32 v240, a52\n"
"v_accvgpr_read_b32 v241, a53\n"
"v_accvgpr_read_b32 v242, a54\n"
"v_accvgpr_read_b32 v243, a55\n"
"v_mul_f32 v240, v240, v10\n"
"v_mul_f32 v241, v241, v10\n"
"v_mul_f32 v242, v242, v10\n"
"v_mul_f32 v243, v243, v10\n"
"v_accvgpr_write_b32 a52, v240\n"
"v_accvgpr_write_b32 a53, v241\n"
"v_accvgpr_write_b32 a54, v242\n"
"v_accvgpr_write_b32 a55, v243\n"
"v_accvgpr_read_b32 v240, a56\n"
"v_accvgpr_read_b32 v241, a57\n"
"v_accvgpr_read_b32 v242, a58\n"
"v_accvgpr_read_b32 v243, a59\n"
"v_mul_f32 v240, v240, v10\n"
"v_mul_f32 v241, v241, v10\n"
"v_mul_f32 v242, v242, v10\n"
"v_mul_f32 v243, v243, v10\n"
"v_accvgpr_write_b32 a56, v240\n"
"v_accvgpr_write_b32 a57, v241\n"
"v_accvgpr_write_b32 a58, v242\n"
"v_accvgpr_write_b32 a59, v243\n"
"v_accvgpr_read_b32 v240, a60\n"
"v_accvgpr_read_b32 v241, a61\n"
"v_accvgpr_read_b32 v242, a62\n"
"v_accvgpr_read_b32 v243, a63\n"
"v_mul_f32 v240, v240, v10\n"
"v_mul_f32 v241, v241, v10\n"
"v_mul_f32 v242, v242, v10\n"
"v_mul_f32 v243, v243, v10\n"
"v_accvgpr_write_b32 a60, v240\n"
"v_accvgpr_write_b32 a61, v241\n"
"v_accvgpr_write_b32 a62, v242\n"
"v_accvgpr_write_b32 a63, v243\n"
"v_accvgpr_read_b32 v240, a64\n"
"v_accvgpr_read_b32 v241, a65\n"
"v_accvgpr_read_b32 v242, a66\n"
"v_accvgpr_read_b32 v243, a67\n"
"v_mul_f32 v240, v240, v11\n"
"v_mul_f32 v241, v241, v11\n"
"v_mul_f32 v242, v242, v11\n"
"v_mul_f32 v243, v243, v11\n"
"v_accvgpr_write_b32 a64, v240\n"
"v_accvgpr_write_b32 a65, v241\n"
"v_accvgpr_write_b32 a66, v242\n"
"v_accvgpr_write_b32 a67, v243\n"
"v_accvgpr_read_b32 v240, a68\n"
"v_accvgpr_read_b32 v241, a69\n"
"v_accvgpr_read_b32 v242, a70\n"
"v_accvgpr_read_b32 v243, a71\n"
"v_mul_f32 v240, v240, v11\n"
"v_mul_f32 v241, v241, v11\n"
"v_mul_f32 v242, v242, v11\n"
"v_mul_f32 v243, v243, v11\n"
"v_accvgpr_write_b32 a68, v240\n"
"v_accvgpr_write_b32 a69, v241\n"
"v_accvgpr_write_b32 a70, v242\n"
"v_accvgpr_write_b32 a71, v243\n"
"v_accvgpr_read_b32 v240, a72\n"
"v_accvgpr_read_b32 v241, a73\n"
"v_accvgpr_read_b32 v242, a74\n"
"v_accvgpr_read_b32 v243, a75\n"
"v_mul_f32 v240, v240, v11\n"
"v_mul_f32 v241, v241, v11\n"
"v_mul_f32 v242, v242, v11\n"
"v_mul_f32 v243, v243, v11\n"
"v_accvgpr_write_b32 a72, v240\n"
"v_accvgpr_write_b32 a73, v241\n"
"v_accvgpr_write_b32 a74, v242\n"
"v_accvgpr_write_b32 a75, v243\n"
"v_accvgpr_read_b32 v240, a76\n"
"v_accvgpr_read_b32 v241, a77\n"
"v_accvgpr_read_b32 v242, a78\n"
"v_accvgpr_read_b32 v243, a79\n"
"v_mul_f32 v240, v240, v11\n"
"v_mul_f32 v241, v241, v11\n"
"v_mul_f32 v242, v242, v11\n"
"v_mul_f32 v243, v243, v11\n"
"v_accvgpr_write_b32 a76, v240\n"
"v_accvgpr_write_b32 a77, v241\n"
"v_accvgpr_write_b32 a78, v242\n"
"v_accvgpr_write_b32 a79, v243\n"
"v_accvgpr_read_b32 v240, a80\n"
"v_accvgpr_read_b32 v241, a81\n"
"v_accvgpr_read_b32 v242, a82\n"
"v_accvgpr_read_b32 v243, a83\n"
"v_mul_f32 v240, v240, v11\n"
"v_mul_f32 v241, v241, v11\n"
"v_mul_f32 v242, v242, v11\n"
"v_mul_f32 v243, v243, v11\n"
"v_accvgpr_write_b32 a80, v240\n"
"v_accvgpr_write_b32 a81, v241\n"
"v_accvgpr_write_b32 a82, v242\n"
"v_accvgpr_write_b32 a83, v243\n"
"v_accvgpr_read_b32 v240, a84\n"
"v_accvgpr_read_b32 v241, a85\n"
"v_accvgpr_read_b32 v242, a86\n"
"v_accvgpr_read_b32 v243, a87\n"
"v_mul_f32 v240, v240, v11\n"
"v_mul_f32 v241, v241, v11\n"
"v_mul_f32 v242, v242, v11\n"
"v_mul_f32 v243, v243, v11\n"
"v_accvgpr_write_b32 a84, v240\n"
"v_accvgpr_write_b32 a85, v241\n"
"v_accvgpr_write_b32 a86, v242\n"
"v_accvgpr_write_b32 a87, v243\n"
"v_accvgpr_read_b32 v240, a88\n"
"v_accvgpr_read_b32 v241, a89\n"
"v_accvgpr_read_b32 v242, a90\n"
"v_accvgpr_read_b32 v243, a91\n"
"v_mul_f32 v240, v240, v11\n"
"v_mul_f32 v241, v241, v11\n"
"v_mul_f32 v242, v242, v11\n"
"v_mul_f32 v243, v243, v11\n"
"v_accvgpr_write_b32 a88, v240\n"
"v_accvgpr_write_b32 a89, v241\n"
"v_accvgpr_write_b32 a90, v242\n"
"v_accvgpr_write_b32 a91, v243\n"
"v_accvgpr_read_b32 v240, a92\n"
"v_accvgpr_read_b32 v241, a93\n"
"v_accvgpr_read_b32 v242, a94\n"
"v_accvgpr_read_b32 v243, a95\n"
"v_mul_f32 v240, v240, v11\n"
"v_mul_f32 v241, v241, v11\n"
"v_mul_f32 v242, v242, v11\n"
"v_mul_f32 v243, v243, v11\n"
"v_accvgpr_write_b32 a92, v240\n"
"v_accvgpr_write_b32 a93, v241\n"
"v_accvgpr_write_b32 a94, v242\n"
"v_accvgpr_write_b32 a95, v243\n"
"v_accvgpr_read_b32 v240, a96\n"
"v_accvgpr_read_b32 v241, a97\n"
"v_accvgpr_read_b32 v242, a98\n"
"v_accvgpr_read_b32 v243, a99\n"
"v_mul_f32 v240, v240, v11\n"
"v_mul_f32 v241, v241, v11\n"
"v_mul_f32 v242, v242, v11\n"
"v_mul_f32 v243, v243, v11\n"
"v_accvgpr_write_b32 a96, v240\n"
"v_accvgpr_write_b32 a97, v241\n"
"v_accvgpr_write_b32 a98, v242\n"
"v_accvgpr_write_b32 a99, v243\n"
"v_accvgpr_read_b32 v240, a100\n"
"v_accvgpr_read_b32 v241, a101\n"
"v_accvgpr_read_b32 v242, a102\n"
"v_accvgpr_read_b32 v243, a103\n"
"v_mul_f32 v240, v240, v11\n"
"v_mul_f32 v241, v241, v11\n"
"v_mul_f32 v242, v242, v11\n"
"v_mul_f32 v243, v243, v11\n"
"v_accvgpr_write_b32 a100, v240\n"
"v_accvgpr_write_b32 a101, v241\n"
"v_accvgpr_write_b32 a102, v242\n"
"v_accvgpr_write_b32 a103, v243\n"
"v_accvgpr_read_b32 v240, a104\n"
"v_accvgpr_read_b32 v241, a105\n"
"v_accvgpr_read_b32 v242, a106\n"
"v_accvgpr_read_b32 v243, a107\n"
"v_mul_f32 v240, v240, v11\n"
"v_mul_f32 v241, v241, v11\n"
"v_mul_f32 v242, v242, v11\n"
"v_mul_f32 v243, v243, v11\n"
"v_accvgpr_write_b32 a104, v240\n"
"v_accvgpr_write_b32 a105, v241\n"
"v_accvgpr_write_b32 a106, v242\n"
"v_accvgpr_write_b32 a107, v243\n"
"v_accvgpr_read_b32 v240, a108\n"
"v_accvgpr_read_b32 v241, a109\n"
"v_accvgpr_read_b32 v242, a110\n"
"v_accvgpr_read_b32 v243, a111\n"
"v_mul_f32 v240, v240, v11\n"
"v_mul_f32 v241, v241, v11\n"
"v_mul_f32 v242, v242, v11\n"
"v_mul_f32 v243, v243, v11\n"
"v_accvgpr_write_b32 a108, v240\n"
"v_accvgpr_write_b32 a109, v241\n"
"v_accvgpr_write_b32 a110, v242\n"
"v_accvgpr_write_b32 a111, v243\n"
"v_accvgpr_read_b32 v240, a112\n"
"v_accvgpr_read_b32 v241, a113\n"
"v_accvgpr_read_b32 v242, a114\n"
"v_accvgpr_read_b32 v243, a115\n"
"v_mul_f32 v240, v240, v11\n"
"v_mul_f32 v241, v241, v11\n"
"v_mul_f32 v242, v242, v11\n"
"v_mul_f32 v243, v243, v11\n"
"v_accvgpr_write_b32 a112, v240\n"
"v_accvgpr_write_b32 a113, v241\n"
"v_accvgpr_write_b32 a114, v242\n"
"v_accvgpr_write_b32 a115, v243\n"
"v_accvgpr_read_b32 v240, a116\n"
"v_accvgpr_read_b32 v241, a117\n"
"v_accvgpr_read_b32 v242, a118\n"
"v_accvgpr_read_b32 v243, a119\n"
"v_mul_f32 v240, v240, v11\n"
"v_mul_f32 v241, v241, v11\n"
"v_mul_f32 v242, v242, v11\n"
"v_mul_f32 v243, v243, v11\n"
"v_accvgpr_write_b32 a116, v240\n"
"v_accvgpr_write_b32 a117, v241\n"
"v_accvgpr_write_b32 a118, v242\n"
"v_accvgpr_write_b32 a119, v243\n"
"v_accvgpr_read_b32 v240, a120\n"
"v_accvgpr_read_b32 v241, a121\n"
"v_accvgpr_read_b32 v242, a122\n"
"v_accvgpr_read_b32 v243, a123\n"
"v_mul_f32 v240, v240, v11\n"
"v_mul_f32 v241, v241, v11\n"
"v_mul_f32 v242, v242, v11\n"
"v_mul_f32 v243, v243, v11\n"
"v_accvgpr_write_b32 a120, v240\n"
"v_accvgpr_write_b32 a121, v241\n"
"v_accvgpr_write_b32 a122, v242\n"
"v_accvgpr_write_b32 a123, v243\n"
"v_accvgpr_read_b32 v240, a124\n"
"v_accvgpr_read_b32 v241, a125\n"
"v_accvgpr_read_b32 v242, a126\n"
"v_accvgpr_read_b32 v243, a127\n"
"v_mul_f32 v240, v240, v11\n"
"v_mul_f32 v241, v241, v11\n"
"v_mul_f32 v242, v242, v11\n"
"v_mul_f32 v243, v243, v11\n"
"v_accvgpr_write_b32 a124, v240\n"
"v_accvgpr_write_b32 a125, v241\n"
"v_accvgpr_write_b32 a126, v242\n"
"v_accvgpr_write_b32 a127, v243\n"
"v_accvgpr_read_b32 v240, a128\n"
"v_accvgpr_read_b32 v241, a129\n"
"v_accvgpr_read_b32 v242, a130\n"
"v_accvgpr_read_b32 v243, a131\n"
"v_mul_f32 v240, v240, v10\n"
"v_mul_f32 v241, v241, v10\n"
"v_mul_f32 v242, v242, v10\n"
"v_mul_f32 v243, v243, v10\n"
"v_accvgpr_write_b32 a128, v240\n"
"v_accvgpr_write_b32 a129, v241\n"
"v_accvgpr_write_b32 a130, v242\n"
"v_accvgpr_write_b32 a131, v243\n"
"v_accvgpr_read_b32 v240, a132\n"
"v_accvgpr_read_b32 v241, a133\n"
"v_accvgpr_read_b32 v242, a134\n"
"v_accvgpr_read_b32 v243, a135\n"
"v_mul_f32 v240, v240, v11\n"
"v_mul_f32 v241, v241, v11\n"
"v_mul_f32 v242, v242, v11\n"
"v_mul_f32 v243, v243, v11\n"
"v_accvgpr_write_b32 a132, v240\n"
"v_accvgpr_write_b32 a133, v241\n"
"v_accvgpr_write_b32 a134, v242\n"
"v_accvgpr_write_b32 a135, v243\n"
"s_nop 1\n"
"s_branch L_r0%=\n"
"L_tail%=:\n"
"v_exp_f32 v80, v80\n"
"v_exp_f32 v81, v81\n"
"v_exp_f32 v82, v82\n"
"buffer_load_dwordx4 v1, s[36:39], s64 offen offset:3072 lds\n"
"s_add_u32 m0, m0, 4096\n"
"v_exp_f32 v83, v83\n"
"v_cvt_pk_bf16_f32 v32, v80, v81\n"
"v_cvt_pk_bf16_f32 v33, v82, v83\n"
"buffer_load_dwordx4 v2, s[36:39], s64 offen offset:0 lds\n"
"v_exp_f32 v84, v84\n"
"v_exp_f32 v85, v85\n"
"v_exp_f32 v86, v86\n"
"buffer_load_dwordx4 v2, s[36:39], s64 offen offset:1024 lds\n"
"v_exp_f32 v87, v87\n"
"v_cvt_pk_bf16_f32 v36, v84, v85\n"
"v_cvt_pk_bf16_f32 v37, v86, v87\n"
"buffer_load_dwordx4 v2, s[36:39], s64 offen offset:2048 lds\n"
"v_exp_f32 v88, v88\n"
"v_exp_f32 v89, v89\n"
"v_exp_f32 v90, v90\n"
"buffer_load_dwordx4 v2, s[36:39], s64 offen offset:3072 lds\n"
"s_add_u32 s64, s64, 65536\n"
"s_sub_u32 s65, s65, 1\n"
"s_cmp_eq_u32 s65, 0\n"
"s_cselect_b32 s64, s68, s64\n"
"s_waitcnt vmcnt(8) lgkmcnt(0)\n"
"s_barrier\n"
"ds_read_b128 v[112:115], v4 offset:32768\n"
"ds_read_b128 v[116:119], v4 offset:33792\n"
"ds_read_b128 v[120:123], v4 offset:34816\n"
"ds_read_b128 v[124:127], v4 offset:35840\n"
"ds_read_b128 v[128:131], v4 offset:36864\n"
"ds_read_b128 v[132:135], v4 offset:37888\n"
"ds_read_b128 v[136:139], v4 offset:38912\n"
"ds_read_b128 v[140:143], v4 offset:39936\n"
"s_cmp_eq_u32 s62, 2\n"
"s_cbranch_scc1 L_spin%=\n"
"s_mov_b32 m0, s71\n"
"v_exp_f32 v91, v91\n"
"v_cvt_pk_bf16_f32 v34, v88, v89\n"
"v_cvt_pk_bf16_f32 v35, v90, v91\n"
"v_exp_f32 v92, v92\n"
"buffer_load_dwordx4 v1, s[36:39], s66 offen offset:0 lds\n"
"v_exp_f32 v93, v93\n"
"v_exp_f32 v94, v94\n"
"v_exp_f32 v95, v95\n"
"v_cvt_pk_bf16_f32 v38, v92, v93\n"
"buffer_load_dwordx4 v1, s[36:39], s66 offen offset:1024 lds\n"
"v_cvt_pk_bf16_f32 v39, v94, v95\n"
"v_exp_f32 v96, v96\n"
"v_exp_f32 v97, v97\n"
"v_exp_f32 v98, v98\n"
"buffer_load_dwordx4 v1, s[36:39], s66 offen offset:2048 lds\n"
"v_exp_f32 v99, v99\n"
"v_cvt_pk_bf16_f32 v40, v96, v97\n"
"v_cvt_pk_bf16_f32 v41, v98, v99\n"
"v_exp_f32 v100, v100\n"
"buffer_load_dwordx4 v1, s[36:39], s66 offen offset:3072 lds\n"
"s_add_u32 m0, m0, 4096\n"
"v_exp_f32 v101, v101\n"
"v_exp_f32 v102, v102\n"
"v_exp_f32 v103, v103\n"
"v_cvt_pk_bf16_f32 v44, v100, v101\n"
"buffer_load_dwordx4 v2, s[36:39], s66 offen offset:0 lds\n"
"v_cvt_pk_bf16_f32 v45, v102, v103\n"
"v_exp_f32 v104, v104\n"
"v_exp_f32 v105, v105\n"
"v_exp_f32 v106, v106\n"
"buffer_load_dwordx4 v2, s[36:39], s66 offen offset:1024 lds\n"
"v_exp_f32 v107, v107\n"
"v_cvt_pk_bf16_f32 v42, v104, v105\n"
"v_cvt_pk_bf16_f32 v43, v106, v107\n"
"v_exp_f32 v108, v108\n"
"buffer_load_dwordx4 v2, s[36:39], s66 offen offset:2048 lds\n"
"v_exp_f32 v109, v109\n"
"v_exp_f32 v110, v110\n"
"v_exp_f32 v111, v111\n"
"v_cvt_pk_bf16_f32 v46, v108, v109\n"
"buffer_load_dwordx4 v2, s[36:39], s66 offen offset:3072 lds\n"
"s_add_u32 s66, s66, 65536\n"
"s_sub_u32 s67, s67, 1\n"
"s_cmp_eq_u32 s67, 0\n"
"s_cselect_b32 s66, s68, s66\n"
"s_or_b32 s66, s66, 32768\n"
"v_cvt_pk_bf16_f32 v47, v110, v111\n"
"s_and_b32 s58, s53, 15\n"
"s_lshl_b32 s58, s58, 2\n"
"s_add_u32 s58, s58, s57\n"
"s_lshl_b32 s58, s58, 14\n"
"s_nop 1\n"
"s_waitcnt lgkmcnt(0)\n"
"v_mfma_f32_16x16x32_bf16 a[0:3], v[112:115], v[32:35], a[0:3]\n"
"ds_read_b128 v[144:147], v4 offset:40960\n"
"v_mfma_f32_16x16x32_bf16 a[64:67], v[112:115], v[36:39], a[64:67]\n"
"buffer_load_dwordx4 a[136:139], v6, s[40:43], s58 offen offset:0\n"
"v_mfma_f32_16x16x32_bf16 a[4:7], v[116:119], v[32:35], a[4:7]\n"
"ds_read_b128 v[148:151], v4 offset:41984\n"
"v_mfma_f32_16x16x32_bf16 a[68:71], v[116:119], v[36:39], a[68:71]\n"
"v_mfma_f32_16x16x32_bf16 a[8:11], v[120:123], v[32:35], a[8:11]\n"
"ds_read_b128 v[152:155], v4 offset:43008\n"
"v_mfma_f32_16x16x32_bf16 a[72:75], v[120:123], v[36:39], a[72:75]\n"
"buffer_load_dwordx4 a[140:143], v6, s[40:43], s58 offen offset:1024\n"
"v_mfma_f32_16x16x32_bf16 a[12:15], v[124:127], v[32:35], a[12:15]\n"
"ds_read_b128 v[156:159], v4 offset:44032\n"
"v_mfma_f32_16x16x32_bf16 a[76:79], v[124:127], v[36:39], a[76:79]\n"
"v_mfma_f32_16x16x32_bf16 a[128:131], v[24:27], v[32:35], a[128:131]\n"
"v_mfma_f32_16x16x32_bf16 a[132:135], v[24:27], v[36:39], a[132:135]\n"
"buffer_load_dwordx4 a[144:147], v6, s[40:43], s58 offen offset:2048\n"
"v_mfma_f32_16x16x32_bf16 a[16:19], v[128:131], v[32:35], a[16:19]\n"
"ds_read_b128 v[160:163], v4 offset:45056\n"
"v_mfma_f32_16x16x32_bf16 a[80:83], v[128:131], v[36:39], a[80:83]\n"
"v_mfma_f32_16x16x32_bf16 a[20:23], v[132:135], v[32:35], a[20:23]\n"
"ds_read_b128 v[164:167], v4 offset:46080\n"
"v_mfma_f32_16x16x32_bf16 a[84:87], v[132:135], v[36:39], a[84:87]\n"
"buffer_load_dwordx4 a[148:151], v6, s[40:43], s58 offen offset:3072\n"
"s_add_u32 s58, s58, 4096\n"
"v_mfma_f32_16x16x32_bf16 a[24:27], v[136:139], v[32:35], a[24:27]\n"
"ds_read_b128 v[168:171], v4 offset:47104\n"
"v_mfma_f32_16x16x32_bf16 a[88:91], v[136:139], v[36:39], a[88:91]\n"
"v_mfma_f32_16x16x32_bf16 a[28:31], v[140:143], v[32:35], a[28:31]\n"
"ds_read_b128 v[172:175], v4 offset:48128\n"
"v_mfma_f32_16x16x32_bf16 a[92:95], v[140:143], v[36:39], a[92:95]\n"
"buffer_load_dwordx4 a[152:155], v6, s[40:43], s58 offen offset:0\n"
"s_waitcnt lgkmcnt(2)\n"
"v_mfma_f32_16x16x32_bf16 a[32:35], v[144:147], v[32:35], a[32:35]\n"
"ds_read_b128 v[112:115], v4 offset:49152\n"
"v_mfma_f32_16x16x32_bf16 a[96:99], v[144:147], v[36:39], a[96:99]\n"
"v_mfma_f32_16x16x32_bf16 a[36:39], v[148:151], v[32:35], a[36:39]\n"
"ds_read_b128 v[116:119], v4 offset:50176\n"
"v_mfma_f32_16x16x32_bf16 a[100:103], v[148:151], v[36:39], a[100:103]\n"
"buffer_load_dwordx4 a[156:159], v6, s[40:43], s58 offen offset:1024\n"
"v_mfma_f32_16x16x32_bf16 a[40:43], v[152:155], v[32:35], a[40:43]\n"
"ds_read_b128 v[120:123], v4 offset:51200\n"
"v_mfma_f32_16x16x32_bf16 a[104:107], v[152:155], v[36:39], a[104:107]\n"
"v_mfma_f32_16x16x32_bf16 a[44:47], v[156:159], v[32:35], a[44:47]\n"
"ds_read_b128 v[124:127], v4 offset:52224\n"
"v_mfma_f32_16x16x32_bf16 a[108:111], v[156:159], v[36:39], a[108:111]\n"
"buffer_load_dwordx4 a[160:163], v6, s[40:43], s58 offen offset:2048\n"
"v_mfma_f32_16x16x32_bf16 a[48:51], v[160:163], v[32:35], a[48:51]\n"
"ds_read_b128 v[128:131], v4 offset:53248\n"
"v_mfma_f32_16x16x32_bf16 a[112:115], v[160:163], v[36:39], a[112:115]\n"
"v_mfma_f32_16x16x32_bf16 a[52:55], v[164:167], v[32:35], a[52:55]\n"
"ds_read_b128 v[132:135], v4 offset:54272\n"
"v_mfma_f32_16x16x32_bf16 a[116:119], v[164:167], v[36:39], a[116:119]\n"
"buffer_load_dwordx4 a[164:167], v6, s[40:43], s58 offen offset:3072\n"
"s_add_u32 s58, s58, 4096\n"
"s_waitcnt lgkmcnt(2)\n"
"v_mfma_f32_16x16x32_bf16 a[56:59], v[168:171], v[32:35], a[56:59]\n"
"ds_read_b128 v[136:139], v4 offset:55296\n"
"v_mfma_f32_16x16x32_bf16 a[120:123], v[168:171], v[36:39], a[120:123]\n"
"v_mfma_f32_16x16x32_bf16 a[60:63], v[172:175], v[32:35], a[60:63]\n"
"ds_read_b128 v[140:143], v4 offset:56320\n"
"v_mfma_f32_16x16x32_bf16 a[124:127], v[172:175], v[36:39], a[124:127]\n"
"buffer_load_dwordx4 a[168:171], v6, s[40:43], s58 offen offset:0\n"
"v_mfma_f32_16x16x32_bf16 a[0:3], v[112:115], v[40:43], a[0:3]\n"
"ds_read_b128 v[144:147], v4 offset:57344\n"
"v_mfma_f32_16x16x32_bf16 a[64:67], v[112:115], v[44:47], a[64:67]\n"
"v_mfma_f32_16x16x32_bf16 a[4:7], v[116:119], v[40:43], a[4:7]\n"
"ds_read_b128 v[148:151], v4 offset:58368\n"
"v_mfma_f32_16x16x32_bf16 a[68:71], v[116:119], v[44:47], a[68:71]\n"
"buffer_load_dwordx4 a[172:175], v6, s[40:43], s58 offen offset:1024\n"
"v_mfma_f32_16x16x32_bf16 a[8:11], v[120:123], v[40:43], a[8:11]\n"
"ds_read_b128 v[152:155], v4 offset:59392\n"
"v_mfma_f32_16x16x32_bf16 a[72:75], v[120:123], v[44:47], a[72:75]\n"
"v_mfma_f32_16x16x32_bf16 a[12:15], v[124:127], v[40:43], a[12:15]\n"
"ds_read_b128 v[156:159], v4 offset:60416\n"
"v_mfma_f32_16x16x32_bf16 a[76:79], v[124:127], v[44:47], a[76:79]\n"
"buffer_load_dwordx4 a[176:179], v6, s[40:43], s58 offen offset:2048\n"
"v_mfma_f32_16x16x32_bf16 a[128:131], v[24:27], v[40:43], a[128:131]\n"
"v_mfma_f32_16x16x32_bf16 a[132:135], v[24:27], v[44:47], a[132:135]\n"
"s_waitcnt lgkmcnt(2)\n"
"v_mfma_f32_16x16x32_bf16 a[16:19], v[128:131], v[40:43], a[16:19]\n"
"ds_read_b128 v[160:163], v4 offset:61440\n"
"v_mfma_f32_16x16x32_bf16 a[80:83], v[128:131], v[44:47], a[80:83]\n"
"buffer_load_dwordx4 a[180:183], v6, s[40:43], s58 offen offset:3072\n"
"s_add_u32 s58, s58, 4096\n"
"v_mfma_f32_16x16x32_bf16 a[20:23], v[132:135], v[40:43], a[20:23]\n"
"ds_read_b128 v[164:167], v4 offset:62464\n"
"v_mfma_f32_16x16x32_bf16 a[84:87], v[132:135], v[44:47], a[84:87]\n"
"v_mfma_f32_16x16x32_bf16 a[24:27], v[136:139], v[40:43], a[24:27]\n"
"ds_read_b128 v[168:171], v4 offset:63488\n"
"v_mfma_f32_16x16x32_bf16 a[88:91], v[136:139], v[44:47], a[88:91]\n"
"buffer_load_dwordx4 a[184:187], v6, s[40:43], s58 offen offset:0\n"
"v_mfma_f32_16x16x32_bf16 a[28:31], v[140:143], v[40:43], a[28:31]\n"
"ds_read_b128 v[172:175], v4 offset:64512\n"
"v_mfma_f32_16x16x32_bf16 a[92:95], v[140:143], v[44:47], a[92:95]\n"
"v_mfma_f32_16x16x32_bf16 a[32:35], v[144:147], v[40:43], a[32:35]\n"
"v_mfma_f32_16x16x32_bf16 a[96:99], v[144:147], v[44:47], a[96:99]\n"
"buffer_load_dwordx4 a[188:191], v6, s[40:43], s58 offen offset:1024\n"
"v_mfma_f32_16x16x32_bf16 a[36:39], v[148:151], v[40:43], a[36:39]\n"
"v_mfma_f32_16x16x32_bf16 a[100:103], v[148:151], v[44:47], a[100:103]\n"
"s_waitcnt lgkmcnt(1)\n"
"v_mfma_f32_16x16x32_bf16 a[40:43], v[152:155], v[40:43], a[40:43]\n"
"v_mfma_f32_16x16x32_bf16 a[104:107], v[152:155], v[44:47], a[104:107]\n"
"buffer_load_dwordx4 a[192:195], v6, s[40:43], s58 offen offset:2048\n"
"v_mfma_f32_16x16x32_bf16 a[44:47], v[156:159], v[40:43], a[44:47]\n"
"v_mfma_f32_16x16x32_bf16 a[108:111], v[156:159], v[44:47], a[108:111]\n"
"v_mfma_f32_16x16x32_bf16 a[48:51], v[160:163], v[40:43], a[48:51]\n"
"v_mfma_f32_16x16x32_bf16 a[112:115], v[160:163], v[44:47], a[112:115]\n"
"buffer_load_dwordx4 a[196:199], v6, s[40:43], s58 offen offset:3072\n"
"s_add_u32 s58, s58, 4096\n"
"v_mfma_f32_16x16x32_bf16 a[52:55], v[164:167], v[40:43], a[52:55]\n"
"v_mfma_f32_16x16x32_bf16 a[116:119], v[164:167], v[44:47], a[116:119]\n"
"v_mfma_f32_16x16x32_bf16 a[56:59], v[168:171], v[40:43], a[56:59]\n"
"v_mfma_f32_16x16x32_bf16 a[120:123], v[168:171], v[44:47], a[120:123]\n"
"s_waitcnt lgkmcnt(0)\n"
"v_mfma_f32_16x16x32_bf16 a[60:63], v[172:175], v[40:43], a[60:63]\n"
"v_mfma_f32_16x16x32_bf16 a[124:127], v[172:175], v[44:47], a[124:127]\n"
"s_branch L_pvd%=\n"
"L_spin%=:\n"
"buffer_load_dword v8, off, s[48:51], s63 sc1\n"
"v_exp_f32 v91, v91\n"
"v_cvt_pk_bf16_f32 v34, v88, v89\n"
"v_cvt_pk_bf16_f32 v35, v90, v91\n"
"v_exp_f32 v92, v92\n"
"v_exp_f32 v93, v93\n"
"v_exp_f32 v94, v94\n"
"v_exp_f32 v95, v95\n"
"v_cvt_pk_bf16_f32 v38, v92, v93\n"
"v_cvt_pk_bf16_f32 v39, v94, v95\n"
"v_exp_f32 v96, v96\n"
"v_exp_f32 v97, v97\n"
"v_exp_f32 v98, v98\n"
"v_exp_f32 v99, v99\n"
"v_cvt_pk_bf16_f32 v40, v96, v97\n"
"v_cvt_pk_bf16_f32 v41, v98, v99\n"
"v_exp_f32 v100, v100\n"
"v_exp_f32 v101, v101\n"
"v_exp_f32 v102, v102\n"
"v_exp_f32 v103, v103\n"
"v_cvt_pk_bf16_f32 v44, v100, v101\n"
"v_cvt_pk_bf16_f32 v45, v102, v103\n"
"v_exp_f32 v104, v104\n"
"v_exp_f32 v105, v105\n"
"v_exp_f32 v106, v106\n"
"v_exp_f32 v107, v107\n"
"v_cvt_pk_bf16_f32 v42, v104, v105\n"
"v_cvt_pk_bf16_f32 v43, v106, v107\n"
"v_exp_f32 v108, v108\n"
"v_exp_f32 v109, v109\n"
"v_exp_f32 v110, v110\n"
"v_exp_f32 v111, v111\n"
"v_cvt_pk_bf16_f32 v46, v108, v109\n"
"v_cvt_pk_bf16_f32 v47, v110, v111\n"
"L_sp2%=:\n"
"s_waitcnt vmcnt(0)\n"
"v_readfirstlane_b32 s58, v8\n"
"s_cmp_eq_u32 s58, 1\n"
"s_cbranch_scc1 L_got%=\n"
"s_sleep 4\n"
"buffer_load_dword v8, off, s[48:51], s63 sc1\n"
"s_branch L_sp2%=\n"
"L_got%=:\n"
"s_add_u32 s58, s76, 16384\n"
"buffer_load_dwordx2 v[250:251], v6, s[48:51], s58 offen sc1\n"
"s_mov_b32 s58, s76\n"
"s_nop 1\n"
"s_waitcnt lgkmcnt(0)\n"
"v_mfma_f32_16x16x32_bf16 a[0:3], v[112:115], v[32:35], a[0:3]\n"
"ds_read_b128 v[144:147], v4 offset:40960\n"
"v_mfma_f32_16x16x32_bf16 a[64:67], v[112:115], v[36:39], a[64:67]\n"
"buffer_load_dwordx4 v[176:179], v6, s[48:51], s58 offen offset:0 sc1\n"
"v_mfma_f32_16x16x32_bf16 a[4:7], v[116:119], v[32:35], a[4:7]\n"
"ds_read_b128 v[148:151], v4 offset:41984\n"
"v_mfma_f32_16x16x32_bf16 a[68:71], v[116:119], v[36:39], a[68:71]\n"
"v_mfma_f32_16x16x32_bf16 a[8:11], v[120:123], v[32:35], a[8:11]\n"
"ds_read_b128 v[152:155], v4 offset:43008\n"
"v_mfma_f32_16x16x32_bf16 a[72:75], v[120:123], v[36:39], a[72:75]\n"
"buffer_load_dwordx4 v[180:183], v6, s[48:51], s58 offen offset:1024 sc1\n"
"v_mfma_f32_16x16x32_bf16 a[12:15], v[124:127], v[32:35], a[12:15]\n"
"ds_read_b128 v[156:159], v4 offset:44032\n"
"v_mfma_f32_16x16x32_bf16 a[76:79], v[124:127], v[36:39], a[76:79]\n"
"v_mfma_f32_16x16x32_bf16 a[128:131], v[24:27], v[32:35], a[128:131]\n"
"v_mfma_f32_16x16x32_bf16 a[132:135], v[24:27], v[36:39], a[132:135]\n"
"buffer_load_dwordx4 v[184:187], v6, s[48:51], s58 offen offset:2048 sc1\n"
"v_mfma_f32_16x16x32_bf16 a[16:19], v[128:131], v[32:35], a[16:19]\n"
"ds_read_b128 v[160:163], v4 offset:45056\n"
"v_mfma_f32_16x16x32_bf16 a[80:83], v[128:131], v[36:39], a[80:83]\n"
"v_mfma_f32_16x16x32_bf16 a[20:23], v[132:135], v[32:35], a[20:23]\n"
"ds_read_b128 v[164:167], v4 offset:46080\n"
"v_mfma_f32_16x16x32_bf16 a[84:87], v[132:135], v[36:39], a[84:87]\n"
"buffer_load_dwordx4 v[188:191], v6, s[48:51], s58 offen offset:3072 sc1\n"
"s_add_u32 s58, s58, 4096\n"
"v_mfma_f32_16x16x32_bf16 a[24:27], v[136:139], v[32:35], a[24:27]\n"
"ds_read_b128 v[168:171], v4 offset:47104\n"
"v_mfma_f32_16x16x32_bf16 a[88:91], v[136:139], v[36:39], a[88:91]\n"
"v_mfma_f32_16x16x32_bf16 a[28:31], v[140:143], v[32:35], a[28:31]\n"
"ds_read_b128 v[172:175], v4 offset:48128\n"
"v_mfma_f32_16x16x32_bf16 a[92:95], v[140:143], v[36:39], a[92:95]\n"
"buffer_load_dwordx4 v[192:195], v6, s[48:51], s58 offen offset:0 sc1\n"
"s_waitcnt lgkmcnt(2)\n"
"v_mfma_f32_16x16x32_bf16 a[32:35], v[144:147], v[32:35], a[32:35]\n"
"ds_read_b128 v[112:115], v4 offset:49152\n"
"v_mfma_f32_16x16x32_bf16 a[96:99], v[144:147], v[36:39], a[96:99]\n"
"v_mfma_f32_16x16x32_bf16 a[36:39], v[148:151], v[32:35], a[36:39]\n"
"ds_read_b128 v[116:119], v4 offset:50176\n"
"v_mfma_f32_16x16x32_bf16 a[100:103], v[148:151], v[36:39], a[100:103]\n"
"buffer_load_dwordx4 v[196:199], v6, s[48:51], s58 offen offset:1024 sc1\n"
"v_mfma_f32_16x16x32_bf16 a[40:43], v[152:155], v[32:35], a[40:43]\n"
"ds_read_b128 v[120:123], v4 offset:51200\n"
"v_mfma_f32_16x16x32_bf16 a[104:107], v[152:155], v[36:39], a[104:107]\n"
"v_mfma_f32_16x16x32_bf16 a[44:47], v[156:159], v[32:35], a[44:47]\n"
"ds_read_b128 v[124:127], v4 offset:52224\n"
"v_mfma_f32_16x16x32_bf16 a[108:111], v[156:159], v[36:39], a[108:111]\n"
"buffer_load_dwordx4 v[200:203], v6, s[48:51], s58 offen offset:2048 sc1\n"
"v_mfma_f32_16x16x32_bf16 a[48:51], v[160:163], v[32:35], a[48:51]\n"
"ds_read_b128 v[128:131], v4 offset:53248\n"
"v_mfma_f32_16x16x32_bf16 a[112:115], v[160:163], v[36:39], a[112:115]\n"
"v_mfma_f32_16x16x32_bf16 a[52:55], v[164:167], v[32:35], a[52:55]\n"
"ds_read_b128 v[132:135], v4 offset:54272\n"
"v_mfma_f32_16x16x32_bf16 a[116:119], v[164:167], v[36:39], a[116:119]\n"
"buffer_load_dwordx4 v[204:207], v6, s[48:51], s58 offen offset:3072 sc1\n"
"s_add_u32 s58, s58, 4096\n"
"s_waitcnt lgkmcnt(2)\n"
"v_mfma_f32_16x16x32_bf16 a[56:59], v[168:171], v[32:35], a[56:59]\n"
"ds_read_b128 v[136:139], v4 offset:55296\n"
"v_mfma_f32_16x16x32_bf16 a[120:123], v[168:171], v[36:39], a[120:123]\n"
"v_mfma_f32_16x16x32_bf16 a[60:63], v[172:175], v[32:35], a[60:63]\n"
"ds_read_b128 v[140:143], v4 offset:56320\n"
"v_mfma_f32_16x16x32_bf16 a[124:127], v[172:175], v[36:39], a[124:127]\n"
"buffer_load_dwordx4 v[208:211], v6, s[48:51], s58 offen offset:0 sc1\n"
"v_mfma_f32_16x16x32_bf16 a[0:3], v[112:115], v[40:43], a[0:3]\n"
"ds_read_b128 v[144:147], v4 offset:57344\n"
"v_mfma_f32_16x16x32_bf16 a[64:67], v[112:115], v[44:47], a[64:67]\n"
"v_mfma_f32_16x16x32_bf16 a[4:7], v[116:119], v[40:43], a[4:7]\n"
"ds_read_b128 v[148:151], v4 offset:58368\n"
"v_mfma_f32_16x16x32_bf16 a[68:71], v[116:119], v[44:47], a[68:71]\n"
"buffer_load_dwordx4 v[212:215], v6, s[48:51], s58 offen offset:1024 sc1\n"
"v_mfma_f32_16x16x32_bf16 a[8:11], v[120:123], v[40:43], a[8:11]\n"
"ds_read_b128 v[152:155], v4 offset:59392\n"
"v_mfma_f32_16x16x32_bf16 a[72:75], v[120:123], v[44:47], a[72:75]\n"
"v_mfma_f32_16x16x32_bf16 a[12:15], v[124:127], v[40:43], a[12:15]\n"
"ds_read_b128 v[156:159], v4 offset:60416\n"
"v_mfma_f32_16x16x32_bf16 a[76:79], v[124:127], v[44:47], a[76:79]\n"
"buffer_load_dwordx4 v[216:219], v6, s[48:51], s58 offen offset:2048 sc1\n"
"v_mfma_f32_16x16x32_bf16 a[128:131], v[24:27], v[40:43], a[128:131]\n"
"v_mfma_f32_16x16x32_bf16 a[132:135], v[24:27], v[44:47], a[132:135]\n"
"s_waitcnt lgkmcnt(2)\n"
"v_mfma_f32_16x16x32_bf16 a[16:19], v[128:131], v[40:43], a[16:19]\n"
"ds_read_b128 v[160:163], v4 offset:61440\n"
"v_mfma_f32_16x16x32_bf16 a[80:83], v[128:131], v[44:47], a[80:83]\n"
"buffer_load_dwordx4 v[220:223], v6, s[48:51], s58 offen offset:3072 sc1\n"
"s_add_u32 s58, s58, 4096\n"
"v_mfma_f32_16x16x32_bf16 a[20:23], v[132:135], v[40:43], a[20:23]\n"
"ds_read_b128 v[164:167], v4 offset:62464\n"
"v_mfma_f32_16x16x32_bf16 a[84:87], v[132:135], v[44:47], a[84:87]\n"
"v_mfma_f32_16x16x32_bf16 a[24:27], v[136:139], v[40:43], a[24:27]\n"
"ds_read_b128 v[168:171], v4 offset:63488\n"
"v_mfma_f32_16x16x32_bf16 a[88:91], v[136:139], v[44:47], a[88:91]\n"
"buffer_load_dwordx4 v[224:227], v6, s[48:51], s58 offen offset:0 sc1\n"
"v_mfma_f32_16x16x32_bf16 a[28:31], v[140:143], v[40:43], a[28:31]\n"
"ds_read_b128 v[172:175], v4 offset:64512\n"
"v_mfma_f32_16x16x32_bf16 a[92:95], v[140:143], v[44:47], a[92:95]\n"
"v_mfma_f32_16x16x32_bf16 a[32:35], v[144:147], v[40:43], a[32:35]\n"
"v_mfma_f32_16x16x32_bf16 a[96:99], v[144:147], v[44:47], a[96:99]\n"
"buffer_load_dwordx4 v[228:231], v6, s[48:51], s58 offen offset:1024 sc1\n"
"v_mfma_f32_16x16x32_bf16 a[36:39], v[148:151], v[40:43], a[36:39]\n"
"v_mfma_f32_16x16x32_bf16 a[100:103], v[148:151], v[44:47], a[100:103]\n"
"s_waitcnt lgkmcnt(1)\n"
"v_mfma_f32_16x16x32_bf16 a[40:43], v[152:155], v[40:43], a[40:43]\n"
"v_mfma_f32_16x16x32_bf16 a[104:107], v[152:155], v[44:47], a[104:107]\n"
"buffer_load_dwordx4 v[232:235], v6, s[48:51], s58 offen offset:2048 sc1\n"
"v_mfma_f32_16x16x32_bf16 a[44:47], v[156:159], v[40:43], a[44:47]\n"
"v_mfma_f32_16x16x32_bf16 a[108:111], v[156:159], v[44:47], a[108:111]\n"
"v_mfma_f32_16x16x32_bf16 a[48:51], v[160:163], v[40:43], a[48:51]\n"
"v_mfma_f32_16x16x32_bf16 a[112:115], v[160:163], v[44:47], a[112:115]\n"
"buffer_load_dwordx4 v[236:239], v6, s[48:51], s58 offen offset:3072 sc1\n"
"s_add_u32 s58, s58, 4096\n"
"v_mfma_f32_16x16x32_bf16 a[52:55], v[164:167], v[40:43], a[52:55]\n"
"v_mfma_f32_16x16x32_bf16 a[116:119], v[164:167], v[44:47], a[116:119]\n"
"v_mfma_f32_16x16x32_bf16 a[56:59], v[168:171], v[40:43], a[56:59]\n"
"v_mfma_f32_16x16x32_bf16 a[120:123], v[168:171], v[44:47], a[120:123]\n"
"s_waitcnt lgkmcnt(0)\n"
"v_mfma_f32_16x16x32_bf16 a[60:63], v[172:175], v[40:43], a[60:63]\n"
"v_mfma_f32_16x16x32_bf16 a[124:127], v[172:175], v[44:47], a[124:127]\n"
"L_pvd%=:\n"
"s_nop 7\n"
"v_accvgpr_read_b32 v240, a128\n"
"v_rcp_f32 v8, v240\n"
"v_log_f32 v28, v240\n"
"s_nop 0\n"
"v_sub_f32 v28, v28, v16\n"
"v_accvgpr_read_b32 v241, a132\n"
"v_rcp_f32 v10, v241\n"
"v_log_f32 v29, v241\n"
"s_nop 0\n"
"v_sub_f32 v29, v29, v20\n"
"s_cmp_eq_u32 s62, 1\n"
"s_cbranch_scc1 L_e1%=\n"
"s_cmp_eq_u32 s62, 2\n"
"s_cbranch_scc1 L_e2%=\n"
"v_mul_f32 v8, v8, v248\n"
"v_mov_b32 v9, v8\n"
"v_mul_f32 v10, v10, v249\n"
"v_mov_b32 v11, v10\n"
"s_mov_b32 s58, s60\n"
"v_accvgpr_read_b32 v48, a0\n"
"v_accvgpr_read_b32 v49, a1\n"
"v_accvgpr_read_b32 v50, a2\n"
"v_accvgpr_read_b32 v51, a3\n"
"v_accvgpr_read_b32 v52, a4\n"
"v_accvgpr_read_b32 v53, a5\n"
"v_accvgpr_read_b32 v54, a6\n"
"v_accvgpr_read_b32 v55, a7\n"
"v_pk_mul_f32 v[48:49], v[48:49], v[8:9]\n"
"v_pk_mul_f32 v[50:51], v[50:51], v[8:9]\n"
"v_pk_mul_f32 v[52:53], v[52:53], v[8:9]\n"
"v_pk_mul_f32 v[54:55], v[54:55], v[8:9]\n"
"v_cvt_pk_bf16_f32 v48, v48, v49\n"
"v_cvt_pk_bf16_f32 v49, v50, v51\n"
"v_cvt_pk_bf16_f32 v50, v52, v53\n"
"v_cvt_pk_bf16_f32 v51, v54, v55\n"
"buffer_store_dwordx4 v[48:51], v6, s[44:47], s58 offen offset:0 sc1\n"
"v_accvgpr_read_b32 v64, a8\n"
"v_accvgpr_read_b32 v65, a9\n"
"v_accvgpr_read_b32 v66, a10\n"
"v_accvgpr_read_b32 v67, a11\n"
"v_accvgpr_read_b32 v68, a12\n"
"v_accvgpr_read_b32 v69, a13\n"
"v_accvgpr_read_b32 v70, a14\n"
"v_accvgpr_read_b32 v71, a15\n"
"v_pk_mul_f32 v[64:65], v[64:65], v[8:9]\n"
"v_pk_mul_f32 v[66:67], v[66:67], v[8:9]\n"
"v_pk_mul_f32 v[68:69], v[68:69], v[8:9]\n"
"v_pk_mul_f32 v[70:71], v[70:71], v[8:9]\n"
"v_cvt_pk_bf16_f32 v64, v64, v65\n"
"v_cvt_pk_bf16_f32 v65, v66, v67\n"
"v_cvt_pk_bf16_f32 v66, v68, v69\n"
"v_cvt_pk_bf16_f32 v67, v70, v71\n"
"buffer_store_dwordx4 v[64:67], v6, s[44:47], s58 offen offset:1024 sc1\n"
"v_accvgpr_read_b32 v80, a16\n"
"v_accvgpr_read_b32 v81, a17\n"
"v_accvgpr_read_b32 v82, a18\n"
"v_accvgpr_read_b32 v83, a19\n"
"v_accvgpr_read_b32 v84, a20\n"
"v_accvgpr_read_b32 v85, a21\n"
"v_accvgpr_read_b32 v86, a22\n"
"v_accvgpr_read_b32 v87, a23\n"
"v_pk_mul_f32 v[80:81], v[80:81], v[8:9]\n"
"v_pk_mul_f32 v[82:83], v[82:83], v[8:9]\n"
"v_pk_mul_f32 v[84:85], v[84:85], v[8:9]\n"
"v_pk_mul_f32 v[86:87], v[86:87], v[8:9]\n"
"v_cvt_pk_bf16_f32 v80, v80, v81\n"
"v_cvt_pk_bf16_f32 v81, v82, v83\n"
"v_cvt_pk_bf16_f32 v82, v84, v85\n"
"v_cvt_pk_bf16_f32 v83, v86, v87\n"
"buffer_store_dwordx4 v[80:83], v6, s[44:47], s58 offen offset:2048 sc1\n"
"v_accvgpr_read_b32 v96, a24\n"
"v_accvgpr_read_b32 v97, a25\n"
"v_accvgpr_read_b32 v98, a26\n"
"v_accvgpr_read_b32 v99, a27\n"
"v_accvgpr_read_b32 v100, a28\n"
"v_accvgpr_read_b32 v101, a29\n"
"v_accvgpr_read_b32 v102, a30\n"
"v_accvgpr_read_b32 v103, a31\n"
"v_pk_mul_f32 v[96:97], v[96:97], v[8:9]\n"
"v_pk_mul_f32 v[98:99], v[98:99], v[8:9]\n"
"v_pk_mul_f32 v[100:101], v[100:101], v[8:9]\n"
"v_pk_mul_f32 v[102:103], v[102:103], v[8:9]\n"
"v_cvt_pk_bf16_f32 v96, v96, v97\n"
"v_cvt_pk_bf16_f32 v97, v98, v99\n"
"v_cvt_pk_bf16_f32 v98, v100, v101\n"
"v_cvt_pk_bf16_f32 v99, v102, v103\n"
"buffer_store_dwordx4 v[96:99], v6, s[44:47], s58 offen offset:3072 sc1\n"
"s_add_u32 s58, s58, 4096\n"
"v_accvgpr_read_b32 v48, a32\n"
"v_accvgpr_read_b32 v49, a33\n"
"v_accvgpr_read_b32 v50, a34\n"
"v_accvgpr_read_b32 v51, a35\n"
"v_accvgpr_read_b32 v52, a36\n"
"v_accvgpr_read_b32 v53, a37\n"
"v_accvgpr_read_b32 v54, a38\n"
"v_accvgpr_read_b32 v55, a39\n"
"v_pk_mul_f32 v[48:49], v[48:49], v[8:9]\n"
"v_pk_mul_f32 v[50:51], v[50:51], v[8:9]\n"
"v_pk_mul_f32 v[52:53], v[52:53], v[8:9]\n"
"v_pk_mul_f32 v[54:55], v[54:55], v[8:9]\n"
"v_cvt_pk_bf16_f32 v48, v48, v49\n"
"v_cvt_pk_bf16_f32 v49, v50, v51\n"
"v_cvt_pk_bf16_f32 v50, v52, v53\n"
"v_cvt_pk_bf16_f32 v51, v54, v55\n"
"buffer_store_dwordx4 v[48:51], v6, s[44:47], s58 offen offset:0 sc1\n"
"v_accvgpr_read_b32 v64, a40\n"
"v_accvgpr_read_b32 v65, a41\n"
"v_accvgpr_read_b32 v66, a42\n"
"v_accvgpr_read_b32 v67, a43\n"
"v_accvgpr_read_b32 v68, a44\n"
"v_accvgpr_read_b32 v69, a45\n"
"v_accvgpr_read_b32 v70, a46\n"
"v_accvgpr_read_b32 v71, a47\n"
"v_pk_mul_f32 v[64:65], v[64:65], v[8:9]\n"
"v_pk_mul_f32 v[66:67], v[66:67], v[8:9]\n"
"v_pk_mul_f32 v[68:69], v[68:69], v[8:9]\n"
"v_pk_mul_f32 v[70:71], v[70:71], v[8:9]\n"
"v_cvt_pk_bf16_f32 v64, v64, v65\n"
"v_cvt_pk_bf16_f32 v65, v66, v67\n"
"v_cvt_pk_bf16_f32 v66, v68, v69\n"
"v_cvt_pk_bf16_f32 v67, v70, v71\n"
"buffer_store_dwordx4 v[64:67], v6, s[44:47], s58 offen offset:1024 sc1\n"
"v_accvgpr_read_b32 v80, a48\n"
"v_accvgpr_read_b32 v81, a49\n"
"v_accvgpr_read_b32 v82, a50\n"
"v_accvgpr_read_b32 v83, a51\n"
"v_accvgpr_read_b32 v84, a52\n"
"v_accvgpr_read_b32 v85, a53\n"
"v_accvgpr_read_b32 v86, a54\n"
"v_accvgpr_read_b32 v87, a55\n"
"v_pk_mul_f32 v[80:81], v[80:81], v[8:9]\n"
"v_pk_mul_f32 v[82:83], v[82:83], v[8:9]\n"
"v_pk_mul_f32 v[84:85], v[84:85], v[8:9]\n"
"v_pk_mul_f32 v[86:87], v[86:87], v[8:9]\n"
"v_cvt_pk_bf16_f32 v80, v80, v81\n"
"v_cvt_pk_bf16_f32 v81, v82, v83\n"
"v_cvt_pk_bf16_f32 v82, v84, v85\n"
"v_cvt_pk_bf16_f32 v83, v86, v87\n"
"buffer_store_dwordx4 v[80:83], v6, s[44:47], s58 offen offset:2048 sc1\n"
"v_accvgpr_read_b32 v96, a56\n"
"v_accvgpr_read_b32 v97, a57\n"
"v_accvgpr_read_b32 v98, a58\n"
"v_accvgpr_read_b32 v99, a59\n"
"v_accvgpr_read_b32 v100, a60\n"
"v_accvgpr_read_b32 v101, a61\n"
"v_accvgpr_read_b32 v102, a62\n"
"v_accvgpr_read_b32 v103, a63\n"
"v_pk_mul_f32 v[96:97], v[96:97], v[8:9]\n"
"v_pk_mul_f32 v[98:99], v[98:99], v[8:9]\n"
"v_pk_mul_f32 v[100:101], v[100:101], v[8:9]\n"
"v_pk_mul_f32 v[102:103], v[102:103], v[8:9]\n"
"v_cvt_pk_bf16_f32 v96, v96, v97\n"
"v_cvt_pk_bf16_f32 v97, v98, v99\n"
"v_cvt_pk_bf16_f32 v98, v100, v101\n"
"v_cvt_pk_bf16_f32 v99, v102, v103\n"
"buffer_store_dwordx4 v[96:99], v6, s[44:47], s58 offen offset:3072 sc1\n"
"s_add_u32 s58, s58, 184320\n"
"v_accvgpr_read_b32 v48, a64\n"
"v_accvgpr_read_b32 v49, a65\n"
"v_accvgpr_read_b32 v50, a66\n"
"v_accvgpr_read_b32 v51, a67\n"
"v_accvgpr_read_b32 v52, a68\n"
"v_accvgpr_read_b32 v53, a69\n"
"v_accvgpr_read_b32 v54, a70\n"
"v_accvgpr_read_b32 v55, a71\n"
"v_pk_mul_f32 v[48:49], v[48:49], v[10:11]\n"
"v_pk_mul_f32 v[50:51], v[50:51], v[10:11]\n"
"v_pk_mul_f32 v[52:53], v[52:53], v[10:11]\n"
"v_pk_mul_f32 v[54:55], v[54:55], v[10:11]\n"
"v_cvt_pk_bf16_f32 v48, v48, v49\n"
"v_cvt_pk_bf16_f32 v49, v50, v51\n"
"v_cvt_pk_bf16_f32 v50, v52, v53\n"
"v_cvt_pk_bf16_f32 v51, v54, v55\n"
"buffer_store_dwordx4 v[48:51], v6, s[44:47], s58 offen offset:0 sc1\n"
"v_accvgpr_read_b32 v64, a72\n"
"v_accvgpr_read_b32 v65, a73\n"
"v_accvgpr_read_b32 v66, a74\n"
"v_accvgpr_read_b32 v67, a75\n"
"v_accvgpr_read_b32 v68, a76\n"
"v_accvgpr_read_b32 v69, a77\n"
"v_accvgpr_read_b32 v70, a78\n"
"v_accvgpr_read_b32 v71, a79\n"
"v_pk_mul_f32 v[64:65], v[64:65], v[10:11]\n"
"v_pk_mul_f32 v[66:67], v[66:67], v[10:11]\n"
"v_pk_mul_f32 v[68:69], v[68:69], v[10:11]\n"
"v_pk_mul_f32 v[70:71], v[70:71], v[10:11]\n"
"v_cvt_pk_bf16_f32 v64, v64, v65\n"
"v_cvt_pk_bf16_f32 v65, v66, v67\n"
"v_cvt_pk_bf16_f32 v66, v68, v69\n"
"v_cvt_pk_bf16_f32 v67, v70, v71\n"
"buffer_store_dwordx4 v[64:67], v6, s[44:47], s58 offen offset:1024 sc1\n"
"v_accvgpr_read_b32 v80, a80\n"
"v_accvgpr_read_b32 v81, a81\n"
"v_accvgpr_read_b32 v82, a82\n"
"v_accvgpr_read_b32 v83, a83\n"
"v_accvgpr_read_b32 v84, a84\n"
"v_accvgpr_read_b32 v85, a85\n"
"v_accvgpr_read_b32 v86, a86\n"
"v_accvgpr_read_b32 v87, a87\n"
"v_pk_mul_f32 v[80:81], v[80:81], v[10:11]\n"
"v_pk_mul_f32 v[82:83], v[82:83], v[10:11]\n"
"v_pk_mul_f32 v[84:85], v[84:85], v[10:11]\n"
"v_pk_mul_f32 v[86:87], v[86:87], v[10:11]\n"
"v_cvt_pk_bf16_f32 v80, v80, v81\n"
"v_cvt_pk_bf16_f32 v81, v82, v83\n"
"v_cvt_pk_bf16_f32 v82, v84, v85\n"
"v_cvt_pk_bf16_f32 v83, v86, v87\n"
"buffer_store_dwordx4 v[80:83], v6, s[44:47], s58 offen offset:2048 sc1\n"
"v_accvgpr_read_b32 v96, a88\n"
"v_accvgpr_read_b32 v97, a89\n"
"v_accvgpr_read_b32 v98, a90\n"
"v_accvgpr_read_b32 v99, a91\n"
"v_accvgpr_read_b32 v100, a92\n"
"v_accvgpr_read_b32 v101, a93\n"
"v_accvgpr_read_b32 v102, a94\n"
"v_accvgpr_read_b32 v103, a95\n"
"v_pk_mul_f32 v[96:97], v[96:97], v[10:11]\n"
"v_pk_mul_f32 v[98:99], v[98:99], v[10:11]\n"
"v_pk_mul_f32 v[100:101], v[100:101], v[10:11]\n"
"v_pk_mul_f32 v[102:103], v[102:103], v[10:11]\n"
"v_cvt_pk_bf16_f32 v96, v96, v97\n"
"v_cvt_pk_bf16_f32 v97, v98, v99\n"
"v_cvt_pk_bf16_f32 v98, v100, v101\n"
"v_cvt_pk_bf16_f32 v99, v102, v103\n"
"buffer_store_dwordx4 v[96:99], v6, s[44:47], s58 offen offset:3072 sc1\n"
"s_add_u32 s58, s58, 4096\n"
"v_accvgpr_read_b32 v48, a96\n"
"v_accvgpr_read_b32 v49, a97\n"
"v_accvgpr_read_b32 v50, a98\n"
"v_accvgpr_read_b32 v51, a99\n"
"v_accvgpr_read_b32 v52, a100\n"
"v_accvgpr_read_b32 v53, a101\n"
"v_accvgpr_read_b32 v54, a102\n"
"v_accvgpr_read_b32 v55, a103\n"
"v_pk_mul_f32 v[48:49], v[48:49], v[10:11]\n"
"v_pk_mul_f32 v[50:51], v[50:51], v[10:11]\n"
"v_pk_mul_f32 v[52:53], v[52:53], v[10:11]\n"
"v_pk_mul_f32 v[54:55], v[54:55], v[10:11]\n"
"v_cvt_pk_bf16_f32 v48, v48, v49\n"
"v_cvt_pk_bf16_f32 v49, v50, v51\n"
"v_cvt_pk_bf16_f32 v50, v52, v53\n"
"v_cvt_pk_bf16_f32 v51, v54, v55\n"
"buffer_store_dwordx4 v[48:51], v6, s[44:47], s58 offen offset:0 sc1\n"
"v_accvgpr_read_b32 v64, a104\n"
"v_accvgpr_read_b32 v65, a105\n"
"v_accvgpr_read_b32 v66, a106\n"
"v_accvgpr_read_b32 v67, a107\n"
"v_accvgpr_read_b32 v68, a108\n"
"v_accvgpr_read_b32 v69, a109\n"
"v_accvgpr_read_b32 v70, a110\n"
"v_accvgpr_read_b32 v71, a111\n"
"v_pk_mul_f32 v[64:65], v[64:65], v[10:11]\n"
"v_pk_mul_f32 v[66:67], v[66:67], v[10:11]\n"
"v_pk_mul_f32 v[68:69], v[68:69], v[10:11]\n"
"v_pk_mul_f32 v[70:71], v[70:71], v[10:11]\n"
"v_cvt_pk_bf16_f32 v64, v64, v65\n"
"v_cvt_pk_bf16_f32 v65, v66, v67\n"
"v_cvt_pk_bf16_f32 v66, v68, v69\n"
"v_cvt_pk_bf16_f32 v67, v70, v71\n"
"buffer_store_dwordx4 v[64:67], v6, s[44:47], s58 offen offset:1024 sc1\n"
"v_accvgpr_read_b32 v80, a112\n"
"v_accvgpr_read_b32 v81, a113\n"
"v_accvgpr_read_b32 v82, a114\n"
"v_accvgpr_read_b32 v83, a115\n"
"v_accvgpr_read_b32 v84, a116\n"
"v_accvgpr_read_b32 v85, a117\n"
"v_accvgpr_read_b32 v86, a118\n"
"v_accvgpr_read_b32 v87, a119\n"
"v_pk_mul_f32 v[80:81], v[80:81], v[10:11]\n"
"v_pk_mul_f32 v[82:83], v[82:83], v[10:11]\n"
"v_pk_mul_f32 v[84:85], v[84:85], v[10:11]\n"
"v_pk_mul_f32 v[86:87], v[86:87], v[10:11]\n"
"v_cvt_pk_bf16_f32 v80, v80, v81\n"
"v_cvt_pk_bf16_f32 v81, v82, v83\n"
"v_cvt_pk_bf16_f32 v82, v84, v85\n"
"v_cvt_pk_bf16_f32 v83, v86, v87\n"
"buffer_store_dwordx4 v[80:83], v6, s[44:47], s58 offen offset:2048 sc1\n"
"v_accvgpr_read_b32 v96, a120\n"
"v_accvgpr_read_b32 v97, a121\n"
"v_accvgpr_read_b32 v98, a122\n"
"v_accvgpr_read_b32 v99, a123\n"
"v_accvgpr_read_b32 v100, a124\n"
"v_accvgpr_read_b32 v101, a125\n"
"v_accvgpr_read_b32 v102, a126\n"
"v_accvgpr_read_b32 v103, a127\n"
"v_pk_mul_f32 v[96:97], v[96:97], v[10:11]\n"
"v_pk_mul_f32 v[98:99], v[98:99], v[10:11]\n"
"v_pk_mul_f32 v[100:101], v[100:101], v[10:11]\n"
"v_pk_mul_f32 v[102:103], v[102:103], v[10:11]\n"
"v_cvt_pk_bf16_f32 v96, v96, v97\n"
"v_cvt_pk_bf16_f32 v97, v98, v99\n"
"v_cvt_pk_bf16_f32 v98, v100, v101\n"
"v_cvt_pk_bf16_f32 v99, v102, v103\n"
"buffer_store_dwordx4 v[96:99], v6, s[44:47], s58 offen offset:3072 sc1\n"
"s_add_u32 s58, s58, 4096\n"
"s_and_b32 s58, s52, 15\n"
"s_lshl_b32 s58, s58, 7\n"
"s_add_u32 s58, s58, 32768\n"
"s_cmp_eq_u32 s57, 0\n"
"s_cselect_b32 s74, s58, 0\n"
"s_branch L_ee%=\n"
"L_e1%=:\n"
"v_mov_b32 v9, v8\n"
"v_mov_b32 v11, v10\n"
"s_mov_b32 s58, s76\n"
"v_accvgpr_read_b32 v48, a0\n"
"v_accvgpr_read_b32 v49, a1\n"
"v_accvgpr_read_b32 v50, a2\n"
"v_accvgpr_read_b32 v51, a3\n"
"v_accvgpr_read_b32 v52, a4\n"
"v_accvgpr_read_b32 v53, a5\n"
"v_accvgpr_read_b32 v54, a6\n"
"v_accvgpr_read_b32 v55, a7\n"
"v_pk_mul_f32 v[48:49], v[48:49], v[8:9]\n"
"v_pk_mul_f32 v[50:51], v[50:51], v[8:9]\n"
"v_pk_mul_f32 v[52:53], v[52:53], v[8:9]\n"
"v_pk_mul_f32 v[54:55], v[54:55], v[8:9]\n"
"v_cvt_pk_bf16_f32 v48, v48, v49\n"
"v_cvt_pk_bf16_f32 v49, v50, v51\n"
"v_cvt_pk_bf16_f32 v50, v52, v53\n"
"v_cvt_pk_bf16_f32 v51, v54, v55\n"
"buffer_store_dwordx4 v[48:51], v6, s[48:51], s58 offen offset:0 sc1\n"
"v_accvgpr_read_b32 v64, a8\n"
"v_accvgpr_read_b32 v65, a9\n"
"v_accvgpr_read_b32 v66, a10\n"
"v_accvgpr_read_b32 v67, a11\n"
"v_accvgpr_read_b32 v68, a12\n"
"v_accvgpr_read_b32 v69, a13\n"
"v_accvgpr_read_b32 v70, a14\n"
"v_accvgpr_read_b32 v71, a15\n"
"v_pk_mul_f32 v[64:65], v[64:65], v[8:9]\n"
"v_pk_mul_f32 v[66:67], v[66:67], v[8:9]\n"
"v_pk_mul_f32 v[68:69], v[68:69], v[8:9]\n"
"v_pk_mul_f32 v[70:71], v[70:71], v[8:9]\n"
"v_cvt_pk_bf16_f32 v64, v64, v65\n"
"v_cvt_pk_bf16_f32 v65, v66, v67\n"
"v_cvt_pk_bf16_f32 v66, v68, v69\n"
"v_cvt_pk_bf16_f32 v67, v70, v71\n"
"buffer_store_dwordx4 v[64:67], v6, s[48:51], s58 offen offset:1024 sc1\n"
"v_accvgpr_read_b32 v80, a16\n"
"v_accvgpr_read_b32 v81, a17\n"
"v_accvgpr_read_b32 v82, a18\n"
"v_accvgpr_read_b32 v83, a19\n"
"v_accvgpr_read_b32 v84, a20\n"
"v_accvgpr_read_b32 v85, a21\n"
"v_accvgpr_read_b32 v86, a22\n"
"v_accvgpr_read_b32 v87, a23\n"
"v_pk_mul_f32 v[80:81], v[80:81], v[8:9]\n"
"v_pk_mul_f32 v[82:83], v[82:83], v[8:9]\n"
"v_pk_mul_f32 v[84:85], v[84:85], v[8:9]\n"
"v_pk_mul_f32 v[86:87], v[86:87], v[8:9]\n"
"v_cvt_pk_bf16_f32 v80, v80, v81\n"
"v_cvt_pk_bf16_f32 v81, v82, v83\n"
"v_cvt_pk_bf16_f32 v82, v84, v85\n"
"v_cvt_pk_bf16_f32 v83, v86, v87\n"
"buffer_store_dwordx4 v[80:83], v6, s[48:51], s58 offen offset:2048 sc1\n"
"v_accvgpr_read_b32 v96, a24\n"
"v_accvgpr_read_b32 v97, a25\n"
"v_accvgpr_read_b32 v98, a26\n"
"v_accvgpr_read_b32 v99, a27\n"
"v_accvgpr_read_b32 v100, a28\n"
"v_accvgpr_read_b32 v101, a29\n"
"v_accvgpr_read_b32 v102, a30\n"
"v_accvgpr_read_b32 v103, a31\n"
"v_pk_mul_f32 v[96:97], v[96:97], v[8:9]\n"
"v_pk_mul_f32 v[98:99], v[98:99], v[8:9]\n"
"v_pk_mul_f32 v[100:101], v[100:101], v[8:9]\n"
"v_pk_mul_f32 v[102:103], v[102:103], v[8:9]\n"
"v_cvt_pk_bf16_f32 v96, v96, v97\n"
"v_cvt_pk_bf16_f32 v97, v98, v99\n"
"v_cvt_pk_bf16_f32 v98, v100, v101\n"
"v_cvt_pk_bf16_f32 v99, v102, v103\n"
"buffer_store_dwordx4 v[96:99], v6, s[48:51], s58 offen offset:3072 sc1\n"
"s_add_u32 s58, s58, 4096\n"
"v_accvgpr_read_b32 v48, a32\n"
"v_accvgpr_read_b32 v49, a33\n"
"v_accvgpr_read_b32 v50, a34\n"
"v_accvgpr_read_b32 v51, a35\n"
"v_accvgpr_read_b32 v52, a36\n"
"v_accvgpr_read_b32 v53, a37\n"
"v_accvgpr_read_b32 v54, a38\n"
"v_accvgpr_read_b32 v55, a39\n"
"v_pk_mul_f32 v[48:49], v[48:49], v[8:9]\n"
"v_pk_mul_f32 v[50:51], v[50:51], v[8:9]\n"
"v_pk_mul_f32 v[52:53], v[52:53], v[8:9]\n"
"v_pk_mul_f32 v[54:55], v[54:55], v[8:9]\n"
"v_cvt_pk_bf16_f32 v48, v48, v49\n"
"v_cvt_pk_bf16_f32 v49, v50, v51\n"
"v_cvt_pk_bf16_f32 v50, v52, v53\n"
"v_cvt_pk_bf16_f32 v51, v54, v55\n"
"buffer_store_dwordx4 v[48:51], v6, s[48:51], s58 offen offset:0 sc1\n"
"v_accvgpr_read_b32 v64, a40\n"
"v_accvgpr_read_b32 v65, a41\n"
"v_accvgpr_read_b32 v66, a42\n"
"v_accvgpr_read_b32 v67, a43\n"
"v_accvgpr_read_b32 v68, a44\n"
"v_accvgpr_read_b32 v69, a45\n"
"v_accvgpr_read_b32 v70, a46\n"
"v_accvgpr_read_b32 v71, a47\n"
"v_pk_mul_f32 v[64:65], v[64:65], v[8:9]\n"
"v_pk_mul_f32 v[66:67], v[66:67], v[8:9]\n"
"v_pk_mul_f32 v[68:69], v[68:69], v[8:9]\n"
"v_pk_mul_f32 v[70:71], v[70:71], v[8:9]\n"
"v_cvt_pk_bf16_f32 v64, v64, v65\n"
"v_cvt_pk_bf16_f32 v65, v66, v67\n"
"v_cvt_pk_bf16_f32 v66, v68, v69\n"
"v_cvt_pk_bf16_f32 v67, v70, v71\n"
"buffer_store_dwordx4 v[64:67], v6, s[48:51], s58 offen offset:1024 sc1\n"
"v_accvgpr_read_b32 v80, a48\n"
"v_accvgpr_read_b32 v81, a49\n"
"v_accvgpr_read_b32 v82, a50\n"
"v_accvgpr_read_b32 v83, a51\n"
"v_accvgpr_read_b32 v84, a52\n"
"v_accvgpr_read_b32 v85, a53\n"
"v_accvgpr_read_b32 v86, a54\n"
"v_accvgpr_read_b32 v87, a55\n"
"v_pk_mul_f32 v[80:81], v[80:81], v[8:9]\n"
"v_pk_mul_f32 v[82:83], v[82:83], v[8:9]\n"
"v_pk_mul_f32 v[84:85], v[84:85], v[8:9]\n"
"v_pk_mul_f32 v[86:87], v[86:87], v[8:9]\n"
"v_cvt_pk_bf16_f32 v80, v80, v81\n"
"v_cvt_pk_bf16_f32 v81, v82, v83\n"
"v_cvt_pk_bf16_f32 v82, v84, v85\n"
"v_cvt_pk_bf16_f32 v83, v86, v87\n"
"buffer_store_dwordx4 v[80:83], v6, s[48:51], s58 offen offset:2048 sc1\n"
"v_accvgpr_read_b32 v96, a56\n"
"v_accvgpr_read_b32 v97, a57\n"
"v_accvgpr_read_b32 v98, a58\n"
"v_accvgpr_read_b32 v99, a59\n"
"v_accvgpr_read_b32 v100, a60\n"
"v_accvgpr_read_b32 v101, a61\n"
"v_accvgpr_read_b32 v102, a62\n"
"v_accvgpr_read_b32 v103, a63\n"
"v_pk_mul_f32 v[96:97], v[96:97], v[8:9]\n"
"v_pk_mul_f32 v[98:99], v[98:99], v[8:9]\n"
"v_pk_mul_f32 v[100:101], v[100:101], v[8:9]\n"
"v_pk_mul_f32 v[102:103], v[102:103], v[8:9]\n"
"v_cvt_pk_bf16_f32 v96, v96, v97\n"
"v_cvt_pk_bf16_f32 v97, v98, v99\n"
"v_cvt_pk_bf16_f32 v98, v100, v101\n"
"v_cvt_pk_bf16_f32 v99, v102, v103\n"
"buffer_store_dwordx4 v[96:99], v6, s[48:51], s58 offen offset:3072 sc1\n"
"s_add_u32 s58, s58, 4096\n"
"v_accvgpr_read_b32 v48, a64\n"
"v_accvgpr_read_b32 v49, a65\n"
"v_accvgpr_read_b32 v50, a66\n"
"v_accvgpr_read_b32 v51, a67\n"
"v_accvgpr_read_b32 v52, a68\n"
"v_accvgpr_read_b32 v53, a69\n"
"v_accvgpr_read_b32 v54, a70\n"
"v_accvgpr_read_b32 v55, a71\n"
"v_pk_mul_f32 v[48:49], v[48:49], v[10:11]\n"
"v_pk_mul_f32 v[50:51], v[50:51], v[10:11]\n"
"v_pk_mul_f32 v[52:53], v[52:53], v[10:11]\n"
"v_pk_mul_f32 v[54:55], v[54:55], v[10:11]\n"
"v_cvt_pk_bf16_f32 v48, v48, v49\n"
"v_cvt_pk_bf16_f32 v49, v50, v51\n"
"v_cvt_pk_bf16_f32 v50, v52, v53\n"
"v_cvt_pk_bf16_f32 v51, v54, v55\n"
"buffer_store_dwordx4 v[48:51], v6, s[48:51], s58 offen offset:0 sc1\n"
"v_accvgpr_read_b32 v64, a72\n"
"v_accvgpr_read_b32 v65, a73\n"
"v_accvgpr_read_b32 v66, a74\n"
"v_accvgpr_read_b32 v67, a75\n"
"v_accvgpr_read_b32 v68, a76\n"
"v_accvgpr_read_b32 v69, a77\n"
"v_accvgpr_read_b32 v70, a78\n"
"v_accvgpr_read_b32 v71, a79\n"
"v_pk_mul_f32 v[64:65], v[64:65], v[10:11]\n"
"v_pk_mul_f32 v[66:67], v[66:67], v[10:11]\n"
"v_pk_mul_f32 v[68:69], v[68:69], v[10:11]\n"
"v_pk_mul_f32 v[70:71], v[70:71], v[10:11]\n"
"v_cvt_pk_bf16_f32 v64, v64, v65\n"
"v_cvt_pk_bf16_f32 v65, v66, v67\n"
"v_cvt_pk_bf16_f32 v66, v68, v69\n"
"v_cvt_pk_bf16_f32 v67, v70, v71\n"
"buffer_store_dwordx4 v[64:67], v6, s[48:51], s58 offen offset:1024 sc1\n"
"v_accvgpr_read_b32 v80, a80\n"
"v_accvgpr_read_b32 v81, a81\n"
"v_accvgpr_read_b32 v82, a82\n"
"v_accvgpr_read_b32 v83, a83\n"
"v_accvgpr_read_b32 v84, a84\n"
"v_accvgpr_read_b32 v85, a85\n"
"v_accvgpr_read_b32 v86, a86\n"
"v_accvgpr_read_b32 v87, a87\n"
"v_pk_mul_f32 v[80:81], v[80:81], v[10:11]\n"
"v_pk_mul_f32 v[82:83], v[82:83], v[10:11]\n"
"v_pk_mul_f32 v[84:85], v[84:85], v[10:11]\n"
"v_pk_mul_f32 v[86:87], v[86:87], v[10:11]\n"
"v_cvt_pk_bf16_f32 v80, v80, v81\n"
"v_cvt_pk_bf16_f32 v81, v82, v83\n"
"v_cvt_pk_bf16_f32 v82, v84, v85\n"
"v_cvt_pk_bf16_f32 v83, v86, v87\n"
"buffer_store_dwordx4 v[80:83], v6, s[48:51], s58 offen offset:2048 sc1\n"
"v_accvgpr_read_b32 v96, a88\n"
"v_accvgpr_read_b32 v97, a89\n"
"v_accvgpr_read_b32 v98, a90\n"
"v_accvgpr_read_b32 v99, a91\n"
"v_accvgpr_read_b32 v100, a92\n"
"v_accvgpr_read_b32 v101, a93\n"
"v_accvgpr_read_b32 v102, a94\n"
"v_accvgpr_read_b32 v103, a95\n"
"v_pk_mul_f32 v[96:97], v[96:97], v[10:11]\n"
"v_pk_mul_f32 v[98:99], v[98:99], v[10:11]\n"
"v_pk_mul_f32 v[100:101], v[100:101], v[10:11]\n"
"v_pk_mul_f32 v[102:103], v[102:103], v[10:11]\n"
"v_cvt_pk_bf16_f32 v96, v96, v97\n"
"v_cvt_pk_bf16_f32 v97, v98, v99\n"
"v_cvt_pk_bf16_f32 v98, v100, v101\n"
"v_cvt_pk_bf16_f32 v99, v102, v103\n"
"buffer_store_dwordx4 v[96:99], v6, s[48:51], s58 offen offset:3072 sc1\n"
"s_add_u32 s58, s58, 4096\n"
"v_accvgpr_read_b32 v48, a96\n"
"v_accvgpr_read_b32 v49, a97\n"
"v_accvgpr_read_b32 v50, a98\n"
"v_accvgpr_read_b32 v51, a99\n"
"v_accvgpr_read_b32 v52, a100\n"
"v_accvgpr_read_b32 v53, a101\n"
"v_accvgpr_read_b32 v54, a102\n"
"v_accvgpr_read_b32 v55, a103\n"
"v_pk_mul_f32 v[48:49], v[48:49], v[10:11]\n"
"v_pk_mul_f32 v[50:51], v[50:51], v[10:11]\n"
"v_pk_mul_f32 v[52:53], v[52:53], v[10:11]\n"
"v_pk_mul_f32 v[54:55], v[54:55], v[10:11]\n"
"v_cvt_pk_bf16_f32 v48, v48, v49\n"
"v_cvt_pk_bf16_f32 v49, v50, v51\n"
"v_cvt_pk_bf16_f32 v50, v52, v53\n"
"v_cvt_pk_bf16_f32 v51, v54, v55\n"
"buffer_store_dwordx4 v[48:51], v6, s[48:51], s58 offen offset:0 sc1\n"
"v_accvgpr_read_b32 v64, a104\n"
"v_accvgpr_read_b32 v65, a105\n"
"v_accvgpr_read_b32 v66, a106\n"
"v_accvgpr_read_b32 v67, a107\n"
"v_accvgpr_read_b32 v68, a108\n"
"v_accvgpr_read_b32 v69, a109\n"
"v_accvgpr_read_b32 v70, a110\n"
"v_accvgpr_read_b32 v71, a111\n"
"v_pk_mul_f32 v[64:65], v[64:65], v[10:11]\n"
"v_pk_mul_f32 v[66:67], v[66:67], v[10:11]\n"
"v_pk_mul_f32 v[68:69], v[68:69], v[10:11]\n"
"v_pk_mul_f32 v[70:71], v[70:71], v[10:11]\n"
"v_cvt_pk_bf16_f32 v64, v64, v65\n"
"v_cvt_pk_bf16_f32 v65, v66, v67\n"
"v_cvt_pk_bf16_f32 v66, v68, v69\n"
"v_cvt_pk_bf16_f32 v67, v70, v71\n"
"buffer_store_dwordx4 v[64:67], v6, s[48:51], s58 offen offset:1024 sc1\n"
"v_accvgpr_read_b32 v80, a112\n"
"v_accvgpr_read_b32 v81, a113\n"
"v_accvgpr_read_b32 v82, a114\n"
"v_accvgpr_read_b32 v83, a115\n"
"v_accvgpr_read_b32 v84, a116\n"
"v_accvgpr_read_b32 v85, a117\n"
"v_accvgpr_read_b32 v86, a118\n"
"v_accvgpr_read_b32 v87, a119\n"
"v_pk_mul_f32 v[80:81], v[80:81], v[10:11]\n"
"v_pk_mul_f32 v[82:83], v[82:83], v[10:11]\n"
"v_pk_mul_f32 v[84:85], v[84:85], v[10:11]\n"
"v_pk_mul_f32 v[86:87], v[86:87], v[10:11]\n"
"v_cvt_pk_bf16_f32 v80, v80, v81\n"
"v_cvt_pk_bf16_f32 v81, v82, v83\n"
"v_cvt_pk_bf16_f32 v82, v84, v85\n"
"v_cvt_pk_bf16_f32 v83, v86, v87\n"
"buffer_store_dwordx4 v[80:83], v6, s[48:51], s58 offen offset:2048 sc1\n"
"v_accvgpr_read_b32 v96, a120\n"
"v_accvgpr_read_b32 v97, a121\n"
"v_accvgpr_read_b32 v98, a122\n"
"v_accvgpr_read_b32 v99, a123\n"
"v_accvgpr_read_b32 v100, a124\n"
"v_accvgpr_read_b32 v101, a125\n"
"v_accvgpr_read_b32 v102, a126\n"
"v_accvgpr_read_b32 v103, a127\n"
"v_pk_mul_f32 v[96:97], v[96:97], v[10:11]\n"
"v_pk_mul_f32 v[98:99], v[98:99], v[10:11]\n"
"v_pk_mul_f32 v[100:101], v[100:101], v[10:11]\n"
"v_pk_mul_f32 v[102:103], v[102:103], v[10:11]\n"
"v_cvt_pk_bf16_f32 v96, v96, v97\n"
"v_cvt_pk_bf16_f32 v97, v98, v99\n"
"v_cvt_pk_bf16_f32 v98, v100, v101\n"
"v_cvt_pk_bf16_f32 v99, v102, v103\n"
"buffer_store_dwordx4 v[96:99], v6, s[48:51], s58 offen offset:3072 sc1\n"
"s_add_u32 s58, s58, 4096\n"
"buffer_store_dwordx2 v[28:29], v6, s[48:51], s58 offen sc1\n"
"s_cmp_eq_u32 s57, 0\n"
"s_cselect_b32 s74, s63, 0\n"
"s_branch L_ee%=\n"
"L_e2%=:\n"
"s_waitcnt vmcnt(16)\n"
"v_sub_f32 v240, v250, v28\n"
"v_exp_f32 v241, v240\n"
"v_exp_f32 v242, -v240\n"
"v_add_f32 v241, 1.0, v241\n"
"v_add_f32 v242, 1.0, v242\n"
"v_rcp_f32 v241, v241\n"
"v_rcp_f32 v242, v242\n"
"v_mul_f32 v8, v8, v241\n"
"v_mul_f32 v12, v248, v242\n"
"v_mov_b32 v13, v12\n"
"v_mul_f32 v8, v8, v248\n"
"v_mov_b32 v9, v8\n"
"v_sub_f32 v240, v251, v29\n"
"v_exp_f32 v241, v240\n"
"v_exp_f32 v242, -v240\n"
"v_add_f32 v241, 1.0, v241\n"
"v_add_f32 v242, 1.0, v242\n"
"v_rcp_f32 v241, v241\n"
"v_rcp_f32 v242, v242\n"
"v_mul_f32 v10, v10, v241\n"
"v_mul_f32 v14, v249, v242\n"
"v_mov_b32 v15, v14\n"
"v_mul_f32 v10, v10, v249\n"
"v_mov_b32 v11, v10\n"
"s_mov_b32 s58, s60\n"
"v_accvgpr_read_b32 v48, a0\n"
"v_accvgpr_read_b32 v49, a1\n"
"v_accvgpr_read_b32 v50, a2\n"
"v_accvgpr_read_b32 v51, a3\n"
"v_accvgpr_read_b32 v52, a4\n"
"v_accvgpr_read_b32 v53, a5\n"
"v_accvgpr_read_b32 v54, a6\n"
"v_accvgpr_read_b32 v55, a7\n"
"v_pk_mul_f32 v[48:49], v[48:49], v[8:9]\n"
"v_pk_mul_f32 v[50:51], v[50:51], v[8:9]\n"
"v_pk_mul_f32 v[52:53], v[52:53], v[8:9]\n"
"v_pk_mul_f32 v[54:55], v[54:55], v[8:9]\n"
"s_waitcnt vmcnt(15)\n"
"v_lshlrev_b32 v56, 16, v176\n"
"v_and_b32 v57, 0xffff0000, v176\n"
"v_lshlrev_b32 v58, 16, v177\n"
"v_and_b32 v59, 0xffff0000, v177\n"
"v_lshlrev_b32 v60, 16, v178\n"
"v_and_b32 v61, 0xffff0000, v178\n"
"v_lshlrev_b32 v62, 16, v179\n"
"v_and_b32 v63, 0xffff0000, v179\n"
"v_pk_fma_f32 v[48:49], v[56:57], v[12:13], v[48:49]\n"
"v_pk_fma_f32 v[50:51], v[58:59], v[12:13], v[50:51]\n"
"v_pk_fma_f32 v[52:53], v[60:61], v[12:13], v[52:53]\n"
"v_pk_fma_f32 v[54:55], v[62:63], v[12:13], v[54:55]\n"
"v_cvt_pk_bf16_f32 v48, v48, v49\n"
"v_cvt_pk_bf16_f32 v49, v50, v51\n"
"v_cvt_pk_bf16_f32 v50, v52, v53\n"
"v_cvt_pk_bf16_f32 v51, v54, v55\n"
"buffer_store_dwordx4 v[48:51], v6, s[44:47], s58 offen offset:0 sc1\n"
"v_accvgpr_read_b32 v64, a8\n"
"v_accvgpr_read_b32 v65, a9\n"
"v_accvgpr_read_b32 v66, a10\n"
"v_accvgpr_read_b32 v67, a11\n"
"v_accvgpr_read_b32 v68, a12\n"
"v_accvgpr_read_b32 v69, a13\n"
"v_accvgpr_read_b32 v70, a14\n"
"v_accvgpr_read_b32 v71, a15\n"
"v_pk_mul_f32 v[64:65], v[64:65], v[8:9]\n"
"v_pk_mul_f32 v[66:67], v[66:67], v[8:9]\n"
"v_pk_mul_f32 v[68:69], v[68:69], v[8:9]\n"
"v_pk_mul_f32 v[70:71], v[70:71], v[8:9]\n"
"s_waitcnt vmcnt(15)\n"
"v_lshlrev_b32 v72, 16, v180\n"
"v_and_b32 v73, 0xffff0000, v180\n"
"v_lshlrev_b32 v74, 16, v181\n"
"v_and_b32 v75, 0xffff0000, v181\n"
"v_lshlrev_b32 v76, 16, v182\n"
"v_and_b32 v77, 0xffff0000, v182\n"
"v_lshlrev_b32 v78, 16, v183\n"
"v_and_b32 v79, 0xffff0000, v183\n"
"v_pk_fma_f32 v[64:65], v[72:73], v[12:13], v[64:65]\n"
"v_pk_fma_f32 v[66:67], v[74:75], v[12:13], v[66:67]\n"
"v_pk_fma_f32 v[68:69], v[76:77], v[12:13], v[68:69]\n"
"v_pk_fma_f32 v[70:71], v[78:79], v[12:13], v[70:71]\n"
"v_cvt_pk_bf16_f32 v64, v64, v65\n"
"v_cvt_pk_bf16_f32 v65, v66, v67\n"
"v_cvt_pk_bf16_f32 v66, v68, v69\n"
"v_cvt_pk_bf16_f32 v67, v70, v71\n"
"buffer_store_dwordx4 v[64:67], v6, s[44:47], s58 offen offset:1024 sc1\n"
"v_accvgpr_read_b32 v80, a16\n"
"v_accvgpr_read_b32 v81, a17\n"
"v_accvgpr_read_b32 v82, a18\n"
"v_accvgpr_read_b32 v83, a19\n"
"v_accvgpr_read_b32 v84, a20\n"
"v_accvgpr_read_b32 v85, a21\n"
"v_accvgpr_read_b32 v86, a22\n"
"v_accvgpr_read_b32 v87, a23\n"
"v_pk_mul_f32 v[80:81], v[80:81], v[8:9]\n"
"v_pk_mul_f32 v[82:83], v[82:83], v[8:9]\n"
"v_pk_mul_f32 v[84:85], v[84:85], v[8:9]\n"
"v_pk_mul_f32 v[86:87], v[86:87], v[8:9]\n"
"s_waitcnt vmcnt(15)\n"
"v_lshlrev_b32 v88, 16, v184\n"
"v_and_b32 v89, 0xffff0000, v184\n"
"v_lshlrev_b32 v90, 16, v185\n"
"v_and_b32 v91, 0xffff0000, v185\n"
"v_lshlrev_b32 v92, 16, v186\n"
"v_and_b32 v93, 0xffff0000, v186\n"
"v_lshlrev_b32 v94, 16, v187\n"
"v_and_b32 v95, 0xffff0000, v187\n"
"v_pk_fma_f32 v[80:81], v[88:89], v[12:13], v[80:81]\n"
"v_pk_fma_f32 v[82:83], v[90:91], v[12:13], v[82:83]\n"
"v_pk_fma_f32 v[84:85], v[92:93], v[12:13], v[84:85]\n"
"v_pk_fma_f32 v[86:87], v[94:95], v[12:13], v[86:87]\n"
"v_cvt_pk_bf16_f32 v80, v80, v81\n"
"v_cvt_pk_bf16_f32 v81, v82, v83\n"
"v_cvt_pk_bf16_f32 v82, v84, v85\n"
"v_cvt_pk_bf16_f32 v83, v86, v87\n"
"buffer_store_dwordx4 v[80:83], v6, s[44:47], s58 offen offset:2048 sc1\n"
"v_accvgpr_read_b32 v96, a24\n"
"v_accvgpr_read_b32 v97, a25\n"
"v_accvgpr_read_b32 v98, a26\n"
"v_accvgpr_read_b32 v99, a27\n"
"v_accvgpr_read_b32 v100, a28\n"
"v_accvgpr_read_b32 v101, a29\n"
"v_accvgpr_read_b32 v102, a30\n"
"v_accvgpr_read_b32 v103, a31\n"
"v_pk_mul_f32 v[96:97], v[96:97], v[8:9]\n"
"v_pk_mul_f32 v[98:99], v[98:99], v[8:9]\n"
"v_pk_mul_f32 v[100:101], v[100:101], v[8:9]\n"
"v_pk_mul_f32 v[102:103], v[102:103], v[8:9]\n"
"s_waitcnt vmcnt(15)\n"
"v_lshlrev_b32 v104, 16, v188\n"
"v_and_b32 v105, 0xffff0000, v188\n"
"v_lshlrev_b32 v106, 16, v189\n"
"v_and_b32 v107, 0xffff0000, v189\n"
"v_lshlrev_b32 v108, 16, v190\n"
"v_and_b32 v109, 0xffff0000, v190\n"
"v_lshlrev_b32 v110, 16, v191\n"
"v_and_b32 v111, 0xffff0000, v191\n"
"v_pk_fma_f32 v[96:97], v[104:105], v[12:13], v[96:97]\n"
"v_pk_fma_f32 v[98:99], v[106:107], v[12:13], v[98:99]\n"
"v_pk_fma_f32 v[100:101], v[108:109], v[12:13], v[100:101]\n"
"v_pk_fma_f32 v[102:103], v[110:111], v[12:13], v[102:103]\n"
"v_cvt_pk_bf16_f32 v96, v96, v97\n"
"v_cvt_pk_bf16_f32 v97, v98, v99\n"
"v_cvt_pk_bf16_f32 v98, v100, v101\n"
"v_cvt_pk_bf16_f32 v99, v102, v103\n"
"buffer_store_dwordx4 v[96:99], v6, s[44:47], s58 offen offset:3072 sc1\n"
"s_add_u32 s58, s58, 4096\n"
"v_accvgpr_read_b32 v48, a32\n"
"v_accvgpr_read_b32 v49, a33\n"
"v_accvgpr_read_b32 v50, a34\n"
"v_accvgpr_read_b32 v51, a35\n"
"v_accvgpr_read_b32 v52, a36\n"
"v_accvgpr_read_b32 v53, a37\n"
"v_accvgpr_read_b32 v54, a38\n"
"v_accvgpr_read_b32 v55, a39\n"
"v_pk_mul_f32 v[48:49], v[48:49], v[8:9]\n"
"v_pk_mul_f32 v[50:51], v[50:51], v[8:9]\n"
"v_pk_mul_f32 v[52:53], v[52:53], v[8:9]\n"
"v_pk_mul_f32 v[54:55], v[54:55], v[8:9]\n"
"s_waitcnt vmcnt(15)\n"
"v_lshlrev_b32 v56, 16, v192\n"
"v_and_b32 v57, 0xffff0000, v192\n"
"v_lshlrev_b32 v58, 16, v193\n"
"v_and_b32 v59, 0xffff0000, v193\n"
"v_lshlrev_b32 v60, 16, v194\n"
"v_and_b32 v61, 0xffff0000, v194\n"
"v_lshlrev_b32 v62, 16, v195\n"
"v_and_b32 v63, 0xffff0000, v195\n"
"v_pk_fma_f32 v[48:49], v[56:57], v[12:13], v[48:49]\n"
"v_pk_fma_f32 v[50:51], v[58:59], v[12:13], v[50:51]\n"
"v_pk_fma_f32 v[52:53], v[60:61], v[12:13], v[52:53]\n"
"v_pk_fma_f32 v[54:55], v[62:63], v[12:13], v[54:55]\n"
"v_cvt_pk_bf16_f32 v48, v48, v49\n"
"v_cvt_pk_bf16_f32 v49, v50, v51\n"
"v_cvt_pk_bf16_f32 v50, v52, v53\n"
"v_cvt_pk_bf16_f32 v51, v54, v55\n"
"buffer_store_dwordx4 v[48:51], v6, s[44:47], s58 offen offset:0 sc1\n"
"v_accvgpr_read_b32 v64, a40\n"
"v_accvgpr_read_b32 v65, a41\n"
"v_accvgpr_read_b32 v66, a42\n"
"v_accvgpr_read_b32 v67, a43\n"
"v_accvgpr_read_b32 v68, a44\n"
"v_accvgpr_read_b32 v69, a45\n"
"v_accvgpr_read_b32 v70, a46\n"
"v_accvgpr_read_b32 v71, a47\n"
"v_pk_mul_f32 v[64:65], v[64:65], v[8:9]\n"
"v_pk_mul_f32 v[66:67], v[66:67], v[8:9]\n"
"v_pk_mul_f32 v[68:69], v[68:69], v[8:9]\n"
"v_pk_mul_f32 v[70:71], v[70:71], v[8:9]\n"
"s_waitcnt vmcnt(15)\n"
"v_lshlrev_b32 v72, 16, v196\n"
"v_and_b32 v73, 0xffff0000, v196\n"
"v_lshlrev_b32 v74, 16, v197\n"
"v_and_b32 v75, 0xffff0000, v197\n"
"v_lshlrev_b32 v76, 16, v198\n"
"v_and_b32 v77, 0xffff0000, v198\n"
"v_lshlrev_b32 v78, 16, v199\n"
"v_and_b32 v79, 0xffff0000, v199\n"
"v_pk_fma_f32 v[64:65], v[72:73], v[12:13], v[64:65]\n"
"v_pk_fma_f32 v[66:67], v[74:75], v[12:13], v[66:67]\n"
"v_pk_fma_f32 v[68:69], v[76:77], v[12:13], v[68:69]\n"
"v_pk_fma_f32 v[70:71], v[78:79], v[12:13], v[70:71]\n"
"v_cvt_pk_bf16_f32 v64, v64, v65\n"
"v_cvt_pk_bf16_f32 v65, v66, v67\n"
"v_cvt_pk_bf16_f32 v66, v68, v69\n"
"v_cvt_pk_bf16_f32 v67, v70, v71\n"
"buffer_store_dwordx4 v[64:67], v6, s[44:47], s58 offen offset:1024 sc1\n"
"v_accvgpr_read_b32 v80, a48\n"
"v_accvgpr_read_b32 v81, a49\n"
"v_accvgpr_read_b32 v82, a50\n"
"v_accvgpr_read_b32 v83, a51\n"
"v_accvgpr_read_b32 v84, a52\n"
"v_accvgpr_read_b32 v85, a53\n"
"v_accvgpr_read_b32 v86, a54\n"
"v_accvgpr_read_b32 v87, a55\n"
"v_pk_mul_f32 v[80:81], v[80:81], v[8:9]\n"
"v_pk_mul_f32 v[82:83], v[82:83], v[8:9]\n"
"v_pk_mul_f32 v[84:85], v[84:85], v[8:9]\n"
"v_pk_mul_f32 v[86:87], v[86:87], v[8:9]\n"
"s_waitcnt vmcnt(15)\n"
"v_lshlrev_b32 v88, 16, v200\n"
"v_and_b32 v89, 0xffff0000, v200\n"
"v_lshlrev_b32 v90, 16, v201\n"
"v_and_b32 v91, 0xffff0000, v201\n"
"v_lshlrev_b32 v92, 16, v202\n"
"v_and_b32 v93, 0xffff0000, v202\n"
"v_lshlrev_b32 v94, 16, v203\n"
"v_and_b32 v95, 0xffff0000, v203\n"
"v_pk_fma_f32 v[80:81], v[88:89], v[12:13], v[80:81]\n"
"v_pk_fma_f32 v[82:83], v[90:91], v[12:13], v[82:83]\n"
"v_pk_fma_f32 v[84:85], v[92:93], v[12:13], v[84:85]\n"
"v_pk_fma_f32 v[86:87], v[94:95], v[12:13], v[86:87]\n"
"v_cvt_pk_bf16_f32 v80, v80, v81\n"
"v_cvt_pk_bf16_f32 v81, v82, v83\n"
"v_cvt_pk_bf16_f32 v82, v84, v85\n"
"v_cvt_pk_bf16_f32 v83, v86, v87\n"
"buffer_store_dwordx4 v[80:83], v6, s[44:47], s58 offen offset:2048 sc1\n"
"v_accvgpr_read_b32 v96, a56\n"
"v_accvgpr_read_b32 v97, a57\n"
"v_accvgpr_read_b32 v98, a58\n"
"v_accvgpr_read_b32 v99, a59\n"
"v_accvgpr_read_b32 v100, a60\n"
"v_accvgpr_read_b32 v101, a61\n"
"v_accvgpr_read_b32 v102, a62\n"
"v_accvgpr_read_b32 v103, a63\n"
"v_pk_mul_f32 v[96:97], v[96:97], v[8:9]\n"
"v_pk_mul_f32 v[98:99], v[98:99], v[8:9]\n"
"v_pk_mul_f32 v[100:101], v[100:101], v[8:9]\n"
"v_pk_mul_f32 v[102:103], v[102:103], v[8:9]\n"
"s_waitcnt vmcnt(15)\n"
"v_lshlrev_b32 v104, 16, v204\n"
"v_and_b32 v105, 0xffff0000, v204\n"
"v_lshlrev_b32 v106, 16, v205\n"
"v_and_b32 v107, 0xffff0000, v205\n"
"v_lshlrev_b32 v108, 16, v206\n"
"v_and_b32 v109, 0xffff0000, v206\n"
"v_lshlrev_b32 v110, 16, v207\n"
"v_and_b32 v111, 0xffff0000, v207\n"
"v_pk_fma_f32 v[96:97], v[104:105], v[12:13], v[96:97]\n"
"v_pk_fma_f32 v[98:99], v[106:107], v[12:13], v[98:99]\n"
"v_pk_fma_f32 v[100:101], v[108:109], v[12:13], v[100:101]\n"
"v_pk_fma_f32 v[102:103], v[110:111], v[12:13], v[102:103]\n"
"v_cvt_pk_bf16_f32 v96, v96, v97\n"
"v_cvt_pk_bf16_f32 v97, v98, v99\n"
"v_cvt_pk_bf16_f32 v98, v100, v101\n"
"v_cvt_pk_bf16_f32 v99, v102, v103\n"
"buffer_store_dwordx4 v[96:99], v6, s[44:47], s58 offen offset:3072 sc1\n"
"s_add_u32 s58, s58, 184320\n"
"v_accvgpr_read_b32 v48, a64\n"
"v_accvgpr_read_b32 v49, a65\n"
"v_accvgpr_read_b32 v50, a66\n"
"v_accvgpr_read_b32 v51, a67\n"
"v_accvgpr_read_b32 v52, a68\n"
"v_accvgpr_read_b32 v53, a69\n"
"v_accvgpr_read_b32 v54, a70\n"
"v_accvgpr_read_b32 v55, a71\n"
"v_pk_mul_f32 v[48:49], v[48:49], v[10:11]\n"
"v_pk_mul_f32 v[50:51], v[50:51], v[10:11]\n"
"v_pk_mul_f32 v[52:53], v[52:53], v[10:11]\n"
"v_pk_mul_f32 v[54:55], v[54:55], v[10:11]\n"
"s_waitcnt vmcnt(15)\n"
"v_lshlrev_b32 v56, 16, v208\n"
"v_and_b32 v57, 0xffff0000, v208\n"
"v_lshlrev_b32 v58, 16, v209\n"
"v_and_b32 v59, 0xffff0000, v209\n"
"v_lshlrev_b32 v60, 16, v210\n"
"v_and_b32 v61, 0xffff0000, v210\n"
"v_lshlrev_b32 v62, 16, v211\n"
"v_and_b32 v63, 0xffff0000, v211\n"
"v_pk_fma_f32 v[48:49], v[56:57], v[14:15], v[48:49]\n"
"v_pk_fma_f32 v[50:51], v[58:59], v[14:15], v[50:51]\n"
"v_pk_fma_f32 v[52:53], v[60:61], v[14:15], v[52:53]\n"
"v_pk_fma_f32 v[54:55], v[62:63], v[14:15], v[54:55]\n"
"v_cvt_pk_bf16_f32 v48, v48, v49\n"
"v_cvt_pk_bf16_f32 v49, v50, v51\n"
"v_cvt_pk_bf16_f32 v50, v52, v53\n"
"v_cvt_pk_bf16_f32 v51, v54, v55\n"
"buffer_store_dwordx4 v[48:51], v6, s[44:47], s58 offen offset:0 sc1\n"
"v_accvgpr_read_b32 v64, a72\n"
"v_accvgpr_read_b32 v65, a73\n"
"v_accvgpr_read_b32 v66, a74\n"
"v_accvgpr_read_b32 v67, a75\n"
"v_accvgpr_read_b32 v68, a76\n"
"v_accvgpr_read_b32 v69, a77\n"
"v_accvgpr_read_b32 v70, a78\n"
"v_accvgpr_read_b32 v71, a79\n"
"v_pk_mul_f32 v[64:65], v[64:65], v[10:11]\n"
"v_pk_mul_f32 v[66:67], v[66:67], v[10:11]\n"
"v_pk_mul_f32 v[68:69], v[68:69], v[10:11]\n"
"v_pk_mul_f32 v[70:71], v[70:71], v[10:11]\n"
"s_waitcnt vmcnt(15)\n"
"v_lshlrev_b32 v72, 16, v212\n"
"v_and_b32 v73, 0xffff0000, v212\n"
"v_lshlrev_b32 v74, 16, v213\n"
"v_and_b32 v75, 0xffff0000, v213\n"
"v_lshlrev_b32 v76, 16, v214\n"
"v_and_b32 v77, 0xffff0000, v214\n"
"v_lshlrev_b32 v78, 16, v215\n"
"v_and_b32 v79, 0xffff0000, v215\n"
"v_pk_fma_f32 v[64:65], v[72:73], v[14:15], v[64:65]\n"
"v_pk_fma_f32 v[66:67], v[74:75], v[14:15], v[66:67]\n"
"v_pk_fma_f32 v[68:69], v[76:77], v[14:15], v[68:69]\n"
"v_pk_fma_f32 v[70:71], v[78:79], v[14:15], v[70:71]\n"
"v_cvt_pk_bf16_f32 v64, v64, v65\n"
"v_cvt_pk_bf16_f32 v65, v66, v67\n"
"v_cvt_pk_bf16_f32 v66, v68, v69\n"
"v_cvt_pk_bf16_f32 v67, v70, v71\n"
"buffer_store_dwordx4 v[64:67], v6, s[44:47], s58 offen offset:1024 sc1\n"
"v_accvgpr_read_b32 v80, a80\n"
"v_accvgpr_read_b32 v81, a81\n"
"v_accvgpr_read_b32 v82, a82\n"
"v_accvgpr_read_b32 v83, a83\n"
"v_accvgpr_read_b32 v84, a84\n"
"v_accvgpr_read_b32 v85, a85\n"
"v_accvgpr_read_b32 v86, a86\n"
"v_accvgpr_read_b32 v87, a87\n"
"v_pk_mul_f32 v[80:81], v[80:81], v[10:11]\n"
"v_pk_mul_f32 v[82:83], v[82:83], v[10:11]\n"
"v_pk_mul_f32 v[84:85], v[84:85], v[10:11]\n"
"v_pk_mul_f32 v[86:87], v[86:87], v[10:11]\n"
"s_waitcnt vmcnt(15)\n"
"v_lshlrev_b32 v88, 16, v216\n"
"v_and_b32 v89, 0xffff0000, v216\n"
"v_lshlrev_b32 v90, 16, v217\n"
"v_and_b32 v91, 0xffff0000, v217\n"
"v_lshlrev_b32 v92, 16, v218\n"
"v_and_b32 v93, 0xffff0000, v218\n"
"v_lshlrev_b32 v94, 16, v219\n"
"v_and_b32 v95, 0xffff0000, v219\n"
"v_pk_fma_f32 v[80:81], v[88:89], v[14:15], v[80:81]\n"
"v_pk_fma_f32 v[82:83], v[90:91], v[14:15], v[82:83]\n"
"v_pk_fma_f32 v[84:85], v[92:93], v[14:15], v[84:85]\n"
"v_pk_fma_f32 v[86:87], v[94:95], v[14:15], v[86:87]\n"
"v_cvt_pk_bf16_f32 v80, v80, v81\n"
"v_cvt_pk_bf16_f32 v81, v82, v83\n"
"v_cvt_pk_bf16_f32 v82, v84, v85\n"
"v_cvt_pk_bf16_f32 v83, v86, v87\n"
"buffer_store_dwordx4 v[80:83], v6, s[44:47], s58 offen offset:2048 sc1\n"
"v_accvgpr_read_b32 v96, a88\n"
"v_accvgpr_read_b32 v97, a89\n"
"v_accvgpr_read_b32 v98, a90\n"
"v_accvgpr_read_b32 v99, a91\n"
"v_accvgpr_read_b32 v100, a92\n"
"v_accvgpr_read_b32 v101, a93\n"
"v_accvgpr_read_b32 v102, a94\n"
"v_accvgpr_read_b32 v103, a95\n"
"v_pk_mul_f32 v[96:97], v[96:97], v[10:11]\n"
"v_pk_mul_f32 v[98:99], v[98:99], v[10:11]\n"
"v_pk_mul_f32 v[100:101], v[100:101], v[10:11]\n"
"v_pk_mul_f32 v[102:103], v[102:103], v[10:11]\n"
"s_waitcnt vmcnt(15)\n"
"v_lshlrev_b32 v104, 16, v220\n"
"v_and_b32 v105, 0xffff0000, v220\n"
"v_lshlrev_b32 v106, 16, v221\n"
"v_and_b32 v107, 0xffff0000, v221\n"
"v_lshlrev_b32 v108, 16, v222\n"
"v_and_b32 v109, 0xffff0000, v222\n"
"v_lshlrev_b32 v110, 16, v223\n"
"v_and_b32 v111, 0xffff0000, v223\n"
"v_pk_fma_f32 v[96:97], v[104:105], v[14:15], v[96:97]\n"
"v_pk_fma_f32 v[98:99], v[106:107], v[14:15], v[98:99]\n"
"v_pk_fma_f32 v[100:101], v[108:109], v[14:15], v[100:101]\n"
"v_pk_fma_f32 v[102:103], v[110:111], v[14:15], v[102:103]\n"
"v_cvt_pk_bf16_f32 v96, v96, v97\n"
"v_cvt_pk_bf16_f32 v97, v98, v99\n"
"v_cvt_pk_bf16_f32 v98, v100, v101\n"
"v_cvt_pk_bf16_f32 v99, v102, v103\n"
"buffer_store_dwordx4 v[96:99], v6, s[44:47], s58 offen offset:3072 sc1\n"
"s_add_u32 s58, s58, 4096\n"
"v_accvgpr_read_b32 v48, a96\n"
"v_accvgpr_read_b32 v49, a97\n"
"v_accvgpr_read_b32 v50, a98\n"
"v_accvgpr_read_b32 v51, a99\n"
"v_accvgpr_read_b32 v52, a100\n"
"v_accvgpr_read_b32 v53, a101\n"
"v_accvgpr_read_b32 v54, a102\n"
"v_accvgpr_read_b32 v55, a103\n"
"v_pk_mul_f32 v[48:49], v[48:49], v[10:11]\n"
"v_pk_mul_f32 v[50:51], v[50:51], v[10:11]\n"
"v_pk_mul_f32 v[52:53], v[52:53], v[10:11]\n"
"v_pk_mul_f32 v[54:55], v[54:55], v[10:11]\n"
"s_waitcnt vmcnt(15)\n"
"v_lshlrev_b32 v56, 16, v224\n"
"v_and_b32 v57, 0xffff0000, v224\n"
"v_lshlrev_b32 v58, 16, v225\n"
"v_and_b32 v59, 0xffff0000, v225\n"
"v_lshlrev_b32 v60, 16, v226\n"
"v_and_b32 v61, 0xffff0000, v226\n"
"v_lshlrev_b32 v62, 16, v227\n"
"v_and_b32 v63, 0xffff0000, v227\n"
"v_pk_fma_f32 v[48:49], v[56:57], v[14:15], v[48:49]\n"
"v_pk_fma_f32 v[50:51], v[58:59], v[14:15], v[50:51]\n"
"v_pk_fma_f32 v[52:53], v[60:61], v[14:15], v[52:53]\n"
"v_pk_fma_f32 v[54:55], v[62:63], v[14:15], v[54:55]\n"
"v_cvt_pk_bf16_f32 v48, v48, v49\n"
"v_cvt_pk_bf16_f32 v49, v50, v51\n"
"v_cvt_pk_bf16_f32 v50, v52, v53\n"
"v_cvt_pk_bf16_f32 v51, v54, v55\n"
"buffer_store_dwordx4 v[48:51], v6, s[44:47], s58 offen offset:0 sc1\n"
"v_accvgpr_read_b32 v64, a104\n"
"v_accvgpr_read_b32 v65, a105\n"
"v_accvgpr_read_b32 v66, a106\n"
"v_accvgpr_read_b32 v67, a107\n"
"v_accvgpr_read_b32 v68, a108\n"
"v_accvgpr_read_b32 v69, a109\n"
"v_accvgpr_read_b32 v70, a110\n"
"v_accvgpr_read_b32 v71, a111\n"
"v_pk_mul_f32 v[64:65], v[64:65], v[10:11]\n"
"v_pk_mul_f32 v[66:67], v[66:67], v[10:11]\n"
"v_pk_mul_f32 v[68:69], v[68:69], v[10:11]\n"
"v_pk_mul_f32 v[70:71], v[70:71], v[10:11]\n"
"s_waitcnt vmcnt(15)\n"
"v_lshlrev_b32 v72, 16, v228\n"
"v_and_b32 v73, 0xffff0000, v228\n"
"v_lshlrev_b32 v74, 16, v229\n"
"v_and_b32 v75, 0xffff0000, v229\n"
"v_lshlrev_b32 v76, 16, v230\n"
"v_and_b32 v77, 0xffff0000, v230\n"
"v_lshlrev_b32 v78, 16, v231\n"
"v_and_b32 v79, 0xffff0000, v231\n"
"v_pk_fma_f32 v[64:65], v[72:73], v[14:15], v[64:65]\n"
"v_pk_fma_f32 v[66:67], v[74:75], v[14:15], v[66:67]\n"
"v_pk_fma_f32 v[68:69], v[76:77], v[14:15], v[68:69]\n"
"v_pk_fma_f32 v[70:71], v[78:79], v[14:15], v[70:71]\n"
"v_cvt_pk_bf16_f32 v64, v64, v65\n"
"v_cvt_pk_bf16_f32 v65, v66, v67\n"
"v_cvt_pk_bf16_f32 v66, v68, v69\n"
"v_cvt_pk_bf16_f32 v67, v70, v71\n"
"buffer_store_dwordx4 v[64:67], v6, s[44:47], s58 offen offset:1024 sc1\n"
"v_accvgpr_read_b32 v80, a112\n"
"v_accvgpr_read_b32 v81, a113\n"
"v_accvgpr_read_b32 v82, a114\n"
"v_accvgpr_read_b32 v83, a115\n"
"v_accvgpr_read_b32 v84, a116\n"
"v_accvgpr_read_b32 v85, a117\n"
"v_accvgpr_read_b32 v86, a118\n"
"v_accvgpr_read_b32 v87, a119\n"
"v_pk_mul_f32 v[80:81], v[80:81], v[10:11]\n"
"v_pk_mul_f32 v[82:83], v[82:83], v[10:11]\n"
"v_pk_mul_f32 v[84:85], v[84:85], v[10:11]\n"
"v_pk_mul_f32 v[86:87], v[86:87], v[10:11]\n"
"s_waitcnt vmcnt(15)\n"
"v_lshlrev_b32 v88, 16, v232\n"
"v_and_b32 v89, 0xffff0000, v232\n"
"v_lshlrev_b32 v90, 16, v233\n"
"v_and_b32 v91, 0xffff0000, v233\n"
"v_lshlrev_b32 v92, 16, v234\n"
"v_and_b32 v93, 0xffff0000, v234\n"
"v_lshlrev_b32 v94, 16, v235\n"
"v_and_b32 v95, 0xffff0000, v235\n"
"v_pk_fma_f32 v[80:81], v[88:89], v[14:15], v[80:81]\n"
"v_pk_fma_f32 v[82:83], v[90:91], v[14:15], v[82:83]\n"
"v_pk_fma_f32 v[84:85], v[92:93], v[14:15], v[84:85]\n"
"v_pk_fma_f32 v[86:87], v[94:95], v[14:15], v[86:87]\n"
"v_cvt_pk_bf16_f32 v80, v80, v81\n"
"v_cvt_pk_bf16_f32 v81, v82, v83\n"
"v_cvt_pk_bf16_f32 v82, v84, v85\n"
"v_cvt_pk_bf16_f32 v83, v86, v87\n"
"buffer_store_dwordx4 v[80:83], v6, s[44:47], s58 offen offset:2048 sc1\n"
"v_accvgpr_read_b32 v96, a120\n"
"v_accvgpr_read_b32 v97, a121\n"
"v_accvgpr_read_b32 v98, a122\n"
"v_accvgpr_read_b32 v99, a123\n"
"v_accvgpr_read_b32 v100, a124\n"
"v_accvgpr_read_b32 v101, a125\n"
"v_accvgpr_read_b32 v102, a126\n"
"v_accvgpr_read_b32 v103, a127\n"
"v_pk_mul_f32 v[96:97], v[96:97], v[10:11]\n"
"v_pk_mul_f32 v[98:99], v[98:99], v[10:11]\n"
"v_pk_mul_f32 v[100:101], v[100:101], v[10:11]\n"
"v_pk_mul_f32 v[102:103], v[102:103], v[10:11]\n"
"s_waitcnt vmcnt(15)\n"
"v_lshlrev_b32 v104, 16, v236\n"
"v_and_b32 v105, 0xffff0000, v236\n"
"v_lshlrev_b32 v106, 16, v237\n"
"v_and_b32 v107, 0xffff0000, v237\n"
"v_lshlrev_b32 v108, 16, v238\n"
"v_and_b32 v109, 0xffff0000, v238\n"
"v_lshlrev_b32 v110, 16, v239\n"
"v_and_b32 v111, 0xffff0000, v239\n"
"v_pk_fma_f32 v[96:97], v[104:105], v[14:15], v[96:97]\n"
"v_pk_fma_f32 v[98:99], v[106:107], v[14:15], v[98:99]\n"
"v_pk_fma_f32 v[100:101], v[108:109], v[14:15], v[100:101]\n"
"v_pk_fma_f32 v[102:103], v[110:111], v[14:15], v[102:103]\n"
"v_cvt_pk_bf16_f32 v96, v96, v97\n"
"v_cvt_pk_bf16_f32 v97, v98, v99\n"
"v_cvt_pk_bf16_f32 v98, v100, v101\n"
"v_cvt_pk_bf16_f32 v99, v102, v103\n"
"buffer_store_dwordx4 v[96:99], v6, s[44:47], s58 offen offset:3072 sc1\n"
"s_add_u32 s58, s58, 4096\n"
"s_and_b32 s58, s52, 15\n"
"s_lshl_b32 s58, s58, 7\n"
"s_add_u32 s58, s58, 32768\n"
"s_cmp_eq_u32 s57, 0\n"
"s_cselect_b32 s74, s58, 0\n"
"L_ee%=:\n"
"s_mov_b32 s52, s53\n"
"s_mov_b32 s53, s54\n"
"s_mov_b32 s54, s55\n"
"s_mov_b32 s55, s56\n"
"s_waitcnt vmcnt(16)\n"
"s_mov_b64 vcc, 0\n"
"s_cmp_lg_u32 s52, 0\n"
"s_cbranch_scc1 L_seg%=\n"
"s_waitcnt vmcnt(0)\n"
"s_barrier\n"
"s_cmp_eq_u32 s74, 0\n"
"s_cbranch_scc1 L_nfe%=\n"
"v_mov_b32 v12, 1\n"
"s_mov_b64 exec, 1\n"
"buffer_atomic_add v12, off, s[48:51], s74 sc1\n"
"s_mov_b64 exec, -1\n"
"s_mov_b32 s74, 0\n"
"L_nfe%=:\n"
        ::[kv] "s"(kv), [pf] "s"(pf), [q] "s"(q), [x] "s"(cx), [a] "s"(ax), [s0] "s"(s0), [s1] "s"(s1), [s2] "s"(s2), [s3] "s"(s3), [sm] "s"(smo), [wv] "s"(wvi)
        : "v1","v2","v3","v4","v5","v6","v7","v8","v9","v10","v11","v12","v13","v14","v15","v16","v17","v18","v19","v20","v21","v22","v23","v24","v25","v26","v27","v28","v29","v30","v31","v32","v33","v34","v35","v36","v37","v38","v39","v40","v41","v42","v43","v44","v45","v46","v47","v48","v49","v50","v51","v52","v53","v54","v55","v56","v57","v58","v59","v60","v61","v62","v63","v64","v65","v66","v67","v68","v69","v70","v71","v72","v73","v74","v75","v76","v77","v78","v79","v80","v81","v82","v83","v84","v85","v86","v87","v88","v89","v90","v91","v92","v93","v94","v95","v96","v97","v98","v99","v100","v101","v102","v103","v104","v105","v106","v107","v108","v109","v110","v111","v112","v113","v114","v115","v116","v117","v118","v119","v120","v121","v122","v123","v124","v125","v126","v127","v128","v129","v130","v131","v132","v133","v134","v135","v136","v137","v138","v139","v140","v141","v142","v143","v144","v145","v146","v147","v148","v149","v150","v151","v152","v153","v154","v155","v156","v157","v158","v159","v160","v161","v162","v163","v164","v165","v166","v167","v168","v169","v170","v171","v172","v173","v174","v175","v176","v177","v178","v179","v180","v181","v182","v183","v184","v185","v186","v187","v188","v189","v190","v191","v192","v193","v194","v195","v196","v197","v198","v199","v200","v201","v202","v203","v204","v205","v206","v207","v208","v209","v210","v211","v212","v213","v214","v215","v216","v217","v218","v219","v220","v221","v222","v223","v224","v225","v226","v227","v228","v229","v230","v231","v232","v233","v234","v235","v236","v237","v238","v239","v240","v241","v242","v243","v244","v245","v246","v247","v248","v249","v250","v251","v252","v253","v254","v255","a0","a1","a2","a3","a4","a5","a6","a7","a8","a9","a10","a11","a12","a13","a14","a15","a16","a17","a18","a19","a20","a21","a22","a23","a24","a25","a26","a27","a28","a29","a30","a31","a32","a33","a34","a35","a36","a37","a38","a39","a40","a41","a42","a43","a44","a45","a46","a47","a48","a49","a50","a51","a52","a53","a54","a55","a56","a57","a58","a59","a60","a61","a62","a63","a64","a65","a66","a67","a68","a69","a70","a71","a72","a73","a74","a75","a76","a77","a78","a79","a80","a81","a82","a83","a84","a85","a86","a87","a88","a89","a90","a91","a92","a93","a94","a95","a96","a97","a98","a99","a100","a101","a102","a103","a104","a105","a106","a107","a108","a109","a110","a111","a112","a113","a114","a115","a116","a117","a118","a119","a120","a121","a122","a123","a124","a125","a126","a127","a128","a129","a130","a131","a132","a133","a134","a135","a136","a137","a138","a139","a140","a141","a142","a143","a144","a145","a146","a147","a148","a149","a150","a151","a152","a153","a154","a155","a156","a157","a158","a159","a160","a161","a162","a163","a164","a165","a166","a167","a168","a169","a170","a171","a172","a173","a174","a175","a176","a177","a178","a179","a180","a181","a182","a183","a184","a185","a186","a187","a188","a189","a190","a191","a192","a193","a194","a195","a196","a197","a198","a199","a200","a201","a202","a203","a204","a205","a206","a207","a208","a209","a210","a211","a212","a213","a214","a215","a216","a217","a218","a219","a220","a221","a222","a223","a224","a225","a226","a227","a228","a229","a230","a231","a232","a233","a234","a235","a236","a237","a238","a239","a240","a241","a242","a243","a244","a245","a246","a247","a248","a249","a250","a251","a252","a253","a254","a255","s36","s37","s38","s39","s40","s41","s42","s43","s44","s45","s46","s47","s48","s49","s50","s51","s52","s53","s54","s55","s56","s57","s58","s59","s60","s61","s62","s63","s64","s65","s66","s67","s68","s69","s70","s71","s72","s73","s74","s75","s76","s77","s78","s79","s80","s81","s82","s83","s84","s85","s86","s87","memory","vcc","scc");
  } else {
    b2(sm, threadIdx.x, xc * 16 + 4 * (id - 2 * 14), x, ws);
  }
  if (id >= 2 * 14 - 2) {
    const int j = id - 2 * 14, u0 = j >= 0 ? 6 * j : 28 + 2 * j, it = j >= 0 ? 6 : 2;
    const char *wp = ws + OFF_W3 + u0 * 57344, *xp = ws + OFF_CTX + (size_t)xc * (16 * CSTR) + (128 + 2 * u0) * 1024;
    const char *ap = ws + OFF_XN + xc * (16 * 14336), *gp = ws + OFF_AUX + xc * 1024;
    asm volatile(
"v_mbcnt_lo_u32_b32 v254, -1, 0\n"
"v_mbcnt_hi_u32_b32 v254, -1, v254\n"
"v_lshl_add_u32 v254, %[wv], 6, v254\n"
"s_mov_b64 s[36:37], %[w]\n"
"s_mov_b32 s38, 344064\n"
"s_mov_b32 s39, 0x20000\n"
"s_mov_b64 s[40:41], %[x]\n"
"s_mov_b32 s42, 3014656\n"
"s_mov_b32 s43, 0x20000\n"
"s_mov_b64 s[44:45], %[a]\n"
"s_mov_b32 s46, 229376\n"
"s_mov_b32 s47, 0x20000\n"
"v_lshrrev_b32 v18, 6, v254\n"
"v_and_b32 v19, 63, v254\n"
"s_nop 0\n"
"v_readfirstlane_b32 s48, v18\n"
"v_lshlrev_b32 v1, 4, v19\n"
"v_add_u32 v2, %[sm], v1\n"
"v_add_u32 v3, 57344, v2\n"
"s_mul_i32 s50, s48, 14336\n"
"s_add_u32 s52, s50, %[sm]\n"
"s_add_u32 s53, s52, 57344\n"
"v_lshlrev_b32 v24, 7, v18\n"
"global_load_dword v25, v24, %[fl] sc1\n"
"s_mov_b32 m0, s52\n"
"s_nop 0\n"
"buffer_load_dwordx4 v1, s[36:39], s50 offen lds\n"
"s_add_u32 s50, s50, 1024\n"
"s_add_u32 m0, m0, 1024\n"
"s_nop 0\n"
"buffer_load_dwordx4 v1, s[36:39], s50 offen lds\n"
"s_add_u32 s50, s50, 1024\n"
"s_add_u32 m0, m0, 1024\n"
"s_nop 0\n"
"buffer_load_dwordx4 v1, s[36:39], s50 offen lds\n"
"s_add_u32 s50, s50, 1024\n"
"s_add_u32 m0, m0, 1024\n"
"s_nop 0\n"
"buffer_load_dwordx4 v1, s[36:39], s50 offen lds\n"
"s_add_u32 s50, s50, 1024\n"
"s_add_u32 m0, m0, 1024\n"
"s_nop 0\n"
"buffer_load_dwordx4 v1, s[36:39], s50 offen lds\n"
"s_add_u32 s50, s50, 1024\n"
"s_add_u32 m0, m0, 1024\n"
"s_nop 0\n"
"buffer_load_dwordx4 v1, s[36:39], s50 offen lds\n"
"s_add_u32 s50, s50, 1024\n"
"s_add_u32 m0, m0, 1024\n"
"s_nop 0\n"
"buffer_load_dwordx4 v1, s[36:39], s50 offen lds\n"
"s_add_u32 s50, s50, 1024\n"
"s_add_u32 m0, m0, 1024\n"
"s_nop 0\n"
"buffer_load_dwordx4 v1, s[36:39], s50 offen lds\n"
"s_add_u32 s50, s50, 1024\n"
"s_add_u32 m0, m0, 1024\n"
"s_nop 0\n"
"buffer_load_dwordx4 v1, s[36:39], s50 offen lds\n"
"s_add_u32 s50, s50, 1024\n"
"s_add_u32 m0, m0, 1024\n"
"s_nop 0\n"
"buffer_load_dwordx4 v1, s[36:39], s50 offen lds\n"
"s_add_u32 s50, s50, 1024\n"
"s_add_u32 m0, m0, 1024\n"
"s_nop 0\n"
"buffer_load_dwordx4 v1, s[36:39], s50 offen lds\n"
"s_add_u32 s50, s50, 1024\n"
"s_add_u32 m0, m0, 1024\n"
"s_nop 0\n"
"buffer_load_dwordx4 v1, s[36:39], s50 offen lds\n"
"s_add_u32 s50, s50, 1024\n"
"s_add_u32 m0, m0, 1024\n"
"s_nop 0\n"
"buffer_load_dwordx4 v1, s[36:39], s50 offen lds\n"
"s_add_u32 s50, s50, 1024\n"
"s_add_u32 m0, m0, 1024\n"
"s_nop 0\n"
"buffer_load_dwordx4 v1, s[36:39], s50 offen lds\n"
"s_add_u32 s50, s50, 44032\n"
"s_waitcnt vmcnt(14)\n"
"L_b3s%=:\n"
"v_readfirstlane_b32 s49, v25\n"
"s_cmp_eq_u32 s49, 1\n"
"s_cbranch_scc1 L_b3g%=\n"
"global_load_dword v25, v24, %[fl] sc1\n"
"s_waitcnt vmcnt(0)\n"
"s_branch L_b3s%=\n"
"L_b3g%=:\n"
"v_and_b32 v19, 15, v19\n"
"v_lshl_add_u32 v18, v18, 6, v19\n"
"v_lshlrev_b32 v18, 2, v18\n"
"global_load_dword v10, v18, %[g] offset:0\n"
"global_load_dword v12, v18, %[g] offset:64\n"
"global_load_dword v14, v18, %[g] offset:128\n"
"global_load_dword v16, v18, %[g] offset:192\n"
"s_mul_i32 s62, s48, 57344\n"
"s_add_u32 s63, s62, 14336\n"
"s_add_u32 s64, s62, 28672\n"
"s_add_u32 s65, s62, 43008\n"
"buffer_load_dwordx4 a[0:3], v1, s[44:47], s62 offen offset:0 sc1\n"
"buffer_load_dwordx4 a[56:59], v1, s[44:47], s63 offen offset:0 sc1\n"
"buffer_load_dwordx4 a[112:115], v1, s[44:47], s64 offen offset:0 sc1\n"
"buffer_load_dwordx4 a[168:171], v1, s[44:47], s65 offen offset:0 sc1\n"
"buffer_load_dwordx4 a[4:7], v1, s[44:47], s62 offen offset:1024 sc1\n"
"buffer_load_dwordx4 a[60:63], v1, s[44:47], s63 offen offset:1024 sc1\n"
"buffer_load_dwordx4 a[116:119], v1, s[44:47], s64 offen offset:1024 sc1\n"
"buffer_load_dwordx4 a[172:175], v1, s[44:47], s65 offen offset:1024 sc1\n"
"buffer_load_dwordx4 a[8:11], v1, s[44:47], s62 offen offset:2048 sc1\n"
"buffer_load_dwordx4 a[64:67], v1, s[44:47], s63 offen offset:2048 sc1\n"
"buffer_load_dwordx4 a[120:123], v1, s[44:47], s64 offen offset:2048 sc1\n"
"buffer_load_dwordx4 a[176:179], v1, s[44:47], s65 offen offset:2048 sc1\n"
"buffer_load_dwordx4 a[12:15], v1, s[44:47], s62 offen offset:3072 sc1\n"
"s_add_u32 s62, s62, 4096\n"
"buffer_load_dwordx4 a[68:71], v1, s[44:47], s63 offen offset:3072 sc1\n"
"s_add_u32 s63, s63, 4096\n"
"buffer_load_dwordx4 a[124:127], v1, s[44:47], s64 offen offset:3072 sc1\n"
"s_add_u32 s64, s64, 4096\n"
"buffer_load_dwordx4 a[180:183], v1, s[44:47], s65 offen offset:3072 sc1\n"
"s_add_u32 s65, s65, 4096\n"
"buffer_load_dwordx4 a[16:19], v1, s[44:47], s62 offen offset:0 sc1\n"
"buffer_load_dwordx4 a[72:75], v1, s[44:47], s63 offen offset:0 sc1\n"
"buffer_load_dwordx4 a[128:131], v1, s[44:47], s64 offen offset:0 sc1\n"
"buffer_load_dwordx4 a[184:187], v1, s[44:47], s65 offen offset:0 sc1\n"
"buffer_load_dwordx4 a[20:23], v1, s[44:47], s62 offen offset:1024 sc1\n"
"buffer_load_dwordx4 a[76:79], v1, s[44:47], s63 offen offset:1024 sc1\n"
"buffer_load_dwordx4 a[132:135], v1, s[44:47], s64 offen offset:1024 sc1\n"
"buffer_load_dwordx4 a[188:191], v1, s[44:47], s65 offen offset:1024 sc1\n"
"v_mov_b32 v4, 0xc09d30fc\n"
"v_mov_b32 v5, 0xc09d30fc\n"
"s_mul_i32 s54, s48, 753664\n"
"s_add_u32 s55, s54, 188416\n"
"s_add_u32 s56, s54, 376832\n"
"s_add_u32 s57, s54, 565248\n"
"s_mov_b32 s51, %[it]\n"
"s_waitcnt vmcnt(24)\n"
"v_sub_f32 v10, 1.0, v10\n"
"v_max_f32 v10, 0x1f800000, v10\n"
"v_rcp_f32 v10, v10\n"
"s_nop 0\n"
"v_mul_f32 v10, 0x3eeff558, v10\n"
"v_sub_f32 v12, 1.0, v12\n"
"v_max_f32 v12, 0x1f800000, v12\n"
"v_rcp_f32 v12, v12\n"
"s_nop 0\n"
"v_mul_f32 v12, 0x3eeff558, v12\n"
"v_sub_f32 v14, 1.0, v14\n"
"v_max_f32 v14, 0x1f800000, v14\n"
"v_rcp_f32 v14, v14\n"
"s_nop 0\n"
"v_mul_f32 v14, 0x3eeff558, v14\n"
"v_sub_f32 v16, 1.0, v16\n"
"v_max_f32 v16, 0x1f800000, v16\n"
"v_rcp_f32 v16, v16\n"
"s_nop 0\n"
"v_mul_f32 v16, 0x3eeff558, v16\n"
"s_cmp_eq_u32 s51, 1\n"
"s_cselect_b32 s38, 0, s38\n"
"s_nop 4\n"
"s_barrier\n"
"ds_read_b128 v[32:35], v2 offset:0\n"
"ds_read_b128 v[36:39], v2 offset:1024\n"
"ds_read_b128 v[40:43], v2 offset:2048\n"
"ds_read_b128 v[44:47], v2 offset:3072\n"
"s_waitcnt vmcnt(20)\n"
"s_waitcnt lgkmcnt(3)\n"
"v_mfma_f32_16x16x32_bf16 v[64:67], v[32:35], a[0:3], 0\n"
"ds_read_b128 v[48:51], v2 offset:4096\n"
"v_mfma_f32_16x16x32_bf16 v[80:83], v[32:35], a[56:59], 0\n"
"buffer_load_dwordx4 a[24:27], v1, s[44:47], s62 offen offset:2048 sc1\n"
"v_mfma_f32_16x16x32_bf16 v[96:99], v[32:35], a[112:115], 0\n"
"v_mfma_f32_16x16x32_bf16 v[112:115], v[32:35], a[168:171], 0\n"
"s_waitcnt lgkmcnt(3)\n"
"v_mfma_f32_16x16x32_bf16 v[68:71], v[36:39], a[0:3], 0\n"
"ds_read_b128 v[52:55], v2 offset:5120\n"
"v_mfma_f32_16x16x32_bf16 v[84:87], v[36:39], a[56:59], 0\n"
"buffer_load_dwordx4 a[80:83], v1, s[44:47], s63 offen offset:2048 sc1\n"
"v_mfma_f32_16x16x32_bf16 v[100:103], v[36:39], a[112:115], 0\n"
"v_mfma_f32_16x16x32_bf16 v[116:119], v[36:39], a[168:171], 0\n"
"s_waitcnt lgkmcnt(1)\n"
"v_mfma_f32_16x16x32_bf16 v[72:75], v[40:43], a[0:3], 0\n"
"ds_read_b128 v[56:59], v2 offset:6144\n"
"v_mfma_f32_16x16x32_bf16 v[88:91], v[40:43], a[56:59], 0\n"
"buffer_load_dwordx4 a[136:139], v1, s[44:47], s64 offen offset:2048 sc1\n"
"v_mfma_f32_16x16x32_bf16 v[104:107], v[40:43], a[112:115], 0\n"
"v_mfma_f32_16x16x32_bf16 v[120:123], v[40:43], a[168:171], 0\n"
"v_mfma_f32_16x16x32_bf16 v[76:79], v[44:47], a[0:3], 0\n"
"ds_read_b128 v[60:63], v2 offset:7168\n"
"v_mfma_f32_16x16x32_bf16 v[92:95], v[44:47], a[56:59], 0\n"
"buffer_load_dwordx4 a[192:195], v1, s[44:47], s65 offen offset:2048 sc1\n"
"v_mfma_f32_16x16x32_bf16 v[108:111], v[44:47], a[112:115], 0\n"
"v_mfma_f32_16x16x32_bf16 v[124:127], v[44:47], a[168:171], 0\n"
"s_waitcnt vmcnt(20)\n"
"v_mfma_f32_16x16x32_bf16 v[64:67], v[48:51], a[4:7], v[64:67]\n"
"ds_read_b128 v[32:35], v2 offset:8192\n"
"v_mfma_f32_16x16x32_bf16 v[80:83], v[48:51], a[60:63], v[80:83]\n"
"buffer_load_dwordx4 a[28:31], v1, s[44:47], s62 offen offset:3072 sc1\n"
"s_add_u32 s62, s62, 4096\n"
"v_mfma_f32_16x16x32_bf16 v[96:99], v[48:51], a[116:119], v[96:99]\n"
"v_mfma_f32_16x16x32_bf16 v[112:115], v[48:51], a[172:175], v[112:115]\n"
"s_waitcnt lgkmcnt(1)\n"
"v_mfma_f32_16x16x32_bf16 v[68:71], v[52:55], a[4:7], v[68:71]\n"
"ds_read_b128 v[36:39], v2 offset:9216\n"
"v_mfma_f32_16x16x32_bf16 v[84:87], v[52:55], a[60:63], v[84:87]\n"
"buffer_load_dwordx4 a[84:87], v1, s[44:47], s63 offen offset:3072 sc1\n"
"s_add_u32 s63, s63, 4096\n"
"v_mfma_f32_16x16x32_bf16 v[100:103], v[52:55], a[116:119], v[100:103]\n"
"v_mfma_f32_16x16x32_bf16 v[116:119], v[52:55], a[172:175], v[116:119]\n"
"v_mfma_f32_16x16x32_bf16 v[72:75], v[56:59], a[4:7], v[72:75]\n"
"ds_read_b128 v[40:43], v2 offset:10240\n"
"v_mfma_f32_16x16x32_bf16 v[88:91], v[56:59], a[60:63], v[88:91]\n"
"buffer_load_dwordx4 a[140:143], v1, s[44:47], s64 offen offset:3072 sc1\n"
"s_add_u32 s64, s64, 4096\n"
"v_mfma_f32_16x16x32_bf16 v[104:107], v[56:59], a[116:119], v[104:107]\n"
"v_mfma_f32_16x16x32_bf16 v[120:123], v[56:59], a[172:175], v[120:123]\n"
"v_mfma_f32_16x16x32_bf16 v[76:79], v[60:63], a[4:7], v[76:79]\n"
"ds_read_b128 v[44:47], v2 offset:11264\n"
"v_mfma_f32_16x16x32_bf16 v[92:95], v[60:63], a[60:63], v[92:95]\n"
"buffer_load_dwordx4 a[196:199], v1, s[44:47], s65 offen offset:3072 sc1\n"
"s_add_u32 s65, s65, 4096\n"
"v_mfma_f32_16x16x32_bf16 v[108:111], v[60:63], a[116:119], v[108:111]\n"
"v_mfma_f32_16x16x32_bf16 v[124:127], v[60:63], a[172:175], v[124:127]\n"
"s_waitcnt vmcnt(20)\n"
"s_waitcnt lgkmcnt(1)\n"
"v_mfma_f32_16x16x32_bf16 v[64:67], v[32:35], a[8:11], v[64:67]\n"
"ds_read_b128 v[48:51], v2 offset:12288\n"
"v_mfma_f32_16x16x32_bf16 v[80:83], v[32:35], a[64:67], v[80:83]\n"
"buffer_load_dwordx4 a[32:35], v1, s[44:47], s62 offen offset:0 sc1\n"
"v_mfma_f32_16x16x32_bf16 v[96:99], v[32:35], a[120:123], v[96:99]\n"
"v_mfma_f32_16x16x32_bf16 v[112:115], v[32:35], a[176:179], v[112:115]\n"
"v_mfma_f32_16x16x32_bf16 v[68:71], v[36:39], a[8:11], v[68:71]\n"
"ds_read_b128 v[52:55], v2 offset:13312\n"
"v_mfma_f32_16x16x32_bf16 v[84:87], v[36:39], a[64:67], v[84:87]\n"
"buffer_load_dwordx4 a[88:91], v1, s[44:47], s63 offen offset:0 sc1\n"
"v_mfma_f32_16x16x32_bf16 v[100:103], v[36:39], a[120:123], v[100:103]\n"
"v_mfma_f32_16x16x32_bf16 v[116:119], v[36:39], a[176:179], v[116:119]\n"
"v_mfma_f32_16x16x32_bf16 v[72:75], v[40:43], a[8:11], v[72:75]\n"
"ds_read_b128 v[56:59], v2 offset:14336\n"
"v_mfma_f32_16x16x32_bf16 v[88:91], v[40:43], a[64:67], v[88:91]\n"
"buffer_load_dwordx4 a[144:147], v1, s[44:47], s64 offen offset:0 sc1\n"
"v_mfma_f32_16x16x32_bf16 v[104:107], v[40:43], a[120:123], v[104:107]\n"
"v_mfma_f32_16x16x32_bf16 v[120:123], v[40:43], a[176:179], v[120:123]\n"
"s_waitcnt lgkmcnt(1)\n"
"v_mfma_f32_16x16x32_bf16 v[76:79], v[44:47], a[8:11], v[76:79]\n"
"ds_read_b128 v[60:63], v2 offset:15360\n"
"v_mfma_f32_16x16x32_bf16 v[92:95], v[44:47], a[64:67], v[92:95]\n"
"buffer_load_dwordx4 a[200:203], v1, s[44:47], s65 offen offset:0 sc1\n"
"v_mfma_f32_16x16x32_bf16 v[108:111], v[44:47], a[120:123], v[108:111]\n"
"v_mfma_f32_16x16x32_bf16 v[124:127], v[44:47], a[176:179], v[124:127]\n"
"s_waitcnt vmcnt(20)\n"
"v_mfma_f32_16x16x32_bf16 v[64:67], v[48:51], a[12:15], v[64:67]\n"
"ds_read_b128 v[32:35], v2 offset:16384\n"
"v_mfma_f32_16x16x32_bf16 v[80:83], v[48:51], a[68:71], v[80:83]\n"
"buffer_load_dwordx4 a[36:39], v1, s[44:47], s62 offen offset:1024 sc1\n"
"v_mfma_f32_16x16x32_bf16 v[96:99], v[48:51], a[124:127], v[96:99]\n"
"v_mfma_f32_16x16x32_bf16 v[112:115], v[48:51], a[180:183], v[112:115]\n"
"v_mfma_f32_16x16x32_bf16 v[68:71], v[52:55], a[12:15], v[68:71]\n"
"ds_read_b128 v[36:39], v2 offset:17408\n"
"v_mfma_f32_16x16x32_bf16 v[84:87], v[52:55], a[68:71], v[84:87]\n"
"buffer_load_dwordx4 a[92:95], v1, s[44:47], s63 offen offset:1024 sc1\n"
"v_mfma_f32_16x16x32_bf16 v[100:103], v[52:55], a[124:127], v[100:103]\n"
"v_mfma_f32_16x16x32_bf16 v[116:119], v[52:55], a[180:183], v[116:119]\n"
"s_waitcnt lgkmcnt(1)\n"
"v_mfma_f32_16x16x32_bf16 v[72:75], v[56:59], a[12:15], v[72:75]\n"
"ds_read_b128 v[40:43], v2 offset:18432\n"
"v_mfma_f32_16x16x32_bf16 v[88:91], v[56:59], a[68:71], v[88:91]\n"
"buffer_load_dwordx4 a[148:151], v1, s[44:47], s64 offen offset:1024 sc1\n"
"v_mfma_f32_16x16x32_bf16 v[104:107], v[56:59], a[124:127], v[104:107]\n"
"v_mfma_f32_16x16x32_bf16 v[120:123], v[56:59], a[180:183], v[120:123]\n"
"v_mfma_f32_16x16x32_bf16 v[76:79], v[60:63], a[12:15], v[76:79]\n"
"ds_read_b128 v[44:47], v2 offset:19456\n"
"v_mfma_f32_16x16x32_bf16 v[92:95], v[60:63], a[68:71], v[92:95]\n"
"buffer_load_dwordx4 a[204:207], v1, s[44:47], s65 offen offset:1024 sc1\n"
"v_mfma_f32_16x16x32_bf16 v[108:111], v[60:63], a[124:127], v[108:111]\n"
"v_mfma_f32_16x16x32_bf16 v[124:127], v[60:63], a[180:183], v[124:127]\n"
"s_waitcnt vmcnt(20)\n"
"v_mfma_f32_16x16x32_bf16 v[64:67], v[32:35], a[16:19], v[64:67]\n"
"ds_read_b128 v[48:51], v2 offset:20480\n"
"v_mfma_f32_16x16x32_bf16 v[80:83], v[32:35], a[72:75], v[80:83]\n"
"buffer_load_dwordx4 a[40:43], v1, s[44:47], s62 offen offset:2048 sc1\n"
"v_mfma_f32_16x16x32_bf16 v[96:99], v[32:35], a[128:131], v[96:99]\n"
"v_mfma_f32_16x16x32_bf16 v[112:115], v[32:35], a[184:187], v[112:115]\n"
"s_waitcnt lgkmcnt(1)\n"
"v_mfma_f32_16x16x32_bf16 v[68:71], v[36:39], a[16:19], v[68:71]\n"
"ds_read_b128 v[52:55], v2 offset:21504\n"
"v_mfma_f32_16x16x32_bf16 v[84:87], v[36:39], a[72:75], v[84:87]\n"
"buffer_load_dwordx4 a[96:99], v1, s[44:47], s63 offen offset:2048 sc1\n"
"v_mfma_f32_16x16x32_bf16 v[100:103], v[36:39], a[128:131], v[100:103]\n"
"v_mfma_f32_16x16x32_bf16 v[116:119], v[36:39], a[184:187], v[116:119]\n"
"v_mfma_f32_16x16x32_bf16 v[72:75], v[40:43], a[16:19], v[72:75]\n"
"ds_read_b128 v[56:59], v2 offset:22528\n"
"v_mfma_f32_16x16x32_bf16 v[88:91], v[40:43], a[72:75], v[88:91]\n"
"buffer_load_dwordx4 a[152:155], v1, s[44:47], s64 offen offset:2048 sc1\n"
"v_mfma_f32_16x16x32_bf16 v[104:107], v[40:43], a[128:131], v[104:107]\n"
"v_mfma_f32_16x16x32_bf16 v[120:123], v[40:43], a[184:187], v[120:123]\n"
"v_mfma_f32_16x16x32_bf16 v[76:79], v[44:47], a[16:19], v[76:79]\n"
"ds_read_b128 v[60:63], v2 offset:23552\n"
"v_mfma_f32_16x16x32_bf16 v[92:95], v[44:47], a[72:75], v[92:95]\n"
"buffer_load_dwordx4 a[208:211], v1, s[44:47], s65 offen offset:2048 sc1\n"
"v_mfma_f32_16x16x32_bf16 v[108:111], v[44:47], a[128:131], v[108:111]\n"
"v_mfma_f32_16x16x32_bf16 v[124:127], v[44:47], a[184:187], v[124:127]\n"
"s_waitcnt vmcnt(20)\n"
"s_waitcnt lgkmcnt(1)\n"
"v_mfma_f32_16x16x32_bf16 v[64:67], v[48:51], a[20:23], v[64:67]\n"
"ds_read_b128 v[32:35], v2 offset:24576\n"
"v_mfma_f32_16x16x32_bf16 v[80:83], v[48:51], a[76:79], v[80:83]\n"
"buffer_load_dwordx4 a[44:47], v1, s[44:47], s62 offen offset:3072 sc1\n"
"s_add_u32 s62, s62, 4096\n"
"v_mfma_f32_16x16x32_bf16 v[96:99], v[48:51], a[132:135], v[96:99]\n"
"v_mfma_f32_16x16x32_bf16 v[112:115], v[48:51], a[188:191], v[112:115]\n"
"v_mfma_f32_16x16x32_bf16 v[68:71], v[52:55], a[20:23], v[68:71]\n"
"ds_read_b128 v[36:39], v2 offset:25600\n"
"v_mfma_f32_16x16x32_bf16 v[84:87], v[52:55], a[76:79], v[84:87]\n"
"buffer_load_dwordx4 a[100:103], v1, s[44:47], s63 offen offset:3072 sc1\n"
"s_add_u32 s63, s63, 4096\n"
"v_mfma_f32_16x16x32_bf16 v[100:103], v[52:55], a[132:135], v[100:103]\n"
"v_mfma_f32_16x16x32_bf16 v[116:119], v[52:55], a[188:191], v[116:119]\n"
"v_mfma_f32_16x16x32_bf16 v[72:75], v[56:59], a[20:23], v[72:75]\n"
"ds_read_b128 v[40:43], v2 offset:26624\n"
"v_mfma_f32_16x16x32_bf16 v[88:91], v[56:59], a[76:79], v[88:91]\n"
"buffer_load_dwordx4 a[156:159], v1, s[44:47], s64 offen offset:3072 sc1\n"
"s_add_u32 s64, s64, 4096\n"
"v_mfma_f32_16x16x32_bf16 v[104:107], v[56:59], a[132:135], v[104:107]\n"
"v_mfma_f32_16x16x32_bf16 v[120:123], v[56:59], a[188:191], v[120:123]\n"
"s_waitcnt lgkmcnt(1)\n"
"v_mfma_f32_16x16x32_bf16 v[76:79], v[60:63], a[20:23], v[76:79]\n"
"ds_read_b128 v[44:47], v2 offset:27648\n"
"v_mfma_f32_16x16x32_bf16 v[92:95], v[60:63], a[76:79], v[92:95]\n"
"buffer_load_dwordx4 a[212:215], v1, s[44:47], s65 offen offset:3072 sc1\n"
"s_add_u32 s65, s65, 4096\n"
"v_mfma_f32_16x16x32_bf16 v[108:111], v[60:63], a[132:135], v[108:111]\n"
"v_mfma_f32_16x16x32_bf16 v[124:127], v[60:63], a[188:191], v[124:127]\n"
"s_waitcnt vmcnt(20)\n"
"v_mfma_f32_16x16x32_bf16 v[64:67], v[32:35], a[24:27], v[64:67]\n"
"ds_read_b128 v[48:51], v2 offset:28672\n"
"v_mfma_f32_16x16x32_bf16 v[80:83], v[32:35], a[80:83], v[80:83]\n"
"buffer_load_dwordx4 a[48:51], v1, s[44:47], s62 offen offset:0 sc1\n"
"v_mfma_f32_16x16x32_bf16 v[96:99], v[32:35], a[136:139], v[96:99]\n"
"v_mfma_f32_16x16x32_bf16 v[112:115], v[32:35], a[192:195], v[112:115]\n"
"v_mfma_f32_16x16x32_bf16 v[68:71], v[36:39], a[24:27], v[68:71]\n"
"ds_read_b128 v[52:55], v2 offset:29696\n"
"v_mfma_f32_16x16x32_bf16 v[84:87], v[36:39], a[80:83], v[84:87]\n"
"buffer_load_dwordx4 a[104:107], v1, s[44:47], s63 offen offset:0 sc1\n"
"v_mfma_f32_16x16x32_bf16 v[100:103], v[36:39], a[136:139], v[100:103]\n"
"v_mfma_f32_16x16x32_bf16 v[116:119], v[36:39], a[192:195], v[116:119]\n"
"s_waitcnt lgkmcnt(1)\n"
"v_mfma_f32_16x16x32_bf16 v[72:75], v[40:43], a[24:27], v[72:75]\n"
"ds_read_b128 v[56:59], v2 offset:30720\n"
"v_mfma_f32_16x16x32_bf16 v[88:91], v[40:43], a[80:83], v[88:91]\n"
"buffer_load_dwordx4 a[160:163], v1, s[44:47], s64 offen offset:0 sc1\n"
"v_mfma_f32_16x16x32_bf16 v[104:107], v[40:43], a[136:139], v[104:107]\n"
"v_mfma_f32_16x16x32_bf16 v[120:123], v[40:43], a[192:195], v[120:123]\n"
"v_mfma_f32_16x16x32_bf16 v[76:79], v[44:47], a[24:27], v[76:79]\n"
"ds_read_b128 v[60:63], v2 offset:31744\n"
"v_mfma_f32_16x16x32_bf16 v[92:95], v[44:47], a[80:83], v[92:95]\n"
"buffer_load_dwordx4 a[216:219], v1, s[44:47], s65 offen offset:0 sc1\n"
"v_mfma_f32_16x16x32_bf16 v[108:111], v[44:47], a[136:139], v[108:111]\n"
"v_mfma_f32_16x16x32_bf16 v[124:127], v[44:47], a[192:195], v[124:127]\n"
"s_waitcnt vmcnt(20)\n"
"v_mfma_f32_16x16x32_bf16 v[64:67], v[48:51], a[28:31], v[64:67]\n"
"ds_read_b128 v[32:35], v2 offset:32768\n"
"v_mfma_f32_16x16x32_bf16 v[80:83], v[48:51], a[84:87], v[80:83]\n"
"buffer_load_dwordx4 a[52:55], v1, s[44:47], s62 offen offset:1024 sc1\n"
"v_mfma_f32_16x16x32_bf16 v[96:99], v[48:51], a[140:143], v[96:99]\n"
"v_mfma_f32_16x16x32_bf16 v[112:115], v[48:51], a[196:199], v[112:115]\n"
"s_waitcnt lgkmcnt(1)\n"
"v_mfma_f32_16x16x32_bf16 v[68:71], v[52:55], a[28:31], v[68:71]\n"
"ds_read_b128 v[36:39], v2 offset:33792\n"
"v_mfma_f32_16x16x32_bf16 v[84:87], v[52:55], a[84:87], v[84:87]\n"
"buffer_load_dwordx4 a[108:111], v1, s[44:47], s63 offen offset:1024 sc1\n"
"v_mfma_f32_16x16x32_bf16 v[100:103], v[52:55], a[140:143], v[100:103]\n"
"v_mfma_f32_16x16x32_bf16 v[116:119], v[52:55], a[196:199], v[116:119]\n"
"v_mfma_f32_16x16x32_bf16 v[72:75], v[56:59], a[28:31], v[72:75]\n"
"ds_read_b128 v[40:43], v2 offset:34816\n"
"v_mfma_f32_16x16x32_bf16 v[88:91], v[56:59], a[84:87], v[88:91]\n"
"buffer_load_dwordx4 a[164:167], v1, s[44:47], s64 offen offset:1024 sc1\n"
"v_mfma_f32_16x16x32_bf16 v[104:107], v[56:59], a[140:143], v[104:107]\n"
"v_mfma_f32_16x16x32_bf16 v[120:123], v[56:59], a[196:199], v[120:123]\n"
"v_mfma_f32_16x16x32_bf16 v[76:79], v[60:63], a[28:31], v[76:79]\n"
"ds_read_b128 v[44:47], v2 offset:35840\n"
"v_mfma_f32_16x16x32_bf16 v[92:95], v[60:63], a[84:87], v[92:95]\n"
"buffer_load_dwordx4 a[220:223], v1, s[44:47], s65 offen offset:1024 sc1\n"
"v_mfma_f32_16x16x32_bf16 v[108:111], v[60:63], a[140:143], v[108:111]\n"
"v_mfma_f32_16x16x32_bf16 v[124:127], v[60:63], a[196:199], v[124:127]\n"
"s_waitcnt vmcnt(20)\n"
"s_waitcnt lgkmcnt(1)\n"
"v_mfma_f32_16x16x32_bf16 v[64:67], v[32:35], a[32:35], v[64:67]\n"
"ds_read_b128 v[48:51], v2 offset:36864\n"
"v_mfma_f32_16x16x32_bf16 v[80:83], v[32:35], a[88:91], v[80:83]\n"
"s_mov_b32 m0, s53\n"
"v_mfma_f32_16x16x32_bf16 v[96:99], v[32:35], a[144:147], v[96:99]\n"
"buffer_load_dwordx4 v1, s[36:39], s50 offen lds\n"
"s_add_u32 s50, s50, 1024\n"
"v_mfma_f32_16x16x32_bf16 v[112:115], v[32:35], a[200:203], v[112:115]\n"
"v_mfma_f32_16x16x32_bf16 v[68:71], v[36:39], a[32:35], v[68:71]\n"
"ds_read_b128 v[52:55], v2 offset:37888\n"
"v_mfma_f32_16x16x32_bf16 v[84:87], v[36:39], a[88:91], v[84:87]\n"
"v_mfma_f32_16x16x32_bf16 v[100:103], v[36:39], a[144:147], v[100:103]\n"
"v_mfma_f32_16x16x32_bf16 v[116:119], v[36:39], a[200:203], v[116:119]\n"
"s_add_u32 m0, m0, 1024\n"
"v_mfma_f32_16x16x32_bf16 v[72:75], v[40:43], a[32:35], v[72:75]\n"
"ds_read_b128 v[56:59], v2 offset:38912\n"
"buffer_load_dwordx4 v1, s[36:39], s50 offen lds\n"
"s_add_u32 s50, s50, 1024\n"
"v_mfma_f32_16x16x32_bf16 v[88:91], v[40:43], a[88:91], v[88:91]\n"
"v_mfma_f32_16x16x32_bf16 v[104:107], v[40:43], a[144:147], v[104:107]\n"
"v_mfma_f32_16x16x32_bf16 v[120:123], v[40:43], a[200:203], v[120:123]\n"
"s_waitcnt lgkmcnt(1)\n"
"v_mfma_f32_16x16x32_bf16 v[76:79], v[44:47], a[32:35], v[76:79]\n"
"ds_read_b128 v[60:63], v2 offset:39936\n"
"v_mfma_f32_16x16x32_bf16 v[92:95], v[44:47], a[88:91], v[92:95]\n"
"s_add_u32 m0, m0, 1024\n"
"v_mfma_f32_16x16x32_bf16 v[108:111], v[44:47], a[144:147], v[108:111]\n"
"buffer_load_dwordx4 v1, s[36:39], s50 offen lds\n"
"s_add_u32 s50, s50, 1024\n"
"v_mfma_f32_16x16x32_bf16 v[124:127], v[44:47], a[200:203], v[124:127]\n"
"s_waitcnt vmcnt(19)\n"
"v_mfma_f32_16x16x32_bf16 v[64:67], v[48:51], a[36:39], v[64:67]\n"
"ds_read_b128 v[32:35], v2 offset:40960\n"
"v_mfma_f32_16x16x32_bf16 v[80:83], v[48:51], a[92:95], v[80:83]\n"
"v_mfma_f32_16x16x32_bf16 v[96:99], v[48:51], a[148:151], v[96:99]\n"
"v_mfma_f32_16x16x32_bf16 v[112:115], v[48:51], a[204:207], v[112:115]\n"
"s_add_u32 m0, m0, 1024\n"
"v_mfma_f32_16x16x32_bf16 v[68:71], v[52:55], a[36:39], v[68:71]\n"
"ds_read_b128 v[36:39], v2 offset:41984\n"
"buffer_load_dwordx4 v1, s[36:39], s50 offen lds\n"
"s_add_u32 s50, s50, 1024\n"
"v_mfma_f32_16x16x32_bf16 v[84:87], v[52:55], a[92:95], v[84:87]\n"
"v_mfma_f32_16x16x32_bf16 v[100:103], v[52:55], a[148:151], v[100:103]\n"
"v_mfma_f32_16x16x32_bf16 v[116:119], v[52:55], a[204:207], v[116:119]\n"
"s_waitcnt lgkmcnt(1)\n"
"v_mfma_f32_16x16x32_bf16 v[72:75], v[56:59], a[36:39], v[72:75]\n"
"ds_read_b128 v[40:43], v2 offset:43008\n"
"v_mfma_f32_16x16x32_bf16 v[88:91], v[56:59], a[92:95], v[88:91]\n"
"s_add_u32 m0, m0, 1024\n"
"v_mfma_f32_16x16x32_bf16 v[104:107], v[56:59], a[148:151], v[104:107]\n"
"buffer_load_dwordx4 v1, s[36:39], s50 offen lds\n"
"s_add_u32 s50, s50, 1024\n"
"v_mfma_f32_16x16x32_bf16 v[120:123], v[56:59], a[204:207], v[120:123]\n"
"v_mfma_f32_16x16x32_bf16 v[76:79], v[60:63], a[36:39], v[76:79]\n"
"ds_read_b128 v[44:47], v2 offset:44032\n"
"v_mfma_f32_16x16x32_bf16 v[92:95], v[60:63], a[92:95], v[92:95]\n"
"v_mfma_f32_16x16x32_bf16 v[108:111], v[60:63], a[148:151], v[108:111]\n"
"v_mfma_f32_16x16x32_bf16 v[124:127], v[60:63], a[204:207], v[124:127]\n"
"s_add_u32 m0, m0, 1024\n"
"s_waitcnt vmcnt(17)\n"
"v_mfma_f32_16x16x32_bf16 v[64:67], v[32:35], a[40:43], v[64:67]\n"
"ds_read_b128 v[48:51], v2 offset:45056\n"
"buffer_load_dwordx4 v1, s[36:39], s50 offen lds\n"
"s_add_u32 s50, s50, 1024\n"
"v_mfma_f32_16x16x32_bf16 v[80:83], v[32:35], a[96:99], v[80:83]\n"
"v_mfma_f32_16x16x32_bf16 v[96:99], v[32:35], a[152:155], v[96:99]\n"
"v_mfma_f32_16x16x32_bf16 v[112:115], v[32:35], a[208:211], v[112:115]\n"
"s_waitcnt lgkmcnt(1)\n"
"v_mfma_f32_16x16x32_bf16 v[68:71], v[36:39], a[40:43], v[68:71]\n"
"ds_read_b128 v[52:55], v2 offset:46080\n"
"v_mfma_f32_16x16x32_bf16 v[84:87], v[36:39], a[96:99], v[84:87]\n"
"s_add_u32 m0, m0, 1024\n"
"v_mfma_f32_16x16x32_bf16 v[100:103], v[36:39], a[152:155], v[100:103]\n"
"buffer_load_dwordx4 v1, s[36:39], s50 offen lds\n"
"s_add_u32 s50, s50, 1024\n"
"v_mfma_f32_16x16x32_bf16 v[116:119], v[36:39], a[208:211], v[116:119]\n"
"v_mfma_f32_16x16x32_bf16 v[72:75], v[40:43], a[40:43], v[72:75]\n"
"ds_read_b128 v[56:59], v2 offset:47104\n"
"v_mfma_f32_16x16x32_bf16 v[88:91], v[40:43], a[96:99], v[88:91]\n"
"v_mfma_f32_16x16x32_bf16 v[104:107], v[40:43], a[152:155], v[104:107]\n"
"v_mfma_f32_16x16x32_bf16 v[120:123], v[40:43], a[208:211], v[120:123]\n"
"s_add_u32 m0, m0, 1024\n"
"v_mfma_f32_16x16x32_bf16 v[76:79], v[44:47], a[40:43], v[76:79]\n"
"ds_read_b128 v[60:63], v2 offset:48128\n"
"buffer_load_dwordx4 v1, s[36:39], s50 offen lds\n"
"s_add_u32 s50, s50, 1024\n"
"v_mfma_f32_16x16x32_bf16 v[92:95], v[44:47], a[96:99], v[92:95]\n"
"v_mfma_f32_16x16x32_bf16 v[108:111], v[44:47], a[152:155], v[108:111]\n"
"v_mfma_f32_16x16x32_bf16 v[124:127], v[44:47], a[208:211], v[124:127]\n"
"s_waitcnt vmcnt(16)\n"
"s_waitcnt lgkmcnt(1)\n"
"v_mfma_f32_16x16x32_bf16 v[64:67], v[48:51], a[44:47], v[64:67]\n"
"ds_read_b128 v[32:35], v2 offset:49152\n"
"v_mfma_f32_16x16x32_bf16 v[80:83], v[48:51], a[100:103], v[80:83]\n"
"s_add_u32 m0, m0, 1024\n"
"v_mfma_f32_16x16x32_bf16 v[96:99], v[48:51], a[156:159], v[96:99]\n"
"buffer_load_dwordx4 v1, s[36:39], s50 offen lds\n"
"s_add_u32 s50, s50, 1024\n"
"v_mfma_f32_16x16x32_bf16 v[112:115], v[48:51], a[212:215], v[112:115]\n"
"v_mfma_f32_16x16x32_bf16 v[68:71], v[52:55], a[44:47], v[68:71]\n"
"ds_read_b128 v[36:39], v2 offset:50176\n"
"v_mfma_f32_16x16x32_bf16 v[84:87], v[52:55], a[100:103], v[84:87]\n"
"v_mfma_f32_16x16x32_bf16 v[100:103], v[52:55], a[156:159], v[100:103]\n"
"v_mfma_f32_16x16x32_bf16 v[116:119], v[52:55], a[212:215], v[116:119]\n"
"s_add_u32 m0, m0, 1024\n"
"v_mfma_f32_16x16x32_bf16 v[72:75], v[56:59], a[44:47], v[72:75]\n"
"ds_read_b128 v[40:43], v2 offset:51200\n"
"buffer_load_dwordx4 v1, s[36:39], s50 offen lds\n"
"s_add_u32 s50, s50, 1024\n"
"v_mfma_f32_16x16x32_bf16 v[88:91], v[56:59], a[100:103], v[88:91]\n"
"v_mfma_f32_16x16x32_bf16 v[104:107], v[56:59], a[156:159], v[104:107]\n"
"v_mfma_f32_16x16x32_bf16 v[120:123], v[56:59], a[212:215], v[120:123]\n"
"s_waitcnt lgkmcnt(1)\n"
"v_mfma_f32_16x16x32_bf16 v[76:79], v[60:63], a[44:47], v[76:79]\n"
"ds_read_b128 v[44:47], v2 offset:52224\n"
"v_mfma_f32_16x16x32_bf16 v[92:95], v[60:63], a[100:103], v[92:95]\n"
"s_add_u32 m0, m0, 1024\n"
"v_mfma_f32_16x16x32_bf16 v[108:111], v[60:63], a[156:159], v[108:111]\n"
"buffer_load_dwordx4 v1, s[36:39], s50 offen lds\n"
"s_add_u32 s50, s50, 1024\n"
"v_mfma_f32_16x16x32_bf16 v[124:127], v[60:63], a[212:215], v[124:127]\n"
"s_waitcnt vmcnt(15)\n"
"v_mfma_f32_16x16x32_bf16 v[64:67], v[32:35], a[48:51], v[64:67]\n"
"ds_read_b128 v[48:51], v2 offset:53248\n"
"v_mfma_f32_16x16x32_bf16 v[80:83], v[32:35], a[104:107], v[80:83]\n"
"v_mfma_f32_16x16x32_bf16 v[96:99], v[32:35], a[160:163], v[96:99]\n"
"v_mfma_f32_16x16x32_bf16 v[112:115], v[32:35], a[216:219], v[112:115]\n"
"s_add_u32 m0, m0, 1024\n"
"v_mfma_f32_16x16x32_bf16 v[68:71], v[36:39], a[48:51], v[68:71]\n"
"ds_read_b128 v[52:55], v2 offset:54272\n"
"buffer_load_dwordx4 v1, s[36:39], s50 offen lds\n"
"s_add_u32 s50, s50, 1024\n"
"v_mfma_f32_16x16x32_bf16 v[84:87], v[36:39], a[104:107], v[84:87]\n"
"v_mfma_f32_16x16x32_bf16 v[100:103], v[36:39], a[160:163], v[100:103]\n"
"v_mfma_f32_16x16x32_bf16 v[116:119], v[36:39], a[216:219], v[116:119]\n"
"s_waitcnt lgkmcnt(1)\n"
"v_mfma_f32_16x16x32_bf16 v[72:75], v[40:43], a[48:51], v[72:75]\n"
"ds_read_b128 v[56:59], v2 offset:55296\n"
"v_mfma_f32_16x16x32_bf16 v[88:91], v[40:43], a[104:107], v[88:91]\n"
"s_add_u32 m0, m0, 1024\n"
"v_mfma_f32_16x16x32_bf16 v[104:107], v[40:43], a[160:163], v[104:107]\n"
"buffer_load_dwordx4 v1, s[36:39], s50 offen lds\n"
"s_add_u32 s50, s50, 1024\n"
"v_mfma_f32_16x16x32_bf16 v[120:123], v[40:43], a[216:219], v[120:123]\n"
"v_mfma_f32_16x16x32_bf16 v[76:79], v[44:47], a[48:51], v[76:79]\n"
"ds_read_b128 v[60:63], v2 offset:56320\n"
"v_mfma_f32_16x16x32_bf16 v[92:95], v[44:47], a[104:107], v[92:95]\n"
"v_mfma_f32_16x16x32_bf16 v[108:111], v[44:47], a[160:163], v[108:111]\n"
"v_mfma_f32_16x16x32_bf16 v[124:127], v[44:47], a[216:219], v[124:127]\n"
"s_add_u32 m0, m0, 1024\n"
"s_waitcnt vmcnt(13)\n"
"v_mfma_f32_16x16x32_bf16 v[64:67], v[48:51], a[52:55], v[64:67]\n"
"buffer_load_dwordx4 v1, s[36:39], s50 offen lds\n"
"s_add_u32 s50, s50, 44032\n"
"v_mfma_f32_16x16x32_bf16 v[80:83], v[48:51], a[108:111], v[80:83]\n"
"v_mfma_f32_16x16x32_bf16 v[96:99], v[48:51], a[164:167], v[96:99]\n"
"v_mfma_f32_16x16x32_bf16 v[112:115], v[48:51], a[220:223], v[112:115]\n"
"s_waitcnt lgkmcnt(0)\n"
"v_mfma_f32_16x16x32_bf16 v[68:71], v[52:55], a[52:55], v[68:71]\n"
"v_mfma_f32_16x16x32_bf16 v[84:87], v[52:55], a[108:111], v[84:87]\n"
"v_mfma_f32_16x16x32_bf16 v[100:103], v[52:55], a[164:167], v[100:103]\n"
"v_mfma_f32_16x16x32_bf16 v[116:119], v[52:55], a[220:223], v[116:119]\n"
"v_mfma_f32_16x16x32_bf16 v[72:75], v[56:59], a[52:55], v[72:75]\n"
"v_mfma_f32_16x16x32_bf16 v[88:91], v[56:59], a[108:111], v[88:91]\n"
"v_mfma_f32_16x16x32_bf16 v[104:107], v[56:59], a[164:167], v[104:107]\n"
"v_mfma_f32_16x16x32_bf16 v[120:123], v[56:59], a[220:223], v[120:123]\n"
"v_mfma_f32_16x16x32_bf16 v[76:79], v[60:63], a[52:55], v[76:79]\n"
"v_mfma_f32_16x16x32_bf16 v[92:95], v[60:63], a[108:111], v[92:95]\n"
"v_mfma_f32_16x16x32_bf16 v[108:111], v[60:63], a[164:167], v[108:111]\n"
"v_mfma_f32_16x16x32_bf16 v[124:127], v[60:63], a[220:223], v[124:127]\n"
"s_sub_u32 s51, s51, 1\n"
"s_cmp_eq_u32 s51, 0\n"
"s_waitcnt vmcnt(0)\n"
"L_b3%=:\n"
"s_cmp_eq_u32 s51, 1\n"
"s_cselect_b32 s38, 0, s38\n"
"s_nop 4\n"
"s_barrier\n"
"ds_read_b128 v[32:35], v3 offset:0\n"
"ds_read_b128 v[36:39], v3 offset:1024\n"
"ds_read_b128 v[40:43], v3 offset:2048\n"
"ds_read_b128 v[44:47], v3 offset:3072\n"
"v_fma_f32 v18, -v64, v64, v4\n"
"v_mul_f32 v18, v18, v64\n"
"v_exp_f32 v18, v18\n"
"v_fma_f32 v19, -v65, v65, v4\n"
"v_mul_f32 v19, v19, v65\n"
"v_fma_f32 v18, v18, v10, v10\n"
"s_waitcnt lgkmcnt(3)\n"
"v_mfma_f32_16x16x32_bf16 v[128:131], v[32:35], a[0:3], 0\n"
"ds_read_b128 v[48:51], v3 offset:4096\n"
"v_exp_f32 v19, v19\n"
"v_mfma_f32_16x16x32_bf16 v[144:147], v[32:35], a[56:59], 0\n"
"s_mov_b32 m0, s52\n"
"v_fma_f32 v20, -v66, v66, v4\n"
"v_mul_f32 v20, v20, v66\n"
"v_mfma_f32_16x16x32_bf16 v[160:163], v[32:35], a[112:115], 0\n"
"buffer_load_dwordx4 v1, s[36:39], s50 offen lds\n"
"s_add_u32 s50, s50, 1024\n"
"v_mfma_f32_16x16x32_bf16 v[176:179], v[32:35], a[168:171], 0\n"
"v_rcp_f32 v18, v18\n"
"s_waitcnt lgkmcnt(0)\n"
"v_mfma_f32_16x16x32_bf16 v[132:135], v[36:39], a[0:3], 0\n"
"ds_read_b128 v[52:55], v3 offset:5120\n"
"v_fma_f32 v19, v19, v10, v10\n"
"v_mul_f32 v64, v64, v18\n"
"v_exp_f32 v20, v20\n"
"v_mfma_f32_16x16x32_bf16 v[148:151], v[36:39], a[56:59], 0\n"
"v_fma_f32 v18, -v67, v67, v4\n"
"v_mul_f32 v18, v18, v67\n"
"v_mfma_f32_16x16x32_bf16 v[164:167], v[36:39], a[112:115], 0\n"
"v_rcp_f32 v19, v19\n"
"v_mfma_f32_16x16x32_bf16 v[180:183], v[36:39], a[168:171], 0\n"
"s_add_u32 m0, m0, 1024\n"
"v_fma_f32 v20, v20, v10, v10\n"
"v_mul_f32 v65, v65, v19\n"
"v_exp_f32 v18, v18\n"
"v_mfma_f32_16x16x32_bf16 v[136:139], v[40:43], a[0:3], 0\n"
"ds_read_b128 v[56:59], v3 offset:6144\n"
"buffer_load_dwordx4 v1, s[36:39], s50 offen lds\n"
"s_add_u32 s50, s50, 1024\n"
"v_mfma_f32_16x16x32_bf16 v[152:155], v[40:43], a[56:59], 0\n"
"v_fma_f32 v19, -v68, v68, v4\n"
"v_mul_f32 v19, v19, v68\n"
"v_mfma_f32_16x16x32_bf16 v[168:171], v[40:43], a[112:115], 0\n"
"v_rcp_f32 v20, v20\n"
"v_mfma_f32_16x16x32_bf16 v[184:187], v[40:43], a[168:171], 0\n"
"v_fma_f32 v18, v18, v10, v10\n"
"v_cvt_pk_bf16_f32 v24, v64, v65\n"
"v_mul_f32 v66, v66, v20\n"
"v_mfma_f32_16x16x32_bf16 v[140:143], v[44:47], a[0:3], 0\n"
"ds_read_b128 v[60:63], v3 offset:7168\n"
"v_exp_f32 v19, v19\n"
"v_mfma_f32_16x16x32_bf16 v[156:159], v[44:47], a[56:59], 0\n"
"s_add_u32 m0, m0, 1024\n"
"v_fma_f32 v20, -v69, v69, v4\n"
"v_mul_f32 v20, v20, v69\n"
"v_mfma_f32_16x16x32_bf16 v[172:175], v[44:47], a[112:115], 0\n"
"buffer_load_dwordx4 v1, s[36:39], s50 offen lds\n"
"s_add_u32 s50, s50, 1024\n"
"v_rcp_f32 v18, v18\n"
"v_mfma_f32_16x16x32_bf16 v[188:191], v[44:47], a[168:171], 0\n"
"v_fma_f32 v19, v19, v10, v10\n"
"v_mul_f32 v67, v67, v18\n"
"v_mfma_f32_16x16x32_bf16 v[128:131], v[48:51], a[4:7], v[128:131]\n"
"ds_read_b128 v[32:35], v3 offset:8192\n"
"v_exp_f32 v20, v20\n"
"v_mfma_f32_16x16x32_bf16 v[144:147], v[48:51], a[60:63], v[144:147]\n"
"v_fma_f32 v18, -v70, v70, v4\n"
"v_mul_f32 v18, v18, v70\n"
"v_mfma_f32_16x16x32_bf16 v[160:163], v[48:51], a[116:119], v[160:163]\n"
"v_rcp_f32 v19, v19\n"
"v_mfma_f32_16x16x32_bf16 v[176:179], v[48:51], a[172:175], v[176:179]\n"
"s_add_u32 m0, m0, 1024\n"
"v_fma_f32 v20, v20, v10, v10\n"
"v_cvt_pk_bf16_f32 v25, v66, v67\n"
"v_mul_f32 v68, v68, v19\n"
"s_waitcnt lgkmcnt(0)\n"
"v_mfma_f32_16x16x32_bf16 v[132:135], v[52:55], a[4:7], v[132:135]\n"
"ds_read_b128 v[36:39], v3 offset:9216\n"
"buffer_load_dwordx4 v1, s[36:39], s50 offen lds\n"
"s_add_u32 s50, s50, 1024\n"
"v_mfma_f32_16x16x32_bf16 v[148:151], v[52:55], a[60:63], v[148:151]\n"
"v_exp_f32 v18, v18\n"
"v_mfma_f32_16x16x32_bf16 v[164:167], v[52:55], a[116:119], v[164:167]\n"
"v_fma_f32 v19, -v71, v71, v4\n"
"v_mul_f32 v19, v19, v71\n"
"v_rcp_f32 v20, v20\n"
"v_mfma_f32_16x16x32_bf16 v[180:183], v[52:55], a[172:175], v[180:183]\n"
"v_fma_f32 v18, v18, v10, v10\n"
"v_mul_f32 v69, v69, v20\n"
"v_exp_f32 v19, v19\n"
"v_mfma_f32_16x16x32_bf16 v[136:139], v[56:59], a[4:7], v[136:139]\n"
"ds_read_b128 v[40:43], v3 offset:10240\n"
"v_fma_f32 v20, -v72, v72, v4\n"
"v_mfma_f32_16x16x32_bf16 v[152:155], v[56:59], a[60:63], v[152:155]\n"
"s_add_u32 m0, m0, 1024\n"
"v_mul_f32 v20, v20, v72\n"
"v_mfma_f32_16x16x32_bf16 v[168:171], v[56:59], a[116:119], v[168:171]\n"
"buffer_load_dwordx4 v1, s[36:39], s50 offen lds\n"
"s_add_u32 s50, s50, 1024\n"
"v_rcp_f32 v18, v18\n"
"v_mfma_f32_16x16x32_bf16 v[184:187], v[56:59], a[172:175], v[184:187]\n"
"v_fma_f32 v19, v19, v10, v10\n"
"v_cvt_pk_bf16_f32 v26, v68, v69\n"
"v_mul_f32 v70, v70, v18\n"
"v_mfma_f32_16x16x32_bf16 v[140:143], v[60:63], a[4:7], v[140:143]\n"
"ds_read_b128 v[44:47], v3 offset:11264\n"
"v_exp_f32 v20, v20\n"
"v_mfma_f32_16x16x32_bf16 v[156:159], v[60:63], a[60:63], v[156:159]\n"
"v_fma_f32 v18, -v73, v73, v4\n"
"v_mul_f32 v18, v18, v73\n"
"v_mfma_f32_16x16x32_bf16 v[172:175], v[60:63], a[116:119], v[172:175]\n"
"v_rcp_f32 v19, v19\n"
"v_mfma_f32_16x16x32_bf16 v[188:191], v[60:63], a[172:175], v[188:191]\n"
"s_add_u32 m0, m0, 1024\n"
"v_fma_f32 v20, v20, v10, v10\n"
"v_mul_f32 v71, v71, v19\n"
"v_exp_f32 v18, v18\n"
"v_mfma_f32_16x16x32_bf16 v[128:131], v[32:35], a[8:11], v[128:131]\n"
"ds_read_b128 v[48:51], v3 offset:12288\n"
"buffer_load_dwordx4 v1, s[36:39], s50 offen lds\n"
"s_add_u32 s50, s50, 1024\n"
"v_mfma_f32_16x16x32_bf16 v[144:147], v[32:35], a[64:67], v[144:147]\n"
"v_fma_f32 v19, -v74, v74, v4\n"
"v_mul_f32 v19, v19, v74\n"
"v_mfma_f32_16x16x32_bf16 v[160:163], v[32:35], a[120:123], v[160:163]\n"
"v_rcp_f32 v20, v20\n"
"v_mfma_f32_16x16x32_bf16 v[176:179], v[32:35], a[176:179], v[176:179]\n"
"v_fma_f32 v18, v18, v10, v10\n"
"v_cvt_pk_bf16_f32 v27, v70, v71\n"
"buffer_store_dwordx4 v[24:27], v1, s[40:43], s54 offen offset:0 sc1\n"
"v_mul_f32 v72, v72, v20\n"
"s_waitcnt lgkmcnt(0)\n"
"v_mfma_f32_16x16x32_bf16 v[132:135], v[36:39], a[8:11], v[132:135]\n"
"ds_read_b128 v[52:55], v3 offset:13312\n"
"v_exp_f32 v19, v19\n"
"v_mfma_f32_16x16x32_bf16 v[148:151], v[36:39], a[64:67], v[148:151]\n"
"s_add_u32 m0, m0, 1024\n"
"v_fma_f32 v20, -v75, v75, v4\n"
"v_mul_f32 v20, v20, v75\n"
"v_mfma_f32_16x16x32_bf16 v[164:167], v[36:39], a[120:123], v[164:167]\n"
"buffer_load_dwordx4 v1, s[36:39], s50 offen lds\n"
"s_add_u32 s50, s50, 1024\n"
"v_mfma_f32_16x16x32_bf16 v[180:183], v[36:39], a[176:179], v[180:183]\n"
"v_rcp_f32 v18, v18\n"
"v_mfma_f32_16x16x32_bf16 v[136:139], v[40:43], a[8:11], v[136:139]\n"
"ds_read_b128 v[56:59], v3 offset:14336\n"
"v_fma_f32 v19, v19, v10, v10\n"
"v_mul_f32 v73, v73, v18\n"
"v_exp_f32 v20, v20\n"
"v_mfma_f32_16x16x32_bf16 v[152:155], v[40:43], a[64:67], v[152:155]\n"
"v_fma_f32 v18, -v76, v76, v4\n"
"v_mul_f32 v18, v18, v76\n"
"v_mfma_f32_16x16x32_bf16 v[168:171], v[40:43], a[120:123], v[168:171]\n"
"v_rcp_f32 v19, v19\n"
"v_mfma_f32_16x16x32_bf16 v[184:187], v[40:43], a[176:179], v[184:187]\n"
"s_add_u32 m0, m0, 1024\n"
"v_fma_f32 v20, v20, v10, v10\n"
"v_cvt_pk_bf16_f32 v28, v72, v73\n"
"v_mul_f32 v74, v74, v19\n"
"v_mfma_f32_16x16x32_bf16 v[140:143], v[44:47], a[8:11], v[140:143]\n"
"ds_read_b128 v[60:63], v3 offset:15360\n"
"buffer_load_dwordx4 v1, s[36:39], s50 offen lds\n"
"s_add_u32 s50, s50, 1024\n"
"v_mfma_f32_16x16x32_bf16 v[156:159], v[44:47], a[64:67], v[156:159]\n"
"v_exp_f32 v18, v18\n"
"v_mfma_f32_16x16x32_bf16 v[172:175], v[44:47], a[120:123], v[172:175]\n"
"v_fma_f32 v19, -v77, v77, v4\n"
"v_mul_f32 v19, v19, v77\n"
"v_rcp_f32 v20, v20\n"
"v_mfma_f32_16x16x32_bf16 v[188:191], v[44:47], a[176:179], v[188:191]\n"
"v_fma_f32 v18, v18, v10, v10\n"
"v_mul_f32 v75, v75, v20\n"
"v_mfma_f32_16x16x32_bf16 v[128:131], v[48:51], a[12:15], v[128:131]\n"
"ds_read_b128 v[32:35], v3 offset:16384\n"
"v_exp_f32 v19, v19\n"
"v_mfma_f32_16x16x32_bf16 v[144:147], v[48:51], a[68:71], v[144:147]\n"
"s_add_u32 m0, m0, 1024\n"
"v_fma_f32 v20, -v78, v78, v4\n"
"v_mul_f32 v20, v20, v78\n"
"v_mfma_f32_16x16x32_bf16 v[160:163], v[48:51], a[124:127], v[160:163]\n"
"buffer_load_dwordx4 v1, s[36:39], s50 offen lds\n"
"s_add_u32 s50, s50, 1024\n"
"v_rcp_f32 v18, v18\n"
"v_mfma_f32_16x16x32_bf16 v[176:179], v[48:51], a[180:183], v[176:179]\n"
"v_fma_f32 v19, v19, v10, v10\n"
"v_cvt_pk_bf16_f32 v29, v74, v75\n"
"s_waitcnt lgkmcnt(0)\n"
"v_mfma_f32_16x16x32_bf16 v[132:135], v[52:55], a[12:15], v[132:135]\n"
"ds_read_b128 v[36:39], v3 offset:17408\n"
"v_mul_f32 v76, v76, v18\n"
"v_mfma_f32_16x16x32_bf16 v[148:151], v[52:55], a[68:71], v[148:151]\n"
"v_exp_f32 v20, v20\n"
"v_mfma_f32_16x16x32_bf16 v[164:167], v[52:55], a[124:127], v[164:167]\n"
"v_fma_f32 v18, -v79, v79, v4\n"
"v_mul_f32 v18, v18, v79\n"
"v_rcp_f32 v19, v19\n"
"v_mfma_f32_16x16x32_bf16 v[180:183], v[52:55], a[180:183], v[180:183]\n"
"s_add_u32 m0, m0, 1024\n"
"v_fma_f32 v20, v20, v10, v10\n"
"v_mul_f32 v77, v77, v19\n"
"v_mfma_f32_16x16x32_bf16 v[136:139], v[56:59], a[12:15], v[136:139]\n"
"ds_read_b128 v[40:43], v3 offset:18432\n"
"buffer_load_dwordx4 v1, s[36:39], s50 offen lds\n"
"s_add_u32 s50, s50, 1024\n"
"v_mfma_f32_16x16x32_bf16 v[152:155], v[56:59], a[68:71], v[152:155]\n"
"v_exp_f32 v18, v18\n"
"v_mfma_f32_16x16x32_bf16 v[168:171], v[56:59], a[124:127], v[168:171]\n"
"v_fma_f32 v19, -v80, v80, v4\n"
"v_mul_f32 v19, v19, v80\n"
"v_rcp_f32 v20, v20\n"
"v_mfma_f32_16x16x32_bf16 v[184:187], v[56:59], a[180:183], v[184:187]\n"
"v_fma_f32 v18, v18, v10, v10\n"
"v_cvt_pk_bf16_f32 v30, v76, v77\n"
"v_mul_f32 v78, v78, v20\n"
"v_mfma_f32_16x16x32_bf16 v[140:143], v[60:63], a[12:15], v[140:143]\n"
"ds_read_b128 v[44:47], v3 offset:19456\n"
"v_exp_f32 v19, v19\n"
"v_mfma_f32_16x16x32_bf16 v[156:159], v[60:63], a[68:71], v[156:159]\n"
"s_add_u32 m0, m0, 1024\n"
"v_fma_f32 v20, -v81, v81, v4\n"
"v_mul_f32 v20, v20, v81\n"
"v_mfma_f32_16x16x32_bf16 v[172:175], v[60:63], a[124:127], v[172:175]\n"
"buffer_load_dwordx4 v1, s[36:39], s50 offen lds\n"
"s_add_u32 s50, s50, 1024\n"
"v_mfma_f32_16x16x32_bf16 v[188:191], v[60:63], a[180:183], v[188:191]\n"
"v_rcp_f32 v18, v18\n"
"v_mfma_f32_16x16x32_bf16 v[128:131], v[32:35], a[16:19], v[128:131]\n"
"ds_read_b128 v[48:51], v3 offset:20480\n"
"v_fma_f32 v19, v19, v12, v12\n"
"v_mul_f32 v79, v79, v18\n"
"v_exp_f32 v20, v20\n"
"v_mfma_f32_16x16x32_bf16 v[144:147], v[32:35], a[72:75], v[144:147]\n"
"v_fma_f32 v18, -v82, v82, v4\n"
"v_mul_f32 v18, v18, v82\n"
"v_mfma_f32_16x16x32_bf16 v[160:163], v[32:35], a[128:131], v[160:163]\n"
"v_rcp_f32 v19, v19\n"
"v_mfma_f32_16x16x32_bf16 v[176:179], v[32:35], a[184:187], v[176:179]\n"
"s_add_u32 m0, m0, 1024\n"
"v_fma_f32 v20, v20, v12, v12\n"
"v_cvt_pk_bf16_f32 v31, v78, v79\n"
"buffer_store_dwordx4 v[28:31], v1, s[40:43], s54 offen offset:1024 sc1\n"
"s_waitcnt lgkmcnt(0)\n"
"v_mfma_f32_16x16x32_bf16 v[132:135], v[36:39], a[16:19], v[132:135]\n"
"ds_read_b128 v[52:55], v3 offset:21504\n"
"buffer_load_dwordx4 v1, s[36:39], s50 offen lds\n"
"s_add_u32 s50, s50, 1024\n"
"v_mfma_f32_16x16x32_bf16 v[148:151], v[36:39], a[72:75], v[148:151]\n"
"s_add_u32 s54, s54, 2048\n"
"v_mul_f32 v80, v80, v19\n"
"v_mfma_f32_16x16x32_bf16 v[164:167], v[36:39], a[128:131], v[164:167]\n"
"v_exp_f32 v18, v18\n"
"v_mfma_f32_16x16x32_bf16 v[180:183], v[36:39], a[184:187], v[180:183]\n"
"v_fma_f32 v19, -v83, v83, v4\n"
"v_mul_f32 v19, v19, v83\n"
"v_rcp_f32 v20, v20\n"
"v_mfma_f32_16x16x32_bf16 v[136:139], v[40:43], a[16:19], v[136:139]\n"
"ds_read_b128 v[56:59], v3 offset:22528\n"
"v_fma_f32 v18, v18, v12, v12\n"
"v_mul_f32 v81, v81, v20\n"
"v_mfma_f32_16x16x32_bf16 v[152:155], v[40:43], a[72:75], v[152:155]\n"
"s_add_u32 m0, m0, 1024\n"
"v_exp_f32 v19, v19\n"
"v_mfma_f32_16x16x32_bf16 v[168:171], v[40:43], a[128:131], v[168:171]\n"
"buffer_load_dwordx4 v1, s[36:39], s50 offen lds\n"
"s_add_u32 s50, s50, 1024\n"
"v_fma_f32 v20, -v84, v84, v4\n"
"v_mfma_f32_16x16x32_bf16 v[184:187], v[40:43], a[184:187], v[184:187]\n"
"v_mul_f32 v20, v20, v84\n"
"v_rcp_f32 v18, v18\n"
"v_mfma_f32_16x16x32_bf16 v[140:143], v[44:47], a[16:19], v[140:143]\n"
"ds_read_b128 v[60:63], v3 offset:23552\n"
"v_fma_f32 v19, v19, v12, v12\n"
"v_cvt_pk_bf16_f32 v24, v80, v81\n"
"v_mfma_f32_16x16x32_bf16 v[156:159], v[44:47], a[72:75], v[156:159]\n"
"v_mul_f32 v82, v82, v18\n"
"v_exp_f32 v20, v20\n"
"v_mfma_f32_16x16x32_bf16 v[172:175], v[44:47], a[128:131], v[172:175]\n"
"v_fma_f32 v18, -v85, v85, v4\n"
"v_mul_f32 v18, v18, v85\n"
"v_mfma_f32_16x16x32_bf16 v[188:191], v[44:47], a[184:187], v[188:191]\n"
"s_add_u32 m0, m0, 1024\n"
"v_rcp_f32 v19, v19\n"
"v_mfma_f32_16x16x32_bf16 v[128:131], v[48:51], a[20:23], v[128:131]\n"
"ds_read_b128 v[32:35], v3 offset:24576\n"
"buffer_load_dwordx4 v1, s[36:39], s50 offen lds\n"
"s_add_u32 s50, s50, 44032\n"
"v_fma_f32 v20, v20, v12, v12\n"
"v_mfma_f32_16x16x32_bf16 v[144:147], v[48:51], a[76:79], v[144:147]\n"
"v_mul_f32 v83, v83, v19\n"
"v_exp_f32 v18, v18\n"
"v_mfma_f32_16x16x32_bf16 v[160:163], v[48:51], a[132:135], v[160:163]\n"
"v_fma_f32 v19, -v86, v86, v4\n"
"v_mul_f32 v19, v19, v86\n"
"v_mfma_f32_16x16x32_bf16 v[176:179], v[48:51], a[188:191], v[176:179]\n"
"v_rcp_f32 v20, v20\n"
"s_waitcnt lgkmcnt(0)\n"
"v_mfma_f32_16x16x32_bf16 v[132:135], v[52:55], a[20:23], v[132:135]\n"
"ds_read_b128 v[36:39], v3 offset:25600\n"
"v_fma_f32 v18, v18, v12, v12\n"
"v_cvt_pk_bf16_f32 v25, v82, v83\n"
"v_mul_f32 v84, v84, v20\n"
"v_mfma_f32_16x16x32_bf16 v[148:151], v[52:55], a[76:79], v[148:151]\n"
"v_exp_f32 v19, v19\n"
"v_mfma_f32_16x16x32_bf16 v[164:167], v[52:55], a[132:135], v[164:167]\n"
"v_fma_f32 v20, -v87, v87, v4\n"
"v_mul_f32 v20, v20, v87\n"
"v_rcp_f32 v18, v18\n"
"v_mfma_f32_16x16x32_bf16 v[180:183], v[52:55], a[188:191], v[180:183]\n"
"v_fma_f32 v19, v19, v12, v12\n"
"v_mul_f32 v85, v85, v18\n"
"v_mfma_f32_16x16x32_bf16 v[136:139], v[56:59], a[20:23], v[136:139]\n"
"ds_read_b128 v[40:43], v3 offset:26624\n"
"v_exp_f32 v20, v20\n"
"v_mfma_f32_16x16x32_bf16 v[152:155], v[56:59], a[76:79], v[152:155]\n"
"v_fma_f32 v18, -v88, v88, v4\n"
"v_mul_f32 v18, v18, v88\n"
"v_rcp_f32 v19, v19\n"
"v_mfma_f32_16x16x32_bf16 v[168:171], v[56:59], a[132:135], v[168:171]\n"
"v_fma_f32 v20, v20, v12, v12\n"
"v_cvt_pk_bf16_f32 v26, v84, v85\n"
"v_mfma_f32_16x16x32_bf16 v[184:187], v[56:59], a[188:191], v[184:187]\n"
"v_mul_f32 v86, v86, v19\n"
"v_exp_f32 v18, v18\n"
"v_mfma_f32_16x16x32_bf16 v[140:143], v[60:63], a[20:23], v[140:143]\n"
"ds_read_b128 v[44:47], v3 offset:27648\n"
"v_fma_f32 v19, -v89, v89, v4\n"
"v_mul_f32 v19, v19, v89\n"
"v_mfma_f32_16x16x32_bf16 v[156:159], v[60:63], a[76:79], v[156:159]\n"
"v_rcp_f32 v20, v20\n"
"v_mfma_f32_16x16x32_bf16 v[172:175], v[60:63], a[132:135], v[172:175]\n"
"v_fma_f32 v18, v18, v12, v12\n"
"v_mul_f32 v87, v87, v20\n"
"v_exp_f32 v19, v19\n"
"v_mfma_f32_16x16x32_bf16 v[188:191], v[60:63], a[188:191], v[188:191]\n"
"v_fma_f32 v20, -v90, v90, v4\n"
"v_mul_f32 v20, v20, v90\n"
"v_mfma_f32_16x16x32_bf16 v[128:131], v[32:35], a[24:27], v[128:131]\n"
"ds_read_b128 v[48:51], v3 offset:28672\n"
"v_rcp_f32 v18, v18\n"
"v_mfma_f32_16x16x32_bf16 v[144:147], v[32:35], a[80:83], v[144:147]\n"
"v_fma_f32 v19, v19, v12, v12\n"
"v_cvt_pk_bf16_f32 v27, v86, v87\n"
"buffer_store_dwordx4 v[24:27], v1, s[40:43], s55 offen offset:0 sc1\n"
"v_mul_f32 v88, v88, v18\n"
"v_mfma_f32_16x16x32_bf16 v[160:163], v[32:35], a[136:139], v[160:163]\n"
"v_exp_f32 v20, v20\n"
"v_mfma_f32_16x16x32_bf16 v[176:179], v[32:35], a[192:195], v[176:179]\n"
"v_fma_f32 v18, -v91, v91, v4\n"
"v_mul_f32 v18, v18, v91\n"
"v_rcp_f32 v19, v19\n"
"s_waitcnt lgkmcnt(0)\n"
"v_mfma_f32_16x16x32_bf16 v[132:135], v[36:39], a[24:27], v[132:135]\n"
"ds_read_b128 v[52:55], v3 offset:29696\n"
"v_fma_f32 v20, v20, v12, v12\n"
"v_mfma_f32_16x16x32_bf16 v[148:151], v[36:39], a[80:83], v[148:151]\n"
"v_mul_f32 v89, v89, v19\n"
"v_exp_f32 v18, v18\n"
"v_mfma_f32_16x16x32_bf16 v[164:167], v[36:39], a[136:139], v[164:167]\n"
"v_fma_f32 v19, -v92, v92, v4\n"
"v_mul_f32 v19, v19, v92\n"
"v_mfma_f32_16x16x32_bf16 v[180:183], v[36:39], a[192:195], v[180:183]\n"
"v_rcp_f32 v20, v20\n"
"v_mfma_f32_16x16x32_bf16 v[136:139], v[40:43], a[24:27], v[136:139]\n"
"ds_read_b128 v[56:59], v3 offset:30720\n"
"v_fma_f32 v18, v18, v12, v12\n"
"v_cvt_pk_bf16_f32 v28, v88, v89\n"
"v_mul_f32 v90, v90, v20\n"
"v_mfma_f32_16x16x32_bf16 v[152:155], v[40:43], a[80:83], v[152:155]\n"
"v_exp_f32 v19, v19\n"
"v_mfma_f32_16x16x32_bf16 v[168:171], v[40:43], a[136:139], v[168:171]\n"
"v_fma_f32 v20, -v93, v93, v4\n"
"v_mul_f32 v20, v20, v93\n"
"v_rcp_f32 v18, v18\n"
"v_mfma_f32_16x16x32_bf16 v[184:187], v[40:43], a[192:195], v[184:187]\n"
"v_fma_f32 v19, v19, v12, v12\n"
"v_mul_f32 v91, v91, v18\n"
"v_exp_f32 v20, v20\n"
"v_mfma_f32_16x16x32_bf16 v[140:143], v[44:47], a[24:27], v[140:143]\n"
"ds_read_b128 v[60:63], v3 offset:31744\n"
"v_fma_f32 v18, -v94, v94, v4\n"
"v_mfma_f32_16x16x32_bf16 v[156:159], v[44:47], a[80:83], v[156:159]\n"
"v_mul_f32 v18, v18, v94\n"
"v_rcp_f32 v19, v19\n"
"v_mfma_f32_16x16x32_bf16 v[172:175], v[44:47], a[136:139], v[172:175]\n"
"v_fma_f32 v20, v20, v12, v12\n"
"v_cvt_pk_bf16_f32 v29, v90, v91\n"
"v_mul_f32 v92, v92, v19\n"
"v_mfma_f32_16x16x32_bf16 v[188:191], v[44:47], a[192:195], v[188:191]\n"
"v_exp_f32 v18, v18\n"
"v_mfma_f32_16x16x32_bf16 v[128:131], v[48:51], a[28:31], v[128:131]\n"
"ds_read_b128 v[32:35], v3 offset:32768\n"
"v_fma_f32 v19, -v95, v95, v4\n"
"v_mul_f32 v19, v19, v95\n"
"v_mfma_f32_16x16x32_bf16 v[144:147], v[48:51], a[84:87], v[144:147]\n"
"v_rcp_f32 v20, v20\n"
"v_mfma_f32_16x16x32_bf16 v[160:163], v[48:51], a[140:143], v[160:163]\n"
"v_fma_f32 v18, v18, v12, v12\n"
"v_mul_f32 v93, v93, v20\n"
"v_exp_f32 v19, v19\n"
"v_mfma_f32_16x16x32_bf16 v[176:179], v[48:51], a[196:199], v[176:179]\n"
"v_fma_f32 v20, -v96, v96, v4\n"
"v_mul_f32 v20, v20, v96\n"
"v_rcp_f32 v18, v18\n"
"s_waitcnt lgkmcnt(0)\n"
"v_mfma_f32_16x16x32_bf16 v[132:135], v[52:55], a[28:31], v[132:135]\n"
"ds_read_b128 v[36:39], v3 offset:33792\n"
"v_fma_f32 v19, v19, v12, v12\n"
"v_mfma_f32_16x16x32_bf16 v[148:151], v[52:55], a[84:87], v[148:151]\n"
"v_cvt_pk_bf16_f32 v30, v92, v93\n"
"v_mul_f32 v94, v94, v18\n"
"v_exp_f32 v20, v20\n"
"v_mfma_f32_16x16x32_bf16 v[164:167], v[52:55], a[140:143], v[164:167]\n"
"v_fma_f32 v18, -v97, v97, v4\n"
"v_mul_f32 v18, v18, v97\n"
"v_mfma_f32_16x16x32_bf16 v[180:183], v[52:55], a[196:199], v[180:183]\n"
"v_rcp_f32 v19, v19\n"
"v_mfma_f32_16x16x32_bf16 v[136:139], v[56:59], a[28:31], v[136:139]\n"
"ds_read_b128 v[40:43], v3 offset:34816\n"
"v_fma_f32 v20, v20, v14, v14\n"
"v_mul_f32 v95, v95, v19\n"
"v_mfma_f32_16x16x32_bf16 v[152:155], v[56:59], a[84:87], v[152:155]\n"
"v_exp_f32 v18, v18\n"
"v_mfma_f32_16x16x32_bf16 v[168:171], v[56:59], a[140:143], v[168:171]\n"
"v_fma_f32 v19, -v98, v98, v4\n"
"v_mul_f32 v19, v19, v98\n"
"v_rcp_f32 v20, v20\n"
"v_mfma_f32_16x16x32_bf16 v[184:187], v[56:59], a[196:199], v[184:187]\n"
"v_fma_f32 v18, v18, v14, v14\n"
"v_cvt_pk_bf16_f32 v31, v94, v95\n"
"buffer_store_dwordx4 v[28:31], v1, s[40:43], s55 offen offset:1024 sc1\n"
"s_add_u32 s55, s55, 2048\n"
"v_mfma_f32_16x16x32_bf16 v[140:143], v[60:63], a[28:31], v[140:143]\n"
"ds_read_b128 v[44:47], v3 offset:35840\n"
"v_mul_f32 v96, v96, v20\n"
"v_mfma_f32_16x16x32_bf16 v[156:159], v[60:63], a[84:87], v[156:159]\n"
"v_exp_f32 v19, v19\n"
"v_mfma_f32_16x16x32_bf16 v[172:175], v[60:63], a[140:143], v[172:175]\n"
"v_fma_f32 v20, -v99, v99, v4\n"
"v_mul_f32 v20, v20, v99\n"
"v_rcp_f32 v18, v18\n"
"v_mfma_f32_16x16x32_bf16 v[188:191], v[60:63], a[196:199], v[188:191]\n"
"v_fma_f32 v19, v19, v14, v14\n"
"v_mul_f32 v97, v97, v18\n"
"v_exp_f32 v20, v20\n"
"v_mfma_f32_16x16x32_bf16 v[128:131], v[32:35], a[32:35], v[128:131]\n"
"ds_read_b128 v[48:51], v3 offset:36864\n"
"v_fma_f32 v18, -v100, v100, v4\n"
"v_mfma_f32_16x16x32_bf16 v[144:147], v[32:35], a[88:91], v[144:147]\n"
"v_mul_f32 v18, v18, v100\n"
"v_rcp_f32 v19, v19\n"
"v_mfma_f32_16x16x32_bf16 v[160:163], v[32:35], a[144:147], v[160:163]\n"
"v_fma_f32 v20, v20, v14, v14\n"
"v_cvt_pk_bf16_f32 v24, v96, v97\n"
"v_mul_f32 v98, v98, v19\n"
"v_mfma_f32_16x16x32_bf16 v[176:179], v[32:35], a[200:203], v[176:179]\n"
"v_exp_f32 v18, v18\n"
"s_waitcnt lgkmcnt(0)\n"
"v_mfma_f32_16x16x32_bf16 v[132:135], v[36:39], a[32:35], v[132:135]\n"
"ds_read_b128 v[52:55], v3 offset:37888\n"
"v_fma_f32 v19, -v101, v101, v4\n"
"v_mul_f32 v19, v19, v101\n"
"v_mfma_f32_16x16x32_bf16 v[148:151], v[36:39], a[88:91], v[148:151]\n"
"v_rcp_f32 v20, v20\n"
"v_mfma_f32_16x16x32_bf16 v[164:167], v[36:39], a[144:147], v[164:167]\n"
"v_fma_f32 v18, v18, v14, v14\n"
"v_mul_f32 v99, v99, v20\n"
"v_exp_f32 v19, v19\n"
"v_mfma_f32_16x16x32_bf16 v[180:183], v[36:39], a[200:203], v[180:183]\n"
"v_fma_f32 v20, -v102, v102, v4\n"
"v_mul_f32 v20, v20, v102\n"
"v_rcp_f32 v18, v18\n"
"v_mfma_f32_16x16x32_bf16 v[136:139], v[40:43], a[32:35], v[136:139]\n"
"ds_read_b128 v[56:59], v3 offset:38912\n"
"v_fma_f32 v19, v19, v14, v14\n"
"v_mfma_f32_16x16x32_bf16 v[152:155], v[40:43], a[88:91], v[152:155]\n"
"v_cvt_pk_bf16_f32 v25, v98, v99\n"
"v_mul_f32 v100, v100, v18\n"
"v_mfma_f32_16x16x32_bf16 v[168:171], v[40:43], a[144:147], v[168:171]\n"
"v_exp_f32 v20, v20\n"
"v_mfma_f32_16x16x32_bf16 v[184:187], v[40:43], a[200:203], v[184:187]\n"
"v_fma_f32 v18, -v103, v103, v4\n"
"v_mul_f32 v18, v18, v103\n"
"v_rcp_f32 v19, v19\n"
"v_mfma_f32_16x16x32_bf16 v[140:143], v[44:47], a[32:35], v[140:143]\n"
"ds_read_b128 v[60:63], v3 offset:39936\n"
"v_fma_f32 v20, v20, v14, v14\n"
"v_mul_f32 v101, v101, v19\n"
"v_mfma_f32_16x16x32_bf16 v[156:159], v[44:47], a[88:91], v[156:159]\n"
"v_exp_f32 v18, v18\n"
"v_mfma_f32_16x16x32_bf16 v[172:175], v[44:47], a[144:147], v[172:175]\n"
"v_fma_f32 v19, -v104, v104, v4\n"
"v_mul_f32 v19, v19, v104\n"
"v_rcp_f32 v20, v20\n"
"v_mfma_f32_16x16x32_bf16 v[188:191], v[44:47], a[200:203], v[188:191]\n"
"v_fma_f32 v18, v18, v14, v14\n"
"v_cvt_pk_bf16_f32 v26, v100, v101\n"
"v_mul_f32 v102, v102, v20\n"
"v_mfma_f32_16x16x32_bf16 v[128:131], v[48:51], a[36:39], v[128:131]\n"
"ds_read_b128 v[32:35], v3 offset:40960\n"
"v_exp_f32 v19, v19\n"
"v_mfma_f32_16x16x32_bf16 v[144:147], v[48:51], a[92:95], v[144:147]\n"
"v_fma_f32 v20, -v105, v105, v4\n"
"v_mul_f32 v20, v20, v105\n"
"v_mfma_f32_16x16x32_bf16 v[160:163], v[48:51], a[148:151], v[160:163]\n"
"v_rcp_f32 v18, v18\n"
"v_mfma_f32_16x16x32_bf16 v[176:179], v[48:51], a[204:207], v[176:179]\n"
"v_fma_f32 v19, v19, v14, v14\n"
"v_mul_f32 v103, v103, v18\n"
"v_exp_f32 v20, v20\n"
"s_waitcnt lgkmcnt(0)\n"
"v_mfma_f32_16x16x32_bf16 v[132:135], v[52:55], a[36:39], v[132:135]\n"
"ds_read_b128 v[36:39], v3 offset:41984\n"
"v_fma_f32 v18, -v106, v106, v4\n"
"v_mul_f32 v18, v18, v106\n"
"v_mfma_f32_16x16x32_bf16 v[148:151], v[52:55], a[92:95], v[148:151]\n"
"v_rcp_f32 v19, v19\n"
"v_mfma_f32_16x16x32_bf16 v[164:167], v[52:55], a[148:151], v[164:167]\n"
"v_fma_f32 v20, v20, v14, v14\n"
"v_cvt_pk_bf16_f32 v27, v102, v103\n"
"buffer_store_dwordx4 v[24:27], v1, s[40:43], s56 offen offset:0 sc1\n"
"v_mul_f32 v104, v104, v19\n"
"v_mfma_f32_16x16x32_bf16 v[180:183], v[52:55], a[204:207], v[180:183]\n"
"v_exp_f32 v18, v18\n"
"v_mfma_f32_16x16x32_bf16 v[136:139], v[56:59], a[36:39], v[136:139]\n"
"ds_read_b128 v[40:43], v3 offset:43008\n"
"v_fma_f32 v19, -v107, v107, v4\n"
"v_mul_f32 v19, v19, v107\n"
"v_rcp_f32 v20, v20\n"
"v_mfma_f32_16x16x32_bf16 v[152:155], v[56:59], a[92:95], v[152:155]\n"
"v_fma_f32 v18, v18, v14, v14\n"
"v_mul_f32 v105, v105, v20\n"
"v_mfma_f32_16x16x32_bf16 v[168:171], v[56:59], a[148:151], v[168:171]\n"
"v_exp_f32 v19, v19\n"
"v_mfma_f32_16x16x32_bf16 v[184:187], v[56:59], a[204:207], v[184:187]\n"
"v_fma_f32 v20, -v108, v108, v4\n"
"v_mul_f32 v20, v20, v108\n"
"v_rcp_f32 v18, v18\n"
"v_mfma_f32_16x16x32_bf16 v[140:143], v[60:63], a[36:39], v[140:143]\n"
"ds_read_b128 v[44:47], v3 offset:44032\n"
"v_fma_f32 v19, v19, v14, v14\n"
"v_cvt_pk_bf16_f32 v28, v104, v105\n"
"v_mfma_f32_16x16x32_bf16 v[156:159], v[60:63], a[92:95], v[156:159]\n"
"v_mul_f32 v106, v106, v18\n"
"v_exp_f32 v20, v20\n"
"v_mfma_f32_16x16x32_bf16 v[172:175], v[60:63], a[148:151], v[172:175]\n"
"v_fma_f32 v18, -v109, v109, v4\n"
"v_mul_f32 v18, v18, v109\n"
"v_mfma_f32_16x16x32_bf16 v[188:191], v[60:63], a[204:207], v[188:191]\n"
"v_rcp_f32 v19, v19\n"
"v_mfma_f32_16x16x32_bf16 v[128:131], v[32:35], a[40:43], v[128:131]\n"
"ds_read_b128 v[48:51], v3 offset:45056\n"
"v_fma_f32 v20, v20, v14, v14\n"
"v_mul_f32 v107, v107, v19\n"
"v_exp_f32 v18, v18\n"
"v_mfma_f32_16x16x32_bf16 v[144:147], v[32:35], a[96:99], v[144:147]\n"
"v_fma_f32 v19, -v110, v110, v4\n"
"v_mul_f32 v19, v19, v110\n"
"v_mfma_f32_16x16x32_bf16 v[160:163], v[32:35], a[152:155], v[160:163]\n"
"v_rcp_f32 v20, v20\n"
"v_mfma_f32_16x16x32_bf16 v[176:179], v[32:35], a[208:211], v[176:179]\n"
"v_fma_f32 v18, v18, v14, v14\n"
"v_cvt_pk_bf16_f32 v29, v106, v107\n"
"v_mul_f32 v108, v108, v20\n"
"s_waitcnt lgkmcnt(0)\n"
"v_mfma_f32_16x16x32_bf16 v[132:135], v[36:39], a[40:43], v[132:135]\n"
"ds_read_b128 v[52:55], v3 offset:46080\n"
"v_exp_f32 v19, v19\n"
"v_mfma_f32_16x16x32_bf16 v[148:151], v[36:39], a[96:99], v[148:151]\n"
"v_fma_f32 v20, -v111, v111, v4\n"
"v_mul_f32 v20, v20, v111\n"
"v_rcp_f32 v18, v18\n"
"v_mfma_f32_16x16x32_bf16 v[164:167], v[36:39], a[152:155], v[164:167]\n"
"v_fma_f32 v19, v19, v14, v14\n"
"v_mul_f32 v109, v109, v18\n"
"v_mfma_f32_16x16x32_bf16 v[180:183], v[36:39], a[208:211], v[180:183]\n"
"v_exp_f32 v20, v20\n"
"v_mfma_f32_16x16x32_bf16 v[136:139], v[40:43], a[40:43], v[136:139]\n"
"ds_read_b128 v[56:59], v3 offset:47104\n"
"v_fma_f32 v18, -v112, v112, v4\n"
"v_mul_f32 v18, v18, v112\n"
"v_rcp_f32 v19, v19\n"
"v_mfma_f32_16x16x32_bf16 v[152:155], v[40:43], a[96:99], v[152:155]\n"
"v_fma_f32 v20, v20, v14, v14\n"
"v_cvt_pk_bf16_f32 v30, v108, v109\n"
"v_mfma_f32_16x16x32_bf16 v[168:171], v[40:43], a[152:155], v[168:171]\n"
"v_mul_f32 v110, v110, v19\n"
"v_exp_f32 v18, v18\n"
"v_mfma_f32_16x16x32_bf16 v[184:187], v[40:43], a[208:211], v[184:187]\n"
"v_fma_f32 v19, -v113, v113, v4\n"
"v_mul_f32 v19, v19, v113\n"
"v_mfma_f32_16x16x32_bf16 v[140:143], v[44:47], a[40:43], v[140:143]\n"
"ds_read_b128 v[60:63], v3 offset:48128\n"
"v_rcp_f32 v20, v20\n"
"v_mfma_f32_16x16x32_bf16 v[156:159], v[44:47], a[96:99], v[156:159]\n"
"v_fma_f32 v18, v18, v16, v16\n"
"v_mul_f32 v111, v111, v20\n"
"v_exp_f32 v19, v19\n"
"v_mfma_f32_16x16x32_bf16 v[172:175], v[44:47], a[152:155], v[172:175]\n"
"v_fma_f32 v20, -v114, v114, v4\n"
"v_mul_f32 v20, v20, v114\n"
"v_mfma_f32_16x16x32_bf16 v[188:191], v[44:47], a[208:211], v[188:191]\n"
"v_rcp_f32 v18, v18\n"
"v_mfma_f32_16x16x32_bf16 v[128:131], v[48:51], a[44:47], v[128:131]\n"
"ds_read_b128 v[32:35], v3 offset:49152\n"
"v_fma_f32 v19, v19, v16, v16\n"
"v_cvt_pk_bf16_f32 v31, v110, v111\n"
"buffer_store_dwordx4 v[28:31], v1, s[40:43], s56 offen offset:1024 sc1\n"
"v_mfma_f32_16x16x32_bf16 v[144:147], v[48:51], a[100:103], v[144:147]\n"
"s_add_u32 s56, s56, 2048\n"
"v_mul_f32 v112, v112, v18\n"
"v_exp_f32 v20, v20\n"
"v_mfma_f32_16x16x32_bf16 v[160:163], v[48:51], a[156:159], v[160:163]\n"
"v_fma_f32 v18, -v115, v115, v4\n"
"v_mul_f32 v18, v18, v115\n"
"v_mfma_f32_16x16x32_bf16 v[176:179], v[48:51], a[212:215], v[176:179]\n"
"v_rcp_f32 v19, v19\n"
"s_waitcnt lgkmcnt(0)\n"
"v_mfma_f32_16x16x32_bf16 v[132:135], v[52:55], a[44:47], v[132:135]\n"
"ds_read_b128 v[36:39], v3 offset:50176\n"
"v_fma_f32 v20, v20, v16, v16\n"
"v_mul_f32 v113, v113, v19\n"
"v_mfma_f32_16x16x32_bf16 v[148:151], v[52:55], a[100:103], v[148:151]\n"
"v_exp_f32 v18, v18\n"
"v_mfma_f32_16x16x32_bf16 v[164:167], v[52:55], a[156:159], v[164:167]\n"
"v_fma_f32 v19, -v116, v116, v4\n"
"v_mul_f32 v19, v19, v116\n"
"v_rcp_f32 v20, v20\n"
"v_mfma_f32_16x16x32_bf16 v[180:183], v[52:55], a[212:215], v[180:183]\n"
"v_fma_f32 v18, v18, v16, v16\n"
"v_cvt_pk_bf16_f32 v24, v112, v113\n"
"v_mul_f32 v114, v114, v20\n"
"v_mfma_f32_16x16x32_bf16 v[136:139], v[56:59], a[44:47], v[136:139]\n"
"ds_read_b128 v[40:43], v3 offset:51200\n"
"v_exp_f32 v19, v19\n"
"v_mfma_f32_16x16x32_bf16 v[152:155], v[56:59], a[100:103], v[152:155]\n"
"v_fma_f32 v20, -v117, v117, v4\n"
"v_mul_f32 v20, v20, v117\n"
"v_rcp_f32 v18, v18\n"
"v_mfma_f32_16x16x32_bf16 v[168:171], v[56:59], a[156:159], v[168:171]\n"
"v_fma_f32 v19, v19, v16, v16\n"
"v_mul_f32 v115, v115, v18\n"
"v_mfma_f32_16x16x32_bf16 v[184:187], v[56:59], a[212:215], v[184:187]\n"
"v_exp_f32 v20, v20\n"
"v_mfma_f32_16x16x32_bf16 v[140:143], v[60:63], a[44:47], v[140:143]\n"
"ds_read_b128 v[44:47], v3 offset:52224\n"
"v_fma_f32 v18, -v118, v118, v4\n"
"v_mul_f32 v18, v18, v118\n"
"v_mfma_f32_16x16x32_bf16 v[156:159], v[60:63], a[100:103], v[156:159]\n"
"v_rcp_f32 v19, v19\n"
"v_mfma_f32_16x16x32_bf16 v[172:175], v[60:63], a[156:159], v[172:175]\n"
"v_fma_f32 v20, v20, v16, v16\n"
"v_cvt_pk_bf16_f32 v25, v114, v115\n"
"v_mul_f32 v116, v116, v19\n"
"v_exp_f32 v18, v18\n"
"v_mfma_f32_16x16x32_bf16 v[188:191], v[60:63], a[212:215], v[188:191]\n"
"v_fma_f32 v19, -v119, v119, v4\n"
"v_mul_f32 v19, v19, v119\n"
"v_mfma_f32_16x16x32_bf16 v[128:131], v[32:35], a[48:51], v[128:131]\n"
"ds_read_b128 v[48:51], v3 offset:53248\n"
"v_rcp_f32 v20, v20\n"
"v_mfma_f32_16x16x32_bf16 v[144:147], v[32:35], a[104:107], v[144:147]\n"
"v_fma_f32 v18, v18, v16, v16\n"
"v_mul_f32 v117, v117, v20\n"
"v_exp_f32 v19, v19\n"
"v_mfma_f32_16x16x32_bf16 v[160:163], v[32:35], a[160:163], v[160:163]\n"
"v_fma_f32 v20, -v120, v120, v4\n"
"v_mul_f32 v20, v20, v120\n"
"v_mfma_f32_16x16x32_bf16 v[176:179], v[32:35], a[216:219], v[176:179]\n"
"v_rcp_f32 v18, v18\n"
"s_waitcnt lgkmcnt(0)\n"
"v_mfma_f32_16x16x32_bf16 v[132:135], v[36:39], a[48:51], v[132:135]\n"
"ds_read_b128 v[52:55], v3 offset:54272\n"
"v_fma_f32 v19, v19, v16, v16\n"
"v_cvt_pk_bf16_f32 v26, v116, v117\n"
"v_mul_f32 v118, v118, v18\n"
"v_mfma_f32_16x16x32_bf16 v[148:151], v[36:39], a[104:107], v[148:151]\n"
"v_exp_f32 v20, v20\n"
"v_mfma_f32_16x16x32_bf16 v[164:167], v[36:39], a[160:163], v[164:167]\n"
"v_fma_f32 v18, -v121, v121, v4\n"
"v_mul_f32 v18, v18, v121\n"
"v_rcp_f32 v19, v19\n"
"v_mfma_f32_16x16x32_bf16 v[180:183], v[36:39], a[216:219], v[180:183]\n"
"v_fma_f32 v20, v20, v16, v16\n"
"v_mul_f32 v119, v119, v19\n"
"v_mfma_f32_16x16x32_bf16 v[136:139], v[40:43], a[48:51], v[136:139]\n"
"ds_read_b128 v[56:59], v3 offset:55296\n"
"v_exp_f32 v18, v18\n"
"v_mfma_f32_16x16x32_bf16 v[152:155], v[40:43], a[104:107], v[152:155]\n"
"v_fma_f32 v19, -v122, v122, v4\n"
"v_mul_f32 v19, v19, v122\n"
"v_rcp_f32 v20, v20\n"
"v_mfma_f32_16x16x32_bf16 v[168:171], v[40:43], a[160:163], v[168:171]\n"
"v_fma_f32 v18, v18, v16, v16\n"
"v_cvt_pk_bf16_f32 v27, v118, v119\n"
"v_mfma_f32_16x16x32_bf16 v[184:187], v[40:43], a[216:219], v[184:187]\n"
"buffer_store_dwordx4 v[24:27], v1, s[40:43], s57 offen offset:0 sc1\n"
"v_mul_f32 v120, v120, v20\n"
"v_exp_f32 v19, v19\n"
"v_mfma_f32_16x16x32_bf16 v[140:143], v[44:47], a[48:51], v[140:143]\n"
"ds_read_b128 v[60:63], v3 offset:56320\n"
"v_fma_f32 v20, -v123, v123, v4\n"
"v_mfma_f32_16x16x32_bf16 v[156:159], v[44:47], a[104:107], v[156:159]\n"
"v_mul_f32 v20, v20, v123\n"
"v_rcp_f32 v18, v18\n"
"v_mfma_f32_16x16x32_bf16 v[172:175], v[44:47], a[160:163], v[172:175]\n"
"v_fma_f32 v19, v19, v16, v16\n"
"v_mul_f32 v121, v121, v18\n"
"v_mfma_f32_16x16x32_bf16 v[188:191], v[44:47], a[216:219], v[188:191]\n"
"v_exp_f32 v20, v20\n"
"v_mfma_f32_16x16x32_bf16 v[128:131], v[48:51], a[52:55], v[128:131]\n"
"v_fma_f32 v18, -v124, v124, v4\n"
"v_mul_f32 v18, v18, v124\n"
"v_rcp_f32 v19, v19\n"
"v_mfma_f32_16x16x32_bf16 v[144:147], v[48:51], a[108:111], v[144:147]\n"
"v_fma_f32 v20, v20, v16, v16\n"
"v_cvt_pk_bf16_f32 v28, v120, v121\n"
"v_mul_f32 v122, v122, v19\n"
"v_mfma_f32_16x16x32_bf16 v[160:163], v[48:51], a[164:167], v[160:163]\n"
"v_exp_f32 v18, v18\n"
"v_mfma_f32_16x16x32_bf16 v[176:179], v[48:51], a[220:223], v[176:179]\n"
"v_fma_f32 v19, -v125, v125, v4\n"
"v_mul_f32 v19, v19, v125\n"
"v_rcp_f32 v20, v20\n"
"s_waitcnt lgkmcnt(0)\n"
"v_mfma_f32_16x16x32_bf16 v[132:135], v[52:55], a[52:55], v[132:135]\n"
"v_fma_f32 v18, v18, v16, v16\n"
"v_mul_f32 v123, v123, v20\n"
"v_exp_f32 v19, v19\n"
"v_mfma_f32_16x16x32_bf16 v[148:151], v[52:55], a[108:111], v[148:151]\n"
"v_fma_f32 v20, -v126, v126, v4\n"
"v_mul_f32 v20, v20, v126\n"
"v_mfma_f32_16x16x32_bf16 v[164:167], v[52:55], a[164:167], v[164:167]\n"
"v_rcp_f32 v18, v18\n"
"v_mfma_f32_16x16x32_bf16 v[180:183], v[52:55], a[220:223], v[180:183]\n"
"v_fma_f32 v19, v19, v16, v16\n"
"v_cvt_pk_bf16_f32 v29, v122, v123\n"
"v_mul_f32 v124, v124, v18\n"
"v_mfma_f32_16x16x32_bf16 v[136:139], v[56:59], a[52:55], v[136:139]\n"
"v_exp_f32 v20, v20\n"
"v_mfma_f32_16x16x32_bf16 v[152:155], v[56:59], a[108:111], v[152:155]\n"
"v_fma_f32 v18, -v127, v127, v4\n"
"v_mul_f32 v18, v18, v127\n"
"v_rcp_f32 v19, v19\n"
"v_mfma_f32_16x16x32_bf16 v[168:171], v[56:59], a[164:167], v[168:171]\n"
"v_fma_f32 v20, v20, v16, v16\n"
"v_mul_f32 v125, v125, v19\n"
"v_exp_f32 v18, v18\n"
"v_mfma_f32_16x16x32_bf16 v[184:187], v[56:59], a[220:223], v[184:187]\n"
"v_rcp_f32 v20, v20\n"
"v_mfma_f32_16x16x32_bf16 v[140:143], v[60:63], a[52:55], v[140:143]\n"
"v_fma_f32 v18, v18, v16, v16\n"
"v_cvt_pk_bf16_f32 v30, v124, v125\n"
"v_mul_f32 v126, v126, v20\n"
"v_mfma_f32_16x16x32_bf16 v[156:159], v[60:63], a[108:111], v[156:159]\n"
"v_rcp_f32 v18, v18\n"
"v_mfma_f32_16x16x32_bf16 v[172:175], v[60:63], a[164:167], v[172:175]\n"
"v_mul_f32 v127, v127, v18\n"
"v_cvt_pk_bf16_f32 v31, v126, v127\n"
"buffer_store_dwordx4 v[28:31], v1, s[40:43], s57 offen offset:1024 sc1\n"
"s_add_u32 s57, s57, 2048\n"
"v_mfma_f32_16x16x32_bf16 v[188:191], v[60:63], a[220:223], v[188:191]\n"
"s_sub_u32 s51, s51, 1\n"
"s_cmp_eq_u32 s51, 0\n"
"s_waitcnt vmcnt(6)\n"
"s_cbranch_scc1 L_b3e%=\n"
"s_cmp_eq_u32 s51, 1\n"
"s_cselect_b32 s38, 0, s38\n"
"s_nop 4\n"
"s_barrier\n"
"ds_read_b128 v[32:35], v2 offset:0\n"
"ds_read_b128 v[36:39], v2 offset:1024\n"
"ds_read_b128 v[40:43], v2 offset:2048\n"
"ds_read_b128 v[44:47], v2 offset:3072\n"
"v_fma_f32 v18, -v128, v128, v4\n"
"v_mul_f32 v18, v18, v128\n"
"v_exp_f32 v18, v18\n"
"v_fma_f32 v19, -v129, v129, v4\n"
"v_mul_f32 v19, v19, v129\n"
"v_fma_f32 v18, v18, v10, v10\n"
"s_waitcnt lgkmcnt(3)\n"
"v_mfma_f32_16x16x32_bf16 v[64:67], v[32:35], a[0:3], 0\n"
"ds_read_b128 v[48:51], v2 offset:4096\n"
"v_exp_f32 v19, v19\n"
"v_mfma_f32_16x16x32_bf16 v[80:83], v[32:35], a[56:59], 0\n"
"s_mov_b32 m0, s53\n"
"v_fma_f32 v20, -v130, v130, v4\n"
"v_mul_f32 v20, v20, v130\n"
"v_mfma_f32_16x16x32_bf16 v[96:99], v[32:35], a[112:115], 0\n"
"buffer_load_dwordx4 v1, s[36:39], s50 offen lds\n"
"s_add_u32 s50, s50, 1024\n"
"v_mfma_f32_16x16x32_bf16 v[112:115], v[32:35], a[168:171], 0\n"
"v_rcp_f32 v18, v18\n"
"s_waitcnt lgkmcnt(0)\n"
"v_mfma_f32_16x16x32_bf16 v[68:71], v[36:39], a[0:3], 0\n"
"ds_read_b128 v[52:55], v2 offset:5120\n"
"v_fma_f32 v19, v19, v10, v10\n"
"v_mul_f32 v128, v128, v18\n"
"v_exp_f32 v20, v20\n"
"v_mfma_f32_16x16x32_bf16 v[84:87], v[36:39], a[56:59], 0\n"
"v_fma_f32 v18, -v131, v131, v4\n"
"v_mul_f32 v18, v18, v131\n"
"v_mfma_f32_16x16x32_bf16 v[100:103], v[36:39], a[112:115], 0\n"
"v_rcp_f32 v19, v19\n"
"v_mfma_f32_16x16x32_bf16 v[116:119], v[36:39], a[168:171], 0\n"
"s_add_u32 m0, m0, 1024\n"
"v_fma_f32 v20, v20, v10, v10\n"
"v_mul_f32 v129, v129, v19\n"
"v_exp_f32 v18, v18\n"
"v_mfma_f32_16x16x32_bf16 v[72:75], v[40:43], a[0:3], 0\n"
"ds_read_b128 v[56:59], v2 offset:6144\n"
"buffer_load_dwordx4 v1, s[36:39], s50 offen lds\n"
"s_add_u32 s50, s50, 1024\n"
"v_mfma_f32_16x16x32_bf16 v[88:91], v[40:43], a[56:59], 0\n"
"v_fma_f32 v19, -v132, v132, v4\n"
"v_mul_f32 v19, v19, v132\n"
"v_mfma_f32_16x16x32_bf16 v[104:107], v[40:43], a[112:115], 0\n"
"v_rcp_f32 v20, v20\n"
"v_mfma_f32_16x16x32_bf16 v[120:123], v[40:43], a[168:171], 0\n"
"v_fma_f32 v18, v18, v10, v10\n"
"v_cvt_pk_bf16_f32 v24, v128, v129\n"
"v_mul_f32 v130, v130, v20\n"
"v_mfma_f32_16x16x32_bf16 v[76:79], v[44:47], a[0:3], 0\n"
"ds_read_b128 v[60:63], v2 offset:7168\n"
"v_exp_f32 v19, v19\n"
"v_mfma_f32_16x16x32_bf16 v[92:95], v[44:47], a[56:59], 0\n"
"s_add_u32 m0, m0, 1024\n"
"v_fma_f32 v20, -v133, v133, v4\n"
"v_mul_f32 v20, v20, v133\n"
"v_mfma_f32_16x16x32_bf16 v[108:111], v[44:47], a[112:115], 0\n"
"buffer_load_dwordx4 v1, s[36:39], s50 offen lds\n"
"s_add_u32 s50, s50, 1024\n"
"v_rcp_f32 v18, v18\n"
"v_mfma_f32_16x16x32_bf16 v[124:127], v[44:47], a[168:171], 0\n"
"v_fma_f32 v19, v19, v10, v10\n"
"v_mul_f32 v131, v131, v18\n"
"v_mfma_f32_16x16x32_bf16 v[64:67], v[48:51], a[4:7], v[64:67]\n"
"ds_read_b128 v[32:35], v2 offset:8192\n"
"v_exp_f32 v20, v20\n"
"v_mfma_f32_16x16x32_bf16 v[80:83], v[48:51], a[60:63], v[80:83]\n"
"v_fma_f32 v18, -v134, v134, v4\n"
"v_mul_f32 v18, v18, v134\n"
"v_mfma_f32_16x16x32_bf16 v[96:99], v[48:51], a[116:119], v[96:99]\n"
"v_rcp_f32 v19, v19\n"
"v_mfma_f32_16x16x32_bf16 v[112:115], v[48:51], a[172:175], v[112:115]\n"
"s_add_u32 m0, m0, 1024\n"
"v_fma_f32 v20, v20, v10, v10\n"
"v_cvt_pk_bf16_f32 v25, v130, v131\n"
"v_mul_f32 v132, v132, v19\n"
"s_waitcnt lgkmcnt(0)\n"
"v_mfma_f32_16x16x32_bf16 v[68:71], v[52:55], a[4:7], v[68:71]\n"
"ds_read_b128 v[36:39], v2 offset:9216\n"
"buffer_load_dwordx4 v1, s[36:39], s50 offen lds\n"
"s_add_u32 s50, s50, 1024\n"
"v_mfma_f32_16x16x32_bf16 v[84:87], v[52:55], a[60:63], v[84:87]\n"
"v_exp_f32 v18, v18\n"
"v_mfma_f32_16x16x32_bf16 v[100:103], v[52:55], a[116:119], v[100:103]\n"
"v_fma_f32 v19, -v135, v135, v4\n"
"v_mul_f32 v19, v19, v135\n"
"v_rcp_f32 v20, v20\n"
"v_mfma_f32_16x16x32_bf16 v[116:119], v[52:55], a[172:175], v[116:119]\n"
"v_fma_f32 v18, v18, v10, v10\n"
"v_mul_f32 v133, v133, v20\n"
"v_exp_f32 v19, v19\n"
"v_mfma_f32_16x16x32_bf16 v[72:75], v[56:59], a[4:7], v[72:75]\n"
"ds_read_b128 v[40:43], v2 offset:10240\n"
"v_fma_f32 v20, -v136, v136, v4\n"
"v_mfma_f32_16x16x32_bf16 v[88:91], v[56:59], a[60:63], v[88:91]\n"
"s_add_u32 m0, m0, 1024\n"
"v_mul_f32 v20, v20, v136\n"
"v_mfma_f32_16x16x32_bf16 v[104:107], v[56:59], a[116:119], v[104:107]\n"
"buffer_load_dwordx4 v1, s[36:39], s50 offen lds\n"
"s_add_u32 s50, s50, 1024\n"
"v_rcp_f32 v18, v18\n"
"v_mfma_f32_16x16x32_bf16 v[120:123], v[56:59], a[172:175], v[120:123]\n"
"v_fma_f32 v19, v19, v10, v10\n"
"v_cvt_pk_bf16_f32 v26, v132, v133\n"
"v_mul_f32 v134, v134, v18\n"
"v_mfma_f32_16x16x32_bf16 v[76:79], v[60:63], a[4:7], v[76:79]\n"
"ds_read_b128 v[44:47], v2 offset:11264\n"
"v_exp_f32 v20, v20\n"
"v_mfma_f32_16x16x32_bf16 v[92:95], v[60:63], a[60:63], v[92:95]\n"
"v_fma_f32 v18, -v137, v137, v4\n"
"v_mul_f32 v18, v18, v137\n"
"v_mfma_f32_16x16x32_bf16 v[108:111], v[60:63], a[116:119], v[108:111]\n"
"v_rcp_f32 v19, v19\n"
"v_mfma_f32_16x16x32_bf16 v[124:127], v[60:63], a[172:175], v[124:127]\n"
"s_add_u32 m0, m0, 1024\n"
"v_fma_f32 v20, v20, v10, v10\n"
"v_mul_f32 v135, v135, v19\n"
"v_exp_f32 v18, v18\n"
"v_mfma_f32_16x16x32_bf16 v[64:67], v[32:35], a[8:11], v[64:67]\n"
"ds_read_b128 v[48:51], v2 offset:12288\n"
"buffer_load_dwordx4 v1, s[36:39], s50 offen lds\n"
"s_add_u32 s50, s50, 1024\n"
"v_mfma_f32_16x16x32_bf16 v[80:83], v[32:35], a[64:67], v[80:83]\n"
"v_fma_f32 v19, -v138, v138, v4\n"
"v_mul_f32 v19, v19, v138\n"
"v_mfma_f32_16x16x32_bf16 v[96:99], v[32:35], a[120:123], v[96:99]\n"
"v_rcp_f32 v20, v20\n"
"v_mfma_f32_16x16x32_bf16 v[112:115], v[32:35], a[176:179], v[112:115]\n"
"v_fma_f32 v18, v18, v10, v10\n"
"v_cvt_pk_bf16_f32 v27, v134, v135\n"
"buffer_store_dwordx4 v[24:27], v1, s[40:43], s54 offen offset:0 sc1\n"
"v_mul_f32 v136, v136, v20\n"
"s_waitcnt lgkmcnt(0)\n"
"v_mfma_f32_16x16x32_bf16 v[68:71], v[36:39], a[8:11], v[68:71]\n"
"ds_read_b128 v[52:55], v2 offset:13312\n"
"v_exp_f32 v19, v19\n"
"v_mfma_f32_16x16x32_bf16 v[84:87], v[36:39], a[64:67], v[84:87]\n"
"s_add_u32 m0, m0, 1024\n"
"v_fma_f32 v20, -v139, v139, v4\n"
"v_mul_f32 v20, v20, v139\n"
"v_mfma_f32_16x16x32_bf16 v[100:103], v[36:39], a[120:123], v[100:103]\n"
"buffer_load_dwordx4 v1, s[36:39], s50 offen lds\n"
"s_add_u32 s50, s50, 1024\n"
"v_mfma_f32_16x16x32_bf16 v[116:119], v[36:39], a[176:179], v[116:119]\n"
"v_rcp_f32 v18, v18\n"
"v_mfma_f32_16x16x32_bf16 v[72:75], v[40:43], a[8:11], v[72:75]\n"
"ds_read_b128 v[56:59], v2 offset:14336\n"
"v_fma_f32 v19, v19, v10, v10\n"
"v_mul_f32 v137, v137, v18\n"
"v_exp_f32 v20, v20\n"
"v_mfma_f32_16x16x32_bf16 v[88:91], v[40:43], a[64:67], v[88:91]\n"
"v_fma_f32 v18, -v140, v140, v4\n"
"v_mul_f32 v18, v18, v140\n"
"v_mfma_f32_16x16x32_bf16 v[104:107], v[40:43], a[120:123], v[104:107]\n"
"v_rcp_f32 v19, v19\n"
"v_mfma_f32_16x16x32_bf16 v[120:123], v[40:43], a[176:179], v[120:123]\n"
"s_add_u32 m0, m0, 1024\n"
"v_fma_f32 v20, v20, v10, v10\n"
"v_cvt_pk_bf16_f32 v28, v136, v137\n"
"v_mul_f32 v138, v138, v19\n"
"v_mfma_f32_16x16x32_bf16 v[76:79], v[44:47], a[8:11], v[76:79]\n"
"ds_read_b128 v[60:63], v2 offset:15360\n"
"buffer_load_dwordx4 v1, s[36:39], s50 offen lds\n"
"s_add_u32 s50, s50, 1024\n"
"v_mfma_f32_16x16x32_bf16 v[92:95], v[44:47], a[64:67], v[92:95]\n"
"v_exp_f32 v18, v18\n"
"v_mfma_f32_16x16x32_bf16 v[108:111], v[44:47], a[120:123], v[108:111]\n"
"v_fma_f32 v19, -v141, v141, v4\n"
"v_mul_f32 v19, v19, v141\n"
"v_rcp_f32 v20, v20\n"
"v_mfma_f32_16x16x32_bf16 v[124:127], v[44:47], a[176:179], v[124:127]\n"
"v_fma_f32 v18, v18, v10, v10\n"
"v_mul_f32 v139, v139, v20\n"
"v_mfma_f32_16x16x32_bf16 v[64:67], v[48:51], a[12:15], v[64:67]\n"
"ds_read_b128 v[32:35], v2 offset:16384\n"
"v_exp_f32 v19, v19\n"
"v_mfma_f32_16x16x32_bf16 v[80:83], v[48:51], a[68:71], v[80:83]\n"
"s_add_u32 m0, m0, 1024\n"
"v_fma_f32 v20, -v142, v142, v4\n"
"v_mul_f32 v20, v20, v142\n"
"v_mfma_f32_16x16x32_bf16 v[96:99], v[48:51], a[124:127], v[96:99]\n"
"buffer_load_dwordx4 v1, s[36:39], s50 offen lds\n"
"s_add_u32 s50, s50, 1024\n"
"v_rcp_f32 v18, v18\n"
"v_mfma_f32_16x16x32_bf16 v[112:115], v[48:51], a[180:183], v[112:115]\n"
"v_fma_f32 v19, v19, v10, v10\n"
"v_cvt_pk_bf16_f32 v29, v138, v139\n"
"s_waitcnt lgkmcnt(0)\n"
"v_mfma_f32_16x16x32_bf16 v[68:71], v[52:55], a[12:15], v[68:71]\n"
"ds_read_b128 v[36:39], v2 offset:17408\n"
"v_mul_f32 v140, v140, v18\n"
"v_mfma_f32_16x16x32_bf16 v[84:87], v[52:55], a[68:71], v[84:87]\n"
"v_exp_f32 v20, v20\n"
"v_mfma_f32_16x16x32_bf16 v[100:103], v[52:55], a[124:127], v[100:103]\n"
"v_fma_f32 v18, -v143, v143, v4\n"
"v_mul_f32 v18, v18, v143\n"
"v_rcp_f32 v19, v19\n"
"v_mfma_f32_16x16x32_bf16 v[116:119], v[52:55], a[180:183], v[116:119]\n"
"s_add_u32 m0, m0, 1024\n"
"v_fma_f32 v20, v20, v10, v10\n"
"v_mul_f32 v141, v141, v19\n"
"v_mfma_f32_16x16x32_bf16 v[72:75], v[56:59], a[12:15], v[72:75]\n"
"ds_read_b128 v[40:43], v2 offset:18432\n"
"buffer_load_dwordx4 v1, s[36:39], s50 offen lds\n"
"s_add_u32 s50, s50, 1024\n"
"v_mfma_f32_16x16x32_bf16 v[88:91], v[56:59], a[68:71], v[88:91]\n"
"v_exp_f32 v18, v18\n"
"v_mfma_f32_16x16x32_bf16 v[104:107], v[56:59], a[124:127], v[104:107]\n"
"v_fma_f32 v19, -v144, v144, v4\n"
"v_mul_f32 v19, v19, v144\n"
"v_rcp_f32 v20, v20\n"
"v_mfma_f32_16x16x32_bf16 v[120:123], v[56:59], a[180:183], v[120:123]\n"
"v_fma_f32 v18, v18, v10, v10\n"
"v_cvt_pk_bf16_f32 v30, v140, v141\n"
"v_mul_f32 v142, v142, v20\n"
"v_mfma_f32_16x16x32_bf16 v[76:79], v[60:63], a[12:15], v[76:79]\n"
"ds_read_b128 v[44:47], v2 offset:19456\n"
"v_exp_f32 v19, v19\n"
"v_mfma_f32_16x16x32_bf16 v[92:95], v[60:63], a[68:71], v[92:95]\n"
"s_add_u32 m0, m0, 1024\n"
"v_fma_f32 v20, -v145, v145, v4\n"
"v_mul_f32 v20, v20, v145\n"
"v_mfma_f32_16x16x32_bf16 v[108:111], v[60:63], a[124:127], v[108:111]\n"
"buffer_load_dwordx4 v1, s[36:39], s50 offen lds\n"
"s_add_u32 s50, s50, 1024\n"
"v_mfma_f32_16x16x32_bf16 v[124:127], v[60:63], a[180:183], v[124:127]\n"
"v_rcp_f32 v18, v18\n"
"v_mfma_f32_16x16x32_bf16 v[64:67], v[32:35], a[16:19], v[64:67]\n"
"ds_read_b128 v[48:51], v2 offset:20480\n"
"v_fma_f32 v19, v19, v12, v12\n"
"v_mul_f32 v143, v143, v18\n"
"v_exp_f32 v20, v20\n"
"v_mfma_f32_16x16x32_bf16 v[80:83], v[32:35], a[72:75], v[80:83]\n"
"v_fma_f32 v18, -v146, v146, v4\n"
"v_mul_f32 v18, v18, v146\n"
"v_mfma_f32_16x16x32_bf16 v[96:99], v[32:35], a[128:131], v[96:99]\n"
"v_rcp_f32 v19, v19\n"
"v_mfma_f32_16x16x32_bf16 v[112:115], v[32:35], a[184:187], v[112:115]\n"
"s_add_u32 m0, m0, 1024\n"
"v_fma_f32 v20, v20, v12, v12\n"
"v_cvt_pk_bf16_f32 v31, v142, v143\n"
"buffer_store_dwordx4 v[28:31], v1, s[40:43], s54 offen offset:1024 sc1\n"
"s_waitcnt lgkmcnt(0)\n"
"v_mfma_f32_16x16x32_bf16 v[68:71], v[36:39], a[16:19], v[68:71]\n"
"ds_read_b128 v[52:55], v2 offset:21504\n"
"buffer_load_dwordx4 v1, s[36:39], s50 offen lds\n"
"s_add_u32 s50, s50, 1024\n"
"v_mfma_f32_16x16x32_bf16 v[84:87], v[36:39], a[72:75], v[84:87]\n"
"s_add_u32 s54, s54, 2048\n"
"v_mul_f32 v144, v144, v19\n"
"v_mfma_f32_16x16x32_bf16 v[100:103], v[36:39], a[128:131], v[100:103]\n"
"v_exp_f32 v18, v18\n"
"v_mfma_f32_16x16x32_bf16 v[116:119], v[36:39], a[184:187], v[116:119]\n"
"v_fma_f32 v19, -v147, v147, v4\n"
"v_mul_f32 v19, v19, v147\n"
"v_rcp_f32 v20, v20\n"
"v_mfma_f32_16x16x32_bf16 v[72:75], v[40:43], a[16:19], v[72:75]\n"
"ds_read_b128 v[56:59], v2 offset:22528\n"
"v_fma_f32 v18, v18, v12, v12\n"
"v_mul_f32 v145, v145, v20\n"
"v_mfma_f32_16x16x32_bf16 v[88:91], v[40:43], a[72:75], v[88:91]\n"
"s_add_u32 m0, m0, 1024\n"
"v_exp_f32 v19, v19\n"
"v_mfma_f32_16x16x32_bf16 v[104:107], v[40:43], a[128:131], v[104:107]\n"
"buffer_load_dwordx4 v1, s[36:39], s50 offen lds\n"
"s_add_u32 s50, s50, 1024\n"
"v_fma_f32 v20, -v148, v148, v4\n"
"v_mfma_f32_16x16x32_bf16 v[120:123], v[40:43], a[184:187], v[120:123]\n"
"v_mul_f32 v20, v20, v148\n"
"v_rcp_f32 v18, v18\n"
"v_mfma_f32_16x16x32_bf16 v[76:79], v[44:47], a[16:19], v[76:79]\n"
"ds_read_b128 v[60:63], v2 offset:23552\n"
"v_fma_f32 v19, v19, v12, v12\n"
"v_cvt_pk_bf16_f32 v24, v144, v145\n"
"v_mfma_f32_16x16x32_bf16 v[92:95], v[44:47], a[72:75], v[92:95]\n"
"v_mul_f32 v146, v146, v18\n"
"v_exp_f32 v20, v20\n"
"v_mfma_f32_16x16x32_bf16 v[108:111], v[44:47], a[128:131], v[108:111]\n"
"v_fma_f32 v18, -v149, v149, v4\n"
"v_mul_f32 v18, v18, v149\n"
"v_mfma_f32_16x16x32_bf16 v[124:127], v[44:47], a[184:187], v[124:127]\n"
"s_add_u32 m0, m0, 1024\n"
"v_rcp_f32 v19, v19\n"
"v_mfma_f32_16x16x32_bf16 v[64:67], v[48:51], a[20:23], v[64:67]\n"
"ds_read_b128 v[32:35], v2 offset:24576\n"
"buffer_load_dwordx4 v1, s[36:39], s50 offen lds\n"
"s_add_u32 s50, s50, 44032\n"
"v_fma_f32 v20, v20, v12, v12\n"
"v_mfma_f32_16x16x32_bf16 v[80:83], v[48:51], a[76:79], v[80:83]\n"
"v_mul_f32 v147, v147, v19\n"
"v_exp_f32 v18, v18\n"
"v_mfma_f32_16x16x32_bf16 v[96:99], v[48:51], a[132:135], v[96:99]\n"
"v_fma_f32 v19, -v150, v150, v4\n"
"v_mul_f32 v19, v19, v150\n"
"v_mfma_f32_16x16x32_bf16 v[112:115], v[48:51], a[188:191], v[112:115]\n"
"v_rcp_f32 v20, v20\n"
"s_waitcnt lgkmcnt(0)\n"
"v_mfma_f32_16x16x32_bf16 v[68:71], v[52:55], a[20:23], v[68:71]\n"
"ds_read_b128 v[36:39], v2 offset:25600\n"
"v_fma_f32 v18, v18, v12, v12\n"
"v_cvt_pk_bf16_f32 v25, v146, v147\n"
"v_mul_f32 v148, v148, v20\n"
"v_mfma_f32_16x16x32_bf16 v[84:87], v[52:55], a[76:79], v[84:87]\n"
"v_exp_f32 v19, v19\n"
"v_mfma_f32_16x16x32_bf16 v[100:103], v[52:55], a[132:135], v[100:103]\n"
"v_fma_f32 v20, -v151, v151, v4\n"
"v_mul_f32 v20, v20, v151\n"
"v_rcp_f32 v18, v18\n"
"v_mfma_f32_16x16x32_bf16 v[116:119], v[52:55], a[188:191], v[116:119]\n"
"v_fma_f32 v19, v19, v12, v12\n"
"v_mul_f32 v149, v149, v18\n"
"v_mfma_f32_16x16x32_bf16 v[72:75], v[56:59], a[20:23], v[72:75]\n"
"ds_read_b128 v[40:43], v2 offset:26624\n"
"v_exp_f32 v20, v20\n"
"v_mfma_f32_16x16x32_bf16 v[88:91], v[56:59], a[76:79], v[88:91]\n"
"v_fma_f32 v18, -v152, v152, v4\n"
"v_mul_f32 v18, v18, v152\n"
"v_rcp_f32 v19, v19\n"
"v_mfma_f32_16x16x32_bf16 v[104:107], v[56:59], a[132:135], v[104:107]\n"
"v_fma_f32 v20, v20, v12, v12\n"
"v_cvt_pk_bf16_f32 v26, v148, v149\n"
"v_mfma_f32_16x16x32_bf16 v[120:123], v[56:59], a[188:191], v[120:123]\n"
"v_mul_f32 v150, v150, v19\n"
"v_exp_f32 v18, v18\n"
"v_mfma_f32_16x16x32_bf16 v[76:79], v[60:63], a[20:23], v[76:79]\n"
"ds_read_b128 v[44:47], v2 offset:27648\n"
"v_fma_f32 v19, -v153, v153, v4\n"
"v_mul_f32 v19, v19, v153\n"
"v_mfma_f32_16x16x32_bf16 v[92:95], v[60:63], a[76:79], v[92:95]\n"
"v_rcp_f32 v20, v20\n"
"v_mfma_f32_16x16x32_bf16 v[108:111], v[60:63], a[132:135], v[108:111]\n"
"v_fma_f32 v18, v18, v12, v12\n"
"v_mul_f32 v151, v151, v20\n"
"v_exp_f32 v19, v19\n"
"v_mfma_f32_16x16x32_bf16 v[124:127], v[60:63], a[188:191], v[124:127]\n"
"v_fma_f32 v20, -v154, v154, v4\n"
"v_mul_f32 v20, v20, v154\n"
"v_mfma_f32_16x16x32_bf16 v[64:67], v[32:35], a[24:27], v[64:67]\n"
"ds_read_b128 v[48:51], v2 offset:28672\n"
"v_rcp_f32 v18, v18\n"
"v_mfma_f32_16x16x32_bf16 v[80:83], v[32:35], a[80:83], v[80:83]\n"
"v_fma_f32 v19, v19, v12, v12\n"
"v_cvt_pk_bf16_f32 v27, v150, v151\n"
"buffer_store_dwordx4 v[24:27], v1, s[40:43], s55 offen offset:0 sc1\n"
"v_mul_f32 v152, v152, v18\n"
"v_mfma_f32_16x16x32_bf16 v[96:99], v[32:35], a[136:139], v[96:99]\n"
"v_exp_f32 v20, v20\n"
"v_mfma_f32_16x16x32_bf16 v[112:115], v[32:35], a[192:195], v[112:115]\n"
"v_fma_f32 v18, -v155, v155, v4\n"
"v_mul_f32 v18, v18, v155\n"
"v_rcp_f32 v19, v19\n"
"s_waitcnt lgkmcnt(0)\n"
"v_mfma_f32_16x16x32_bf16 v[68:71], v[36:39], a[24:27], v[68:71]\n"
"ds_read_b128 v[52:55], v2 offset:29696\n"
"v_fma_f32 v20, v20, v12, v12\n"
"v_mfma_f32_16x16x32_bf16 v[84:87], v[36:39], a[80:83], v[84:87]\n"
"v_mul_f32 v153, v153, v19\n"
"v_exp_f32 v18, v18\n"
"v_mfma_f32_16x16x32_bf16 v[100:103], v[36:39], a[136:139], v[100:103]\n"
"v_fma_f32 v19, -v156, v156, v4\n"
"v_mul_f32 v19, v19, v156\n"
"v_mfma_f32_16x16x32_bf16 v[116:119], v[36:39], a[192:195], v[116:119]\n"
"v_rcp_f32 v20, v20\n"
"v_mfma_f32_16x16x32_bf16 v[72:75], v[40:43], a[24:27], v[72:75]\n"
"ds_read_b128 v[56:59], v2 offset:30720\n"
"v_fma_f32 v18, v18, v12, v12\n"
"v_cvt_pk_bf16_f32 v28, v152, v153\n"
"v_mul_f32 v154, v154, v20\n"
"v_mfma_f32_16x16x32_bf16 v[88:91], v[40:43], a[80:83], v[88:91]\n"
"v_exp_f32 v19, v19\n"
"v_mfma_f32_16x16x32_bf16 v[104:107], v[40:43], a[136:139], v[104:107]\n"
"v_fma_f32 v20, -v157, v157, v4\n"
"v_mul_f32 v20, v20, v157\n"
"v_rcp_f32 v18, v18\n"
"v_mfma_f32_16x16x32_bf16 v[120:123], v[40:43], a[192:195], v[120:123]\n"
"v_fma_f32 v19, v19, v12, v12\n"
"v_mul_f32 v155, v155, v18\n"
"v_exp_f32 v20, v20\n"
"v_mfma_f32_16x16x32_bf16 v[76:79], v[44:47], a[24:27], v[76:79]\n"
"ds_read_b128 v[60:63], v2 offset:31744\n"
"v_fma_f32 v18, -v158, v158, v4\n"
"v_mfma_f32_16x16x32_bf16 v[92:95], v[44:47], a[80:83], v[92:95]\n"
"v_mul_f32 v18, v18, v158\n"
"v_rcp_f32 v19, v19\n"
"v_mfma_f32_16x16x32_bf16 v[108:111], v[44:47], a[136:139], v[108:111]\n"
"v_fma_f32 v20, v20, v12, v12\n"
"v_cvt_pk_bf16_f32 v29, v154, v155\n"
"v_mul_f32 v156, v156, v19\n"
"v_mfma_f32_16x16x32_bf16 v[124:127], v[44:47], a[192:195], v[124:127]\n"
"v_exp_f32 v18, v18\n"
"v_mfma_f32_16x16x32_bf16 v[64:67], v[48:51], a[28:31], v[64:67]\n"
"ds_read_b128 v[32:35], v2 offset:32768\n"
"v_fma_f32 v19, -v159, v159, v4\n"
"v_mul_f32 v19, v19, v159\n"
"v_mfma_f32_16x16x32_bf16 v[80:83], v[48:51], a[84:87], v[80:83]\n"
"v_rcp_f32 v20, v20\n"
"v_mfma_f32_16x16x32_bf16 v[96:99], v[48:51], a[140:143], v[96:99]\n"
"v_fma_f32 v18, v18, v12, v12\n"
"v_mul_f32 v157, v157, v20\n"
"v_exp_f32 v19, v19\n"
"v_mfma_f32_16x16x32_bf16 v[112:115], v[48:51], a[196:199], v[112:115]\n"
"v_fma_f32 v20, -v160, v160, v4\n"
"v_mul_f32 v20, v20, v160\n"
"v_rcp_f32 v18, v18\n"
"s_waitcnt lgkmcnt(0)\n"
"v_mfma_f32_16x16x32_bf16 v[68:71], v[52:55], a[28:31], v[68:71]\n"
"ds_read_b128 v[36:39], v2 offset:33792\n"
"v_fma_f32 v19, v19, v12, v12\n"
"v_mfma_f32_16x16x32_bf16 v[84:87], v[52:55], a[84:87], v[84:87]\n"
"v_cvt_pk_bf16_f32 v30, v156, v157\n"
"v_mul_f32 v158, v158, v18\n"
"v_exp_f32 v20, v20\n"
"v_mfma_f32_16x16x32_bf16 v[100:103], v[52:55], a[140:143], v[100:103]\n"
"v_fma_f32 v18, -v161, v161, v4\n"
"v_mul_f32 v18, v18, v161\n"
"v_mfma_f32_16x16x32_bf16 v[116:119], v[52:55], a[196:199], v[116:119]\n"
"v_rcp_f32 v19, v19\n"
"v_mfma_f32_16x16x32_bf16 v[72:75], v[56:59], a[28:31], v[72:75]\n"
"ds_read_b128 v[40:43], v2 offset:34816\n"
"v_fma_f32 v20, v20, v14, v14\n"
"v_mul_f32 v159, v159, v19\n"
"v_mfma_f32_16x16x32_bf16 v[88:91], v[56:59], a[84:87], v[88:91]\n"
"v_exp_f32 v18, v18\n"
"v_mfma_f32_16x16x32_bf16 v[104:107], v[56:59], a[140:143], v[104:107]\n"
"v_fma_f32 v19, -v162, v162, v4\n"
"v_mul_f32 v19, v19, v162\n"
"v_rcp_f32 v20, v20\n"
"v_mfma_f32_16x16x32_bf16 v[120:123], v[56:59], a[196:199], v[120:123]\n"
"v_fma_f32 v18, v18, v14, v14\n"
"v_cvt_pk_bf16_f32 v31, v158, v159\n"
"buffer_store_dwordx4 v[28:31], v1, s[40:43], s55 offen offset:1024 sc1\n"
"s_add_u32 s55, s55, 2048\n"
"v_mfma_f32_16x16x32_bf16 v[76:79], v[60:63], a[28:31], v[76:79]\n"
"ds_read_b128 v[44:47], v2 offset:35840\n"
"v_mul_f32 v160, v160, v20\n"
"v_mfma_f32_16x16x32_bf16 v[92:95], v[60:63], a[84:87], v[92:95]\n"
"v_exp_f32 v19, v19\n"
"v_mfma_f32_16x16x32_bf16 v[108:111], v[60:63], a[140:143], v[108:111]\n"
"v_fma_f32 v20, -v163, v163, v4\n"
"v_mul_f32 v20, v20, v163\n"
"v_rcp_f32 v18, v18\n"
"v_mfma_f32_16x16x32_bf16 v[124:127], v[60:63], a[196:199], v[124:127]\n"
"v_fma_f32 v19, v19, v14, v14\n"
"v_mul_f32 v161, v161, v18\n"
"v_exp_f32 v20, v20\n"
"v_mfma_f32_16x16x32_bf16 v[64:67], v[32:35], a[32:35], v[64:67]\n"
"ds_read_b128 v[48:51], v2 offset:36864\n"
"v_fma_f32 v18, -v164, v164, v4\n"
"v_mfma_f32_16x16x32_bf16 v[80:83], v[32:35], a[88:91], v[80:83]\n"
"v_mul_f32 v18, v18, v164\n"
"v_rcp_f32 v19, v19\n"
"v_mfma_f32_16x16x32_bf16 v[96:99], v[32:35], a[144:147], v[96:99]\n"
"v_fma_f32 v20, v20, v14, v14\n"
"v_cvt_pk_bf16_f32 v24, v160, v161\n"
"v_mul_f32 v162, v162, v19\n"
"v_mfma_f32_16x16x32_bf16 v[112:115], v[32:35], a[200:203], v[112:115]\n"
"v_exp_f32 v18, v18\n"
"s_waitcnt lgkmcnt(0)\n"
"v_mfma_f32_16x16x32_bf16 v[68:71], v[36:39], a[32:35], v[68:71]\n"
"ds_read_b128 v[52:55], v2 offset:37888\n"
"v_fma_f32 v19, -v165, v165, v4\n"
"v_mul_f32 v19, v19, v165\n"
"v_mfma_f32_16x16x32_bf16 v[84:87], v[36:39], a[88:91], v[84:87]\n"
"v_rcp_f32 v20, v20\n"
"v_mfma_f32_16x16x32_bf16 v[100:103], v[36:39], a[144:147], v[100:103]\n"
"v_fma_f32 v18, v18, v14, v14\n"
"v_mul_f32 v163, v163, v20\n"
"v_exp_f32 v19, v19\n"
"v_mfma_f32_16x16x32_bf16 v[116:119], v[36:39], a[200:203], v[116:119]\n"
"v_fma_f32 v20, -v166, v166, v4\n"
"v_mul_f32 v20, v20, v166\n"
"v_rcp_f32 v18, v18\n"
"v_mfma_f32_16x16x32_bf16 v[72:75], v[40:43], a[32:35], v[72:75]\n"
"ds_read_b128 v[56:59], v2 offset:38912\n"
"v_fma_f32 v19, v19, v14, v14\n"
"v_mfma_f32_16x16x32_bf16 v[88:91], v[40:43], a[88:91], v[88:91]\n"
"v_cvt_pk_bf16_f32 v25, v162, v163\n"
"v_mul_f32 v164, v164, v18\n"
"v_mfma_f32_16x16x32_bf16 v[104:107], v[40:43], a[144:147], v[104:107]\n"
"v_exp_f32 v20, v20\n"
"v_mfma_f32_16x16x32_bf16 v[120:123], v[40:43], a[200:203], v[120:123]\n"
"v_fma_f32 v18, -v167, v167, v4\n"
"v_mul_f32 v18, v18, v167\n"
"v_rcp_f32 v19, v19\n"
"v_mfma_f32_16x16x32_bf16 v[76:79], v[44:47], a[32:35], v[76:79]\n"
"ds_read_b128 v[60:63], v2 offset:39936\n"
"v_fma_f32 v20, v20, v14, v14\n"
"v_mul_f32 v165, v165, v19\n"
"v_mfma_f32_16x16x32_bf16 v[92:95], v[44:47], a[88:91], v[92:95]\n"
"v_exp_f32 v18, v18\n"
"v_mfma_f32_16x16x32_bf16 v[108:111], v[44:47], a[144:147], v[108:111]\n"
"v_fma_f32 v19, -v168, v168, v4\n"
"v_mul_f32 v19, v19, v168\n"
"v_rcp_f32 v20, v20\n"
"v_mfma_f32_16x16x32_bf16 v[124:127], v[44:47], a[200:203], v[124:127]\n"
"v_fma_f32 v18, v18, v14, v14\n"
"v_cvt_pk_bf16_f32 v26, v164, v165\n"
"v_mul_f32 v166, v166, v20\n"
"v_mfma_f32_16x16x32_bf16 v[64:67], v[48:51], a[36:39], v[64:67]\n"
"ds_read_b128 v[32:35], v2 offset:40960\n"
"v_exp_f32 v19, v19\n"
"v_mfma_f32_16x16x32_bf16 v[80:83], v[48:51], a[92:95], v[80:83]\n"
"v_fma_f32 v20, -v169, v169, v4\n"
"v_mul_f32 v20, v20, v169\n"
"v_mfma_f32_16x16x32_bf16 v[96:99], v[48:51], a[148:151], v[96:99]\n"
"v_rcp_f32 v18, v18\n"
"v_mfma_f32_16x16x32_bf16 v[112:115], v[48:51], a[204:207], v[112:115]\n"
"v_fma_f32 v19, v19, v14, v14\n"
"v_mul_f32 v167, v167, v18\n"
"v_exp_f32 v20, v20\n"
"s_waitcnt lgkmcnt(0)\n"
"v_mfma_f32_16x16x32_bf16 v[68:71], v[52:55], a[36:39], v[68:71]\n"
"ds_read_b128 v[36:39], v2 offset:41984\n"
"v_fma_f32 v18, -v170, v170, v4\n"
"v_mul_f32 v18, v18, v170\n"
"v_mfma_f32_16x16x32_bf16 v[84:87], v[52:55], a[92:95], v[84:87]\n"
"v_rcp_f32 v19, v19\n"
"v_mfma_f32_16x16x32_bf16 v[100:103], v[52:55], a[148:151], v[100:103]\n"
"v_fma_f32 v20, v20, v14, v14\n"
"v_cvt_pk_bf16_f32 v27, v166, v167\n"
"buffer_store_dwordx4 v[24:27], v1, s[40:43], s56 offen offset:0 sc1\n"
"v_mul_f32 v168, v168, v19\n"
"v_mfma_f32_16x16x32_bf16 v[116:119], v[52:55], a[204:207], v[116:119]\n"
"v_exp_f32 v18, v18\n"
"v_mfma_f32_16x16x32_bf16 v[72:75], v[56:59], a[36:39], v[72:75]\n"
"ds_read_b128 v[40:43], v2 offset:43008\n"
"v_fma_f32 v19, -v171, v171, v4\n"
"v_mul_f32 v19, v19, v171\n"
"v_rcp_f32 v20, v20\n"
"v_mfma_f32_16x16x32_bf16 v[88:91], v[56:59], a[92:95], v[88:91]\n"
"v_fma_f32 v18, v18, v14, v14\n"
"v_mul_f32 v169, v169, v20\n"
"v_mfma_f32_16x16x32_bf16 v[104:107], v[56:59], a[148:151], v[104:107]\n"
"v_exp_f32 v19, v19\n"
"v_mfma_f32_16x16x32_bf16 v[120:123], v[56:59], a[204:207], v[120:123]\n"
"v_fma_f32 v20, -v172, v172, v4\n"
"v_mul_f32 v20, v20, v172\n"
"v_rcp_f32 v18, v18\n"
"v_mfma_f32_16x16x32_bf16 v[76:79], v[60:63], a[36:39], v[76:79]\n"
"ds_read_b128 v[44:47], v2 offset:44032\n"
"v_fma_f32 v19, v19, v14, v14\n"
"v_cvt_pk_bf16_f32 v28, v168, v169\n"
"v_mfma_f32_16x16x32_bf16 v[92:95], v[60:63], a[92:95], v[92:95]\n"
"v_mul_f32 v170, v170, v18\n"
"v_exp_f32 v20, v20\n"
"v_mfma_f32_16x16x32_bf16 v[108:111], v[60:63], a[148:151], v[108:111]\n"
"v_fma_f32 v18, -v173, v173, v4\n"
"v_mul_f32 v18, v18, v173\n"
"v_mfma_f32_16x16x32_bf16 v[124:127], v[60:63], a[204:207], v[124:127]\n"
"v_rcp_f32 v19, v19\n"
"v_mfma_f32_16x16x32_bf16 v[64:67], v[32:35], a[40:43], v[64:67]\n"
"ds_read_b128 v[48:51], v2 offset:45056\n"
"v_fma_f32 v20, v20, v14, v14\n"
"v_mul_f32 v171, v171, v19\n"
"v_exp_f32 v18, v18\n"
"v_mfma_f32_16x16x32_bf16 v[80:83], v[32:35], a[96:99], v[80:83]\n"
"v_fma_f32 v19, -v174, v174, v4\n"
"v_mul_f32 v19, v19, v174\n"
"v_mfma_f32_16x16x32_bf16 v[96:99], v[32:35], a[152:155], v[96:99]\n"
"v_rcp_f32 v20, v20\n"
"v_mfma_f32_16x16x32_bf16 v[112:115], v[32:35], a[208:211], v[112:115]\n"
"v_fma_f32 v18, v18, v14, v14\n"
"v_cvt_pk_bf16_f32 v29, v170, v171\n"
"v_mul_f32 v172, v172, v20\n"
"s_waitcnt lgkmcnt(0)\n"
"v_mfma_f32_16x16x32_bf16 v[68:71], v[36:39], a[40:43], v[68:71]\n"
"ds_read_b128 v[52:55], v2 offset:46080\n"
"v_exp_f32 v19, v19\n"
"v_mfma_f32_16x16x32_bf16 v[84:87], v[36:39], a[96:99], v[84:87]\n"
"v_fma_f32 v20, -v175, v175, v4\n"
"v_mul_f32 v20, v20, v175\n"
"v_rcp_f32 v18, v18\n"
"v_mfma_f32_16x16x32_bf16 v[100:103], v[36:39], a[152:155], v[100:103]\n"
"v_fma_f32 v19, v19, v14, v14\n"
"v_mul_f32 v173, v173, v18\n"
"v_mfma_f32_16x16x32_bf16 v[116:119], v[36:39], a[208:211], v[116:119]\n"
"v_exp_f32 v20, v20\n"
"v_mfma_f32_16x16x32_bf16 v[72:75], v[40:43], a[40:43], v[72:75]\n"
"ds_read_b128 v[56:59], v2 offset:47104\n"
"v_fma_f32 v18, -v176, v176, v4\n"
"v_mul_f32 v18, v18, v176\n"
"v_rcp_f32 v19, v19\n"
"v_mfma_f32_16x16x32_bf16 v[88:91], v[40:43], a[96:99], v[88:91]\n"
"v_fma_f32 v20, v20, v14, v14\n"
"v_cvt_pk_bf16_f32 v30, v172, v173\n"
"v_mfma_f32_16x16x32_bf16 v[104:107], v[40:43], a[152:155], v[104:107]\n"
"v_mul_f32 v174, v174, v19\n"
"v_exp_f32 v18, v18\n"
"v_mfma_f32_16x16x32_bf16 v[120:123], v[40:43], a[208:211], v[120:123]\n"
"v_fma_f32 v19, -v177, v177, v4\n"
"v_mul_f32 v19, v19, v177\n"
"v_mfma_f32_16x16x32_bf16 v[76:79], v[44:47], a[40:43], v[76:79]\n"
"ds_read_b128 v[60:63], v2 offset:48128\n"
"v_rcp_f32 v20, v20\n"
"v_mfma_f32_16x16x32_bf16 v[92:95], v[44:47], a[96:99], v[92:95]\n"
"v_fma_f32 v18, v18, v16, v16\n"
"v_mul_f32 v175, v175, v20\n"
"v_exp_f32 v19, v19\n"
"v_mfma_f32_16x16x32_bf16 v[108:111], v[44:47], a[152:155], v[108:111]\n"
"v_fma_f32 v20, -v178, v178, v4\n"
"v_mul_f32 v20, v20, v178\n"
"v_mfma_f32_16x16x32_bf16 v[124:127], v[44:47], a[208:211], v[124:127]\n"
"v_rcp_f32 v18, v18\n"
"v_mfma_f32_16x16x32_bf16 v[64:67], v[48:51], a[44:47], v[64:67]\n"
"ds_read_b128 v[32:35], v2 offset:49152\n"
"v_fma_f32 v19, v19, v16, v16\n"
"v_cvt_pk_bf16_f32 v31, v174, v175\n"
"buffer_store_dwordx4 v[28:31], v1, s[40:43], s56 offen offset:1024 sc1\n"
"v_mfma_f32_16x16x32_bf16 v[80:83], v[48:51], a[100:103], v[80:83]\n"
"s_add_u32 s56, s56, 2048\n"
"v_mul_f32 v176, v176, v18\n"
"v_exp_f32 v20, v20\n"
"v_mfma_f32_16x16x32_bf16 v[96:99], v[48:51], a[156:159], v[96:99]\n"
"v_fma_f32 v18, -v179, v179, v4\n"
"v_mul_f32 v18, v18, v179\n"
"v_mfma_f32_16x16x32_bf16 v[112:115], v[48:51], a[212:215], v[112:115]\n"
"v_rcp_f32 v19, v19\n"
"s_waitcnt lgkmcnt(0)\n"
"v_mfma_f32_16x16x32_bf16 v[68:71], v[52:55], a[44:47], v[68:71]\n"
"ds_read_b128 v[36:39], v2 offset:50176\n"
"v_fma_f32 v20, v20, v16, v16\n"
"v_mul_f32 v177, v177, v19\n"
"v_mfma_f32_16x16x32_bf16 v[84:87], v[52:55], a[100:103], v[84:87]\n"
"v_exp_f32 v18, v18\n"
"v_mfma_f32_16x16x32_bf16 v[100:103], v[52:55], a[156:159], v[100:103]\n"
"v_fma_f32 v19, -v180, v180, v4\n"
"v_mul_f32 v19, v19, v180\n"
"v_rcp_f32 v20, v20\n"
"v_mfma_f32_16x16x32_bf16 v[116:119], v[52:55], a[212:215], v[116:119]\n"
"v_fma_f32 v18, v18, v16, v16\n"
"v_cvt_pk_bf16_f32 v24, v176, v177\n"
"v_mul_f32 v178, v178, v20\n"
"v_mfma_f32_16x16x32_bf16 v[72:75], v[56:59], a[44:47], v[72:75]\n"
"ds_read_b128 v[40:43], v2 offset:51200\n"
"v_exp_f32 v19, v19\n"
"v_mfma_f32_16x16x32_bf16 v[88:91], v[56:59], a[100:103], v[88:91]\n"
"v_fma_f32 v20, -v181, v181, v4\n"
"v_mul_f32 v20, v20, v181\n"
"v_rcp_f32 v18, v18\n"
"v_mfma_f32_16x16x32_bf16 v[104:107], v[56:59], a[156:159], v[104:107]\n"
"v_fma_f32 v19, v19, v16, v16\n"
"v_mul_f32 v179, v179, v18\n"
"v_mfma_f32_16x16x32_bf16 v[120:123], v[56:59], a[212:215], v[120:123]\n"
"v_exp_f32 v20, v20\n"
"v_mfma_f32_16x16x32_bf16 v[76:79], v[60:63], a[44:47], v[76:79]\n"
"ds_read_b128 v[44:47], v2 offset:52224\n"
"v_fma_f32 v18, -v182, v182, v4\n"
"v_mul_f32 v18, v18, v182\n"
"v_mfma_f32_16x16x32_bf16 v[92:95], v[60:63], a[100:103], v[92:95]\n"
"v_rcp_f32 v19, v19\n"
"v_mfma_f32_16x16x32_bf16 v[108:111], v[60:63], a[156:159], v[108:111]\n"
"v_fma_f32 v20, v20, v16, v16\n"
"v_cvt_pk_bf16_f32 v25, v178, v179\n"
"v_mul_f32 v180, v180, v19\n"
"v_exp_f32 v18, v18\n"
"v_mfma_f32_16x16x32_bf16 v[124:127], v[60:63], a[212:215], v[124:127]\n"
"v_fma_f32 v19, -v183, v183, v4\n"
"v_mul_f32 v19, v19, v183\n"
"v_mfma_f32_16x16x32_bf16 v[64:67], v[32:35], a[48:51], v[64:67]\n"
"ds_read_b128 v[48:51], v2 offset:53248\n"
"v_rcp_f32 v20, v20\n"
"v_mfma_f32_16x16x32_bf16 v[80:83], v[32:35], a[104:107], v[80:83]\n"
"v_fma_f32 v18, v18, v16, v16\n"
"v_mul_f32 v181, v181, v20\n"
"v_exp_f32 v19, v19\n"
"v_mfma_f32_16x16x32_bf16 v[96:99], v[32:35], a[160:163], v[96:99]\n"
"v_fma_f32 v20, -v184, v184, v4\n"
"v_mul_f32 v20, v20, v184\n"
"v_mfma_f32_16x16x32_bf16 v[112:115], v[32:35], a[216:219], v[112:115]\n"
"v_rcp_f32 v18, v18\n"
"s_waitcnt lgkmcnt(0)\n"
"v_mfma_f32_16x16x32_bf16 v[68:71], v[36:39], a[48:51], v[68:71]\n"
"ds_read_b128 v[52:55], v2 offset:54272\n"
"v_fma_f32 v19, v19, v16, v16\n"
"v_cvt_pk_bf16_f32 v26, v180, v181\n"
"v_mul_f32 v182, v182, v18\n"
"v_mfma_f32_16x16x32_bf16 v[84:87], v[36:39], a[104:107], v[84:87]\n"
"v_exp_f32 v20, v20\n"
"v_mfma_f32_16x16x32_bf16 v[100:103], v[36:39], a[160:163], v[100:103]\n"
"v_fma_f32 v18, -v185, v185, v4\n"
"v_mul_f32 v18, v18, v185\n"
"v_rcp_f32 v19, v19\n"
"v_mfma_f32_16x16x32_bf16 v[116:119], v[36:39], a[216:219], v[116:119]\n"
"v_fma_f32 v20, v20, v16, v16\n"
"v_mul_f32 v183, v183, v19\n"
"v_mfma_f32_16x16x32_bf16 v[72:75], v[40:43], a[48:51], v[72:75]\n"
"ds_read_b128 v[56:59], v2 offset:55296\n"
"v_exp_f32 v18, v18\n"
"v_mfma_f32_16x16x32_bf16 v[88:91], v[40:43], a[104:107], v[88:91]\n"
"v_fma_f32 v19, -v186, v186, v4\n"
"v_mul_f32 v19, v19, v186\n"
"v_rcp_f32 v20, v20\n"
"v_mfma_f32_16x16x32_bf16 v[104:107], v[40:43], a[160:163], v[104:107]\n"
"v_fma_f32 v18, v18, v16, v16\n"
"v_cvt_pk_bf16_f32 v27, v182, v183\n"
"v_mfma_f32_16x16x32_bf16 v[120:123], v[40:43], a[216:219], v[120:123]\n"
"buffer_store_dwordx4 v[24:27], v1, s[40:43], s57 offen offset:0 sc1\n"
"v_mul_f32 v184, v184, v20\n"
"v_exp_f32 v19, v19\n"
"v_mfma_f32_16x16x32_bf16 v[76:79], v[44:47], a[48:51], v[76:79]\n"
"ds_read_b128 v[60:63], v2 offset:56320\n"
"v_fma_f32 v20, -v187, v187, v4\n"
"v_mfma_f32_16x16x32_bf16 v[92:95], v[44:47], a[104:107], v[92:95]\n"
"v_mul_f32 v20, v20, v187\n"
"v_rcp_f32 v18, v18\n"
"v_mfma_f32_16x16x32_bf16 v[108:111], v[44:47], a[160:163], v[108:111]\n"
"v_fma_f32 v19, v19, v16, v16\n"
"v_mul_f32 v185, v185, v18\n"
"v_mfma_f32_16x16x32_bf16 v[124:127], v[44:47], a[216:219], v[124:127]\n"
"v_exp_f32 v20, v20\n"
"v_mfma_f32_16x16x32_bf16 v[64:67], v[48:51], a[52:55], v[64:67]\n"
"v_fma_f32 v18, -v188, v188, v4\n"
"v_mul_f32 v18, v18, v188\n"
"v_rcp_f32 v19, v19\n"
"v_mfma_f32_16x16x32_bf16 v[80:83], v[48:51], a[108:111], v[80:83]\n"
"v_fma_f32 v20, v20, v16, v16\n"
"v_cvt_pk_bf16_f32 v28, v184, v185\n"
"v_mul_f32 v186, v186, v19\n"
"v_mfma_f32_16x16x32_bf16 v[96:99], v[48:51], a[164:167], v[96:99]\n"
"v_exp_f32 v18, v18\n"
"v_mfma_f32_16x16x32_bf16 v[112:115], v[48:51], a[220:223], v[112:115]\n"
"v_fma_f32 v19, -v189, v189, v4\n"
"v_mul_f32 v19, v19, v189\n"
"v_rcp_f32 v20, v20\n"
"s_waitcnt lgkmcnt(0)\n"
"v_mfma_f32_16x16x32_bf16 v[68:71], v[52:55], a[52:55], v[68:71]\n"
"v_fma_f32 v18, v18, v16, v16\n"
"v_mul_f32 v187, v187, v20\n"
"v_exp_f32 v19, v19\n"
"v_mfma_f32_16x16x32_bf16 v[84:87], v[52:55], a[108:111], v[84:87]\n"
"v_fma_f32 v20, -v190, v190, v4\n"
"v_mul_f32 v20, v20, v190\n"
"v_mfma_f32_16x16x32_bf16 v[100:103], v[52:55], a[164:167], v[100:103]\n"
"v_rcp_f32 v18, v18\n"
"v_mfma_f32_16x16x32_bf16 v[116:119], v[52:55], a[220:223], v[116:119]\n"
"v_fma_f32 v19, v19, v16, v16\n"
"v_cvt_pk_bf16_f32 v29, v186, v187\n"
"v_mul_f32 v188, v188, v18\n"
"v_mfma_f32_16x16x32_bf16 v[72:75], v[56:59], a[52:55], v[72:75]\n"
"v_exp_f32 v20, v20\n"
"v_mfma_f32_16x16x32_bf16 v[88:91], v[56:59], a[108:111], v[88:91]\n"
"v_fma_f32 v18, -v191, v191, v4\n"
"v_mul_f32 v18, v18, v191\n"
"v_rcp_f32 v19, v19\n"
"v_mfma_f32_16x16x32_bf16 v[104:107], v[56:59], a[164:167], v[104:107]\n"
"v_fma_f32 v20, v20, v16, v16\n"
"v_mul_f32 v189, v189, v19\n"
"v_exp_f32 v18, v18\n"
"v_mfma_f32_16x16x32_bf16 v[120:123], v[56:59], a[220:223], v[120:123]\n"
"v_rcp_f32 v20, v20\n"
"v_mfma_f32_16x16x32_bf16 v[76:79], v[60:63], a[52:55], v[76:79]\n"
"v_fma_f32 v18, v18, v16, v16\n"
"v_cvt_pk_bf16_f32 v30, v188, v189\n"
"v_mul_f32 v190, v190, v20\n"
"v_mfma_f32_16x16x32_bf16 v[92:95], v[60:63], a[108:111], v[92:95]\n"
"v_rcp_f32 v18, v18\n"
"v_mfma_f32_16x16x32_bf16 v[108:111], v[60:63], a[164:167], v[108:111]\n"
"v_mul_f32 v191, v191, v18\n"
"v_cvt_pk_bf16_f32 v31, v190, v191\n"
"buffer_store_dwordx4 v[28:31], v1, s[40:43], s57 offen offset:1024 sc1\n"
"s_add_u32 s57, s57, 2048\n"
"v_mfma_f32_16x16x32_bf16 v[124:127], v[60:63], a[220:223], v[124:127]\n"
"s_sub_u32 s51, s51, 1\n"
"s_cmp_eq_u32 s51, 0\n"
"s_waitcnt vmcnt(6)\n"
"s_branch L_b3%=\n"
"L_b3e%=:\n"
"s_nop 7\n"
"v_fma_f32 v18, -v128, v128, v4\n"
"v_mul_f32 v18, v18, v128\n"
"v_exp_f32 v18, v18\n"
"v_fma_f32 v19, -v129, v129, v4\n"
"v_mul_f32 v19, v19, v129\n"
"v_fma_f32 v18, v18, v10, v10\n"
"v_exp_f32 v19, v19\n"
"v_fma_f32 v20, -v130, v130, v4\n"
"v_mul_f32 v20, v20, v130\n"
"v_rcp_f32 v18, v18\n"
"v_fma_f32 v19, v19, v10, v10\n"
"v_mul_f32 v128, v128, v18\n"
"v_exp_f32 v20, v20\n"
"v_fma_f32 v18, -v131, v131, v4\n"
"v_mul_f32 v18, v18, v131\n"
"v_rcp_f32 v19, v19\n"
"v_fma_f32 v20, v20, v10, v10\n"
"v_mul_f32 v129, v129, v19\n"
"v_exp_f32 v18, v18\n"
"v_fma_f32 v19, -v132, v132, v4\n"
"v_mul_f32 v19, v19, v132\n"
"v_rcp_f32 v20, v20\n"
"v_fma_f32 v18, v18, v10, v10\n"
"v_cvt_pk_bf16_f32 v24, v128, v129\n"
"v_mul_f32 v130, v130, v20\n"
"v_exp_f32 v19, v19\n"
"v_fma_f32 v20, -v133, v133, v4\n"
"v_mul_f32 v20, v20, v133\n"
"v_rcp_f32 v18, v18\n"
"v_fma_f32 v19, v19, v10, v10\n"
"v_mul_f32 v131, v131, v18\n"
"v_exp_f32 v20, v20\n"
"v_fma_f32 v18, -v134, v134, v4\n"
"v_mul_f32 v18, v18, v134\n"
"v_rcp_f32 v19, v19\n"
"v_fma_f32 v20, v20, v10, v10\n"
"v_cvt_pk_bf16_f32 v25, v130, v131\n"
"v_mul_f32 v132, v132, v19\n"
"v_exp_f32 v18, v18\n"
"v_fma_f32 v19, -v135, v135, v4\n"
"v_mul_f32 v19, v19, v135\n"
"v_rcp_f32 v20, v20\n"
"v_fma_f32 v18, v18, v10, v10\n"
"v_mul_f32 v133, v133, v20\n"
"v_exp_f32 v19, v19\n"
"v_fma_f32 v20, -v136, v136, v4\n"
"v_mul_f32 v20, v20, v136\n"
"v_rcp_f32 v18, v18\n"
"v_fma_f32 v19, v19, v10, v10\n"
"v_cvt_pk_bf16_f32 v26, v132, v133\n"
"v_mul_f32 v134, v134, v18\n"
"v_exp_f32 v20, v20\n"
"v_fma_f32 v18, -v137, v137, v4\n"
"v_mul_f32 v18, v18, v137\n"
"v_rcp_f32 v19, v19\n"
"v_fma_f32 v20, v20, v10, v10\n"
"v_mul_f32 v135, v135, v19\n"
"v_exp_f32 v18, v18\n"
"v_fma_f32 v19, -v138, v138, v4\n"
"v_mul_f32 v19, v19, v138\n"
"v_rcp_f32 v20, v20\n"
"v_fma_f32 v18, v18, v10, v10\n"
"v_cvt_pk_bf16_f32 v27, v134, v135\n"
"buffer_store_dwordx4 v[24:27], v1, s[40:43], s54 offen offset:0 sc1\n"
"v_mul_f32 v136, v136, v20\n"
"v_exp_f32 v19, v19\n"
"v_fma_f32 v20, -v139, v139, v4\n"
"v_mul_f32 v20, v20, v139\n"
"v_rcp_f32 v18, v18\n"
"v_fma_f32 v19, v19, v10, v10\n"
"v_mul_f32 v137, v137, v18\n"
"v_exp_f32 v20, v20\n"
"v_fma_f32 v18, -v140, v140, v4\n"
"v_mul_f32 v18, v18, v140\n"
"v_rcp_f32 v19, v19\n"
"v_fma_f32 v20, v20, v10, v10\n"
"v_cvt_pk_bf16_f32 v28, v136, v137\n"
"v_mul_f32 v138, v138, v19\n"
"v_exp_f32 v18, v18\n"
"v_fma_f32 v19, -v141, v141, v4\n"
"v_mul_f32 v19, v19, v141\n"
"v_rcp_f32 v20, v20\n"
"v_fma_f32 v18, v18, v10, v10\n"
"v_mul_f32 v139, v139, v20\n"
"v_exp_f32 v19, v19\n"
"v_fma_f32 v20, -v142, v142, v4\n"
"v_mul_f32 v20, v20, v142\n"
"v_rcp_f32 v18, v18\n"
"v_fma_f32 v19, v19, v10, v10\n"
"v_cvt_pk_bf16_f32 v29, v138, v139\n"
"v_mul_f32 v140, v140, v18\n"
"v_exp_f32 v20, v20\n"
"v_fma_f32 v18, -v143, v143, v4\n"
"v_mul_f32 v18, v18, v143\n"
"v_rcp_f32 v19, v19\n"
"v_fma_f32 v20, v20, v10, v10\n"
"v_mul_f32 v141, v141, v19\n"
"v_exp_f32 v18, v18\n"
"v_fma_f32 v19, -v144, v144, v4\n"
"v_mul_f32 v19, v19, v144\n"
"v_rcp_f32 v20, v20\n"
"v_fma_f32 v18, v18, v10, v10\n"
"v_cvt_pk_bf16_f32 v30, v140, v141\n"
"v_mul_f32 v142, v142, v20\n"
"v_exp_f32 v19, v19\n"
"v_fma_f32 v20, -v145, v145, v4\n"
"v_mul_f32 v20, v20, v145\n"
"v_rcp_f32 v18, v18\n"
"v_fma_f32 v19, v19, v12, v12\n"
"v_mul_f32 v143, v143, v18\n"
"v_exp_f32 v20, v20\n"
"v_fma_f32 v18, -v146, v146, v4\n"
"v_mul_f32 v18, v18, v146\n"
"v_rcp_f32 v19, v19\n"
"v_fma_f32 v20, v20, v12, v12\n"
"v_cvt_pk_bf16_f32 v31, v142, v143\n"
"buffer_store_dwordx4 v[28:31], v1, s[40:43], s54 offen offset:1024 sc1\n"
"s_add_u32 s54, s54, 2048\n"
"v_mul_f32 v144, v144, v19\n"
"v_exp_f32 v18, v18\n"
"v_fma_f32 v19, -v147, v147, v4\n"
"v_mul_f32 v19, v19, v147\n"
"v_rcp_f32 v20, v20\n"
"v_fma_f32 v18, v18, v12, v12\n"
"v_mul_f32 v145, v145, v20\n"
"v_exp_f32 v19, v19\n"
"v_fma_f32 v20, -v148, v148, v4\n"
"v_mul_f32 v20, v20, v148\n"
"v_rcp_f32 v18, v18\n"
"v_fma_f32 v19, v19, v12, v12\n"
"v_cvt_pk_bf16_f32 v24, v144, v145\n"
"v_mul_f32 v146, v146, v18\n"
"v_exp_f32 v20, v20\n"
"v_fma_f32 v18, -v149, v149, v4\n"
"v_mul_f32 v18, v18, v149\n"
"v_rcp_f32 v19, v19\n"
"v_fma_f32 v20, v20, v12, v12\n"
"v_mul_f32 v147, v147, v19\n"
"v_exp_f32 v18, v18\n"
"v_fma_f32 v19, -v150, v150, v4\n"
"v_mul_f32 v19, v19, v150\n"
"v_rcp_f32 v20, v20\n"
"v_fma_f32 v18, v18, v12, v12\n"
"v_cvt_pk_bf16_f32 v25, v146, v147\n"
"v_mul_f32 v148, v148, v20\n"
"v_exp_f32 v19, v19\n"
"v_fma_f32 v20, -v151, v151, v4\n"
"v_mul_f32 v20, v20, v151\n"
"v_rcp_f32 v18, v18\n"
"v_fma_f32 v19, v19, v12, v12\n"
"v_mul_f32 v149, v149, v18\n"
"v_exp_f32 v20, v20\n"
"v_fma_f32 v18, -v152, v152, v4\n"
"v_mul_f32 v18, v18, v152\n"
"v_rcp_f32 v19, v19\n"
"v_fma_f32 v20, v20, v12, v12\n"
"v_cvt_pk_bf16_f32 v26, v148, v149\n"
"v_mul_f32 v150, v150, v19\n"
"v_exp_f32 v18, v18\n"
"v_fma_f32 v19, -v153, v153, v4\n"
"v_mul_f32 v19, v19, v153\n"
"v_rcp_f32 v20, v20\n"
"v_fma_f32 v18, v18, v12, v12\n"
"v_mul_f32 v151, v151, v20\n"
"v_exp_f32 v19, v19\n"
"v_fma_f32 v20, -v154, v154, v4\n"
"v_mul_f32 v20, v20, v154\n"
"v_rcp_f32 v18, v18\n"
"v_fma_f32 v19, v19, v12, v12\n"
"v_cvt_pk_bf16_f32 v27, v150, v151\n"
"buffer_store_dwordx4 v[24:27], v1, s[40:43], s55 offen offset:0 sc1\n"
"v_mul_f32 v152, v152, v18\n"
"v_exp_f32 v20, v20\n"
"v_fma_f32 v18, -v155, v155, v4\n"
"v_mul_f32 v18, v18, v155\n"
"v_rcp_f32 v19, v19\n"
"v_fma_f32 v20, v20, v12, v12\n"
"v_mul_f32 v153, v153, v19\n"
"v_exp_f32 v18, v18\n"
"v_fma_f32 v19, -v156, v156, v4\n"
"v_mul_f32 v19, v19, v156\n"
"v_rcp_f32 v20, v20\n"
"v_fma_f32 v18, v18, v12, v12\n"
"v_cvt_pk_bf16_f32 v28, v152, v153\n"
"v_mul_f32 v154, v154, v20\n"
"v_exp_f32 v19, v19\n"
"v_fma_f32 v20, -v157, v157, v4\n"
"v_mul_f32 v20, v20, v157\n"
"v_rcp_f32 v18, v18\n"
"v_fma_f32 v19, v19, v12, v12\n"
"v_mul_f32 v155, v155, v18\n"
"v_exp_f32 v20, v20\n"
"v_fma_f32 v18, -v158, v158, v4\n"
"v_mul_f32 v18, v18, v158\n"
"v_rcp_f32 v19, v19\n"
"v_fma_f32 v20, v20, v12, v12\n"
"v_cvt_pk_bf16_f32 v29, v154, v155\n"
"v_mul_f32 v156, v156, v19\n"
"v_exp_f32 v18, v18\n"
"v_fma_f32 v19, -v159, v159, v4\n"
"v_mul_f32 v19, v19, v159\n"
"v_rcp_f32 v20, v20\n"
"v_fma_f32 v18, v18, v12, v12\n"
"v_mul_f32 v157, v157, v20\n"
"v_exp_f32 v19, v19\n"
"v_fma_f32 v20, -v160, v160, v4\n"
"v_mul_f32 v20, v20, v160\n"
"v_rcp_f32 v18, v18\n"
"v_fma_f32 v19, v19, v12, v12\n"
"v_cvt_pk_bf16_f32 v30, v156, v157\n"
"v_mul_f32 v158, v158, v18\n"
"v_exp_f32 v20, v20\n"
"v_fma_f32 v18, -v161, v161, v4\n"
"v_mul_f32 v18, v18, v161\n"
"v_rcp_f32 v19, v19\n"
"v_fma_f32 v20, v20, v14, v14\n"
"v_mul_f32 v159, v159, v19\n"
"v_exp_f32 v18, v18\n"
"v_fma_f32 v19, -v162, v162, v4\n"
"v_mul_f32 v19, v19, v162\n"
"v_rcp_f32 v20, v20\n"
"v_fma_f32 v18, v18, v14, v14\n"
"v_cvt_pk_bf16_f32 v31, v158, v159\n"
"buffer_store_dwordx4 v[28:31], v1, s[40:43], s55 offen offset:1024 sc1\n"
"s_add_u32 s55, s55, 2048\n"
"v_mul_f32 v160, v160, v20\n"
"v_exp_f32 v19, v19\n"
"v_fma_f32 v20, -v163, v163, v4\n"
"v_mul_f32 v20, v20, v163\n"
"v_rcp_f32 v18, v18\n"
"v_fma_f32 v19, v19, v14, v14\n"
"v_mul_f32 v161, v161, v18\n"
"v_exp_f32 v20, v20\n"
"v_fma_f32 v18, -v164, v164, v4\n"
"v_mul_f32 v18, v18, v164\n"
"v_rcp_f32 v19, v19\n"
"v_fma_f32 v20, v20, v14, v14\n"
"v_cvt_pk_bf16_f32 v24, v160, v161\n"
"v_mul_f32 v162, v162, v19\n"
"v_exp_f32 v18, v18\n"
"v_fma_f32 v19, -v165, v165, v4\n"
"v_mul_f32 v19, v19, v165\n"
"v_rcp_f32 v20, v20\n"
"v_fma_f32 v18, v18, v14, v14\n"
"v_mul_f32 v163, v163, v20\n"
"v_exp_f32 v19, v19\n"
"v_fma_f32 v20, -v166, v166, v4\n"
"v_mul_f32 v20, v20, v166\n"
"v_rcp_f32 v18, v18\n"
"v_fma_f32 v19, v19, v14, v14\n"
"v_cvt_pk_bf16_f32 v25, v162, v163\n"
"v_mul_f32 v164, v164, v18\n"
"v_exp_f32 v20, v20\n"
"v_fma_f32 v18, -v167, v167, v4\n"
"v_mul_f32 v18, v18, v167\n"
"v_rcp_f32 v19, v19\n"
"v_fma_f32 v20, v20, v14, v14\n"
"v_mul_f32 v165, v165, v19\n"
"v_exp_f32 v18, v18\n"
"v_fma_f32 v19, -v168, v168, v4\n"
"v_mul_f32 v19, v19, v168\n"
"v_rcp_f32 v20, v20\n"
"v_fma_f32 v18, v18, v14, v14\n"
"v_cvt_pk_bf16_f32 v26, v164, v165\n"
"v_mul_f32 v166, v166, v20\n"
"v_exp_f32 v19, v19\n"
"v_fma_f32 v20, -v169, v169, v4\n"
"v_mul_f32 v20, v20, v169\n"
"v_rcp_f32 v18, v18\n"
"v_fma_f32 v19, v19, v14, v14\n"
"v_mul_f32 v167, v167, v18\n"
"v_exp_f32 v20, v20\n"
"v_fma_f32 v18, -v170, v170, v4\n"
"v_mul_f32 v18, v18, v170\n"
"v_rcp_f32 v19, v19\n"
"v_fma_f32 v20, v20, v14, v14\n"
"v_cvt_pk_bf16_f32 v27, v166, v167\n"
"buffer_store_dwordx4 v[24:27], v1, s[40:43], s56 offen offset:0 sc1\n"
"v_mul_f32 v168, v168, v19\n"
"v_exp_f32 v18, v18\n"
"v_fma_f32 v19, -v171, v171, v4\n"
"v_mul_f32 v19, v19, v171\n"
"v_rcp_f32 v20, v20\n"
"v_fma_f32 v18, v18, v14, v14\n"
"v_mul_f32 v169, v169, v20\n"
"v_exp_f32 v19, v19\n"
"v_fma_f32 v20, -v172, v172, v4\n"
"v_mul_f32 v20, v20, v172\n"
"v_rcp_f32 v18, v18\n"
"v_fma_f32 v19, v19, v14, v14\n"
"v_cvt_pk_bf16_f32 v28, v168, v169\n"
"v_mul_f32 v170, v170, v18\n"
"v_exp_f32 v20, v20\n"
"v_fma_f32 v18, -v173, v173, v4\n"
"v_mul_f32 v18, v18, v173\n"
"v_rcp_f32 v19, v19\n"
"v_fma_f32 v20, v20, v14, v14\n"
"v_mul_f32 v171, v171, v19\n"
"v_exp_f32 v18, v18\n"
"v_fma_f32 v19, -v174, v174, v4\n"
"v_mul_f32 v19, v19, v174\n"
"v_rcp_f32 v20, v20\n"
"v_fma_f32 v18, v18, v14, v14\n"
"v_cvt_pk_bf16_f32 v29, v170, v171\n"
"v_mul_f32 v172, v172, v20\n"
"v_exp_f32 v19, v19\n"
"v_fma_f32 v20, -v175, v175, v4\n"
"v_mul_f32 v20, v20, v175\n"
"v_rcp_f32 v18, v18\n"
"v_fma_f32 v19, v19, v14, v14\n"
"v_mul_f32 v173, v173, v18\n"
"v_exp_f32 v20, v20\n"
"v_fma_f32 v18, -v176, v176, v4\n"
"v_mul_f32 v18, v18, v176\n"
"v_rcp_f32 v19, v19\n"
"v_fma_f32 v20, v20, v14, v14\n"
"v_cvt_pk_bf16_f32 v30, v172, v173\n"
"v_mul_f32 v174, v174, v19\n"
"v_exp_f32 v18, v18\n"
"v_fma_f32 v19, -v177, v177, v4\n"
"v_mul_f32 v19, v19, v177\n"
"v_rcp_f32 v20, v20\n"
"v_fma_f32 v18, v18, v16, v16\n"
"v_mul_f32 v175, v175, v20\n"
"v_exp_f32 v19, v19\n"
"v_fma_f32 v20, -v178, v178, v4\n"
"v_mul_f32 v20, v20, v178\n"
"v_rcp_f32 v18, v18\n"
"v_fma_f32 v19, v19, v16, v16\n"
"v_cvt_pk_bf16_f32 v31, v174, v175\n"
"buffer_store_dwordx4 v[28:31], v1, s[40:43], s56 offen offset:1024 sc1\n"
"s_add_u32 s56, s56, 2048\n"
"v_mul_f32 v176, v176, v18\n"
"v_exp_f32 v20, v20\n"
"v_fma_f32 v18, -v179, v179, v4\n"
"v_mul_f32 v18, v18, v179\n"
"v_rcp_f32 v19, v19\n"
"v_fma_f32 v20, v20, v16, v16\n"
"v_mul_f32 v177, v177, v19\n"
"v_exp_f32 v18, v18\n"
"v_fma_f32 v19, -v180, v180, v4\n"
"v_mul_f32 v19, v19, v180\n"
"v_rcp_f32 v20, v20\n"
"v_fma_f32 v18, v18, v16, v16\n"
"v_cvt_pk_bf16_f32 v24, v176, v177\n"
"v_mul_f32 v178, v178, v20\n"
"v_exp_f32 v19, v19\n"
"v_fma_f32 v20, -v181, v181, v4\n"
"v_mul_f32 v20, v20, v181\n"
"v_rcp_f32 v18, v18\n"
"v_fma_f32 v19, v19, v16, v16\n"
"v_mul_f32 v179, v179, v18\n"
"v_exp_f32 v20, v20\n"
"v_fma_f32 v18, -v182, v182, v4\n"
"v_mul_f32 v18, v18, v182\n"
"v_rcp_f32 v19, v19\n"
"v_fma_f32 v20, v20, v16, v16\n"
"v_cvt_pk_bf16_f32 v25, v178, v179\n"
"v_mul_f32 v180, v180, v19\n"
"v_exp_f32 v18, v18\n"
"v_fma_f32 v19, -v183, v183, v4\n"
"v_mul_f32 v19, v19, v183\n"
"v_rcp_f32 v20, v20\n"
"v_fma_f32 v18, v18, v16, v16\n"
"v_mul_f32 v181, v181, v20\n"
"v_exp_f32 v19, v19\n"
"v_fma_f32 v20, -v184, v184, v4\n"
"v_mul_f32 v20, v20, v184\n"
"v_rcp_f32 v18, v18\n"
"v_fma_f32 v19, v19, v16, v16\n"
"v_cvt_pk_bf16_f32 v26, v180, v181\n"
"v_mul_f32 v182, v182, v18\n"
"v_exp_f32 v20, v20\n"
"v_fma_f32 v18, -v185, v185, v4\n"
"v_mul_f32 v18, v18, v185\n"
"v_rcp_f32 v19, v19\n"
"v_fma_f32 v20, v20, v16, v16\n"
"v_mul_f32 v183, v183, v19\n"
"v_exp_f32 v18, v18\n"
"v_fma_f32 v19, -v186, v186, v4\n"
"v_mul_f32 v19, v19, v186\n"
"v_rcp_f32 v20, v20\n"
"v_fma_f32 v18, v18, v16, v16\n"
"v_cvt_pk_bf16_f32 v27, v182, v183\n"
"buffer_store_dwordx4 v[24:27], v1, s[40:43], s57 offen offset:0 sc1\n"
"v_mul_f32 v184, v184, v20\n"
"v_exp_f32 v19, v19\n"
"v_fma_f32 v20, -v187, v187, v4\n"
"v_mul_f32 v20, v20, v187\n"
"v_rcp_f32 v18, v18\n"
"v_fma_f32 v19, v19, v16, v16\n"
"v_mul_f32 v185, v185, v18\n"
"v_exp_f32 v20, v20\n"
"v_fma_f32 v18, -v188, v188, v4\n"
"v_mul_f32 v18, v18, v188\n"
"v_rcp_f32 v19, v19\n"
"v_fma_f32 v20, v20, v16, v16\n"
"v_cvt_pk_bf16_f32 v28, v184, v185\n"
"v_mul_f32 v186, v186, v19\n"
"v_exp_f32 v18, v18\n"
"v_fma_f32 v19, -v189, v189, v4\n"
"v_mul_f32 v19, v19, v189\n"
"v_rcp_f32 v20, v20\n"
"v_fma_f32 v18, v18, v16, v16\n"
"v_mul_f32 v187, v187, v20\n"
"v_exp_f32 v19, v19\n"
"v_fma_f32 v20, -v190, v190, v4\n"
"v_mul_f32 v20, v20, v190\n"
"v_rcp_f32 v18, v18\n"
"v_fma_f32 v19, v19, v16, v16\n"
"v_cvt_pk_bf16_f32 v29, v186, v187\n"
"v_mul_f32 v188, v188, v18\n"
"v_exp_f32 v20, v20\n"
"v_fma_f32 v18, -v191, v191, v4\n"
"v_mul_f32 v18, v18, v191\n"
"v_rcp_f32 v19, v19\n"
"v_fma_f32 v20, v20, v16, v16\n"
"v_mul_f32 v189, v189, v19\n"
"v_exp_f32 v18, v18\n"
"v_rcp_f32 v20, v20\n"
"v_fma_f32 v18, v18, v16, v16\n"
"v_cvt_pk_bf16_f32 v30, v188, v189\n"
"v_mul_f32 v190, v190, v20\n"
"v_rcp_f32 v18, v18\n"
"s_nop 0\n"
"v_mul_f32 v191, v191, v18\n"
"v_cvt_pk_bf16_f32 v31, v190, v191\n"
"buffer_store_dwordx4 v[28:31], v1, s[40:43], s57 offen offset:1024 sc1\n"
"s_add_u32 s57, s57, 2048\n"
"v_lshlrev_b32 v18, 3, v1\n"
"v_mov_b32 v19, 1\n"
"s_cmp_eq_u32 s48, 0\n"
"s_waitcnt vmcnt(0)\n"
"s_barrier\n"
"s_cbranch_scc0 L_b3x%=\n"
"s_mov_b64 exec, 3\n"
"global_atomic_add v18, v19, %[cn] sc1\n"
"s_mov_b64 exec, -1\n"
"L_b3x%=:\n"
        ::[w] "s"(wp), [x] "s"(xp), [a] "s"(ap), [g] "s"(gp), [it] "s"(it), [cn] "s"(ws + OFF_FL + (192 + 2 * xc) * 128), [fl] "s"(ws + OFF_FL + (128 + 4 * xc) * 128), [sm] "s"((u32)(size_t)sm), [wv] "s"(wvi)
        : "v1","v2","v3","v4","v5","v6","v7","v8","v9","v10","v11","v12","v13","v14","v15","v16","v17","v18","v19","v20","v21","v22","v23","v24","v25","v26","v27","v28","v29","v30","v31","v32","v33","v34","v35","v36","v37","v38","v39","v40","v41","v42","v43","v44","v45","v46","v47","v48","v49","v50","v51","v52","v53","v54","v55","v56","v57","v58","v59","v60","v61","v62","v63","v64","v65","v66","v67","v68","v69","v70","v71","v72","v73","v74","v75","v76","v77","v78","v79","v80","v81","v82","v83","v84","v85","v86","v87","v88","v89","v90","v91","v92","v93","v94","v95","v96","v97","v98","v99","v100","v101","v102","v103","v104","v105","v106","v107","v108","v109","v110","v111","v112","v113","v114","v115","v116","v117","v118","v119","v120","v121","v122","v123","v124","v125","v126","v127","v128","v129","v130","v131","v132","v133","v134","v135","v136","v137","v138","v139","v140","v141","v142","v143","v144","v145","v146","v147","v148","v149","v150","v151","v152","v153","v154","v155","v156","v157","v158","v159","v160","v161","v162","v163","v164","v165","v166","v167","v168","v169","v170","v171","v172","v173","v174","v175","v176","v177","v178","v179","v180","v181","v182","v183","v184","v185","v186","v187","v188","v189","v190","v191","v192","v193","v194","v195","v196","v197","v198","v199","v200","v201","v202","v203","v204","v205","v206","v207","v208","v209","v210","v211","v212","v213","v214","v215","v216","v217","v218","v219","v220","v221","v222","v223","v224","v225","v226","v227","v228","v229","v230","v231","v232","v233","v234","v235","v236","v237","v238","v239","v240","v241","v242","v243","v244","v245","v246","v247","v248","v249","v250","v251","v252","v253","v254","v255","a0","a1","a2","a3","a4","a5","a6","a7","a8","a9","a10","a11","a12","a13","a14","a15","a16","a17","a18","a19","a20","a21","a22","a23","a24","a25","a26","a27","a28","a29","a30","a31","a32","a33","a34","a35","a36","a37","a38","a39","a40","a41","a42","a43","a44","a45","a46","a47","a48","a49","a50","a51","a52","a53","a54","a55","a56","a57","a58","a59","a60","a61","a62","a63","a64","a65","a66","a67","a68","a69","a70","a71","a72","a73","a74","a75","a76","a77","a78","a79","a80","a81","a82","a83","a84","a85","a86","a87","a88","a89","a90","a91","a92","a93","a94","a95","a96","a97","a98","a99","a100","a101","a102","a103","a104","a105","a106","a107","a108","a109","a110","a111","a112","a113","a114","a115","a116","a117","a118","a119","a120","a121","a122","a123","a124","a125","a126","a127","a128","a129","a130","a131","a132","a133","a134","a135","a136","a137","a138","a139","a140","a141","a142","a143","a144","a145","a146","a147","a148","a149","a150","a151","a152","a153","a154","a155","a156","a157","a158","a159","a160","a161","a162","a163","a164","a165","a166","a167","a168","a169","a170","a171","a172","a173","a174","a175","a176","a177","a178","a179","a180","a181","a182","a183","a184","a185","a186","a187","a188","a189","a190","a191","a192","a193","a194","a195","a196","a197","a198","a199","a200","a201","a202","a203","a204","a205","a206","a207","a208","a209","a210","a211","a212","a213","a214","a215","a216","a217","a218","a219","a220","a221","a222","a223","a224","a225","a226","a227","a228","a229","a230","a231","a232","a233","a234","a235","a236","a237","a238","a239","a240","a241","a242","a243","a244","a245","a246","a247","a248","a249","a250","a251","a252","a253","a254","a255","s36","s37","s38","s39","s40","s41","s42","s43","s44","s45","s46","s47","s48","s49","s50","s51","s52","s53","s54","s55","s56","s57","s58","s59","s60","s61","s62","s63","s64","s65","s66","s67","s68","s69","s70","s71","s72","s73","s74","s75","s76","s77","s78","s79","s80","s81","s82","s83","s84","s85","s86","s87","memory","vcc","scc");
  }
  const int kq = xc >> 1, tile = (xc & 1) * 32 + id, blk0 = (tile >> 2) * 8 + 2 * wvi, cb = id & 3;
  asm volatile(
"v_mbcnt_lo_u32_b32 v14, -1, 0\n"
"v_mbcnt_hi_u32_b32 v14, -1, v14\n"
"v_lshl_add_u32 v14, %[wv], 6, v14\n"
"s_mov_b64 s[36:37], %[a]\n"
"s_mov_b32 s38, 235520\n"
"s_mov_b32 s39, 0x20000\n"
"s_mov_b64 s[40:41], %[w]\n"
"s_mov_b32 s42, 1318912\n"
"s_mov_b32 s43, 0x20000\n"
"s_mov_b64 s[44:45], %[x]\n"
"s_mov_b32 s46, 0\n"
"s_mov_b32 s47, 0x20000\n"
"s_mov_b64 s[48:49], %[p]\n"
"s_mov_b32 s50, 114688\n"
"s_mov_b32 s51, 0x20000\n"
"s_mov_b64 s[52:53], %[o]\n"
"s_mov_b32 s54, 28672\n"
"s_mov_b32 s55, 0x20000\n"
"v_and_b32 v6, 63, v14\n"
"v_lshlrev_b32 v1, 4, v6\n"
"v_add_u32 v2, 4096, v1\n"
"s_mul_i32 s60, %[wv], 28672\n"
"s_mul_i32 s61, %[wv], 7168\n"
"s_add_u32 s61, s61, %[sm]\n"
"s_mov_b32 s58, 0\n"
"s_mov_b32 s59, 188416\n"
"s_cmp_eq_u32 %[wv], %[kq]\n"
"s_cselect_b32 s46, 35840, 0\n"
"s_add_u32 m0, s61, 0\n"
"s_nop 0\n"
"buffer_load_dwordx4 v1, s[40:43], s60 offen offset:0 lds\n"
"buffer_load_dwordx4 v1, s[40:43], s60 offen offset:1024 lds\n"
"buffer_load_dwordx4 v1, s[40:43], s60 offen offset:2048 lds\n"
"buffer_load_dwordx4 v1, s[40:43], s60 offen offset:3072 lds\n"
"s_add_u32 m0, s61, 4096\n"
"s_nop 0\n"
"buffer_load_dwordx4 v2, s[40:43], s60 offen offset:0 lds\n"
"buffer_load_dwordx4 v2, s[40:43], s60 offen offset:1024 lds\n"
"buffer_load_dwordx4 v2, s[40:43], s60 offen offset:2048 lds\n"
"s_add_u32 s60, s60, 114688\n"
"s_add_u32 m0, s61, 28672\n"
"s_nop 0\n"
"buffer_load_dwordx4 v1, s[40:43], s60 offen offset:0 lds\n"
"buffer_load_dwordx4 v1, s[40:43], s60 offen offset:1024 lds\n"
"buffer_load_dwordx4 v1, s[40:43], s60 offen offset:2048 lds\n"
"buffer_load_dwordx4 v1, s[40:43], s60 offen offset:3072 lds\n"
"s_add_u32 m0, s61, 32768\n"
"s_nop 0\n"
"buffer_load_dwordx4 v2, s[40:43], s60 offen offset:0 lds\n"
"buffer_load_dwordx4 v2, s[40:43], s60 offen offset:1024 lds\n"
"buffer_load_dwordx4 v2, s[40:43], s60 offen offset:2048 lds\n"
"s_add_u32 s60, s60, 114688\n"
"v_mov_b32 v10, 0\n"
"s_branch L_k3w%=\n"
"L_k3v%=:\n"
"s_sleep 3\n"
"L_k3w%=:\n"
"global_load_dword v11, v10, %[cn] sc1\n"
"s_waitcnt vmcnt(0)\n"
"v_readfirstlane_b32 s57, v11\n"
"s_cmp_eq_u32 s57, 22\n"
"s_cbranch_scc0 L_k3v%=\n"
"buffer_load_dwordx4 v[128:131], v1, s[36:39], s58 offen offset:0 sc1\n"
"buffer_load_dwordx4 v[132:135], v1, s[36:39], s59 offen offset:0 sc1\n"
"buffer_load_dwordx4 v[136:139], v1, s[36:39], s58 offen offset:1024 sc1\n"
"buffer_load_dwordx4 v[140:143], v1, s[36:39], s59 offen offset:1024 sc1\n"
"buffer_load_dwordx4 v[144:147], v1, s[36:39], s58 offen offset:2048 sc1\n"
"buffer_load_dwordx4 v[148:151], v1, s[36:39], s59 offen offset:2048 sc1\n"
"buffer_load_dwordx4 v[152:155], v1, s[36:39], s58 offen offset:3072 sc1\n"
"s_add_u32 s58, s58, 4096\n"
"buffer_load_dwordx4 v[156:159], v1, s[36:39], s59 offen offset:3072 sc1\n"
"s_add_u32 s59, s59, 4096\n"
"s_mov_b32 s57, 0\n"
"buffer_load_dwordx4 a[0:3], v1, s[44:47], s57 offen sc1\n"
"s_mov_b32 s57, 1024\n"
"buffer_load_dwordx4 a[4:7], v1, s[44:47], s57 offen sc1\n"
"s_mov_b32 s57, 2048\n"
"buffer_load_dwordx4 a[8:11], v1, s[44:47], s57 offen sc1\n"
"s_mov_b32 s57, 3072\n"
"buffer_load_dwordx4 a[12:15], v1, s[44:47], s57 offen sc1\n"
"s_mov_b32 s57, 4096\n"
"buffer_load_dwordx4 a[16:19], v1, s[44:47], s57 offen sc1\n"
"s_mov_b32 s57, 5120\n"
"buffer_load_dwordx4 a[20:23], v1, s[44:47], s57 offen sc1\n"
"s_mov_b32 s57, 6144\n"
"buffer_load_dwordx4 a[24:27], v1, s[44:47], s57 offen sc1\n"
"s_mov_b32 s57, 28672\n"
"buffer_load_dwordx4 a[28:31], v1, s[44:47], s57 offen sc1\n"
"s_mov_b32 s57, 29696\n"
"buffer_load_dwordx4 a[32:35], v1, s[44:47], s57 offen sc1\n"
"s_mov_b32 s57, 30720\n"
"buffer_load_dwordx4 a[36:39], v1, s[44:47], s57 offen sc1\n"
"s_mov_b32 s57, 31744\n"
"buffer_load_dwordx4 a[40:43], v1, s[44:47], s57 offen sc1\n"
"s_mov_b32 s57, 32768\n"
"buffer_load_dwordx4 a[44:47], v1, s[44:47], s57 offen sc1\n"
"s_mov_b32 s57, 33792\n"
"buffer_load_dwordx4 a[48:51], v1, s[44:47], s57 offen sc1\n"
"s_mov_b32 s57, 34816\n"
"buffer_load_dwordx4 a[52:55], v1, s[44:47], s57 offen sc1\n"
"buffer_load_dwordx4 v[160:163], v1, s[36:39], s58 offen offset:0 sc1\n"
"buffer_load_dwordx4 v[164:167], v1, s[36:39], s59 offen offset:0 sc1\n"
"buffer_load_dwordx4 v[168:171], v1, s[36:39], s58 offen offset:1024 sc1\n"
"buffer_load_dwordx4 v[172:175], v1, s[36:39], s59 offen offset:1024 sc1\n"
"buffer_load_dwordx4 v[176:179], v1, s[36:39], s58 offen offset:2048 sc1\n"
"buffer_load_dwordx4 v[180:183], v1, s[36:39], s59 offen offset:2048 sc1\n"
"buffer_load_dwordx4 v[184:187], v1, s[36:39], s58 offen offset:3072 sc1\n"
"s_add_u32 s58, s58, 4096\n"
"buffer_load_dwordx4 v[188:191], v1, s[36:39], s59 offen offset:3072 sc1\n"
"s_add_u32 s59, s59, 4096\n"
"v_add_u32 v3, %[sm], v1\n"
"v_add_u32 v4, 57344, v3\n"
"v_add_u32 v5, 57344, v4\n"
"v_and_b32 v10, 15, v6\n"
"v_mul_u32_u24 v10, 896, v10\n"
"v_lshrrev_b32 v7, 4, v6\n"
"v_lshl_add_u32 v10, v7, 3, v10\n"
"v_and_b32 v6, 3, v6\n"
"s_waitcnt vmcnt(8)\n"
"s_barrier\n"
"ds_read_b128 v[32:35], v3 offset:0\n"
"ds_read_b128 v[36:39], v3 offset:1024\n"
"ds_read_b128 v[40:43], v3 offset:2048\n"
"ds_read_b128 v[44:47], v3 offset:3072\n"
"ds_read_b128 v[48:51], v3 offset:4096\n"
"ds_read_b128 v[52:55], v3 offset:5120\n"
"ds_read_b128 v[56:59], v3 offset:6144\n"
"s_waitcnt lgkmcnt(6)\n"
"v_mfma_f32_16x16x32_bf16 a[0:3], v[32:35], v[128:131], a[0:3]\n"
"ds_read_b128 v[60:63], v3 offset:7168\n"
"v_mfma_f32_16x16x32_bf16 a[28:31], v[32:35], v[132:135], a[28:31]\n"
"buffer_load_dwordx4 v[192:195], v1, s[36:39], s58 offen offset:0 sc1\n"
"s_waitcnt lgkmcnt(4)\n"
"v_mfma_f32_16x16x32_bf16 a[4:7], v[36:39], v[128:131], a[4:7]\n"
"ds_read_b128 v[64:67], v3 offset:8192\n"
"v_mfma_f32_16x16x32_bf16 a[32:35], v[36:39], v[132:135], a[32:35]\n"
"s_add_u32 m0, s61, 57344\n"
"v_mfma_f32_16x16x32_bf16 a[8:11], v[40:43], v[128:131], a[8:11]\n"
"buffer_load_dwordx4 v1, s[40:43], s60 offen offset:0 lds\n"
"ds_read_b128 v[68:71], v3 offset:9216\n"
"v_mfma_f32_16x16x32_bf16 a[36:39], v[40:43], v[132:135], a[36:39]\n"
"v_mfma_f32_16x16x32_bf16 a[12:15], v[44:47], v[128:131], a[12:15]\n"
"ds_read_b128 v[72:75], v3 offset:10240\n"
"v_mfma_f32_16x16x32_bf16 a[40:43], v[44:47], v[132:135], a[40:43]\n"
"s_waitcnt lgkmcnt(2)\n"
"v_mfma_f32_16x16x32_bf16 a[16:19], v[48:51], v[128:131], a[16:19]\n"
"buffer_load_dwordx4 v[196:199], v1, s[36:39], s59 offen offset:0 sc1\n"
"ds_read_b128 v[76:79], v3 offset:11264\n"
"v_mfma_f32_16x16x32_bf16 a[44:47], v[48:51], v[132:135], a[44:47]\n"
"v_mfma_f32_16x16x32_bf16 a[20:23], v[52:55], v[128:131], a[20:23]\n"
"ds_read_b128 v[80:83], v3 offset:12288\n"
"v_mfma_f32_16x16x32_bf16 a[48:51], v[52:55], v[132:135], a[48:51]\n"
"v_mfma_f32_16x16x32_bf16 a[24:27], v[56:59], v[128:131], a[24:27]\n"
"buffer_load_dwordx4 v1, s[40:43], s60 offen offset:1024 lds\n"
"ds_read_b128 v[84:87], v3 offset:13312\n"
"v_mfma_f32_16x16x32_bf16 a[52:55], v[56:59], v[132:135], a[52:55]\n"
"v_mfma_f32_16x16x32_bf16 a[0:3], v[60:63], v[136:139], a[0:3]\n"
"ds_read_b128 v[88:91], v3 offset:14336\n"
"v_mfma_f32_16x16x32_bf16 a[28:31], v[60:63], v[140:143], a[28:31]\n"
"buffer_load_dwordx4 v[200:203], v1, s[36:39], s58 offen offset:1024 sc1\n"
"v_mfma_f32_16x16x32_bf16 a[4:7], v[64:67], v[136:139], a[4:7]\n"
"ds_read_b128 v[92:95], v3 offset:15360\n"
"v_mfma_f32_16x16x32_bf16 a[32:35], v[64:67], v[140:143], a[32:35]\n"
"s_waitcnt lgkmcnt(2)\n"
"v_mfma_f32_16x16x32_bf16 a[8:11], v[68:71], v[136:139], a[8:11]\n"
"ds_read_b128 v[32:35], v3 offset:16384\n"
"v_mfma_f32_16x16x32_bf16 a[36:39], v[68:71], v[140:143], a[36:39]\n"
"buffer_load_dwordx4 v1, s[40:43], s60 offen offset:2048 lds\n"
"v_mfma_f32_16x16x32_bf16 a[12:15], v[72:75], v[136:139], a[12:15]\n"
"ds_read_b128 v[36:39], v3 offset:17408\n"
"v_mfma_f32_16x16x32_bf16 a[40:43], v[72:75], v[140:143], a[40:43]\n"
"v_mfma_f32_16x16x32_bf16 a[16:19], v[76:79], v[136:139], a[16:19]\n"
"ds_read_b128 v[40:43], v3 offset:18432\n"
"v_mfma_f32_16x16x32_bf16 a[44:47], v[76:79], v[140:143], a[44:47]\n"
"buffer_load_dwordx4 v[204:207], v1, s[36:39], s59 offen offset:1024 sc1\n"
"v_mfma_f32_16x16x32_bf16 a[20:23], v[80:83], v[136:139], a[20:23]\n"
"ds_read_b128 v[44:47], v3 offset:19456\n"
"v_mfma_f32_16x16x32_bf16 a[48:51], v[80:83], v[140:143], a[48:51]\n"
"v_mfma_f32_16x16x32_bf16 a[24:27], v[84:87], v[136:139], a[24:27]\n"
"buffer_load_dwordx4 v1, s[40:43], s60 offen offset:3072 lds\n"
"ds_read_b128 v[48:51], v3 offset:20480\n"
"v_mfma_f32_16x16x32_bf16 a[52:55], v[84:87], v[140:143], a[52:55]\n"
"s_waitcnt lgkmcnt(2)\n"
"v_mfma_f32_16x16x32_bf16 a[0:3], v[88:91], v[144:147], a[0:3]\n"
"ds_read_b128 v[52:55], v3 offset:21504\n"
"v_mfma_f32_16x16x32_bf16 a[28:31], v[88:91], v[148:151], a[28:31]\n"
"v_mfma_f32_16x16x32_bf16 a[4:7], v[92:95], v[144:147], a[4:7]\n"
"buffer_load_dwordx4 v[208:211], v1, s[36:39], s58 offen offset:2048 sc1\n"
"ds_read_b128 v[56:59], v3 offset:22528\n"
"v_mfma_f32_16x16x32_bf16 a[32:35], v[92:95], v[148:151], a[32:35]\n"
"v_mfma_f32_16x16x32_bf16 a[8:11], v[32:35], v[144:147], a[8:11]\n"
"ds_read_b128 v[60:63], v3 offset:23552\n"
"v_mfma_f32_16x16x32_bf16 a[36:39], v[32:35], v[148:151], a[36:39]\n"
"s_add_u32 m0, s61, 61440\n"
"v_mfma_f32_16x16x32_bf16 a[12:15], v[36:39], v[144:147], a[12:15]\n"
"buffer_load_dwordx4 v2, s[40:43], s60 offen offset:0 lds\n"
"ds_read_b128 v[64:67], v3 offset:24576\n"
"v_mfma_f32_16x16x32_bf16 a[40:43], v[36:39], v[148:151], a[40:43]\n"
"v_mfma_f32_16x16x32_bf16 a[16:19], v[40:43], v[144:147], a[16:19]\n"
"ds_read_b128 v[68:71], v3 offset:25600\n"
"v_mfma_f32_16x16x32_bf16 a[44:47], v[40:43], v[148:151], a[44:47]\n"
"buffer_load_dwordx4 v[212:215], v1, s[36:39], s59 offen offset:2048 sc1\n"
"s_waitcnt lgkmcnt(2)\n"
"v_mfma_f32_16x16x32_bf16 a[20:23], v[44:47], v[144:147], a[20:23]\n"
"ds_read_b128 v[72:75], v3 offset:26624\n"
"v_mfma_f32_16x16x32_bf16 a[48:51], v[44:47], v[148:151], a[48:51]\n"
"v_mfma_f32_16x16x32_bf16 a[24:27], v[48:51], v[144:147], a[24:27]\n"
"ds_read_b128 v[76:79], v3 offset:27648\n"
"v_mfma_f32_16x16x32_bf16 a[52:55], v[48:51], v[148:151], a[52:55]\n"
"buffer_load_dwordx4 v2, s[40:43], s60 offen offset:1024 lds\n"
"s_waitcnt vmcnt(12)\n"
"s_barrier\n"
"v_mfma_f32_16x16x32_bf16 a[0:3], v[52:55], v[152:155], a[0:3]\n"
"ds_read_b128 v[80:83], v3 offset:28672\n"
"v_mfma_f32_16x16x32_bf16 a[28:31], v[52:55], v[156:159], a[28:31]\n"
"v_mfma_f32_16x16x32_bf16 a[4:7], v[56:59], v[152:155], a[4:7]\n"
"ds_read_b128 v[84:87], v3 offset:29696\n"
"v_mfma_f32_16x16x32_bf16 a[32:35], v[56:59], v[156:159], a[32:35]\n"
"buffer_load_dwordx4 v[216:219], v1, s[36:39], s58 offen offset:3072 sc1\n"
"s_add_u32 s58, s58, 4096\n"
"v_mfma_f32_16x16x32_bf16 a[8:11], v[60:63], v[152:155], a[8:11]\n"
"ds_read_b128 v[88:91], v3 offset:30720\n"
"v_mfma_f32_16x16x32_bf16 a[36:39], v[60:63], v[156:159], a[36:39]\n"
"s_waitcnt lgkmcnt(2)\n"
"v_mfma_f32_16x16x32_bf16 a[12:15], v[64:67], v[152:155], a[12:15]\n"
"buffer_load_dwordx4 v2, s[40:43], s60 offen offset:2048 lds\n"
"s_add_u32 s60, s60, 114688\n"
"ds_read_b128 v[92:95], v3 offset:31744\n"
"v_mfma_f32_16x16x32_bf16 a[40:43], v[64:67], v[156:159], a[40:43]\n"
"v_mfma_f32_16x16x32_bf16 a[16:19], v[68:71], v[152:155], a[16:19]\n"
"ds_read_b128 v[32:35], v3 offset:32768\n"
"v_mfma_f32_16x16x32_bf16 a[44:47], v[68:71], v[156:159], a[44:47]\n"
"v_mfma_f32_16x16x32_bf16 a[20:23], v[72:75], v[152:155], a[20:23]\n"
"buffer_load_dwordx4 v[220:223], v1, s[36:39], s59 offen offset:3072 sc1\n"
"s_add_u32 s59, s59, 4096\n"
"ds_read_b128 v[36:39], v3 offset:33792\n"
"v_mfma_f32_16x16x32_bf16 a[48:51], v[72:75], v[156:159], a[48:51]\n"
"v_mfma_f32_16x16x32_bf16 a[24:27], v[76:79], v[152:155], a[24:27]\n"
"ds_read_b128 v[40:43], v3 offset:34816\n"
"v_mfma_f32_16x16x32_bf16 a[52:55], v[76:79], v[156:159], a[52:55]\n"
"v_mfma_f32_16x16x32_bf16 a[0:3], v[80:83], v[160:163], a[0:3]\n"
"ds_read_b128 v[44:47], v3 offset:35840\n"
"v_mfma_f32_16x16x32_bf16 a[28:31], v[80:83], v[164:167], a[28:31]\n"
"buffer_load_dwordx4 v[224:227], v1, s[36:39], s58 offen offset:0 sc1\n"
"s_waitcnt lgkmcnt(2)\n"
"v_mfma_f32_16x16x32_bf16 a[4:7], v[84:87], v[160:163], a[4:7]\n"
"ds_read_b128 v[48:51], v3 offset:36864\n"
"v_mfma_f32_16x16x32_bf16 a[32:35], v[84:87], v[164:167], a[32:35]\n"
"s_add_u32 m0, s61, 86016\n"
"v_mfma_f32_16x16x32_bf16 a[8:11], v[88:91], v[160:163], a[8:11]\n"
"buffer_load_dwordx4 v1, s[40:43], s60 offen offset:0 lds\n"
"ds_read_b128 v[52:55], v3 offset:37888\n"
"v_mfma_f32_16x16x32_bf16 a[36:39], v[88:91], v[164:167], a[36:39]\n"
"v_mfma_f32_16x16x32_bf16 a[12:15], v[92:95], v[160:163], a[12:15]\n"
"ds_read_b128 v[56:59], v3 offset:38912\n"
"v_mfma_f32_16x16x32_bf16 a[40:43], v[92:95], v[164:167], a[40:43]\n"
"v_mfma_f32_16x16x32_bf16 a[16:19], v[32:35], v[160:163], a[16:19]\n"
"buffer_load_dwordx4 v[228:231], v1, s[36:39], s59 offen offset:0 sc1\n"
"ds_read_b128 v[60:63], v3 offset:39936\n"
"v_mfma_f32_16x16x32_bf16 a[44:47], v[32:35], v[164:167], a[44:47]\n"
"v_mfma_f32_16x16x32_bf16 a[20:23], v[36:39], v[160:163], a[20:23]\n"
"ds_read_b128 v[64:67], v3 offset:40960\n"
"v_mfma_f32_16x16x32_bf16 a[48:51], v[36:39], v[164:167], a[48:51]\n"
"s_waitcnt lgkmcnt(2)\n"
"v_mfma_f32_16x16x32_bf16 a[24:27], v[40:43], v[160:163], a[24:27]\n"
"buffer_load_dwordx4 v1, s[40:43], s60 offen offset:1024 lds\n"
"ds_read_b128 v[68:71], v3 offset:41984\n"
"v_mfma_f32_16x16x32_bf16 a[52:55], v[40:43], v[164:167], a[52:55]\n"
"v_mfma_f32_16x16x32_bf16 a[0:3], v[44:47], v[168:171], a[0:3]\n"
"ds_read_b128 v[72:75], v3 offset:43008\n"
"v_mfma_f32_16x16x32_bf16 a[28:31], v[44:47], v[172:175], a[28:31]\n"
"buffer_load_dwordx4 v[232:235], v1, s[36:39], s58 offen offset:1024 sc1\n"
"v_mfma_f32_16x16x32_bf16 a[4:7], v[48:51], v[168:171], a[4:7]\n"
"ds_read_b128 v[76:79], v3 offset:44032\n"
"v_mfma_f32_16x16x32_bf16 a[32:35], v[48:51], v[172:175], a[32:35]\n"
"v_mfma_f32_16x16x32_bf16 a[8:11], v[52:55], v[168:171], a[8:11]\n"
"ds_read_b128 v[80:83], v3 offset:45056\n"
"v_mfma_f32_16x16x32_bf16 a[36:39], v[52:55], v[172:175], a[36:39]\n"
"buffer_load_dwordx4 v1, s[40:43], s60 offen offset:2048 lds\n"
"v_mfma_f32_16x16x32_bf16 a[12:15], v[56:59], v[168:171], a[12:15]\n"
"ds_read_b128 v[84:87], v3 offset:46080\n"
"v_mfma_f32_16x16x32_bf16 a[40:43], v[56:59], v[172:175], a[40:43]\n"
"s_waitcnt lgkmcnt(2)\n"
"v_mfma_f32_16x16x32_bf16 a[16:19], v[60:63], v[168:171], a[16:19]\n"
"ds_read_b128 v[88:91], v3 offset:47104\n"
"v_mfma_f32_16x16x32_bf16 a[44:47], v[60:63], v[172:175], a[44:47]\n"
"buffer_load_dwordx4 v[236:239], v1, s[36:39], s59 offen offset:1024 sc1\n"
"v_mfma_f32_16x16x32_bf16 a[20:23], v[64:67], v[168:171], a[20:23]\n"
"ds_read_b128 v[92:95], v3 offset:48128\n"
"v_mfma_f32_16x16x32_bf16 a[48:51], v[64:67], v[172:175], a[48:51]\n"
"v_mfma_f32_16x16x32_bf16 a[24:27], v[68:71], v[168:171], a[24:27]\n"
"buffer_load_dwordx4 v1, s[40:43], s60 offen offset:3072 lds\n"
"ds_read_b128 v[32:35], v3 offset:49152\n"
"v_mfma_f32_16x16x32_bf16 a[52:55], v[68:71], v[172:175], a[52:55]\n"
"v_mfma_f32_16x16x32_bf16 a[0:3], v[72:75], v[176:179], a[0:3]\n"
"ds_read_b128 v[36:39], v3 offset:50176\n"
"v_mfma_f32_16x16x32_bf16 a[28:31], v[72:75], v[180:183], a[28:31]\n"
"v_mfma_f32_16x16x32_bf16 a[4:7], v[76:79], v[176:179], a[4:7]\n"
"buffer_load_dwordx4 v[240:243], v1, s[36:39], s58 offen offset:2048 sc1\n"
"ds_read_b128 v[40:43], v3 offset:51200\n"
"v_mfma_f32_16x16x32_bf16 a[32:35], v[76:79], v[180:183], a[32:35]\n"
"s_waitcnt lgkmcnt(2)\n"
"v_mfma_f32_16x16x32_bf16 a[8:11], v[80:83], v[176:179], a[8:11]\n"
"ds_read_b128 v[44:47], v3 offset:52224\n"
"v_mfma_f32_16x16x32_bf16 a[36:39], v[80:83], v[180:183], a[36:39]\n"
"s_add_u32 m0, s61, 90112\n"
"v_mfma_f32_16x16x32_bf16 a[12:15], v[84:87], v[176:179], a[12:15]\n"
"buffer_load_dwordx4 v2, s[40:43], s60 offen offset:0 lds\n"
"ds_read_b128 v[48:51], v3 offset:53248\n"
"v_mfma_f32_16x16x32_bf16 a[40:43], v[84:87], v[180:183], a[40:43]\n"
"v_mfma_f32_16x16x32_bf16 a[16:19], v[88:91], v[176:179], a[16:19]\n"
"ds_read_b128 v[52:55], v3 offset:54272\n"
"v_mfma_f32_16x16x32_bf16 a[44:47], v[88:91], v[180:183], a[44:47]\n"
"buffer_load_dwordx4 v[244:247], v1, s[36:39], s59 offen offset:2048 sc1\n"
"v_mfma_f32_16x16x32_bf16 a[20:23], v[92:95], v[176:179], a[20:23]\n"
"ds_read_b128 v[56:59], v3 offset:55296\n"
"v_mfma_f32_16x16x32_bf16 a[48:51], v[92:95], v[180:183], a[48:51]\n"
"v_mfma_f32_16x16x32_bf16 a[24:27], v[32:35], v[176:179], a[24:27]\n"
"ds_read_b128 v[60:63], v3 offset:56320\n"
"v_mfma_f32_16x16x32_bf16 a[52:55], v[32:35], v[180:183], a[52:55]\n"
"buffer_load_dwordx4 v2, s[40:43], s60 offen offset:1024 lds\n"
"s_waitcnt vmcnt(12)\n"
"s_barrier\n"
"s_waitcnt lgkmcnt(1)\n"
"v_mfma_f32_16x16x32_bf16 a[0:3], v[36:39], v[184:187], a[0:3]\n"
"ds_read_b128 v[64:67], v4 offset:0\n"
"v_mfma_f32_16x16x32_bf16 a[28:31], v[36:39], v[188:191], a[28:31]\n"
"v_mfma_f32_16x16x32_bf16 a[4:7], v[40:43], v[184:187], a[4:7]\n"
"ds_read_b128 v[68:71], v4 offset:1024\n"
"v_mfma_f32_16x16x32_bf16 a[32:35], v[40:43], v[188:191], a[32:35]\n"
"buffer_load_dwordx4 v[248:251], v1, s[36:39], s58 offen offset:3072 sc1\n"
"s_add_u32 s58, s58, 4096\n"
"v_mfma_f32_16x16x32_bf16 a[8:11], v[44:47], v[184:187], a[8:11]\n"
"ds_read_b128 v[72:75], v4 offset:2048\n"
"v_mfma_f32_16x16x32_bf16 a[36:39], v[44:47], v[188:191], a[36:39]\n"
"v_mfma_f32_16x16x32_bf16 a[12:15], v[48:51], v[184:187], a[12:15]\n"
"buffer_load_dwordx4 v2, s[40:43], s60 offen offset:2048 lds\n"
"s_add_u32 s60, s60, 114688\n"
"ds_read_b128 v[76:79], v4 offset:3072\n"
"v_mfma_f32_16x16x32_bf16 a[40:43], v[48:51], v[188:191], a[40:43]\n"
"v_mfma_f32_16x16x32_bf16 a[16:19], v[52:55], v[184:187], a[16:19]\n"
"ds_read_b128 v[80:83], v4 offset:4096\n"
"v_mfma_f32_16x16x32_bf16 a[44:47], v[52:55], v[188:191], a[44:47]\n"
"v_mfma_f32_16x16x32_bf16 a[20:23], v[56:59], v[184:187], a[20:23]\n"
"buffer_load_dwordx4 v[252:255], v1, s[36:39], s59 offen offset:3072 sc1\n"
"s_add_u32 s59, s59, 4096\n"
"ds_read_b128 v[84:87], v4 offset:5120\n"
"v_mfma_f32_16x16x32_bf16 a[48:51], v[56:59], v[188:191], a[48:51]\n"
"s_waitcnt lgkmcnt(2)\n"
"v_mfma_f32_16x16x32_bf16 a[24:27], v[60:63], v[184:187], a[24:27]\n"
"ds_read_b128 v[88:91], v4 offset:6144\n"
"v_mfma_f32_16x16x32_bf16 a[52:55], v[60:63], v[188:191], a[52:55]\n"
"v_mfma_f32_16x16x32_bf16 a[0:3], v[64:67], v[192:195], a[0:3]\n"
"ds_read_b128 v[92:95], v4 offset:7168\n"
"v_mfma_f32_16x16x32_bf16 a[28:31], v[64:67], v[196:199], a[28:31]\n"
"buffer_load_dwordx4 v[128:131], v1, s[36:39], s58 offen offset:0 sc1\n"
"v_mfma_f32_16x16x32_bf16 a[4:7], v[68:71], v[192:195], a[4:7]\n"
"ds_read_b128 v[32:35], v4 offset:8192\n"
"v_mfma_f32_16x16x32_bf16 a[32:35], v[68:71], v[196:199], a[32:35]\n"
"s_add_u32 m0, s61, 114688\n"
"v_mfma_f32_16x16x32_bf16 a[8:11], v[72:75], v[192:195], a[8:11]\n"
"buffer_load_dwordx4 v1, s[40:43], s60 offen offset:0 lds\n"
"ds_read_b128 v[36:39], v4 offset:9216\n"
"v_mfma_f32_16x16x32_bf16 a[36:39], v[72:75], v[196:199], a[36:39]\n"
"v_mfma_f32_16x16x32_bf16 a[12:15], v[76:79], v[192:195], a[12:15]\n"
"ds_read_b128 v[40:43], v4 offset:10240\n"
"v_mfma_f32_16x16x32_bf16 a[40:43], v[76:79], v[196:199], a[40:43]\n"
"s_waitcnt lgkmcnt(2)\n"
"v_mfma_f32_16x16x32_bf16 a[16:19], v[80:83], v[192:195], a[16:19]\n"
"buffer_load_dwordx4 v[132:135], v1, s[36:39], s59 offen offset:0 sc1\n"
"ds_read_b128 v[44:47], v4 offset:11264\n"
"v_mfma_f32_16x16x32_bf16 a[44:47], v[80:83], v[196:199], a[44:47]\n"
"v_mfma_f32_16x16x32_bf16 a[20:23], v[84:87], v[192:195], a[20:23]\n"
"ds_read_b128 v[48:51], v4 offset:12288\n"
"v_mfma_f32_16x16x32_bf16 a[48:51], v[84:87], v[196:199], a[48:51]\n"
"v_mfma_f32_16x16x32_bf16 a[24:27], v[88:91], v[192:195], a[24:27]\n"
"buffer_load_dwordx4 v1, s[40:43], s60 offen offset:1024 lds\n"
"ds_read_b128 v[52:55], v4 offset:13312\n"
"v_mfma_f32_16x16x32_bf16 a[52:55], v[88:91], v[196:199], a[52:55]\n"
"v_mfma_f32_16x16x32_bf16 a[0:3], v[92:95], v[200:203], a[0:3]\n"
"ds_read_b128 v[56:59], v4 offset:14336\n"
"v_mfma_f32_16x16x32_bf16 a[28:31], v[92:95], v[204:207], a[28:31]\n"
"buffer_load_dwordx4 v[136:139], v1, s[36:39], s58 offen offset:1024 sc1\n"
"v_mfma_f32_16x16x32_bf16 a[4:7], v[32:35], v[200:203], a[4:7]\n"
"ds_read_b128 v[60:63], v4 offset:15360\n"
"v_mfma_f32_16x16x32_bf16 a[32:35], v[32:35], v[204:207], a[32:35]\n"
"s_waitcnt lgkmcnt(2)\n"
"v_mfma_f32_16x16x32_bf16 a[8:11], v[36:39], v[200:203], a[8:11]\n"
"ds_read_b128 v[64:67], v4 offset:16384\n"
"v_mfma_f32_16x16x32_bf16 a[36:39], v[36:39], v[204:207], a[36:39]\n"
"buffer_load_dwordx4 v1, s[40:43], s60 offen offset:2048 lds\n"
"v_mfma_f32_16x16x32_bf16 a[12:15], v[40:43], v[200:203], a[12:15]\n"
"ds_read_b128 v[68:71], v4 offset:17408\n"
"v_mfma_f32_16x16x32_bf16 a[40:43], v[40:43], v[204:207], a[40:43]\n"
"v_mfma_f32_16x16x32_bf16 a[16:19], v[44:47], v[200:203], a[16:19]\n"
"ds_read_b128 v[72:75], v4 offset:18432\n"
"v_mfma_f32_16x16x32_bf16 a[44:47], v[44:47], v[204:207], a[44:47]\n"
"buffer_load_dwordx4 v[140:143], v1, s[36:39], s59 offen offset:1024 sc1\n"
"v_mfma_f32_16x16x32_bf16 a[20:23], v[48:51], v[200:203], a[20:23]\n"
"ds_read_b128 v[76:79], v4 offset:19456\n"
"v_mfma_f32_16x16x32_bf16 a[48:51], v[48:51], v[204:207], a[48:51]\n"
"v_mfma_f32_16x16x32_bf16 a[24:27], v[52:55], v[200:203], a[24:27]\n"
"buffer_load_dwordx4 v1, s[40:43], s60 offen offset:3072 lds\n"
"ds_read_b128 v[80:83], v4 offset:20480\n"
"v_mfma_f32_16x16x32_bf16 a[52:55], v[52:55], v[204:207], a[52:55]\n"
"s_waitcnt lgkmcnt(2)\n"
"v_mfma_f32_16x16x32_bf16 a[0:3], v[56:59], v[208:211], a[0:3]\n"
"ds_read_b128 v[84:87], v4 offset:21504\n"
"v_mfma_f32_16x16x32_bf16 a[28:31], v[56:59], v[212:215], a[28:31]\n"
"v_mfma_f32_16x16x32_bf16 a[4:7], v[60:63], v[208:211], a[4:7]\n"
"buffer_load_dwordx4 v[144:147], v1, s[36:39], s58 offen offset:2048 sc1\n"
"ds_read_b128 v[88:91], v4 offset:22528\n"
"v_mfma_f32_16x16x32_bf16 a[32:35], v[60:63], v[212:215], a[32:35]\n"
"v_mfma_f32_16x16x32_bf16 a[8:11], v[64:67], v[208:211], a[8:11]\n"
"ds_read_b128 v[92:95], v4 offset:23552\n"
"v_mfma_f32_16x16x32_bf16 a[36:39], v[64:67], v[212:215], a[36:39]\n"
"s_add_u32 m0, s61, 118784\n"
"v_mfma_f32_16x16x32_bf16 a[12:15], v[68:71], v[208:211], a[12:15]\n"
"buffer_load_dwordx4 v2, s[40:43], s60 offen offset:0 lds\n"
"ds_read_b128 v[32:35], v4 offset:24576\n"
"v_mfma_f32_16x16x32_bf16 a[40:43], v[68:71], v[212:215], a[40:43]\n"
"v_mfma_f32_16x16x32_bf16 a[16:19], v[72:75], v[208:211], a[16:19]\n"
"ds_read_b128 v[36:39], v4 offset:25600\n"
"v_mfma_f32_16x16x32_bf16 a[44:47], v[72:75], v[212:215], a[44:47]\n"
"buffer_load_dwordx4 v[148:151], v1, s[36:39], s59 offen offset:2048 sc1\n"
"s_waitcnt lgkmcnt(2)\n"
"v_mfma_f32_16x16x32_bf16 a[20:23], v[76:79], v[208:211], a[20:23]\n"
"ds_read_b128 v[40:43], v4 offset:26624\n"
"v_mfma_f32_16x16x32_bf16 a[48:51], v[76:79], v[212:215], a[48:51]\n"
"v_mfma_f32_16x16x32_bf16 a[24:27], v[80:83], v[208:211], a[24:27]\n"
"ds_read_b128 v[44:47], v4 offset:27648\n"
"v_mfma_f32_16x16x32_bf16 a[52:55], v[80:83], v[212:215], a[52:55]\n"
"buffer_load_dwordx4 v2, s[40:43], s60 offen offset:1024 lds\n"
"s_waitcnt vmcnt(12)\n"
"s_barrier\n"
"v_mfma_f32_16x16x32_bf16 a[0:3], v[84:87], v[216:219], a[0:3]\n"
"ds_read_b128 v[48:51], v4 offset:28672\n"
"v_mfma_f32_16x16x32_bf16 a[28:31], v[84:87], v[220:223], a[28:31]\n"
"v_mfma_f32_16x16x32_bf16 a[4:7], v[88:91], v[216:219], a[4:7]\n"
"ds_read_b128 v[52:55], v4 offset:29696\n"
"v_mfma_f32_16x16x32_bf16 a[32:35], v[88:91], v[220:223], a[32:35]\n"
"buffer_load_dwordx4 v[152:155], v1, s[36:39], s58 offen offset:3072 sc1\n"
"s_add_u32 s58, s58, 4096\n"
"v_mfma_f32_16x16x32_bf16 a[8:11], v[92:95], v[216:219], a[8:11]\n"
"ds_read_b128 v[56:59], v4 offset:30720\n"
"v_mfma_f32_16x16x32_bf16 a[36:39], v[92:95], v[220:223], a[36:39]\n"
"s_waitcnt lgkmcnt(2)\n"
"v_mfma_f32_16x16x32_bf16 a[12:15], v[32:35], v[216:219], a[12:15]\n"
"buffer_load_dwordx4 v2, s[40:43], s60 offen offset:2048 lds\n"
"s_add_u32 s60, s60, 114688\n"
"ds_read_b128 v[60:63], v4 offset:31744\n"
"v_mfma_f32_16x16x32_bf16 a[40:43], v[32:35], v[220:223], a[40:43]\n"
"v_mfma_f32_16x16x32_bf16 a[16:19], v[36:39], v[216:219], a[16:19]\n"
"ds_read_b128 v[64:67], v4 offset:32768\n"
"v_mfma_f32_16x16x32_bf16 a[44:47], v[36:39], v[220:223], a[44:47]\n"
"v_mfma_f32_16x16x32_bf16 a[20:23], v[40:43], v[216:219], a[20:23]\n"
"buffer_load_dwordx4 v[156:159], v1, s[36:39], s59 offen offset:3072 sc1\n"
"s_add_u32 s59, s59, 4096\n"
"ds_read_b128 v[68:71], v4 offset:33792\n"
"v_mfma_f32_16x16x32_bf16 a[48:51], v[40:43], v[220:223], a[48:51]\n"
"v_mfma_f32_16x16x32_bf16 a[24:27], v[44:47], v[216:219], a[24:27]\n"
"ds_read_b128 v[72:75], v4 offset:34816\n"
"v_mfma_f32_16x16x32_bf16 a[52:55], v[44:47], v[220:223], a[52:55]\n"
"v_mfma_f32_16x16x32_bf16 a[0:3], v[48:51], v[224:227], a[0:3]\n"
"ds_read_b128 v[76:79], v4 offset:35840\n"
"v_mfma_f32_16x16x32_bf16 a[28:31], v[48:51], v[228:231], a[28:31]\n"
"buffer_load_dwordx4 v[160:163], v1, s[36:39], s58 offen offset:0 sc1\n"
"s_waitcnt lgkmcnt(2)\n"
"v_mfma_f32_16x16x32_bf16 a[4:7], v[52:55], v[224:227], a[4:7]\n"
"ds_read_b128 v[80:83], v4 offset:36864\n"
"v_mfma_f32_16x16x32_bf16 a[32:35], v[52:55], v[228:231], a[32:35]\n"
"s_add_u32 m0, s61, 0\n"
"v_mfma_f32_16x16x32_bf16 a[8:11], v[56:59], v[224:227], a[8:11]\n"
"buffer_load_dwordx4 v1, s[40:43], s60 offen offset:0 lds\n"
"ds_read_b128 v[84:87], v4 offset:37888\n"
"v_mfma_f32_16x16x32_bf16 a[36:39], v[56:59], v[228:231], a[36:39]\n"
"v_mfma_f32_16x16x32_bf16 a[12:15], v[60:63], v[224:227], a[12:15]\n"
"ds_read_b128 v[88:91], v4 offset:38912\n"
"v_mfma_f32_16x16x32_bf16 a[40:43], v[60:63], v[228:231], a[40:43]\n"
"v_mfma_f32_16x16x32_bf16 a[16:19], v[64:67], v[224:227], a[16:19]\n"
"buffer_load_dwordx4 v[164:167], v1, s[36:39], s59 offen offset:0 sc1\n"
"ds_read_b128 v[92:95], v4 offset:39936\n"
"v_mfma_f32_16x16x32_bf16 a[44:47], v[64:67], v[228:231], a[44:47]\n"
"v_mfma_f32_16x16x32_bf16 a[20:23], v[68:71], v[224:227], a[20:23]\n"
"ds_read_b128 v[32:35], v4 offset:40960\n"
"v_mfma_f32_16x16x32_bf16 a[48:51], v[68:71], v[228:231], a[48:51]\n"
"s_waitcnt lgkmcnt(2)\n"
"v_mfma_f32_16x16x32_bf16 a[24:27], v[72:75], v[224:227], a[24:27]\n"
"buffer_load_dwordx4 v1, s[40:43], s60 offen offset:1024 lds\n"
"ds_read_b128 v[36:39], v4 offset:41984\n"
"v_mfma_f32_16x16x32_bf16 a[52:55], v[72:75], v[228:231], a[52:55]\n"
"v_mfma_f32_16x16x32_bf16 a[0:3], v[76:79], v[232:235], a[0:3]\n"
"ds_read_b128 v[40:43], v4 offset:43008\n"
"v_mfma_f32_16x16x32_bf16 a[28:31], v[76:79], v[236:239], a[28:31]\n"
"buffer_load_dwordx4 v[168:171], v1, s[36:39], s58 offen offset:1024 sc1\n"
"v_mfma_f32_16x16x32_bf16 a[4:7], v[80:83], v[232:235], a[4:7]\n"
"ds_read_b128 v[44:47], v4 offset:44032\n"
"v_mfma_f32_16x16x32_bf16 a[32:35], v[80:83], v[236:239], a[32:35]\n"
"v_mfma_f32_16x16x32_bf16 a[8:11], v[84:87], v[232:235], a[8:11]\n"
"ds_read_b128 v[48:51], v4 offset:45056\n"
"v_mfma_f32_16x16x32_bf16 a[36:39], v[84:87], v[236:239], a[36:39]\n"
"buffer_load_dwordx4 v1, s[40:43], s60 offen offset:2048 lds\n"
"v_mfma_f32_16x16x32_bf16 a[12:15], v[88:91], v[232:235], a[12:15]\n"
"ds_read_b128 v[52:55], v4 offset:46080\n"
"v_mfma_f32_16x16x32_bf16 a[40:43], v[88:91], v[236:239], a[40:43]\n"
"s_waitcnt lgkmcnt(2)\n"
"v_mfma_f32_16x16x32_bf16 a[16:19], v[92:95], v[232:235], a[16:19]\n"
"ds_read_b128 v[56:59], v4 offset:47104\n"
"v_mfma_f32_16x16x32_bf16 a[44:47], v[92:95], v[236:239], a[44:47]\n"
"buffer_load_dwordx4 v[172:175], v1, s[36:39], s59 offen offset:1024 sc1\n"
"v_mfma_f32_16x16x32_bf16 a[20:23], v[32:35], v[232:235], a[20:23]\n"
"ds_read_b128 v[60:63], v4 offset:48128\n"
"v_mfma_f32_16x16x32_bf16 a[48:51], v[32:35], v[236:239], a[48:51]\n"
"v_mfma_f32_16x16x32_bf16 a[24:27], v[36:39], v[232:235], a[24:27]\n"
"buffer_load_dwordx4 v1, s[40:43], s60 offen offset:3072 lds\n"
"ds_read_b128 v[64:67], v4 offset:49152\n"
"v_mfma_f32_16x16x32_bf16 a[52:55], v[36:39], v[236:239], a[52:55]\n"
"v_mfma_f32_16x16x32_bf16 a[0:3], v[40:43], v[240:243], a[0:3]\n"
"ds_read_b128 v[68:71], v4 offset:50176\n"
"v_mfma_f32_16x16x32_bf16 a[28:31], v[40:43], v[244:247], a[28:31]\n"
"v_mfma_f32_16x16x32_bf16 a[4:7], v[44:47], v[240:243], a[4:7]\n"
"buffer_load_dwordx4 v[176:179], v1, s[36:39], s58 offen offset:2048 sc1\n"
"ds_read_b128 v[72:75], v4 offset:51200\n"
"v_mfma_f32_16x16x32_bf16 a[32:35], v[44:47], v[244:247], a[32:35]\n"
"s_waitcnt lgkmcnt(2)\n"
"v_mfma_f32_16x16x32_bf16 a[8:11], v[48:51], v[240:243], a[8:11]\n"
"ds_read_b128 v[76:79], v4 offset:52224\n"
"v_mfma_f32_16x16x32_bf16 a[36:39], v[48:51], v[244:247], a[36:39]\n"
"s_add_u32 m0, s61, 4096\n"
"v_mfma_f32_16x16x32_bf16 a[12:15], v[52:55], v[240:243], a[12:15]\n"
"buffer_load_dwordx4 v2, s[40:43], s60 offen offset:0 lds\n"
"ds_read_b128 v[80:83], v4 offset:53248\n"
"v_mfma_f32_16x16x32_bf16 a[40:43], v[52:55], v[244:247], a[40:43]\n"
"v_mfma_f32_16x16x32_bf16 a[16:19], v[56:59], v[240:243], a[16:19]\n"
"ds_read_b128 v[84:87], v4 offset:54272\n"
"v_mfma_f32_16x16x32_bf16 a[44:47], v[56:59], v[244:247], a[44:47]\n"
"buffer_load_dwordx4 v[180:183], v1, s[36:39], s59 offen offset:2048 sc1\n"
"v_mfma_f32_16x16x32_bf16 a[20:23], v[60:63], v[240:243], a[20:23]\n"
"ds_read_b128 v[88:91], v4 offset:55296\n"
"v_mfma_f32_16x16x32_bf16 a[48:51], v[60:63], v[244:247], a[48:51]\n"
"v_mfma_f32_16x16x32_bf16 a[24:27], v[64:67], v[240:243], a[24:27]\n"
"ds_read_b128 v[92:95], v4 offset:56320\n"
"v_mfma_f32_16x16x32_bf16 a[52:55], v[64:67], v[244:247], a[52:55]\n"
"buffer_load_dwordx4 v2, s[40:43], s60 offen offset:1024 lds\n"
"s_waitcnt vmcnt(12)\n"
"s_barrier\n"
"s_waitcnt lgkmcnt(1)\n"
"v_mfma_f32_16x16x32_bf16 a[0:3], v[68:71], v[248:251], a[0:3]\n"
"ds_read_b128 v[32:35], v5 offset:0\n"
"v_mfma_f32_16x16x32_bf16 a[28:31], v[68:71], v[252:255], a[28:31]\n"
"v_mfma_f32_16x16x32_bf16 a[4:7], v[72:75], v[248:251], a[4:7]\n"
"ds_read_b128 v[36:39], v5 offset:1024\n"
"v_mfma_f32_16x16x32_bf16 a[32:35], v[72:75], v[252:255], a[32:35]\n"
"buffer_load_dwordx4 v[184:187], v1, s[36:39], s58 offen offset:3072 sc1\n"
"s_add_u32 s58, s58, 4096\n"
"v_mfma_f32_16x16x32_bf16 a[8:11], v[76:79], v[248:251], a[8:11]\n"
"ds_read_b128 v[40:43], v5 offset:2048\n"
"v_mfma_f32_16x16x32_bf16 a[36:39], v[76:79], v[252:255], a[36:39]\n"
"v_mfma_f32_16x16x32_bf16 a[12:15], v[80:83], v[248:251], a[12:15]\n"
"buffer_load_dwordx4 v2, s[40:43], s60 offen offset:2048 lds\n"
"s_add_u32 s60, s60, 114688\n"
"ds_read_b128 v[44:47], v5 offset:3072\n"
"v_mfma_f32_16x16x32_bf16 a[40:43], v[80:83], v[252:255], a[40:43]\n"
"v_mfma_f32_16x16x32_bf16 a[16:19], v[84:87], v[248:251], a[16:19]\n"
"ds_read_b128 v[48:51], v5 offset:4096\n"
"v_mfma_f32_16x16x32_bf16 a[44:47], v[84:87], v[252:255], a[44:47]\n"
"v_mfma_f32_16x16x32_bf16 a[20:23], v[88:91], v[248:251], a[20:23]\n"
"buffer_load_dwordx4 v[188:191], v1, s[36:39], s59 offen offset:3072 sc1\n"
"s_add_u32 s59, s59, 4096\n"
"ds_read_b128 v[52:55], v5 offset:5120\n"
"v_mfma_f32_16x16x32_bf16 a[48:51], v[88:91], v[252:255], a[48:51]\n"
"s_waitcnt lgkmcnt(2)\n"
"v_mfma_f32_16x16x32_bf16 a[24:27], v[92:95], v[248:251], a[24:27]\n"
"ds_read_b128 v[56:59], v5 offset:6144\n"
"v_mfma_f32_16x16x32_bf16 a[52:55], v[92:95], v[252:255], a[52:55]\n"
"v_mfma_f32_16x16x32_bf16 a[0:3], v[32:35], v[128:131], a[0:3]\n"
"ds_read_b128 v[60:63], v5 offset:7168\n"
"v_mfma_f32_16x16x32_bf16 a[28:31], v[32:35], v[132:135], a[28:31]\n"
"buffer_load_dwordx4 v[192:195], v1, s[36:39], s58 offen offset:0 sc1\n"
"v_mfma_f32_16x16x32_bf16 a[4:7], v[36:39], v[128:131], a[4:7]\n"
"ds_read_b128 v[64:67], v5 offset:8192\n"
"v_mfma_f32_16x16x32_bf16 a[32:35], v[36:39], v[132:135], a[32:35]\n"
"s_add_u32 m0, s61, 28672\n"
"v_mfma_f32_16x16x32_bf16 a[8:11], v[40:43], v[128:131], a[8:11]\n"
"buffer_load_dwordx4 v1, s[40:43], s60 offen offset:0 lds\n"
"ds_read_b128 v[68:71], v5 offset:9216\n"
"v_mfma_f32_16x16x32_bf16 a[36:39], v[40:43], v[132:135], a[36:39]\n"
"v_mfma_f32_16x16x32_bf16 a[12:15], v[44:47], v[128:131], a[12:15]\n"
"ds_read_b128 v[72:75], v5 offset:10240\n"
"v_mfma_f32_16x16x32_bf16 a[40:43], v[44:47], v[132:135], a[40:43]\n"
"s_waitcnt lgkmcnt(2)\n"
"v_mfma_f32_16x16x32_bf16 a[16:19], v[48:51], v[128:131], a[16:19]\n"
"buffer_load_dwordx4 v[196:199], v1, s[36:39], s59 offen offset:0 sc1\n"
"ds_read_b128 v[76:79], v5 offset:11264\n"
"v_mfma_f32_16x16x32_bf16 a[44:47], v[48:51], v[132:135], a[44:47]\n"
"v_mfma_f32_16x16x32_bf16 a[20:23], v[52:55], v[128:131], a[20:23]\n"
"ds_read_b128 v[80:83], v5 offset:12288\n"
"v_mfma_f32_16x16x32_bf16 a[48:51], v[52:55], v[132:135], a[48:51]\n"
"v_mfma_f32_16x16x32_bf16 a[24:27], v[56:59], v[128:131], a[24:27]\n"
"buffer_load_dwordx4 v1, s[40:43], s60 offen offset:1024 lds\n"
"ds_read_b128 v[84:87], v5 offset:13312\n"
"v_mfma_f32_16x16x32_bf16 a[52:55], v[56:59], v[132:135], a[52:55]\n"
"v_mfma_f32_16x16x32_bf16 a[0:3], v[60:63], v[136:139], a[0:3]\n"
"ds_read_b128 v[88:91], v5 offset:14336\n"
"v_mfma_f32_16x16x32_bf16 a[28:31], v[60:63], v[140:143], a[28:31]\n"
"buffer_load_dwordx4 v[200:203], v1, s[36:39], s58 offen offset:1024 sc1\n"
"v_mfma_f32_16x16x32_bf16 a[4:7], v[64:67], v[136:139], a[4:7]\n"
"ds_read_b128 v[92:95], v5 offset:15360\n"
"v_mfma_f32_16x16x32_bf16 a[32:35], v[64:67], v[140:143], a[32:35]\n"
"s_waitcnt lgkmcnt(2)\n"
"v_mfma_f32_16x16x32_bf16 a[8:11], v[68:71], v[136:139], a[8:11]\n"
"ds_read_b128 v[32:35], v5 offset:16384\n"
"v_mfma_f32_16x16x32_bf16 a[36:39], v[68:71], v[140:143], a[36:39]\n"
"buffer_load_dwordx4 v1, s[40:43], s60 offen offset:2048 lds\n"
"v_mfma_f32_16x16x32_bf16 a[12:15], v[72:75], v[136:139], a[12:15]\n"
"ds_read_b128 v[36:39], v5 offset:17408\n"
"v_mfma_f32_16x16x32_bf16 a[40:43], v[72:75], v[140:143], a[40:43]\n"
"v_mfma_f32_16x16x32_bf16 a[16:19], v[76:79], v[136:139], a[16:19]\n"
"ds_read_b128 v[40:43], v5 offset:18432\n"
"v_mfma_f32_16x16x32_bf16 a[44:47], v[76:79], v[140:143], a[44:47]\n"
"buffer_load_dwordx4 v[204:207], v1, s[36:39], s59 offen offset:1024 sc1\n"
"v_mfma_f32_16x16x32_bf16 a[20:23], v[80:83], v[136:139], a[20:23]\n"
"ds_read_b128 v[44:47], v5 offset:19456\n"
"v_mfma_f32_16x16x32_bf16 a[48:51], v[80:83], v[140:143], a[48:51]\n"
"v_mfma_f32_16x16x32_bf16 a[24:27], v[84:87], v[136:139], a[24:27]\n"
"buffer_load_dwordx4 v1, s[40:43], s60 offen offset:3072 lds\n"
"ds_read_b128 v[48:51], v5 offset:20480\n"
"v_mfma_f32_16x16x32_bf16 a[52:55], v[84:87], v[140:143], a[52:55]\n"
"s_waitcnt lgkmcnt(2)\n"
"v_mfma_f32_16x16x32_bf16 a[0:3], v[88:91], v[144:147], a[0:3]\n"
"ds_read_b128 v[52:55], v5 offset:21504\n"
"v_mfma_f32_16x16x32_bf16 a[28:31], v[88:91], v[148:151], a[28:31]\n"
"v_mfma_f32_16x16x32_bf16 a[4:7], v[92:95], v[144:147], a[4:7]\n"
"buffer_load_dwordx4 v[208:211], v1, s[36:39], s58 offen offset:2048 sc1\n"
"ds_read_b128 v[56:59], v5 offset:22528\n"
"v_mfma_f32_16x16x32_bf16 a[32:35], v[92:95], v[148:151], a[32:35]\n"
"v_mfma_f32_16x16x32_bf16 a[8:11], v[32:35], v[144:147], a[8:11]\n"
"ds_read_b128 v[60:63], v5 offset:23552\n"
"v_mfma_f32_16x16x32_bf16 a[36:39], v[32:35], v[148:151], a[36:39]\n"
"s_add_u32 m0, s61, 32768\n"
"v_mfma_f32_16x16x32_bf16 a[12:15], v[36:39], v[144:147], a[12:15]\n"
"buffer_load_dwordx4 v2, s[40:43], s60 offen offset:0 lds\n"
"ds_read_b128 v[64:67], v5 offset:24576\n"
"v_mfma_f32_16x16x32_bf16 a[40:43], v[36:39], v[148:151], a[40:43]\n"
"v_mfma_f32_16x16x32_bf16 a[16:19], v[40:43], v[144:147], a[16:19]\n"
"ds_read_b128 v[68:71], v5 offset:25600\n"
"v_mfma_f32_16x16x32_bf16 a[44:47], v[40:43], v[148:151], a[44:47]\n"
"buffer_load_dwordx4 v[212:215], v1, s[36:39], s59 offen offset:2048 sc1\n"
"s_waitcnt lgkmcnt(2)\n"
"v_mfma_f32_16x16x32_bf16 a[20:23], v[44:47], v[144:147], a[20:23]\n"
"ds_read_b128 v[72:75], v5 offset:26624\n"
"v_mfma_f32_16x16x32_bf16 a[48:51], v[44:47], v[148:151], a[48:51]\n"
"v_mfma_f32_16x16x32_bf16 a[24:27], v[48:51], v[144:147], a[24:27]\n"
"ds_read_b128 v[76:79], v5 offset:27648\n"
"v_mfma_f32_16x16x32_bf16 a[52:55], v[48:51], v[148:151], a[52:55]\n"
"buffer_load_dwordx4 v2, s[40:43], s60 offen offset:1024 lds\n"
"s_waitcnt vmcnt(12)\n"
"s_barrier\n"
"v_mfma_f32_16x16x32_bf16 a[0:3], v[52:55], v[152:155], a[0:3]\n"
"ds_read_b128 v[80:83], v3 offset:0\n"
"v_mfma_f32_16x16x32_bf16 a[28:31], v[52:55], v[156:159], a[28:31]\n"
"v_mfma_f32_16x16x32_bf16 a[4:7], v[56:59], v[152:155], a[4:7]\n"
"ds_read_b128 v[84:87], v3 offset:1024\n"
"v_mfma_f32_16x16x32_bf16 a[32:35], v[56:59], v[156:159], a[32:35]\n"
"buffer_load_dwordx4 v[216:219], v1, s[36:39], s58 offen offset:3072 sc1\n"
"s_add_u32 s58, s58, 4096\n"
"v_mfma_f32_16x16x32_bf16 a[8:11], v[60:63], v[152:155], a[8:11]\n"
"ds_read_b128 v[88:91], v3 offset:2048\n"
"v_mfma_f32_16x16x32_bf16 a[36:39], v[60:63], v[156:159], a[36:39]\n"
"s_waitcnt lgkmcnt(2)\n"
"v_mfma_f32_16x16x32_bf16 a[12:15], v[64:67], v[152:155], a[12:15]\n"
"buffer_load_dwordx4 v2, s[40:43], s60 offen offset:2048 lds\n"
"s_add_u32 s60, s60, 114688\n"
"ds_read_b128 v[92:95], v3 offset:3072\n"
"v_mfma_f32_16x16x32_bf16 a[40:43], v[64:67], v[156:159], a[40:43]\n"
"v_mfma_f32_16x16x32_bf16 a[16:19], v[68:71], v[152:155], a[16:19]\n"
"ds_read_b128 v[32:35], v3 offset:4096\n"
"v_mfma_f32_16x16x32_bf16 a[44:47], v[68:71], v[156:159], a[44:47]\n"
"v_mfma_f32_16x16x32_bf16 a[20:23], v[72:75], v[152:155], a[20:23]\n"
"buffer_load_dwordx4 v[220:223], v1, s[36:39], s59 offen offset:3072 sc1\n"
"s_add_u32 s59, s59, 4096\n"
"ds_read_b128 v[36:39], v3 offset:5120\n"
"v_mfma_f32_16x16x32_bf16 a[48:51], v[72:75], v[156:159], a[48:51]\n"
"v_mfma_f32_16x16x32_bf16 a[24:27], v[76:79], v[152:155], a[24:27]\n"
"ds_read_b128 v[40:43], v3 offset:6144\n"
"v_mfma_f32_16x16x32_bf16 a[52:55], v[76:79], v[156:159], a[52:55]\n"
"v_mfma_f32_16x16x32_bf16 a[0:3], v[80:83], v[160:163], a[0:3]\n"
"ds_read_b128 v[44:47], v3 offset:7168\n"
"v_mfma_f32_16x16x32_bf16 a[28:31], v[80:83], v[164:167], a[28:31]\n"
"buffer_load_dwordx4 v[224:227], v1, s[36:39], s58 offen offset:0 sc1\n"
"s_waitcnt lgkmcnt(2)\n"
"v_mfma_f32_16x16x32_bf16 a[4:7], v[84:87], v[160:163], a[4:7]\n"
"ds_read_b128 v[48:51], v3 offset:8192\n"
"v_mfma_f32_16x16x32_bf16 a[32:35], v[84:87], v[164:167], a[32:35]\n"
"s_add_u32 m0, s61, 57344\n"
"v_mfma_f32_16x16x32_bf16 a[8:11], v[88:91], v[160:163], a[8:11]\n"
"buffer_load_dwordx4 v1, s[40:43], s60 offen offset:0 lds\n"
"ds_read_b128 v[52:55], v3 offset:9216\n"
"v_mfma_f32_16x16x32_bf16 a[36:39], v[88:91], v[164:167], a[36:39]\n"
"v_mfma_f32_16x16x32_bf16 a[12:15], v[92:95], v[160:163], a[12:15]\n"
"ds_read_b128 v[56:59], v3 offset:10240\n"
"v_mfma_f32_16x16x32_bf16 a[40:43], v[92:95], v[164:167], a[40:43]\n"
"v_mfma_f32_16x16x32_bf16 a[16:19], v[32:35], v[160:163], a[16:19]\n"
"buffer_load_dwordx4 v[228:231], v1, s[36:39], s59 offen offset:0 sc1\n"
"ds_read_b128 v[60:63], v3 offset:11264\n"
"v_mfma_f32_16x16x32_bf16 a[44:47], v[32:35], v[164:167], a[44:47]\n"
"v_mfma_f32_16x16x32_bf16 a[20:23], v[36:39], v[160:163], a[20:23]\n"
"ds_read_b128 v[64:67], v3 offset:12288\n"
"v_mfma_f32_16x16x32_bf16 a[48:51], v[36:39], v[164:167], a[48:51]\n"
"s_waitcnt lgkmcnt(2)\n"
"v_mfma_f32_16x16x32_bf16 a[24:27], v[40:43], v[160:163], a[24:27]\n"
"buffer_load_dwordx4 v1, s[40:43], s60 offen offset:1024 lds\n"
"ds_read_b128 v[68:71], v3 offset:13312\n"
"v_mfma_f32_16x16x32_bf16 a[52:55], v[40:43], v[164:167], a[52:55]\n"
"v_mfma_f32_16x16x32_bf16 a[0:3], v[44:47], v[168:171], a[0:3]\n"
"ds_read_b128 v[72:75], v3 offset:14336\n"
"v_mfma_f32_16x16x32_bf16 a[28:31], v[44:47], v[172:175], a[28:31]\n"
"buffer_load_dwordx4 v[232:235], v1, s[36:39], s58 offen offset:1024 sc1\n"
"v_mfma_f32_16x16x32_bf16 a[4:7], v[48:51], v[168:171], a[4:7]\n"
"ds_read_b128 v[76:79], v3 offset:15360\n"
"v_mfma_f32_16x16x32_bf16 a[32:35], v[48:51], v[172:175], a[32:35]\n"
"v_mfma_f32_16x16x32_bf16 a[8:11], v[52:55], v[168:171], a[8:11]\n"
"ds_read_b128 v[80:83], v3 offset:16384\n"
"v_mfma_f32_16x16x32_bf16 a[36:39], v[52:55], v[172:175], a[36:39]\n"
"buffer_load_dwordx4 v1, s[40:43], s60 offen offset:2048 lds\n"
"v_mfma_f32_16x16x32_bf16 a[12:15], v[56:59], v[168:171], a[12:15]\n"
"ds_read_b128 v[84:87], v3 offset:17408\n"
"v_mfma_f32_16x16x32_bf16 a[40:43], v[56:59], v[172:175], a[40:43]\n"
"s_waitcnt lgkmcnt(2)\n"
"v_mfma_f32_16x16x32_bf16 a[16:19], v[60:63], v[168:171], a[16:19]\n"
"ds_read_b128 v[88:91], v3 offset:18432\n"
"v_mfma_f32_16x16x32_bf16 a[44:47], v[60:63], v[172:175], a[44:47]\n"
"buffer_load_dwordx4 v[236:239], v1, s[36:39], s59 offen offset:1024 sc1\n"
"v_mfma_f32_16x16x32_bf16 a[20:23], v[64:67], v[168:171], a[20:23]\n"
"ds_read_b128 v[92:95], v3 offset:19456\n"
"v_mfma_f32_16x16x32_bf16 a[48:51], v[64:67], v[172:175], a[48:51]\n"
"v_mfma_f32_16x16x32_bf16 a[24:27], v[68:71], v[168:171], a[24:27]\n"
"buffer_load_dwordx4 v1, s[40:43], s60 offen offset:3072 lds\n"
"ds_read_b128 v[32:35], v3 offset:20480\n"
"v_mfma_f32_16x16x32_bf16 a[52:55], v[68:71], v[172:175], a[52:55]\n"
"v_mfma_f32_16x16x32_bf16 a[0:3], v[72:75], v[176:179], a[0:3]\n"
"ds_read_b128 v[36:39], v3 offset:21504\n"
"v_mfma_f32_16x16x32_bf16 a[28:31], v[72:75], v[180:183], a[28:31]\n"
"v_mfma_f32_16x16x32_bf16 a[4:7], v[76:79], v[176:179], a[4:7]\n"
"buffer_load_dwordx4 v[240:243], v1, s[36:39], s58 offen offset:2048 sc1\n"
"ds_read_b128 v[40:43], v3 offset:22528\n"
"v_mfma_f32_16x16x32_bf16 a[32:35], v[76:79], v[180:183], a[32:35]\n"
"s_waitcnt lgkmcnt(2)\n"
"v_mfma_f32_16x16x32_bf16 a[8:11], v[80:83], v[176:179], a[8:11]\n"
"ds_read_b128 v[44:47], v3 offset:23552\n"
"v_mfma_f32_16x16x32_bf16 a[36:39], v[80:83], v[180:183], a[36:39]\n"
"s_add_u32 m0, s61, 61440\n"
"v_mfma_f32_16x16x32_bf16 a[12:15], v[84:87], v[176:179], a[12:15]\n"
"buffer_load_dwordx4 v2, s[40:43], s60 offen offset:0 lds\n"
"ds_read_b128 v[48:51], v3 offset:24576\n"
"v_mfma_f32_16x16x32_bf16 a[40:43], v[84:87], v[180:183], a[40:43]\n"
"v_mfma_f32_16x16x32_bf16 a[16:19], v[88:91], v[176:179], a[16:19]\n"
"ds_read_b128 v[52:55], v3 offset:25600\n"
"v_mfma_f32_16x16x32_bf16 a[44:47], v[88:91], v[180:183], a[44:47]\n"
"buffer_load_dwordx4 v[244:247], v1, s[36:39], s59 offen offset:2048 sc1\n"
"v_mfma_f32_16x16x32_bf16 a[20:23], v[92:95], v[176:179], a[20:23]\n"
"ds_read_b128 v[56:59], v3 offset:26624\n"
"v_mfma_f32_16x16x32_bf16 a[48:51], v[92:95], v[180:183], a[48:51]\n"
"v_mfma_f32_16x16x32_bf16 a[24:27], v[32:35], v[176:179], a[24:27]\n"
"ds_read_b128 v[60:63], v3 offset:27648\n"
"v_mfma_f32_16x16x32_bf16 a[52:55], v[32:35], v[180:183], a[52:55]\n"
"buffer_load_dwordx4 v2, s[40:43], s60 offen offset:1024 lds\n"
"s_waitcnt vmcnt(12)\n"
"s_barrier\n"
"s_waitcnt lgkmcnt(1)\n"
"v_mfma_f32_16x16x32_bf16 a[0:3], v[36:39], v[184:187], a[0:3]\n"
"ds_read_b128 v[64:67], v3 offset:28672\n"
"v_mfma_f32_16x16x32_bf16 a[28:31], v[36:39], v[188:191], a[28:31]\n"
"v_mfma_f32_16x16x32_bf16 a[4:7], v[40:43], v[184:187], a[4:7]\n"
"ds_read_b128 v[68:71], v3 offset:29696\n"
"v_mfma_f32_16x16x32_bf16 a[32:35], v[40:43], v[188:191], a[32:35]\n"
"buffer_load_dwordx4 v[248:251], v1, s[36:39], s58 offen offset:3072 sc1\n"
"s_add_u32 s58, s58, 4096\n"
"v_mfma_f32_16x16x32_bf16 a[8:11], v[44:47], v[184:187], a[8:11]\n"
"ds_read_b128 v[72:75], v3 offset:30720\n"
"v_mfma_f32_16x16x32_bf16 a[36:39], v[44:47], v[188:191], a[36:39]\n"
"v_mfma_f32_16x16x32_bf16 a[12:15], v[48:51], v[184:187], a[12:15]\n"
"buffer_load_dwordx4 v2, s[40:43], s60 offen offset:2048 lds\n"
"s_add_u32 s60, s60, 114688\n"
"ds_read_b128 v[76:79], v3 offset:31744\n"
"v_mfma_f32_16x16x32_bf16 a[40:43], v[48:51], v[188:191], a[40:43]\n"
"v_mfma_f32_16x16x32_bf16 a[16:19], v[52:55], v[184:187], a[16:19]\n"
"ds_read_b128 v[80:83], v3 offset:32768\n"
"v_mfma_f32_16x16x32_bf16 a[44:47], v[52:55], v[188:191], a[44:47]\n"
"v_mfma_f32_16x16x32_bf16 a[20:23], v[56:59], v[184:187], a[20:23]\n"
"buffer_load_dwordx4 v[252:255], v1, s[36:39], s59 offen offset:3072 sc1\n"
"s_add_u32 s59, s59, 4096\n"
"ds_read_b128 v[84:87], v3 offset:33792\n"
"v_mfma_f32_16x16x32_bf16 a[48:51], v[56:59], v[188:191], a[48:51]\n"
"s_waitcnt lgkmcnt(2)\n"
"v_mfma_f32_16x16x32_bf16 a[24:27], v[60:63], v[184:187], a[24:27]\n"
"ds_read_b128 v[88:91], v3 offset:34816\n"
"v_mfma_f32_16x16x32_bf16 a[52:55], v[60:63], v[188:191], a[52:55]\n"
"v_mfma_f32_16x16x32_bf16 a[0:3], v[64:67], v[192:195], a[0:3]\n"
"ds_read_b128 v[92:95], v3 offset:35840\n"
"v_mfma_f32_16x16x32_bf16 a[28:31], v[64:67], v[196:199], a[28:31]\n"
"buffer_load_dwordx4 v[128:131], v1, s[36:39], s58 offen offset:0 sc1\n"
"v_mfma_f32_16x16x32_bf16 a[4:7], v[68:71], v[192:195], a[4:7]\n"
"ds_read_b128 v[32:35], v3 offset:36864\n"
"v_mfma_f32_16x16x32_bf16 a[32:35], v[68:71], v[196:199], a[32:35]\n"
"s_add_u32 m0, s61, 86016\n"
"v_mfma_f32_16x16x32_bf16 a[8:11], v[72:75], v[192:195], a[8:11]\n"
"buffer_load_dwordx4 v1, s[40:43], s60 offen offset:0 lds\n"
"ds_read_b128 v[36:39], v3 offset:37888\n"
"v_mfma_f32_16x16x32_bf16 a[36:39], v[72:75], v[196:199], a[36:39]\n"
"v_mfma_f32_16x16x32_bf16 a[12:15], v[76:79], v[192:195], a[12:15]\n"
"ds_read_b128 v[40:43], v3 offset:38912\n"
"v_mfma_f32_16x16x32_bf16 a[40:43], v[76:79], v[196:199], a[40:43]\n"
"s_waitcnt lgkmcnt(2)\n"
"v_mfma_f32_16x16x32_bf16 a[16:19], v[80:83], v[192:195], a[16:19]\n"
"buffer_load_dwordx4 v[132:135], v1, s[36:39], s59 offen offset:0 sc1\n"
"ds_read_b128 v[44:47], v3 offset:39936\n"
"v_mfma_f32_16x16x32_bf16 a[44:47], v[80:83], v[196:199], a[44:47]\n"
"v_mfma_f32_16x16x32_bf16 a[20:23], v[84:87], v[192:195], a[20:23]\n"
"ds_read_b128 v[48:51], v3 offset:40960\n"
"v_mfma_f32_16x16x32_bf16 a[48:51], v[84:87], v[196:199], a[48:51]\n"
"v_mfma_f32_16x16x32_bf16 a[24:27], v[88:91], v[192:195], a[24:27]\n"
"buffer_load_dwordx4 v1, s[40:43], s60 offen offset:1024 lds\n"
"ds_read_b128 v[52:55], v3 offset:41984\n"
"v_mfma_f32_16x16x32_bf16 a[52:55], v[88:91], v[196:199], a[52:55]\n"
"v_mfma_f32_16x16x32_bf16 a[0:3], v[92:95], v[200:203], a[0:3]\n"
"ds_read_b128 v[56:59], v3 offset:43008\n"
"v_mfma_f32_16x16x32_bf16 a[28:31], v[92:95], v[204:207], a[28:31]\n"
"buffer_load_dwordx4 v[136:139], v1, s[36:39], s58 offen offset:1024 sc1\n"
"v_mfma_f32_16x16x32_bf16 a[4:7], v[32:35], v[200:203], a[4:7]\n"
"ds_read_b128 v[60:63], v3 offset:44032\n"
"v_mfma_f32_16x16x32_bf16 a[32:35], v[32:35], v[204:207], a[32:35]\n"
"s_waitcnt lgkmcnt(2)\n"
"v_mfma_f32_16x16x32_bf16 a[8:11], v[36:39], v[200:203], a[8:11]\n"
"ds_read_b128 v[64:67], v3 offset:45056\n"
"v_mfma_f32_16x16x32_bf16 a[36:39], v[36:39], v[204:207], a[36:39]\n"
"buffer_load_dwordx4 v1, s[40:43], s60 offen offset:2048 lds\n"
"v_mfma_f32_16x16x32_bf16 a[12:15], v[40:43], v[200:203], a[12:15]\n"
"ds_read_b128 v[68:71], v3 offset:46080\n"
"v_mfma_f32_16x16x32_bf16 a[40:43], v[40:43], v[204:207], a[40:43]\n"
"v_mfma_f32_16x16x32_bf16 a[16:19], v[44:47], v[200:203], a[16:19]\n"
"ds_read_b128 v[72:75], v3 offset:47104\n"
"v_mfma_f32_16x16x32_bf16 a[44:47], v[44:47], v[204:207], a[44:47]\n"
"buffer_load_dwordx4 v[140:143], v1, s[36:39], s59 offen offset:1024 sc1\n"
"v_mfma_f32_16x16x32_bf16 a[20:23], v[48:51], v[200:203], a[20:23]\n"
"ds_read_b128 v[76:79], v3 offset:48128\n"
"v_mfma_f32_16x16x32_bf16 a[48:51], v[48:51], v[204:207], a[48:51]\n"
"v_mfma_f32_16x16x32_bf16 a[24:27], v[52:55], v[200:203], a[24:27]\n"
"buffer_load_dwordx4 v1, s[40:43], s60 offen offset:3072 lds\n"
"ds_read_b128 v[80:83], v3 offset:49152\n"
"v_mfma_f32_16x16x32_bf16 a[52:55], v[52:55], v[204:207], a[52:55]\n"
"s_waitcnt lgkmcnt(2)\n"
"v_mfma_f32_16x16x32_bf16 a[0:3], v[56:59], v[208:211], a[0:3]\n"
"ds_read_b128 v[84:87], v3 offset:50176\n"
"v_mfma_f32_16x16x32_bf16 a[28:31], v[56:59], v[212:215], a[28:31]\n"
"v_mfma_f32_16x16x32_bf16 a[4:7], v[60:63], v[208:211], a[4:7]\n"
"buffer_load_dwordx4 v[144:147], v1, s[36:39], s58 offen offset:2048 sc1\n"
"ds_read_b128 v[88:91], v3 offset:51200\n"
"v_mfma_f32_16x16x32_bf16 a[32:35], v[60:63], v[212:215], a[32:35]\n"
"v_mfma_f32_16x16x32_bf16 a[8:11], v[64:67], v[208:211], a[8:11]\n"
"ds_read_b128 v[92:95], v3 offset:52224\n"
"v_mfma_f32_16x16x32_bf16 a[36:39], v[64:67], v[212:215], a[36:39]\n"
"s_add_u32 m0, s61, 90112\n"
"v_mfma_f32_16x16x32_bf16 a[12:15], v[68:71], v[208:211], a[12:15]\n"
"buffer_load_dwordx4 v2, s[40:43], s60 offen offset:0 lds\n"
"ds_read_b128 v[32:35], v3 offset:53248\n"
"v_mfma_f32_16x16x32_bf16 a[40:43], v[68:71], v[212:215], a[40:43]\n"
"v_mfma_f32_16x16x32_bf16 a[16:19], v[72:75], v[208:211], a[16:19]\n"
"ds_read_b128 v[36:39], v3 offset:54272\n"
"v_mfma_f32_16x16x32_bf16 a[44:47], v[72:75], v[212:215], a[44:47]\n"
"buffer_load_dwordx4 v[148:151], v1, s[36:39], s59 offen offset:2048 sc1\n"
"s_waitcnt lgkmcnt(2)\n"
"v_mfma_f32_16x16x32_bf16 a[20:23], v[76:79], v[208:211], a[20:23]\n"
"ds_read_b128 v[40:43], v3 offset:55296\n"
"v_mfma_f32_16x16x32_bf16 a[48:51], v[76:79], v[212:215], a[48:51]\n"
"v_mfma_f32_16x16x32_bf16 a[24:27], v[80:83], v[208:211], a[24:27]\n"
"ds_read_b128 v[44:47], v3 offset:56320\n"
"v_mfma_f32_16x16x32_bf16 a[52:55], v[80:83], v[212:215], a[52:55]\n"
"buffer_load_dwordx4 v2, s[40:43], s60 offen offset:1024 lds\n"
"s_waitcnt vmcnt(12)\n"
"s_barrier\n"
"v_mfma_f32_16x16x32_bf16 a[0:3], v[84:87], v[216:219], a[0:3]\n"
"ds_read_b128 v[48:51], v4 offset:0\n"
"v_mfma_f32_16x16x32_bf16 a[28:31], v[84:87], v[220:223], a[28:31]\n"
"v_mfma_f32_16x16x32_bf16 a[4:7], v[88:91], v[216:219], a[4:7]\n"
"ds_read_b128 v[52:55], v4 offset:1024\n"
"v_mfma_f32_16x16x32_bf16 a[32:35], v[88:91], v[220:223], a[32:35]\n"
"buffer_load_dwordx4 v[152:155], v1, s[36:39], s58 offen offset:3072 sc1\n"
"s_add_u32 s58, s58, 4096\n"
"v_mfma_f32_16x16x32_bf16 a[8:11], v[92:95], v[216:219], a[8:11]\n"
"ds_read_b128 v[56:59], v4 offset:2048\n"
"v_mfma_f32_16x16x32_bf16 a[36:39], v[92:95], v[220:223], a[36:39]\n"
"s_waitcnt lgkmcnt(2)\n"
"v_mfma_f32_16x16x32_bf16 a[12:15], v[32:35], v[216:219], a[12:15]\n"
"buffer_load_dwordx4 v2, s[40:43], s60 offen offset:2048 lds\n"
"s_add_u32 s60, s60, 114688\n"
"ds_read_b128 v[60:63], v4 offset:3072\n"
"v_mfma_f32_16x16x32_bf16 a[40:43], v[32:35], v[220:223], a[40:43]\n"
"v_mfma_f32_16x16x32_bf16 a[16:19], v[36:39], v[216:219], a[16:19]\n"
"ds_read_b128 v[64:67], v4 offset:4096\n"
"v_mfma_f32_16x16x32_bf16 a[44:47], v[36:39], v[220:223], a[44:47]\n"
"v_mfma_f32_16x16x32_bf16 a[20:23], v[40:43], v[216:219], a[20:23]\n"
"buffer_load_dwordx4 v[156:159], v1, s[36:39], s59 offen offset:3072 sc1\n"
"s_add_u32 s59, s59, 4096\n"
"ds_read_b128 v[68:71], v4 offset:5120\n"
"v_mfma_f32_16x16x32_bf16 a[48:51], v[40:43], v[220:223], a[48:51]\n"
"v_mfma_f32_16x16x32_bf16 a[24:27], v[44:47], v[216:219], a[24:27]\n"
"ds_read_b128 v[72:75], v4 offset:6144\n"
"v_mfma_f32_16x16x32_bf16 a[52:55], v[44:47], v[220:223], a[52:55]\n"
"v_mfma_f32_16x16x32_bf16 a[0:3], v[48:51], v[224:227], a[0:3]\n"
"ds_read_b128 v[76:79], v4 offset:7168\n"
"v_mfma_f32_16x16x32_bf16 a[28:31], v[48:51], v[228:231], a[28:31]\n"
"buffer_load_dwordx4 v[160:163], v1, s[36:39], s58 offen offset:0 sc1\n"
"s_waitcnt lgkmcnt(2)\n"
"v_mfma_f32_16x16x32_bf16 a[4:7], v[52:55], v[224:227], a[4:7]\n"
"ds_read_b128 v[80:83], v4 offset:8192\n"
"v_mfma_f32_16x16x32_bf16 a[32:35], v[52:55], v[228:231], a[32:35]\n"
"s_add_u32 m0, s61, 114688\n"
"v_mfma_f32_16x16x32_bf16 a[8:11], v[56:59], v[224:227], a[8:11]\n"
"buffer_load_dwordx4 v1, s[40:43], s60 offen offset:0 lds\n"
"ds_read_b128 v[84:87], v4 offset:9216\n"
"v_mfma_f32_16x16x32_bf16 a[36:39], v[56:59], v[228:231], a[36:39]\n"
"v_mfma_f32_16x16x32_bf16 a[12:15], v[60:63], v[224:227], a[12:15]\n"
"ds_read_b128 v[88:91], v4 offset:10240\n"
"v_mfma_f32_16x16x32_bf16 a[40:43], v[60:63], v[228:231], a[40:43]\n"
"v_mfma_f32_16x16x32_bf16 a[16:19], v[64:67], v[224:227], a[16:19]\n"
"buffer_load_dwordx4 v[164:167], v1, s[36:39], s59 offen offset:0 sc1\n"
"ds_read_b128 v[92:95], v4 offset:11264\n"
"v_mfma_f32_16x16x32_bf16 a[44:47], v[64:67], v[228:231], a[44:47]\n"
"v_mfma_f32_16x16x32_bf16 a[20:23], v[68:71], v[224:227], a[20:23]\n"
"ds_read_b128 v[32:35], v4 offset:12288\n"
"v_mfma_f32_16x16x32_bf16 a[48:51], v[68:71], v[228:231], a[48:51]\n"
"s_waitcnt lgkmcnt(2)\n"
"v_mfma_f32_16x16x32_bf16 a[24:27], v[72:75], v[224:227], a[24:27]\n"
"buffer_load_dwordx4 v1, s[40:43], s60 offen offset:1024 lds\n"
"ds_read_b128 v[36:39], v4 offset:13312\n"
"v_mfma_f32_16x16x32_bf16 a[52:55], v[72:75], v[228:231], a[52:55]\n"
"v_mfma_f32_16x16x32_bf16 a[0:3], v[76:79], v[232:235], a[0:3]\n"
"ds_read_b128 v[40:43], v4 offset:14336\n"
"v_mfma_f32_16x16x32_bf16 a[28:31], v[76:79], v[236:239], a[28:31]\n"
"buffer_load_dwordx4 v[168:171], v1, s[36:39], s58 offen offset:1024 sc1\n"
"v_mfma_f32_16x16x32_bf16 a[4:7], v[80:83], v[232:235], a[4:7]\n"
"ds_read_b128 v[44:47], v4 offset:15360\n"
"v_mfma_f32_16x16x32_bf16 a[32:35], v[80:83], v[236:239], a[32:35]\n"
"v_mfma_f32_16x16x32_bf16 a[8:11], v[84:87], v[232:235], a[8:11]\n"
"ds_read_b128 v[48:51], v4 offset:16384\n"
"v_mfma_f32_16x16x32_bf16 a[36:39], v[84:87], v[236:239], a[36:39]\n"
"buffer_load_dwordx4 v1, s[40:43], s60 offen offset:2048 lds\n"
"v_mfma_f32_16x16x32_bf16 a[12:15], v[88:91], v[232:235], a[12:15]\n"
"ds_read_b128 v[52:55], v4 offset:17408\n"
"v_mfma_f32_16x16x32_bf16 a[40:43], v[88:91], v[236:239], a[40:43]\n"
"s_waitcnt lgkmcnt(2)\n"
"v_mfma_f32_16x16x32_bf16 a[16:19], v[92:95], v[232:235], a[16:19]\n"
"ds_read_b128 v[56:59], v4 offset:18432\n"
"v_mfma_f32_16x16x32_bf16 a[44:47], v[92:95], v[236:239], a[44:47]\n"
"buffer_load_dwordx4 v[172:175], v1, s[36:39], s59 offen offset:1024 sc1\n"
"v_mfma_f32_16x16x32_bf16 a[20:23], v[32:35], v[232:235], a[20:23]\n"
"ds_read_b128 v[60:63], v4 offset:19456\n"
"v_mfma_f32_16x16x32_bf16 a[48:51], v[32:35], v[236:239], a[48:51]\n"
"v_mfma_f32_16x16x32_bf16 a[24:27], v[36:39], v[232:235], a[24:27]\n"
"buffer_load_dwordx4 v1, s[40:43], s60 offen offset:3072 lds\n"
"ds_read_b128 v[64:67], v4 offset:20480\n"
"v_mfma_f32_16x16x32_bf16 a[52:55], v[36:39], v[236:239], a[52:55]\n"
"v_mfma_f32_16x16x32_bf16 a[0:3], v[40:43], v[240:243], a[0:3]\n"
"ds_read_b128 v[68:71], v4 offset:21504\n"
"v_mfma_f32_16x16x32_bf16 a[28:31], v[40:43], v[244:247], a[28:31]\n"
"v_mfma_f32_16x16x32_bf16 a[4:7], v[44:47], v[240:243], a[4:7]\n"
"buffer_load_dwordx4 v[176:179], v1, s[36:39], s58 offen offset:2048 sc1\n"
"ds_read_b128 v[72:75], v4 offset:22528\n"
"v_mfma_f32_16x16x32_bf16 a[32:35], v[44:47], v[244:247], a[32:35]\n"
"s_waitcnt lgkmcnt(2)\n"
"v_mfma_f32_16x16x32_bf16 a[8:11], v[48:51], v[240:243], a[8:11]\n"
"ds_read_b128 v[76:79], v4 offset:23552\n"
"v_mfma_f32_16x16x32_bf16 a[36:39], v[48:51], v[244:247], a[36:39]\n"
"s_add_u32 m0, s61, 118784\n"
"v_mfma_f32_16x16x32_bf16 a[12:15], v[52:55], v[240:243], a[12:15]\n"
"buffer_load_dwordx4 v2, s[40:43], s60 offen offset:0 lds\n"
"ds_read_b128 v[80:83], v4 offset:24576\n"
"v_mfma_f32_16x16x32_bf16 a[40:43], v[52:55], v[244:247], a[40:43]\n"
"v_mfma_f32_16x16x32_bf16 a[16:19], v[56:59], v[240:243], a[16:19]\n"
"ds_read_b128 v[84:87], v4 offset:25600\n"
"v_mfma_f32_16x16x32_bf16 a[44:47], v[56:59], v[244:247], a[44:47]\n"
"buffer_load_dwordx4 v[180:183], v1, s[36:39], s59 offen offset:2048 sc1\n"
"v_mfma_f32_16x16x32_bf16 a[20:23], v[60:63], v[240:243], a[20:23]\n"
"ds_read_b128 v[88:91], v4 offset:26624\n"
"v_mfma_f32_16x16x32_bf16 a[48:51], v[60:63], v[244:247], a[48:51]\n"
"v_mfma_f32_16x16x32_bf16 a[24:27], v[64:67], v[240:243], a[24:27]\n"
"ds_read_b128 v[92:95], v4 offset:27648\n"
"v_mfma_f32_16x16x32_bf16 a[52:55], v[64:67], v[244:247], a[52:55]\n"
"buffer_load_dwordx4 v2, s[40:43], s60 offen offset:1024 lds\n"
"s_waitcnt vmcnt(12)\n"
"s_barrier\n"
"s_waitcnt lgkmcnt(1)\n"
"v_mfma_f32_16x16x32_bf16 a[0:3], v[68:71], v[248:251], a[0:3]\n"
"ds_read_b128 v[32:35], v4 offset:28672\n"
"v_mfma_f32_16x16x32_bf16 a[28:31], v[68:71], v[252:255], a[28:31]\n"
"v_mfma_f32_16x16x32_bf16 a[4:7], v[72:75], v[248:251], a[4:7]\n"
"ds_read_b128 v[36:39], v4 offset:29696\n"
"v_mfma_f32_16x16x32_bf16 a[32:35], v[72:75], v[252:255], a[32:35]\n"
"buffer_load_dwordx4 v[184:187], v1, s[36:39], s58 offen offset:3072 sc1\n"
"s_add_u32 s58, s58, 4096\n"
"v_mfma_f32_16x16x32_bf16 a[8:11], v[76:79], v[248:251], a[8:11]\n"
"ds_read_b128 v[40:43], v4 offset:30720\n"
"v_mfma_f32_16x16x32_bf16 a[36:39], v[76:79], v[252:255], a[36:39]\n"
"v_mfma_f32_16x16x32_bf16 a[12:15], v[80:83], v[248:251], a[12:15]\n"
"buffer_load_dwordx4 v2, s[40:43], s60 offen offset:2048 lds\n"
"s_add_u32 s60, s60, 114688\n"
"ds_read_b128 v[44:47], v4 offset:31744\n"
"v_mfma_f32_16x16x32_bf16 a[40:43], v[80:83], v[252:255], a[40:43]\n"
"v_mfma_f32_16x16x32_bf16 a[16:19], v[84:87], v[248:251], a[16:19]\n"
"ds_read_b128 v[48:51], v4 offset:32768\n"
"v_mfma_f32_16x16x32_bf16 a[44:47], v[84:87], v[252:255], a[44:47]\n"
"v_mfma_f32_16x16x32_bf16 a[20:23], v[88:91], v[248:251], a[20:23]\n"
"buffer_load_dwordx4 v[188:191], v1, s[36:39], s59 offen offset:3072 sc1\n"
"s_add_u32 s59, s59, 4096\n"
"ds_read_b128 v[52:55], v4 offset:33792\n"
"v_mfma_f32_16x16x32_bf16 a[48:51], v[88:91], v[252:255], a[48:51]\n"
"s_waitcnt lgkmcnt(2)\n"
"v_mfma_f32_16x16x32_bf16 a[24:27], v[92:95], v[248:251], a[24:27]\n"
"ds_read_b128 v[56:59], v4 offset:34816\n"
"v_mfma_f32_16x16x32_bf16 a[52:55], v[92:95], v[252:255], a[52:55]\n"
"v_mfma_f32_16x16x32_bf16 a[0:3], v[32:35], v[128:131], a[0:3]\n"
"ds_read_b128 v[60:63], v4 offset:35840\n"
"v_mfma_f32_16x16x32_bf16 a[28:31], v[32:35], v[132:135], a[28:31]\n"
"buffer_load_dwordx4 v[192:195], v1, s[36:39], s58 offen offset:0 sc1\n"
"v_mfma_f32_16x16x32_bf16 a[4:7], v[36:39], v[128:131], a[4:7]\n"
"ds_read_b128 v[64:67], v4 offset:36864\n"
"v_mfma_f32_16x16x32_bf16 a[32:35], v[36:39], v[132:135], a[32:35]\n"
"s_add_u32 m0, s61, 0\n"
"v_mfma_f32_16x16x32_bf16 a[8:11], v[40:43], v[128:131], a[8:11]\n"
"buffer_load_dwordx4 v1, s[40:43], s60 offen offset:0 lds\n"
"ds_read_b128 v[68:71], v4 offset:37888\n"
"v_mfma_f32_16x16x32_bf16 a[36:39], v[40:43], v[132:135], a[36:39]\n"
"v_mfma_f32_16x16x32_bf16 a[12:15], v[44:47], v[128:131], a[12:15]\n"
"ds_read_b128 v[72:75], v4 offset:38912\n"
"v_mfma_f32_16x16x32_bf16 a[40:43], v[44:47], v[132:135], a[40:43]\n"
"s_waitcnt lgkmcnt(2)\n"
"v_mfma_f32_16x16x32_bf16 a[16:19], v[48:51], v[128:131], a[16:19]\n"
"buffer_load_dwordx4 v[196:199], v1, s[36:39], s59 offen offset:0 sc1\n"
"ds_read_b128 v[76:79], v4 offset:39936\n"
"v_mfma_f32_16x16x32_bf16 a[44:47], v[48:51], v[132:135], a[44:47]\n"
"v_mfma_f32_16x16x32_bf16 a[20:23], v[52:55], v[128:131], a[20:23]\n"
"ds_read_b128 v[80:83], v4 offset:40960\n"
"v_mfma_f32_16x16x32_bf16 a[48:51], v[52:55], v[132:135], a[48:51]\n"
"v_mfma_f32_16x16x32_bf16 a[24:27], v[56:59], v[128:131], a[24:27]\n"
"buffer_load_dwordx4 v1, s[40:43], s60 offen offset:1024 lds\n"
"ds_read_b128 v[84:87], v4 offset:41984\n"
"v_mfma_f32_16x16x32_bf16 a[52:55], v[56:59], v[132:135], a[52:55]\n"
"v_mfma_f32_16x16x32_bf16 a[0:3], v[60:63], v[136:139], a[0:3]\n"
"ds_read_b128 v[88:91], v4 offset:43008\n"
"v_mfma_f32_16x16x32_bf16 a[28:31], v[60:63], v[140:143], a[28:31]\n"
"buffer_load_dwordx4 v[200:203], v1, s[36:39], s58 offen offset:1024 sc1\n"
"v_mfma_f32_16x16x32_bf16 a[4:7], v[64:67], v[136:139], a[4:7]\n"
"ds_read_b128 v[92:95], v4 offset:44032\n"
"v_mfma_f32_16x16x32_bf16 a[32:35], v[64:67], v[140:143], a[32:35]\n"
"s_waitcnt lgkmcnt(2)\n"
"v_mfma_f32_16x16x32_bf16 a[8:11], v[68:71], v[136:139], a[8:11]\n"
"ds_read_b128 v[32:35], v4 offset:45056\n"
"v_mfma_f32_16x16x32_bf16 a[36:39], v[68:71], v[140:143], a[36:39]\n"
"buffer_load_dwordx4 v1, s[40:43], s60 offen offset:2048 lds\n"
"v_mfma_f32_16x16x32_bf16 a[12:15], v[72:75], v[136:139], a[12:15]\n"
"ds_read_b128 v[36:39], v4 offset:46080\n"
"v_mfma_f32_16x16x32_bf16 a[40:43], v[72:75], v[140:143], a[40:43]\n"
"v_mfma_f32_16x16x32_bf16 a[16:19], v[76:79], v[136:139], a[16:19]\n"
"ds_read_b128 v[40:43], v4 offset:47104\n"
"v_mfma_f32_16x16x32_bf16 a[44:47], v[76:79], v[140:143], a[44:47]\n"
"buffer_load_dwordx4 v[204:207], v1, s[36:39], s59 offen offset:1024 sc1\n"
"v_mfma_f32_16x16x32_bf16 a[20:23], v[80:83], v[136:139], a[20:23]\n"
"ds_read_b128 v[44:47], v4 offset:48128\n"
"v_mfma_f32_16x16x32_bf16 a[48:51], v[80:83], v[140:143], a[48:51]\n"
"v_mfma_f32_16x16x32_bf16 a[24:27], v[84:87], v[136:139], a[24:27]\n"
"buffer_load_dwordx4 v1, s[40:43], s60 offen offset:3072 lds\n"
"ds_read_b128 v[48:51], v4 offset:49152\n"
"v_mfma_f32_16x16x32_bf16 a[52:55], v[84:87], v[140:143], a[52:55]\n"
"s_waitcnt lgkmcnt(2)\n"
"v_mfma_f32_16x16x32_bf16 a[0:3], v[88:91], v[144:147], a[0:3]\n"
"ds_read_b128 v[52:55], v4 offset:50176\n"
"v_mfma_f32_16x16x32_bf16 a[28:31], v[88:91], v[148:151], a[28:31]\n"
"v_mfma_f32_16x16x32_bf16 a[4:7], v[92:95], v[144:147], a[4:7]\n"
"buffer_load_dwordx4 v[208:211], v1, s[36:39], s58 offen offset:2048 sc1\n"
"ds_read_b128 v[56:59], v4 offset:51200\n"
"v_mfma_f32_16x16x32_bf16 a[32:35], v[92:95], v[148:151], a[32:35]\n"
"v_mfma_f32_16x16x32_bf16 a[8:11], v[32:35], v[144:147], a[8:11]\n"
"ds_read_b128 v[60:63], v4 offset:52224\n"
"v_mfma_f32_16x16x32_bf16 a[36:39], v[32:35], v[148:151], a[36:39]\n"
"s_add_u32 m0, s61, 4096\n"
"v_mfma_f32_16x16x32_bf16 a[12:15], v[36:39], v[144:147], a[12:15]\n"
"buffer_load_dwordx4 v2, s[40:43], s60 offen offset:0 lds\n"
"ds_read_b128 v[64:67], v4 offset:53248\n"
"v_mfma_f32_16x16x32_bf16 a[40:43], v[36:39], v[148:151], a[40:43]\n"
"v_mfma_f32_16x16x32_bf16 a[16:19], v[40:43], v[144:147], a[16:19]\n"
"ds_read_b128 v[68:71], v4 offset:54272\n"
"v_mfma_f32_16x16x32_bf16 a[44:47], v[40:43], v[148:151], a[44:47]\n"
"buffer_load_dwordx4 v[212:215], v1, s[36:39], s59 offen offset:2048 sc1\n"
"s_waitcnt lgkmcnt(2)\n"
"v_mfma_f32_16x16x32_bf16 a[20:23], v[44:47], v[144:147], a[20:23]\n"
"ds_read_b128 v[72:75], v4 offset:55296\n"
"v_mfma_f32_16x16x32_bf16 a[48:51], v[44:47], v[148:151], a[48:51]\n"
"v_mfma_f32_16x16x32_bf16 a[24:27], v[48:51], v[144:147], a[24:27]\n"
"ds_read_b128 v[76:79], v4 offset:56320\n"
"v_mfma_f32_16x16x32_bf16 a[52:55], v[48:51], v[148:151], a[52:55]\n"
"buffer_load_dwordx4 v2, s[40:43], s60 offen offset:1024 lds\n"
"s_waitcnt vmcnt(12)\n"
"s_barrier\n"
"v_mfma_f32_16x16x32_bf16 a[0:3], v[52:55], v[152:155], a[0:3]\n"
"ds_read_b128 v[80:83], v5 offset:0\n"
"v_mfma_f32_16x16x32_bf16 a[28:31], v[52:55], v[156:159], a[28:31]\n"
"v_mfma_f32_16x16x32_bf16 a[4:7], v[56:59], v[152:155], a[4:7]\n"
"ds_read_b128 v[84:87], v5 offset:1024\n"
"v_mfma_f32_16x16x32_bf16 a[32:35], v[56:59], v[156:159], a[32:35]\n"
"buffer_load_dwordx4 v[216:219], v1, s[36:39], s58 offen offset:3072 sc1\n"
"s_add_u32 s58, s58, 4096\n"
"v_mfma_f32_16x16x32_bf16 a[8:11], v[60:63], v[152:155], a[8:11]\n"
"ds_read_b128 v[88:91], v5 offset:2048\n"
"v_mfma_f32_16x16x32_bf16 a[36:39], v[60:63], v[156:159], a[36:39]\n"
"s_waitcnt lgkmcnt(2)\n"
"v_mfma_f32_16x16x32_bf16 a[12:15], v[64:67], v[152:155], a[12:15]\n"
"buffer_load_dwordx4 v2, s[40:43], s60 offen offset:2048 lds\n"
"s_add_u32 s60, s60, 114688\n"
"ds_read_b128 v[92:95], v5 offset:3072\n"
"v_mfma_f32_16x16x32_bf16 a[40:43], v[64:67], v[156:159], a[40:43]\n"
"v_mfma_f32_16x16x32_bf16 a[16:19], v[68:71], v[152:155], a[16:19]\n"
"ds_read_b128 v[32:35], v5 offset:4096\n"
"v_mfma_f32_16x16x32_bf16 a[44:47], v[68:71], v[156:159], a[44:47]\n"
"v_mfma_f32_16x16x32_bf16 a[20:23], v[72:75], v[152:155], a[20:23]\n"
"buffer_load_dwordx4 v[220:223], v1, s[36:39], s59 offen offset:3072 sc1\n"
"s_add_u32 s59, s59, 4096\n"
"ds_read_b128 v[36:39], v5 offset:5120\n"
"v_mfma_f32_16x16x32_bf16 a[48:51], v[72:75], v[156:159], a[48:51]\n"
"v_mfma_f32_16x16x32_bf16 a[24:27], v[76:79], v[152:155], a[24:27]\n"
"ds_read_b128 v[40:43], v5 offset:6144\n"
"v_mfma_f32_16x16x32_bf16 a[52:55], v[76:79], v[156:159], a[52:55]\n"
"v_mfma_f32_16x16x32_bf16 a[0:3], v[80:83], v[160:163], a[0:3]\n"
"ds_read_b128 v[44:47], v5 offset:7168\n"
"v_mfma_f32_16x16x32_bf16 a[28:31], v[80:83], v[164:167], a[28:31]\n"
"buffer_load_dwordx4 v[224:227], v1, s[36:39], s58 offen offset:0 sc1\n"
"s_waitcnt lgkmcnt(2)\n"
"v_mfma_f32_16x16x32_bf16 a[4:7], v[84:87], v[160:163], a[4:7]\n"
"ds_read_b128 v[48:51], v5 offset:8192\n"
"v_mfma_f32_16x16x32_bf16 a[32:35], v[84:87], v[164:167], a[32:35]\n"
"s_add_u32 m0, s61, 28672\n"
"v_mfma_f32_16x16x32_bf16 a[8:11], v[88:91], v[160:163], a[8:11]\n"
"buffer_load_dwordx4 v1, s[40:43], s60 offen offset:0 lds\n"
"ds_read_b128 v[52:55], v5 offset:9216\n"
"v_mfma_f32_16x16x32_bf16 a[36:39], v[88:91], v[164:167], a[36:39]\n"
"v_mfma_f32_16x16x32_bf16 a[12:15], v[92:95], v[160:163], a[12:15]\n"
"ds_read_b128 v[56:59], v5 offset:10240\n"
"v_mfma_f32_16x16x32_bf16 a[40:43], v[92:95], v[164:167], a[40:43]\n"
"v_mfma_f32_16x16x32_bf16 a[16:19], v[32:35], v[160:163], a[16:19]\n"
"buffer_load_dwordx4 v[228:231], v1, s[36:39], s59 offen offset:0 sc1\n"
"ds_read_b128 v[60:63], v5 offset:11264\n"
"v_mfma_f32_16x16x32_bf16 a[44:47], v[32:35], v[164:167], a[44:47]\n"
"v_mfma_f32_16x16x32_bf16 a[20:23], v[36:39], v[160:163], a[20:23]\n"
"ds_read_b128 v[64:67], v5 offset:12288\n"
"v_mfma_f32_16x16x32_bf16 a[48:51], v[36:39], v[164:167], a[48:51]\n"
"s_waitcnt lgkmcnt(2)\n"
"v_mfma_f32_16x16x32_bf16 a[24:27], v[40:43], v[160:163], a[24:27]\n"
"buffer_load_dwordx4 v1, s[40:43], s60 offen offset:1024 lds\n"
"ds_read_b128 v[68:71], v5 offset:13312\n"
"v_mfma_f32_16x16x32_bf16 a[52:55], v[40:43], v[164:167], a[52:55]\n"
"v_mfma_f32_16x16x32_bf16 a[0:3], v[44:47], v[168:171], a[0:3]\n"
"ds_read_b128 v[72:75], v5 offset:14336\n"
"v_mfma_f32_16x16x32_bf16 a[28:31], v[44:47], v[172:175], a[28:31]\n"
"buffer_load_dwordx4 v[232:235], v1, s[36:39], s58 offen offset:1024 sc1\n"
"v_mfma_f32_16x16x32_bf16 a[4:7], v[48:51], v[168:171], a[4:7]\n"
"ds_read_b128 v[76:79], v5 offset:15360\n"
"v_mfma_f32_16x16x32_bf16 a[32:35], v[48:51], v[172:175], a[32:35]\n"
"v_mfma_f32_16x16x32_bf16 a[8:11], v[52:55], v[168:171], a[8:11]\n"
"ds_read_b128 v[80:83], v5 offset:16384\n"
"v_mfma_f32_16x16x32_bf16 a[36:39], v[52:55], v[172:175], a[36:39]\n"
"buffer_load_dwordx4 v1, s[40:43], s60 offen offset:2048 lds\n"
"v_mfma_f32_16x16x32_bf16 a[12:15], v[56:59], v[168:171], a[12:15]\n"
"ds_read_b128 v[84:87], v5 offset:17408\n"
"v_mfma_f32_16x16x32_bf16 a[40:43], v[56:59], v[172:175], a[40:43]\n"
"s_waitcnt lgkmcnt(2)\n"
"v_mfma_f32_16x16x32_bf16 a[16:19], v[60:63], v[168:171], a[16:19]\n"
"ds_read_b128 v[88:91], v5 offset:18432\n"
"v_mfma_f32_16x16x32_bf16 a[44:47], v[60:63], v[172:175], a[44:47]\n"
"buffer_load_dwordx4 v[236:239], v1, s[36:39], s59 offen offset:1024 sc1\n"
"v_mfma_f32_16x16x32_bf16 a[20:23], v[64:67], v[168:171], a[20:23]\n"
"ds_read_b128 v[92:95], v5 offset:19456\n"
"v_mfma_f32_16x16x32_bf16 a[48:51], v[64:67], v[172:175], a[48:51]\n"
"v_mfma_f32_16x16x32_bf16 a[24:27], v[68:71], v[168:171], a[24:27]\n"
"buffer_load_dwordx4 v1, s[40:43], s60 offen offset:3072 lds\n"
"ds_read_b128 v[32:35], v5 offset:20480\n"
"v_mfma_f32_16x16x32_bf16 a[52:55], v[68:71], v[172:175], a[52:55]\n"
"v_mfma_f32_16x16x32_bf16 a[0:3], v[72:75], v[176:179], a[0:3]\n"
"ds_read_b128 v[36:39], v5 offset:21504\n"
"v_mfma_f32_16x16x32_bf16 a[28:31], v[72:75], v[180:183], a[28:31]\n"
"s_add_u32 m0, s61, 32768\n"
"v_mfma_f32_16x16x32_bf16 a[4:7], v[76:79], v[176:179], a[4:7]\n"
"buffer_load_dwordx4 v2, s[40:43], s60 offen offset:0 lds\n"
"ds_read_b128 v[40:43], v5 offset:22528\n"
"v_mfma_f32_16x16x32_bf16 a[32:35], v[76:79], v[180:183], a[32:35]\n"
"s_waitcnt lgkmcnt(2)\n"
"v_mfma_f32_16x16x32_bf16 a[8:11], v[80:83], v[176:179], a[8:11]\n"
"ds_read_b128 v[44:47], v5 offset:23552\n"
"v_mfma_f32_16x16x32_bf16 a[36:39], v[80:83], v[180:183], a[36:39]\n"
"v_mfma_f32_16x16x32_bf16 a[12:15], v[84:87], v[176:179], a[12:15]\n"
"buffer_load_dwordx4 v2, s[40:43], s60 offen offset:1024 lds\n"
"ds_read_b128 v[48:51], v5 offset:24576\n"
"v_mfma_f32_16x16x32_bf16 a[40:43], v[84:87], v[180:183], a[40:43]\n"
"v_mfma_f32_16x16x32_bf16 a[16:19], v[88:91], v[176:179], a[16:19]\n"
"ds_read_b128 v[52:55], v5 offset:25600\n"
"v_mfma_f32_16x16x32_bf16 a[44:47], v[88:91], v[180:183], a[44:47]\n"
"buffer_load_dwordx4 v2, s[40:43], s60 offen offset:2048 lds\n"
"s_add_u32 s60, s60, 114688\n"
"v_mfma_f32_16x16x32_bf16 a[20:23], v[92:95], v[176:179], a[20:23]\n"
"ds_read_b128 v[56:59], v5 offset:26624\n"
"v_mfma_f32_16x16x32_bf16 a[48:51], v[92:95], v[180:183], a[48:51]\n"
"v_mfma_f32_16x16x32_bf16 a[24:27], v[32:35], v[176:179], a[24:27]\n"
"ds_read_b128 v[60:63], v5 offset:27648\n"
"v_mfma_f32_16x16x32_bf16 a[52:55], v[32:35], v[180:183], a[52:55]\n"
"s_waitcnt vmcnt(11)\n"
"s_barrier\n"
"s_waitcnt lgkmcnt(2)\n"
"v_mfma_f32_16x16x32_bf16 a[0:3], v[36:39], v[184:187], a[0:3]\n"
"ds_read_b128 v[64:67], v3 offset:0\n"
"v_mfma_f32_16x16x32_bf16 a[28:31], v[36:39], v[188:191], a[28:31]\n"
"v_mfma_f32_16x16x32_bf16 a[4:7], v[40:43], v[184:187], a[4:7]\n"
"ds_read_b128 v[68:71], v3 offset:1024\n"
"v_mfma_f32_16x16x32_bf16 a[32:35], v[40:43], v[188:191], a[32:35]\n"
"v_mfma_f32_16x16x32_bf16 a[8:11], v[44:47], v[184:187], a[8:11]\n"
"ds_read_b128 v[72:75], v3 offset:2048\n"
"v_mfma_f32_16x16x32_bf16 a[36:39], v[44:47], v[188:191], a[36:39]\n"
"v_mfma_f32_16x16x32_bf16 a[12:15], v[48:51], v[184:187], a[12:15]\n"
"ds_read_b128 v[76:79], v3 offset:3072\n"
"v_mfma_f32_16x16x32_bf16 a[40:43], v[48:51], v[188:191], a[40:43]\n"
"v_mfma_f32_16x16x32_bf16 a[16:19], v[52:55], v[184:187], a[16:19]\n"
"ds_read_b128 v[80:83], v3 offset:4096\n"
"v_mfma_f32_16x16x32_bf16 a[44:47], v[52:55], v[188:191], a[44:47]\n"
"s_waitcnt lgkmcnt(2)\n"
"v_mfma_f32_16x16x32_bf16 a[20:23], v[56:59], v[184:187], a[20:23]\n"
"ds_read_b128 v[84:87], v3 offset:5120\n"
"v_mfma_f32_16x16x32_bf16 a[48:51], v[56:59], v[188:191], a[48:51]\n"
"v_mfma_f32_16x16x32_bf16 a[24:27], v[60:63], v[184:187], a[24:27]\n"
"ds_read_b128 v[88:91], v3 offset:6144\n"
"v_mfma_f32_16x16x32_bf16 a[52:55], v[60:63], v[188:191], a[52:55]\n"
"v_mfma_f32_16x16x32_bf16 a[0:3], v[64:67], v[192:195], a[0:3]\n"
"ds_read_b128 v[92:95], v3 offset:7168\n"
"v_mfma_f32_16x16x32_bf16 a[28:31], v[64:67], v[196:199], a[28:31]\n"
"v_mfma_f32_16x16x32_bf16 a[4:7], v[68:71], v[192:195], a[4:7]\n"
"ds_read_b128 v[32:35], v3 offset:8192\n"
"v_mfma_f32_16x16x32_bf16 a[32:35], v[68:71], v[196:199], a[32:35]\n"
"v_mfma_f32_16x16x32_bf16 a[8:11], v[72:75], v[192:195], a[8:11]\n"
"ds_read_b128 v[36:39], v3 offset:9216\n"
"v_mfma_f32_16x16x32_bf16 a[36:39], v[72:75], v[196:199], a[36:39]\n"
"s_waitcnt lgkmcnt(2)\n"
"v_mfma_f32_16x16x32_bf16 a[12:15], v[76:79], v[192:195], a[12:15]\n"
"ds_read_b128 v[40:43], v3 offset:10240\n"
"v_mfma_f32_16x16x32_bf16 a[40:43], v[76:79], v[196:199], a[40:43]\n"
"v_mfma_f32_16x16x32_bf16 a[16:19], v[80:83], v[192:195], a[16:19]\n"
"ds_read_b128 v[44:47], v3 offset:11264\n"
"v_mfma_f32_16x16x32_bf16 a[44:47], v[80:83], v[196:199], a[44:47]\n"
"v_mfma_f32_16x16x32_bf16 a[20:23], v[84:87], v[192:195], a[20:23]\n"
"ds_read_b128 v[48:51], v3 offset:12288\n"
"v_mfma_f32_16x16x32_bf16 a[48:51], v[84:87], v[196:199], a[48:51]\n"
"v_mfma_f32_16x16x32_bf16 a[24:27], v[88:91], v[192:195], a[24:27]\n"
"ds_read_b128 v[52:55], v3 offset:13312\n"
"v_mfma_f32_16x16x32_bf16 a[52:55], v[88:91], v[196:199], a[52:55]\n"
"v_mfma_f32_16x16x32_bf16 a[0:3], v[92:95], v[200:203], a[0:3]\n"
"ds_read_b128 v[56:59], v3 offset:14336\n"
"v_mfma_f32_16x16x32_bf16 a[28:31], v[92:95], v[204:207], a[28:31]\n"
"s_waitcnt lgkmcnt(2)\n"
"v_mfma_f32_16x16x32_bf16 a[4:7], v[32:35], v[200:203], a[4:7]\n"
"ds_read_b128 v[60:63], v3 offset:15360\n"
"v_mfma_f32_16x16x32_bf16 a[32:35], v[32:35], v[204:207], a[32:35]\n"
"v_mfma_f32_16x16x32_bf16 a[8:11], v[36:39], v[200:203], a[8:11]\n"
"ds_read_b128 v[64:67], v3 offset:16384\n"
"v_mfma_f32_16x16x32_bf16 a[36:39], v[36:39], v[204:207], a[36:39]\n"
"v_mfma_f32_16x16x32_bf16 a[12:15], v[40:43], v[200:203], a[12:15]\n"
"ds_read_b128 v[68:71], v3 offset:17408\n"
"v_mfma_f32_16x16x32_bf16 a[40:43], v[40:43], v[204:207], a[40:43]\n"
"v_mfma_f32_16x16x32_bf16 a[16:19], v[44:47], v[200:203], a[16:19]\n"
"ds_read_b128 v[72:75], v3 offset:18432\n"
"v_mfma_f32_16x16x32_bf16 a[44:47], v[44:47], v[204:207], a[44:47]\n"
"v_mfma_f32_16x16x32_bf16 a[20:23], v[48:51], v[200:203], a[20:23]\n"
"ds_read_b128 v[76:79], v3 offset:19456\n"
"v_mfma_f32_16x16x32_bf16 a[48:51], v[48:51], v[204:207], a[48:51]\n"
"s_waitcnt lgkmcnt(2)\n"
"v_mfma_f32_16x16x32_bf16 a[24:27], v[52:55], v[200:203], a[24:27]\n"
"ds_read_b128 v[80:83], v3 offset:20480\n"
"v_mfma_f32_16x16x32_bf16 a[52:55], v[52:55], v[204:207], a[52:55]\n"
"v_mfma_f32_16x16x32_bf16 a[0:3], v[56:59], v[208:211], a[0:3]\n"
"ds_read_b128 v[84:87], v3 offset:21504\n"
"v_mfma_f32_16x16x32_bf16 a[28:31], v[56:59], v[212:215], a[28:31]\n"
"v_mfma_f32_16x16x32_bf16 a[4:7], v[60:63], v[208:211], a[4:7]\n"
"ds_read_b128 v[88:91], v3 offset:22528\n"
"v_mfma_f32_16x16x32_bf16 a[32:35], v[60:63], v[212:215], a[32:35]\n"
"v_mfma_f32_16x16x32_bf16 a[8:11], v[64:67], v[208:211], a[8:11]\n"
"ds_read_b128 v[92:95], v3 offset:23552\n"
"v_mfma_f32_16x16x32_bf16 a[36:39], v[64:67], v[212:215], a[36:39]\n"
"v_mfma_f32_16x16x32_bf16 a[12:15], v[68:71], v[208:211], a[12:15]\n"
"ds_read_b128 v[32:35], v3 offset:24576\n"
"v_mfma_f32_16x16x32_bf16 a[40:43], v[68:71], v[212:215], a[40:43]\n"
"s_waitcnt lgkmcnt(2)\n"
"v_mfma_f32_16x16x32_bf16 a[16:19], v[72:75], v[208:211], a[16:19]\n"
"ds_read_b128 v[36:39], v3 offset:25600\n"
"v_mfma_f32_16x16x32_bf16 a[44:47], v[72:75], v[212:215], a[44:47]\n"
"v_mfma_f32_16x16x32_bf16 a[20:23], v[76:79], v[208:211], a[20:23]\n"
"ds_read_b128 v[40:43], v3 offset:26624\n"
"v_mfma_f32_16x16x32_bf16 a[48:51], v[76:79], v[212:215], a[48:51]\n"
"v_mfma_f32_16x16x32_bf16 a[24:27], v[80:83], v[208:211], a[24:27]\n"
"ds_read_b128 v[44:47], v3 offset:27648\n"
"v_mfma_f32_16x16x32_bf16 a[52:55], v[80:83], v[212:215], a[52:55]\n"
"s_waitcnt vmcnt(0)\n"
"s_barrier\n"
"v_mfma_f32_16x16x32_bf16 a[0:3], v[84:87], v[216:219], a[0:3]\n"
"ds_read_b128 v[48:51], v3 offset:28672\n"
"v_mfma_f32_16x16x32_bf16 a[28:31], v[84:87], v[220:223], a[28:31]\n"
"v_mfma_f32_16x16x32_bf16 a[4:7], v[88:91], v[216:219], a[4:7]\n"
"ds_read_b128 v[52:55], v3 offset:29696\n"
"v_mfma_f32_16x16x32_bf16 a[32:35], v[88:91], v[220:223], a[32:35]\n"
"s_waitcnt lgkmcnt(2)\n"
"v_mfma_f32_16x16x32_bf16 a[8:11], v[92:95], v[216:219], a[8:11]\n"
"ds_read_b128 v[56:59], v3 offset:30720\n"
"v_mfma_f32_16x16x32_bf16 a[36:39], v[92:95], v[220:223], a[36:39]\n"
"v_mfma_f32_16x16x32_bf16 a[12:15], v[32:35], v[216:219], a[12:15]\n"
"ds_read_b128 v[60:63], v3 offset:31744\n"
"v_mfma_f32_16x16x32_bf16 a[40:43], v[32:35], v[220:223], a[40:43]\n"
"v_mfma_f32_16x16x32_bf16 a[16:19], v[36:39], v[216:219], a[16:19]\n"
"ds_read_b128 v[64:67], v3 offset:32768\n"
"v_mfma_f32_16x16x32_bf16 a[44:47], v[36:39], v[220:223], a[44:47]\n"
"v_mfma_f32_16x16x32_bf16 a[20:23], v[40:43], v[216:219], a[20:23]\n"
"ds_read_b128 v[68:71], v3 offset:33792\n"
"v_mfma_f32_16x16x32_bf16 a[48:51], v[40:43], v[220:223], a[48:51]\n"
"v_mfma_f32_16x16x32_bf16 a[24:27], v[44:47], v[216:219], a[24:27]\n"
"ds_read_b128 v[72:75], v3 offset:34816\n"
"v_mfma_f32_16x16x32_bf16 a[52:55], v[44:47], v[220:223], a[52:55]\n"
"s_waitcnt lgkmcnt(2)\n"
"v_mfma_f32_16x16x32_bf16 a[0:3], v[48:51], v[224:227], a[0:3]\n"
"ds_read_b128 v[76:79], v3 offset:35840\n"
"v_mfma_f32_16x16x32_bf16 a[28:31], v[48:51], v[228:231], a[28:31]\n"
"v_mfma_f32_16x16x32_bf16 a[4:7], v[52:55], v[224:227], a[4:7]\n"
"ds_read_b128 v[80:83], v3 offset:36864\n"
"v_mfma_f32_16x16x32_bf16 a[32:35], v[52:55], v[228:231], a[32:35]\n"
"v_mfma_f32_16x16x32_bf16 a[8:11], v[56:59], v[224:227], a[8:11]\n"
"ds_read_b128 v[84:87], v3 offset:37888\n"
"v_mfma_f32_16x16x32_bf16 a[36:39], v[56:59], v[228:231], a[36:39]\n"
"v_mfma_f32_16x16x32_bf16 a[12:15], v[60:63], v[224:227], a[12:15]\n"
"ds_read_b128 v[88:91], v3 offset:38912\n"
"v_mfma_f32_16x16x32_bf16 a[40:43], v[60:63], v[228:231], a[40:43]\n"
"v_mfma_f32_16x16x32_bf16 a[16:19], v[64:67], v[224:227], a[16:19]\n"
"ds_read_b128 v[92:95], v3 offset:39936\n"
"v_mfma_f32_16x16x32_bf16 a[44:47], v[64:67], v[228:231], a[44:47]\n"
"s_waitcnt lgkmcnt(2)\n"
"v_mfma_f32_16x16x32_bf16 a[20:23], v[68:71], v[224:227], a[20:23]\n"
"ds_read_b128 v[32:35], v3 offset:40960\n"
"v_mfma_f32_16x16x32_bf16 a[48:51], v[68:71], v[228:231], a[48:51]\n"
"v_mfma_f32_16x16x32_bf16 a[24:27], v[72:75], v[224:227], a[24:27]\n"
"ds_read_b128 v[36:39], v3 offset:41984\n"
"v_mfma_f32_16x16x32_bf16 a[52:55], v[72:75], v[228:231], a[52:55]\n"
"v_mfma_f32_16x16x32_bf16 a[0:3], v[76:79], v[232:235], a[0:3]\n"
"v_mfma_f32_16x16x32_bf16 a[28:31], v[76:79], v[236:239], a[28:31]\n"
"v_mfma_f32_16x16x32_bf16 a[4:7], v[80:83], v[232:235], a[4:7]\n"
"v_mfma_f32_16x16x32_bf16 a[32:35], v[80:83], v[236:239], a[32:35]\n"
"v_mfma_f32_16x16x32_bf16 a[8:11], v[84:87], v[232:235], a[8:11]\n"
"v_mfma_f32_16x16x32_bf16 a[36:39], v[84:87], v[236:239], a[36:39]\n"
"s_waitcnt lgkmcnt(0)\n"
"v_mfma_f32_16x16x32_bf16 a[12:15], v[88:91], v[232:235], a[12:15]\n"
"v_mfma_f32_16x16x32_bf16 a[40:43], v[88:91], v[236:239], a[40:43]\n"
"v_mfma_f32_16x16x32_bf16 a[16:19], v[92:95], v[232:235], a[16:19]\n"
"v_mfma_f32_16x16x32_bf16 a[44:47], v[92:95], v[236:239], a[44:47]\n"
"v_mfma_f32_16x16x32_bf16 a[20:23], v[32:35], v[232:235], a[20:23]\n"
"v_mfma_f32_16x16x32_bf16 a[48:51], v[32:35], v[236:239], a[48:51]\n"
"v_mfma_f32_16x16x32_bf16 a[24:27], v[36:39], v[232:235], a[24:27]\n"
"v_mfma_f32_16x16x32_bf16 a[52:55], v[36:39], v[236:239], a[52:55]\n"
"s_cmp_eq_u32 %[wv], %[kq]\n"
"s_cbranch_scc1 L_k3c%=\n"
"s_lshl_b32 s57, %[wv], 2\n"
"s_add_u32 s57, s57, %[kq]\n"
"s_mul_i32 s58, s57, 7168\n"
"s_nop 2\n"
"v_accvgpr_read_b32 v32, a0\n"
"v_accvgpr_read_b32 v33, a1\n"
"v_accvgpr_read_b32 v34, a2\n"
"v_accvgpr_read_b32 v35, a3\n"
"v_accvgpr_read_b32 v36, a4\n"
"v_accvgpr_read_b32 v37, a5\n"
"v_accvgpr_read_b32 v38, a6\n"
"v_accvgpr_read_b32 v39, a7\n"
"v_cvt_pk_bf16_f32 v32, v32, v33\n"
"v_cvt_pk_bf16_f32 v33, v34, v35\n"
"v_cvt_pk_bf16_f32 v34, v36, v37\n"
"v_cvt_pk_bf16_f32 v35, v38, v39\n"
"buffer_store_dwordx4 v[32:35], v1, s[48:51], s58 offen offset:0 sc1\n"
"v_accvgpr_read_b32 v40, a8\n"
"v_accvgpr_read_b32 v41, a9\n"
"v_accvgpr_read_b32 v42, a10\n"
"v_accvgpr_read_b32 v43, a11\n"
"v_accvgpr_read_b32 v44, a12\n"
"v_accvgpr_read_b32 v45, a13\n"
"v_accvgpr_read_b32 v46, a14\n"
"v_accvgpr_read_b32 v47, a15\n"
"v_cvt_pk_bf16_f32 v40, v40, v41\n"
"v_cvt_pk_bf16_f32 v41, v42, v43\n"
"v_cvt_pk_bf16_f32 v42, v44, v45\n"
"v_cvt_pk_bf16_f32 v43, v46, v47\n"
"buffer_store_dwordx4 v[40:43], v1, s[48:51], s58 offen offset:1024 sc1\n"
"v_accvgpr_read_b32 v48, a16\n"
"v_accvgpr_read_b32 v49, a17\n"
"v_accvgpr_read_b32 v50, a18\n"
"v_accvgpr_read_b32 v51, a19\n"
"v_accvgpr_read_b32 v52, a20\n"
"v_accvgpr_read_b32 v53, a21\n"
"v_accvgpr_read_b32 v54, a22\n"
"v_accvgpr_read_b32 v55, a23\n"
"v_cvt_pk_bf16_f32 v48, v48, v49\n"
"v_cvt_pk_bf16_f32 v49, v50, v51\n"
"v_cvt_pk_bf16_f32 v50, v52, v53\n"
"v_cvt_pk_bf16_f32 v51, v54, v55\n"
"buffer_store_dwordx4 v[48:51], v1, s[48:51], s58 offen offset:2048 sc1\n"
"v_accvgpr_read_b32 v56, a24\n"
"v_accvgpr_read_b32 v57, a25\n"
"v_accvgpr_read_b32 v58, a26\n"
"v_accvgpr_read_b32 v59, a27\n"
"v_accvgpr_read_b32 v60, a28\n"
"v_accvgpr_read_b32 v61, a29\n"
"v_accvgpr_read_b32 v62, a30\n"
"v_accvgpr_read_b32 v63, a31\n"
"v_cvt_pk_bf16_f32 v56, v56, v57\n"
"v_cvt_pk_bf16_f32 v57, v58, v59\n"
"v_cvt_pk_bf16_f32 v58, v60, v61\n"
"v_cvt_pk_bf16_f32 v59, v62, v63\n"
"buffer_store_dwordx4 v[56:59], v1, s[48:51], s58 offen offset:3072 sc1\n"
"v_accvgpr_read_b32 v64, a32\n"
"v_accvgpr_read_b32 v65, a33\n"
"v_accvgpr_read_b32 v66, a34\n"
"v_accvgpr_read_b32 v67, a35\n"
"v_accvgpr_read_b32 v68, a36\n"
"v_accvgpr_read_b32 v69, a37\n"
"v_accvgpr_read_b32 v70, a38\n"
"v_accvgpr_read_b32 v71, a39\n"
"v_cvt_pk_bf16_f32 v64, v64, v65\n"
"v_cvt_pk_bf16_f32 v65, v66, v67\n"
"v_cvt_pk_bf16_f32 v66, v68, v69\n"
"v_cvt_pk_bf16_f32 v67, v70, v71\n"
"buffer_store_dwordx4 v[64:67], v2, s[48:51], s58 offen offset:0 sc1\n"
"v_accvgpr_read_b32 v72, a40\n"
"v_accvgpr_read_b32 v73, a41\n"
"v_accvgpr_read_b32 v74, a42\n"
"v_accvgpr_read_b32 v75, a43\n"
"v_accvgpr_read_b32 v76, a44\n"
"v_accvgpr_read_b32 v77, a45\n"
"v_accvgpr_read_b32 v78, a46\n"
"v_accvgpr_read_b32 v79, a47\n"
"v_cvt_pk_bf16_f32 v72, v72, v73\n"
"v_cvt_pk_bf16_f32 v73, v74, v75\n"
"v_cvt_pk_bf16_f32 v74, v76, v77\n"
"v_cvt_pk_bf16_f32 v75, v78, v79\n"
"buffer_store_dwordx4 v[72:75], v2, s[48:51], s58 offen offset:1024 sc1\n"
"v_accvgpr_read_b32 v80, a48\n"
"v_accvgpr_read_b32 v81, a49\n"
"v_accvgpr_read_b32 v82, a50\n"
"v_accvgpr_read_b32 v83, a51\n"
"v_accvgpr_read_b32 v84, a52\n"
"v_accvgpr_read_b32 v85, a53\n"
"v_accvgpr_read_b32 v86, a54\n"
"v_accvgpr_read_b32 v87, a55\n"
"v_cvt_pk_bf16_f32 v80, v80, v81\n"
"v_cvt_pk_bf16_f32 v81, v82, v83\n"
"v_cvt_pk_bf16_f32 v82, v84, v85\n"
"v_cvt_pk_bf16_f32 v83, v86, v87\n"
"buffer_store_dwordx4 v[80:83], v2, s[48:51], s58 offen offset:2048 sc1\n"
"s_lshl_b32 s57, %[wv], 5\n"
"s_add_u32 s57, s57, %[kq]\n"
"s_lshl_b32 s57, s57, 2\n"
"v_mov_b32 v6, s57\n"
"v_mov_b32 v7, 1\n"
"s_mov_b64 exec, 1\n"
"s_waitcnt vmcnt(0)\n"
"global_store_dword v6, v7, %[fl] sc1\n"
"s_mov_b64 exec, -1\n"
"s_branch L_k3e%=\n"
"L_k3c%=:\n"
"s_lshl_b32 s57, %[kq], 5\n"
"v_cmp_eq_u32_e64 s[36:37], %[kq], v6\n"
"v_add_lshl_u32 v6, v6, s57, 2\n"
"s_add_u32 s57, %[kq], 1\n"
"s_and_b32 s57, s57, 3\n"
"s_lshl_b32 s40, %[kq], 2\n"
"s_add_u32 s57, s57, s40\n"
"s_mul_i32 s40, s57, 7168\n"
"s_add_u32 s57, %[kq], 2\n"
"s_and_b32 s57, s57, 3\n"
"s_lshl_b32 s41, %[kq], 2\n"
"s_add_u32 s57, s57, s41\n"
"s_mul_i32 s41, s57, 7168\n"
"s_add_u32 s57, %[kq], 3\n"
"s_and_b32 s57, s57, 3\n"
"s_lshl_b32 s42, %[kq], 2\n"
"s_add_u32 s57, s57, s42\n"
"s_mul_i32 s42, s57, 7168\n"
"v_accvgpr_read_b32 v200, a0\n"
"v_accvgpr_read_b32 v201, a1\n"
"v_accvgpr_read_b32 v202, a2\n"
"v_accvgpr_read_b32 v203, a3\n"
"v_accvgpr_read_b32 v204, a4\n"
"v_accvgpr_read_b32 v205, a5\n"
"v_accvgpr_read_b32 v206, a6\n"
"v_accvgpr_read_b32 v207, a7\n"
"v_accvgpr_read_b32 v208, a8\n"
"v_accvgpr_read_b32 v209, a9\n"
"v_accvgpr_read_b32 v210, a10\n"
"v_accvgpr_read_b32 v211, a11\n"
"v_accvgpr_read_b32 v212, a12\n"
"v_accvgpr_read_b32 v213, a13\n"
"v_accvgpr_read_b32 v214, a14\n"
"v_accvgpr_read_b32 v215, a15\n"
"v_accvgpr_read_b32 v216, a16\n"
"v_accvgpr_read_b32 v217, a17\n"
"v_accvgpr_read_b32 v218, a18\n"
"v_accvgpr_read_b32 v219, a19\n"
"v_accvgpr_read_b32 v220, a20\n"
"v_accvgpr_read_b32 v221, a21\n"
"v_accvgpr_read_b32 v222, a22\n"
"v_accvgpr_read_b32 v223, a23\n"
"v_accvgpr_read_b32 v224, a24\n"
"v_accvgpr_read_b32 v225, a25\n"
"v_accvgpr_read_b32 v226, a26\n"
"v_accvgpr_read_b32 v227, a27\n"
"v_accvgpr_read_b32 v228, a28\n"
"v_accvgpr_read_b32 v229, a29\n"
"v_accvgpr_read_b32 v230, a30\n"
"v_accvgpr_read_b32 v231, a31\n"
"v_accvgpr_read_b32 v232, a32\n"
"v_accvgpr_read_b32 v233, a33\n"
"v_accvgpr_read_b32 v234, a34\n"
"v_accvgpr_read_b32 v235, a35\n"
"v_accvgpr_read_b32 v236, a36\n"
"v_accvgpr_read_b32 v237, a37\n"
"v_accvgpr_read_b32 v238, a38\n"
"v_accvgpr_read_b32 v239, a39\n"
"v_accvgpr_read_b32 v240, a40\n"
"v_accvgpr_read_b32 v241, a41\n"
"v_accvgpr_read_b32 v242, a42\n"
"v_accvgpr_read_b32 v243, a43\n"
"v_accvgpr_read_b32 v244, a44\n"
"v_accvgpr_read_b32 v245, a45\n"
"v_accvgpr_read_b32 v246, a46\n"
"v_accvgpr_read_b32 v247, a47\n"
"v_accvgpr_read_b32 v248, a48\n"
"v_accvgpr_read_b32 v249, a49\n"
"v_accvgpr_read_b32 v250, a50\n"
"v_accvgpr_read_b32 v251, a51\n"
"v_accvgpr_read_b32 v252, a52\n"
"v_accvgpr_read_b32 v253, a53\n"
"v_accvgpr_read_b32 v254, a54\n"
"v_accvgpr_read_b32 v255, a55\n"
"s_mov_b32 s57, 14336\n"
"s_mov_b32 s58, 0x3f80\n"
"s_mov_b32 s59, 0x3f800000\n"
"L_k3s%=:\n"
"global_load_dword v7, v6, %[fl] sc1\n"
"s_waitcnt vmcnt(0)\n"
"v_cmp_eq_u32 vcc, 1, v7\n"
"s_nop 3\n"
"s_or_b64 s[38:39], vcc, s[36:37]\n"
"s_cmp_eq_u64 s[38:39], -1\n"
"s_cbranch_scc0 L_k3s%=\n"
"buffer_load_dwordx4 v[32:35], v1, s[48:51], s40 offen offset:0 sc1\n"
"buffer_load_dwordx4 v[36:39], v1, s[48:51], s41 offen offset:0 sc1\n"
"buffer_load_dwordx4 v[40:43], v1, s[48:51], s42 offen offset:0 sc1\n"
"buffer_load_dwordx4 v[44:47], v1, s[48:51], s40 offen offset:1024 sc1\n"
"buffer_load_dwordx4 v[48:51], v1, s[48:51], s41 offen offset:1024 sc1\n"
"buffer_load_dwordx4 v[52:55], v1, s[48:51], s42 offen offset:1024 sc1\n"
"buffer_load_dwordx4 v[56:59], v1, s[48:51], s40 offen offset:2048 sc1\n"
"buffer_load_dwordx4 v[60:63], v1, s[48:51], s41 offen offset:2048 sc1\n"
"buffer_load_dwordx4 v[64:67], v1, s[48:51], s42 offen offset:2048 sc1\n"
"buffer_load_dwordx4 v[68:71], v1, s[48:51], s40 offen offset:3072 sc1\n"
"buffer_load_dwordx4 v[72:75], v1, s[48:51], s41 offen offset:3072 sc1\n"
"buffer_load_dwordx4 v[76:79], v1, s[48:51], s42 offen offset:3072 sc1\n"
"buffer_load_dwordx4 v[80:83], v2, s[48:51], s40 offen offset:0 sc1\n"
"buffer_load_dwordx4 v[84:87], v2, s[48:51], s41 offen offset:0 sc1\n"
"buffer_load_dwordx4 v[88:91], v2, s[48:51], s42 offen offset:0 sc1\n"
"buffer_load_dwordx4 v[92:95], v2, s[48:51], s40 offen offset:1024 sc1\n"
"buffer_load_dwordx4 v[96:99], v2, s[48:51], s41 offen offset:1024 sc1\n"
"buffer_load_dwordx4 v[100:103], v2, s[48:51], s42 offen offset:1024 sc1\n"
"buffer_load_dwordx4 v[104:107], v2, s[48:51], s40 offen offset:2048 sc1\n"
"buffer_load_dwordx4 v[108:111], v2, s[48:51], s41 offen offset:2048 sc1\n"
"buffer_load_dwordx4 v[112:115], v2, s[48:51], s42 offen offset:2048 sc1\n"
"s_waitcnt vmcnt(20)\n"
"v_dot2c_f32_bf16 v200, s58, v32\n"
"v_dot2c_f32_bf16 v201, s59, v32\n"
"v_dot2c_f32_bf16 v202, s58, v33\n"
"v_dot2c_f32_bf16 v203, s59, v33\n"
"v_dot2c_f32_bf16 v204, s58, v34\n"
"v_dot2c_f32_bf16 v205, s59, v34\n"
"v_dot2c_f32_bf16 v206, s58, v35\n"
"v_dot2c_f32_bf16 v207, s59, v35\n"
"s_waitcnt vmcnt(19)\n"
"v_dot2c_f32_bf16 v200, s58, v36\n"
"v_dot2c_f32_bf16 v201, s59, v36\n"
"v_dot2c_f32_bf16 v202, s58, v37\n"
"v_dot2c_f32_bf16 v203, s59, v37\n"
"v_dot2c_f32_bf16 v204, s58, v38\n"
"v_dot2c_f32_bf16 v205, s59, v38\n"
"v_dot2c_f32_bf16 v206, s58, v39\n"
"v_dot2c_f32_bf16 v207, s59, v39\n"
"s_waitcnt vmcnt(18)\n"
"v_dot2c_f32_bf16 v200, s58, v40\n"
"v_dot2c_f32_bf16 v201, s59, v40\n"
"v_dot2c_f32_bf16 v202, s58, v41\n"
"v_dot2c_f32_bf16 v203, s59, v41\n"
"v_dot2c_f32_bf16 v204, s58, v42\n"
"v_dot2c_f32_bf16 v205, s59, v42\n"
"v_dot2c_f32_bf16 v206, s58, v43\n"
"v_dot2c_f32_bf16 v207, s59, v43\n"
"s_waitcnt vmcnt(17)\n"
"v_dot2c_f32_bf16 v208, s58, v44\n"
"v_dot2c_f32_bf16 v209, s59, v44\n"
"v_dot2c_f32_bf16 v210, s58, v45\n"
"v_dot2c_f32_bf16 v211, s59, v45\n"
"v_dot2c_f32_bf16 v212, s58, v46\n"
"v_dot2c_f32_bf16 v213, s59, v46\n"
"v_dot2c_f32_bf16 v214, s58, v47\n"
"v_dot2c_f32_bf16 v215, s59, v47\n"
"s_waitcnt vmcnt(16)\n"
"v_dot2c_f32_bf16 v208, s58, v48\n"
"v_dot2c_f32_bf16 v209, s59, v48\n"
"v_dot2c_f32_bf16 v210, s58, v49\n"
"v_dot2c_f32_bf16 v211, s59, v49\n"
"v_dot2c_f32_bf16 v212, s58, v50\n"
"v_dot2c_f32_bf16 v213, s59, v50\n"
"v_dot2c_f32_bf16 v214, s58, v51\n"
"v_dot2c_f32_bf16 v215, s59, v51\n"
"s_waitcnt vmcnt(15)\n"
"v_dot2c_f32_bf16 v208, s58, v52\n"
"v_dot2c_f32_bf16 v209, s59, v52\n"
"v_dot2c_f32_bf16 v210, s58, v53\n"
"v_dot2c_f32_bf16 v211, s59, v53\n"
"v_dot2c_f32_bf16 v212, s58, v54\n"
"v_dot2c_f32_bf16 v213, s59, v54\n"
"v_dot2c_f32_bf16 v214, s58, v55\n"
"v_dot2c_f32_bf16 v215, s59, v55\n"
"s_waitcnt vmcnt(14)\n"
"v_dot2c_f32_bf16 v216, s58, v56\n"
"v_dot2c_f32_bf16 v217, s59, v56\n"
"v_dot2c_f32_bf16 v218, s58, v57\n"
"v_dot2c_f32_bf16 v219, s59, v57\n"
"v_dot2c_f32_bf16 v220, s58, v58\n"
"v_dot2c_f32_bf16 v221, s59, v58\n"
"v_dot2c_f32_bf16 v222, s58, v59\n"
"v_dot2c_f32_bf16 v223, s59, v59\n"
"s_waitcnt vmcnt(13)\n"
"v_dot2c_f32_bf16 v216, s58, v60\n"
"v_dot2c_f32_bf16 v217, s59, v60\n"
"v_dot2c_f32_bf16 v218, s58, v61\n"
"v_dot2c_f32_bf16 v219, s59, v61\n"
"v_dot2c_f32_bf16 v220, s58, v62\n"
"v_dot2c_f32_bf16 v221, s59, v62\n"
"v_dot2c_f32_bf16 v222, s58, v63\n"
"v_dot2c_f32_bf16 v223, s59, v63\n"
"s_waitcnt vmcnt(12)\n"
"v_dot2c_f32_bf16 v216, s58, v64\n"
"v_dot2c_f32_bf16 v217, s59, v64\n"
"v_dot2c_f32_bf16 v218, s58, v65\n"
"v_dot2c_f32_bf16 v219, s59, v65\n"
"v_dot2c_f32_bf16 v220, s58, v66\n"
"v_dot2c_f32_bf16 v221, s59, v66\n"
"v_dot2c_f32_bf16 v222, s58, v67\n"
"v_dot2c_f32_bf16 v223, s59, v67\n"
"s_waitcnt vmcnt(11)\n"
"v_dot2c_f32_bf16 v224, s58, v68\n"
"v_dot2c_f32_bf16 v225, s59, v68\n"
"v_dot2c_f32_bf16 v226, s58, v69\n"
"v_dot2c_f32_bf16 v227, s59, v69\n"
"v_dot2c_f32_bf16 v228, s58, v70\n"
"v_dot2c_f32_bf16 v229, s59, v70\n"
"v_dot2c_f32_bf16 v230, s58, v71\n"
"v_dot2c_f32_bf16 v231, s59, v71\n"
"s_waitcnt vmcnt(10)\n"
"v_dot2c_f32_bf16 v224, s58, v72\n"
"v_dot2c_f32_bf16 v225, s59, v72\n"
"v_dot2c_f32_bf16 v226, s58, v73\n"
"v_dot2c_f32_bf16 v227, s59, v73\n"
"v_dot2c_f32_bf16 v228, s58, v74\n"
"v_dot2c_f32_bf16 v229, s59, v74\n"
"v_dot2c_f32_bf16 v230, s58, v75\n"
"v_dot2c_f32_bf16 v231, s59, v75\n"
"s_waitcnt vmcnt(9)\n"
"v_dot2c_f32_bf16 v224, s58, v76\n"
"v_dot2c_f32_bf16 v225, s59, v76\n"
"v_dot2c_f32_bf16 v226, s58, v77\n"
"v_dot2c_f32_bf16 v227, s59, v77\n"
"v_dot2c_f32_bf16 v228, s58, v78\n"
"v_dot2c_f32_bf16 v229, s59, v78\n"
"v_dot2c_f32_bf16 v230, s58, v79\n"
"v_dot2c_f32_bf16 v231, s59, v79\n"
"s_waitcnt vmcnt(8)\n"
"v_dot2c_f32_bf16 v232, s58, v80\n"
"v_dot2c_f32_bf16 v233, s59, v80\n"
"v_dot2c_f32_bf16 v234, s58, v81\n"
"v_dot2c_f32_bf16 v235, s59, v81\n"
"v_dot2c_f32_bf16 v236, s58, v82\n"
"v_dot2c_f32_bf16 v237, s59, v82\n"
"v_dot2c_f32_bf16 v238, s58, v83\n"
"v_dot2c_f32_bf16 v239, s59, v83\n"
"s_waitcnt vmcnt(7)\n"
"v_dot2c_f32_bf16 v232, s58, v84\n"
"v_dot2c_f32_bf16 v233, s59, v84\n"
"v_dot2c_f32_bf16 v234, s58, v85\n"
"v_dot2c_f32_bf16 v235, s59, v85\n"
"v_dot2c_f32_bf16 v236, s58, v86\n"
"v_dot2c_f32_bf16 v237, s59, v86\n"
"v_dot2c_f32_bf16 v238, s58, v87\n"
"v_dot2c_f32_bf16 v239, s59, v87\n"
"s_waitcnt vmcnt(6)\n"
"v_dot2c_f32_bf16 v232, s58, v88\n"
"v_dot2c_f32_bf16 v233, s59, v88\n"
"v_dot2c_f32_bf16 v234, s58, v89\n"
"v_dot2c_f32_bf16 v235, s59, v89\n"
"v_dot2c_f32_bf16 v236, s58, v90\n"
"v_dot2c_f32_bf16 v237, s59, v90\n"
"v_dot2c_f32_bf16 v238, s58, v91\n"
"v_dot2c_f32_bf16 v239, s59, v91\n"
"s_waitcnt vmcnt(5)\n"
"v_dot2c_f32_bf16 v240, s58, v92\n"
"v_dot2c_f32_bf16 v241, s59, v92\n"
"v_dot2c_f32_bf16 v242, s58, v93\n"
"v_dot2c_f32_bf16 v243, s59, v93\n"
"v_dot2c_f32_bf16 v244, s58, v94\n"
"v_dot2c_f32_bf16 v245, s59, v94\n"
"v_dot2c_f32_bf16 v246, s58, v95\n"
"v_dot2c_f32_bf16 v247, s59, v95\n"
"s_waitcnt vmcnt(4)\n"
"v_dot2c_f32_bf16 v240, s58, v96\n"
"v_dot2c_f32_bf16 v241, s59, v96\n"
"v_dot2c_f32_bf16 v242, s58, v97\n"
"v_dot2c_f32_bf16 v243, s59, v97\n"
"v_dot2c_f32_bf16 v244, s58, v98\n"
"v_dot2c_f32_bf16 v245, s59, v98\n"
"v_dot2c_f32_bf16 v246, s58, v99\n"
"v_dot2c_f32_bf16 v247, s59, v99\n"
"s_waitcnt vmcnt(3)\n"
"v_dot2c_f32_bf16 v240, s58, v100\n"
"v_dot2c_f32_bf16 v241, s59, v100\n"
"v_dot2c_f32_bf16 v242, s58, v101\n"
"v_dot2c_f32_bf16 v243, s59, v101\n"
"v_dot2c_f32_bf16 v244, s58, v102\n"
"v_dot2c_f32_bf16 v245, s59, v102\n"
"v_dot2c_f32_bf16 v246, s58, v103\n"
"v_dot2c_f32_bf16 v247, s59, v103\n"
"s_waitcnt vmcnt(2)\n"
"v_dot2c_f32_bf16 v248, s58, v104\n"
"v_dot2c_f32_bf16 v249, s59, v104\n"
"v_dot2c_f32_bf16 v250, s58, v105\n"
"v_dot2c_f32_bf16 v251, s59, v105\n"
"v_dot2c_f32_bf16 v252, s58, v106\n"
"v_dot2c_f32_bf16 v253, s59, v106\n"
"v_dot2c_f32_bf16 v254, s58, v107\n"
"v_dot2c_f32_bf16 v255, s59, v107\n"
"s_waitcnt vmcnt(1)\n"
"v_dot2c_f32_bf16 v248, s58, v108\n"
"v_dot2c_f32_bf16 v249, s59, v108\n"
"v_dot2c_f32_bf16 v250, s58, v109\n"
"v_dot2c_f32_bf16 v251, s59, v109\n"
"v_dot2c_f32_bf16 v252, s58, v110\n"
"v_dot2c_f32_bf16 v253, s59, v110\n"
"v_dot2c_f32_bf16 v254, s58, v111\n"
"v_dot2c_f32_bf16 v255, s59, v111\n"
"s_waitcnt vmcnt(0)\n"
"v_dot2c_f32_bf16 v248, s58, v112\n"
"v_dot2c_f32_bf16 v249, s59, v112\n"
"v_dot2c_f32_bf16 v250, s58, v113\n"
"v_dot2c_f32_bf16 v251, s59, v113\n"
"v_dot2c_f32_bf16 v252, s58, v114\n"
"v_dot2c_f32_bf16 v253, s59, v114\n"
"v_dot2c_f32_bf16 v254, s58, v115\n"
"v_dot2c_f32_bf16 v255, s59, v115\n"
"s_nop 2\n"
"v_cvt_pk_bf16_f32 v200, v200, v201\n"
"v_cvt_pk_bf16_f32 v201, v202, v203\n"
"v_cvt_pk_bf16_f32 v204, v204, v205\n"
"v_cvt_pk_bf16_f32 v205, v206, v207\n"
"v_cvt_pk_bf16_f32 v208, v208, v209\n"
"v_cvt_pk_bf16_f32 v209, v210, v211\n"
"v_cvt_pk_bf16_f32 v212, v212, v213\n"
"v_cvt_pk_bf16_f32 v213, v214, v215\n"
"v_cvt_pk_bf16_f32 v216, v216, v217\n"
"v_cvt_pk_bf16_f32 v217, v218, v219\n"
"v_cvt_pk_bf16_f32 v220, v220, v221\n"
"v_cvt_pk_bf16_f32 v221, v222, v223\n"
"v_cvt_pk_bf16_f32 v224, v224, v225\n"
"v_cvt_pk_bf16_f32 v225, v226, v227\n"
"v_cvt_pk_bf16_f32 v228, v228, v229\n"
"v_cvt_pk_bf16_f32 v229, v230, v231\n"
"v_cvt_pk_bf16_f32 v232, v232, v233\n"
"v_cvt_pk_bf16_f32 v233, v234, v235\n"
"v_cvt_pk_bf16_f32 v236, v236, v237\n"
"v_cvt_pk_bf16_f32 v237, v238, v239\n"
"v_cvt_pk_bf16_f32 v240, v240, v241\n"
"v_cvt_pk_bf16_f32 v241, v242, v243\n"
"v_cvt_pk_bf16_f32 v244, v244, v245\n"
"v_cvt_pk_bf16_f32 v245, v246, v247\n"
"v_cvt_pk_bf16_f32 v248, v248, v249\n"
"v_cvt_pk_bf16_f32 v249, v250, v251\n"
"v_cvt_pk_bf16_f32 v252, v252, v253\n"
"v_cvt_pk_bf16_f32 v253, v254, v255\n"
"buffer_store_dwordx2 v[200:201], v10, s[52:55], 0 offen offset:0\n"
"buffer_store_dwordx2 v[204:205], v10, s[52:55], 0 offen offset:32\n"
"buffer_store_dwordx2 v[208:209], v10, s[52:55], 0 offen offset:64\n"
"buffer_store_dwordx2 v[212:213], v10, s[52:55], 0 offen offset:96\n"
"buffer_store_dwordx2 v[216:217], v10, s[52:55], 0 offen offset:128\n"
"buffer_store_dwordx2 v[220:221], v10, s[52:55], 0 offen offset:160\n"
"buffer_store_dwordx2 v[224:225], v10, s[52:55], 0 offen offset:192\n"
"buffer_store_dwordx2 v[228:229], v10, s[52:55], s57 offen offset:0\n"
"buffer_store_dwordx2 v[232:233], v10, s[52:55], s57 offen offset:32\n"
"buffer_store_dwordx2 v[236:237], v10, s[52:55], s57 offen offset:64\n"
"buffer_store_dwordx2 v[240:241], v10, s[52:55], s57 offen offset:96\n"
"buffer_store_dwordx2 v[244:245], v10, s[52:55], s57 offen offset:128\n"
"buffer_store_dwordx2 v[248:249], v10, s[52:55], s57 offen offset:160\n"
"buffer_store_dwordx2 v[252:253], v10, s[52:55], s57 offen offset:192\n"
"L_k3e%=:\n"
      ::[a] "s"(ws + OFF_CTX + (size_t)blk0 * CSTR + kq * 47104), [w] "s"(ws + OFF_WF + kq * (46 * 28672) + cb * 7168),
      [x] "s"(ws + OFF_X1 + (blk0 * 28 + cb * 7) * 1024), [p] "s"(ws + OFF_P + tile * (16 * 7168)), [o] "s"(out + blk0 * 14336 + cb * 224),
      [fl] "s"(ws + OFF_FL + (256 + tile * 4) * 128), [cn] "s"(ws + OFF_FL + (192 + (tile >> 2)) * 128), [kq] "s"(kq), [sm] "s"((u32)(size_t)sm), [wv] "s"(wvi)
      : "v1","v2","v3","v4","v5","v6","v7","v8","v9","v10","v11","v12","v13","v14","v15","v16","v17","v18","v19","v20","v21","v22","v23","v24","v25","v26","v27","v28","v29","v30","v31","v32","v33","v34","v35","v36","v37","v38","v39","v40","v41","v42","v43","v44","v45","v46","v47","v48","v49","v50","v51","v52","v53","v54","v55","v56","v57","v58","v59","v60","v61","v62","v63","v64","v65","v66","v67","v68","v69","v70","v71","v72","v73","v74","v75","v76","v77","v78","v79","v80","v81","v82","v83","v84","v85","v86","v87","v88","v89","v90","v91","v92","v93","v94","v95","v96","v97","v98","v99","v100","v101","v102","v103","v104","v105","v106","v107","v108","v109","v110","v111","v112","v113","v114","v115","v116","v117","v118","v119","v120","v121","v122","v123","v124","v125","v126","v127","v128","v129","v130","v131","v132","v133","v134","v135","v136","v137","v138","v139","v140","v141","v142","v143","v144","v145","v146","v147","v148","v149","v150","v151","v152","v153","v154","v155","v156","v157","v158","v159","v160","v161","v162","v163","v164","v165","v166","v167","v168","v169","v170","v171","v172","v173","v174","v175","v176","v177","v178","v179","v180","v181","v182","v183","v184","v185","v186","v187","v188","v189","v190","v191","v192","v193","v194","v195","v196","v197","v198","v199","v200","v201","v202","v203","v204","v205","v206","v207","v208","v209","v210","v211","v212","v213","v214","v215","v216","v217","v218","v219","v220","v221","v222","v223","v224","v225","v226","v227","v228","v229","v230","v231","v232","v233","v234","v235","v236","v237","v238","v239","v240","v241","v242","v243","v244","v245","v246","v247","v248","v249","v250","v251","v252","v253","v254","v255","a0","a1","a2","a3","a4","a5","a6","a7","a8","a9","a10","a11","a12","a13","a14","a15","a16","a17","a18","a19","a20","a21","a22","a23","a24","a25","a26","a27","a28","a29","a30","a31","a32","a33","a34","a35","a36","a37","a38","a39","a40","a41","a42","a43","a44","a45","a46","a47","a48","a49","a50","a51","a52","a53","a54","a55","a56","a57","a58","a59","a60","a61","a62","a63","a64","a65","a66","a67","a68","a69","a70","a71","a72","a73","a74","a75","a76","a77","a78","a79","a80","a81","a82","a83","a84","a85","a86","a87","a88","a89","a90","a91","a92","a93","a94","a95","a96","a97","a98","a99","a100","a101","a102","a103","a104","a105","a106","a107","a108","a109","a110","a111","a112","a113","a114","a115","a116","a117","a118","a119","a120","a121","a122","a123","a124","a125","a126","a127","a128","a129","a130","a131","a132","a133","a134","a135","a136","a137","a138","a139","a140","a141","a142","a143","a144","a145","a146","a147","a148","a149","a150","a151","a152","a153","a154","a155","a156","a157","a158","a159","a160","a161","a162","a163","a164","a165","a166","a167","a168","a169","a170","a171","a172","a173","a174","a175","a176","a177","a178","a179","a180","a181","a182","a183","a184","a185","a186","a187","a188","a189","a190","a191","a192","a193","a194","a195","a196","a197","a198","a199","a200","a201","a202","a203","a204","a205","a206","a207","a208","a209","a210","a211","a212","a213","a214","a215","a216","a217","a218","a219","a220","a221","a222","a223","a224","a225","a226","a227","a228","a229","a230","a231","a232","a233","a234","a235","a236","a237","a238","a239","a240","a241","a242","a243","a244","a245","a246","a247","a248","a249","a250","a251","a252","a253","a254","a255","s36","s37","s38","s39","s40","s41","s42","s43","s44","s45","s46","s47","s48","s49","s50","s51","s52","s53","s54","s55","s56","s57","s58","s59","s60","s61","s62","s63","s64","s65","s66","s67","s68","s69","s70","s71","s72","s73","s74","s75","s76","s77","s78","s79","s80","s81","s82","s83","s84","s85","s86","s87","memory","vcc","scc");
}


extern "C" void kernel_launch(void* const* d_in, const int* in_sizes, int n_in, void* d_out, int out_size, void* d_ws, size_t ws_size,
                              hipStream_t stream) {
  const char* const* in = (const char* const*)d_in;
  char* ws = (char*)d_ws;
  k1<<<256, 256, 0, stream>>>(in[0], in[1], in[2], in[3], in[4], in[7], in[8], in[11], in[5], in[9], in[10], ws);
  k2<<<256, 256, 0, stream>>>(in[0], ws, (char*)d_out);
}
```
